```python
import torch

def _softmax(x, axis=-1):
    return torch.softmax(x, dim=axis)

def _gelu(x):
    return torch.nn.functional.gelu(x, approximate='tanh')

def _silu(x):
    return torch.nn.functional.silu(x)

def _sigmoid(x):
    return torch.sigmoid(x)

def _layernorm(x, axis=-1, eps=1e-05):
    m = x.mean(dim=axis, keepdim=True)
    v = x.var(dim=axis, keepdim=True, unbiased=False)
    return (x - m) / torch.sqrt(v + eps)

def _scatter_add(tgt, idx, src, axis=0):
    return tgt.clone().index_add_(axis, idx, src)

def _segment_sum(src, seg, n):
    out = torch.zeros((n,) + src.shape[1:], dtype=src.dtype)
    return out.index_add_(0, seg, src)

def _pad(x, pw):
    flat = tuple((v for pair in reversed(pw) for v in pair))
    return torch.nn.functional.pad(x, flat)
kernel_name = 'adaLN_cond__ssm_conv_s4__mixer_gmlp__prompt'

def setup_inputs():

    _w = lambda a: (lambda t: t.to(torch.bfloat16) if t.is_floating_point() else t)(torch.as_tensor(a))

    def _A_setup():
        B, L, D, N = (1, 2048, 512, 128)
        A_re = -(lambda _x: torch.exp(torch.as_tensor(_x)))((lambda *s: torch.randn(*s, dtype=torch.bfloat16) * 3.2)(D, N))
        A_im = torch.pi * (lambda *s: torch.randn(*s, dtype=torch.bfloat16) * 0.2)(D, N)
        B_re = (lambda *s: torch.randn(*s, dtype=torch.bfloat16) * 0.2)(D, N)
        B_im = (lambda *s: torch.randn(*s, dtype=torch.bfloat16) * 0.2)(D, N)
        C_re = (lambda *s: torch.randn(*s, dtype=torch.bfloat16) * 0.2)(D, N)
        C_im = (lambda *s: torch.randn(*s, dtype=torch.bfloat16) * 0.2)(D, N)
        log_dt = (lambda *s: torch.randn(*s, dtype=torch.bfloat16) * 0.2)(D) * 0.5 - 4.0
        Dskip = (lambda *s: torch.randn(*s, dtype=torch.bfloat16) * 0.2)(D)
        return {'u': _w((lambda *s: torch.randn(*s, dtype=torch.bfloat16) * 0.05)(B, L, D)), 'A_re': _w(A_re), 'A_im': _w(A_im), 'B_re': _w(B_re), 'B_im': _w(B_im), 'C_re': _w(C_re), 'C_im': _w(C_im), 'log_dt': _w(log_dt), 'Dskip': _w(Dskip)}
    _A_in = _A_setup()

    def _B_setup():
        B, S, D, F = (1, 2048, 512, 1792)
        Fh = F // 2
        return {'x': _w((lambda *s: torch.randn(*s, dtype=torch.bfloat16) * 0.05)(B, S, D)), 'w_in': _w((lambda *s: torch.randn(*s, dtype=torch.bfloat16) * 0.2)(D, 2 * Fh)), 'w_sp': _w(0.01 * (lambda *s: torch.randn(*s, dtype=torch.bfloat16) * 0.2)(S, S)), 'b_sp': _w(1.0 + 0.01 * (lambda *s: torch.randn(*s, dtype=torch.bfloat16) * 0.05)(S)), 'w_out': _w((lambda *s: torch.randn(*s, dtype=torch.bfloat16) * 0.05)(Fh, D))}
    _B_in = _B_setup()
    return {'A_u': _A_in['u'], 'A_A_re': _A_in['A_re'], 'A_A_im': _A_in['A_im'], 'A_B_re': _A_in['B_re'], 'A_B_im': _A_in['B_im'], 'A_C_re': _A_in['C_re'], 'A_C_im': _A_in['C_im'], 'A_log_dt': _A_in['log_dt'], 'A_Dskip': _A_in['Dskip'], 'B_x': _B_in['x'], 'B_w_in': _B_in['w_in'], 'B_w_sp': _B_in['w_sp'], 'B_b_sp': _B_in['b_sp'], 'B_w_out': _B_in['w_out'], 'c': _w((lambda *s: torch.randn(*s, dtype=torch.bfloat16) * 0.1)(1, 512) * 8.0), 'w_mod': _w((lambda *s: torch.randn(*s, dtype=torch.bfloat16) * 0.1)(512, 6 * 512) * 512 ** (-0.5) * 8.0)}

def reference(A_u, A_A_re, A_A_im, A_B_re, A_B_im, A_C_re, A_C_im, A_log_dt, A_Dskip, B_x, B_w_in, B_w_sp, B_b_sp, B_w_out, c, w_mod):

    def _A_ref(u, A_re, A_im, B_re, B_im, C_re, C_im, log_dt, Dskip):
        Bsz, L, D = u.shape
        dt = torch.exp(log_dt)[:, None]
        A = A_re + 1j * A_im
        Bc = B_re + 1j * B_im
        Cc = C_re + 1j * C_im
        logdA = dt * A
        dB = dt * Bc
        l = (lambda x, dt: x.to(dt))(torch.arange(L), A_re.dtype)
        P = torch.exp(logdA[:, :, None] * l[None, None, :])
        K = torch.einsum('dn,dnl,dn->dl', Cc, P, dB).real
        L2 = 2 * L
        Kp = _pad(K, ((0, 0), (0, L)))
        up = (lambda x, *d: x.permute(*d))(u, 0, 2, 1)
        up = _pad(up, ((0, 0), (0, 0), (0, L)))
        Kf = (lambda x, axes=(-2, -1): torch.fft.rfft2(x.float(), dim=axes))(Kp[:, None, :], axes=(-2, -1))
        Uf = (lambda x, axes=(-2, -1): torch.fft.rfft2(x.float(), dim=axes))(up[:, :, None, :], axes=(-2, -1))
        Yf = Uf * Kf[None, :, :, :]
        y = (lambda x, s, axes=(-2, -1): torch.fft.irfft2(x, s=s, dim=axes).to(torch.bfloat16))(Yf, (1, L2), axes=(-2, -1))[:, :, 0, :L]
        y = (lambda x, *d: x.permute(*d))(y, 0, 2, 1) + u * Dskip[None, None, :]
        return y

    def _B_ref(x, w_in, w_sp, b_sp, w_out):
        B, S, D = x.shape
        F = w_in.shape[-1]
        Fh = F // 2
        z = _gelu(torch.einsum('bsd,df->bsf', x, w_in))
        z1 = z[..., :Fh].clone()
        z2 = _layernorm(z[..., Fh:], axis=-1)
        g = torch.einsum('ts,bsf->btf', w_sp, z2) + b_sp[None, :, None]
        return torch.einsum('bsf,fd->bsd', z1 * g, w_out)
    x = A_u
    mod = (_silu(c) @ w_mod).reshape(c.shape[0], 6, 1, -1)
    sa, ca, ga, sb, cb, gb = (mod[:, 0].clone(), mod[:, 1].clone(), mod[:, 2].clone(), mod[:, 3].clone(), mod[:, 4].clone(), mod[:, 5].clone())
    h = _layernorm(x, axis=-1) * (1 + ca) + sa
    x = x + ga * _A_ref(h, A_A_re, A_A_im, A_B_re, A_B_im, A_C_re, A_C_im, A_log_dt, A_Dskip)
    h = _layernorm(x, axis=-1) * (1 + cb) + sb
    x = x + gb * _B_ref(h, B_w_in, B_w_sp, B_b_sp, B_w_out)
    return x
```

```cpp
#include <hip/hip_runtime.h>
#include <hip/hip_bf16.h>
typedef unsigned short u16;
__device__ __forceinline__ float bf2f(u16 x){ return __uint_as_float(((unsigned)x)<<16); }
__device__ __forceinline__ u16 f2bf(float f){ __hip_bfloat16 b = __float2bfloat16(f); return *(u16*)&b; }
__device__ __forceinline__ float rb(float f){ return bf2f(f2bf(f)); }
constexpr int L=2048, D=512, NS=128, F=1792, FH=896;

__global__ void k_mod(const u16* c, const u16* wmod, float* mod){
  int j = blockIdx.x*blockDim.x+threadIdx.x;
  float acc=0;
  for(int i=0;i<D;i++){ float x=bf2f(c[i]); float s=rb(x/(1.f+expf(-x))); acc+=s*bf2f(wmod[i*3072+j]); }
  mod[j]=rb(acc);
}
__device__ float block_sum(float v, float* sh){
  for(int o=32;o>0;o>>=1) v+=__shfl_xor(v,o);
  int w=threadIdx.x>>6; __syncthreads(); if((threadIdx.x&63)==0) sh[w]=v; __syncthreads();
  float s=0; for(int i=0;i<(int)(blockDim.x>>6);i++) s+=sh[i]; return s;
}
__global__ void k_ln(const u16* x, const float* shift, const float* scale, u16* h){
  __shared__ float sh[16];
  int t=blockIdx.x; int d=threadIdx.x;
  float v=bf2f(x[t*D+d]);
  float m=block_sum(v,sh)/D; float dv=v-m; float var=block_sum(dv*dv,sh)/D;
  float mb=rb(m), vb=rb(var);
  float sd=rb(sqrtf(rb(vb+1e-5f)));
  float xh=rb(rb(v-mb)/sd);
  h[t*D+d]=f2bf(rb(xh*rb(1.f+scale[d]))+shift[d]);
}
__global__ void k_kgen(const u16* Are,const u16* Aim,const u16* Bre,const u16* Bim,const u16* Cre,const u16* Cim,const u16* logdt,float* K){
  int d=blockIdx.y; int l=blockIdx.x*blockDim.x+threadIdx.x;
  float dt=rb(expf(bf2f(logdt[d])));
  float lf=rb((float)l);
  float acc=0;
  for(int n=0;n<NS;n++){
    float ar=dt*bf2f(Are[d*NS+n]), ai=dt*bf2f(Aim[d*NS+n]);
    float br=dt*bf2f(Bre[d*NS+n]), bi=dt*bf2f(Bim[d*NS+n]);
    float cr=bf2f(Cre[d*NS+n]), ci=bf2f(Cim[d*NS+n]);
    float wr=cr*br-ci*bi, wi=cr*bi+ci*br;
    float e=expf(ar*lf); float s,c; sincosf(ai*lf,&s,&c);
    acc+=e*(wr*c-wi*s);
  }
  K[d*L+l]=acc;
}
__global__ void k_conv(const u16* x,const u16* h,const float* K,const u16* Dskip,const float* ga,u16* x1){
  int t=blockIdx.x; int d=threadIdx.x;
  float acc=0;
  for(int j=0;j<=t;j++) acc+=K[d*L+j]*bf2f(h[(t-j)*D+d]);
  float y=rb(rb(acc)+rb(bf2f(h[t*D+d])*bf2f(Dskip[d])));
  x1[t*D+d]=f2bf(bf2f(x[t*D+d])+rb(ga[d]*y));
}
template<int MODE>
__global__ void k_gemm(const u16* A,const u16* B,int M,int N,int K,u16* C,const u16* e0,const u16* e1,const float* e2){
  __shared__ float sa[16][17], sb[16][17];
  int tx=threadIdx.x, ty=threadIdx.y; int r=blockIdx.y*16+ty, c=blockIdx.x*16+tx;
  float acc=0;
  for(int k=0;k<K;k+=16){
    sa[ty][tx]=bf2f(A[r*K+k+tx]); sb[ty][tx]=bf2f(B[(k+ty)*N+c]); __syncthreads();
    for(int i=0;i<16;i++) acc+=sa[ty][i]*sb[i][tx];
    __syncthreads();
  }
  if(MODE==0){ float z=rb(acc); float g=0.5f*z*(1.f+tanhf(0.7978845608028654f*(z+0.044715f*z*z*z))); C[r*N+c]=f2bf(g);}
  if(MODE==1){ float g=rb(rb(acc)+bf2f(e0[r])); C[r*N+c]=f2bf(bf2f(e1[r*F+c])*g);}
  if(MODE==2){ C[r*N+c]=f2bf(bf2f(e0[r*N+c])+rb(e2[c]*rb(acc))); }
}
__global__ void k_ln2(const u16* z,u16* zn){
  __shared__ float sh[16];
  int t=blockIdx.x; int f=threadIdx.x;
  float v= f<FH? bf2f(z[t*F+FH+f]):0.f;
  float m=block_sum(v,sh)/FH; float dv=f<FH? v-m:0.f; float var=block_sum(dv*dv,sh)/FH;
  float mb=rb(m), vb=rb(var); float sd=rb(sqrtf(rb(vb+1e-5f)));
  if(f<FH) zn[t*FH+f]=f2bf(rb(v-mb)/sd);
}
extern "C" void kernel_launch(void* const* d_in, const int* in_sizes, int n_in, void* d_out, int out_size, void* d_ws, size_t ws_size, hipStream_t stream){
  const u16* u=(const u16*)d_in[0]; const u16* Are=(const u16*)d_in[1]; const u16* Aim=(const u16*)d_in[2];
  const u16* Bre=(const u16*)d_in[3]; const u16* Bim=(const u16*)d_in[4]; const u16* Cre=(const u16*)d_in[5]; const u16* Cim=(const u16*)d_in[6];
  const u16* logdt=(const u16*)d_in[7]; const u16* Dskip=(const u16*)d_in[8];
  const u16* win=(const u16*)d_in[10]; const u16* wsp=(const u16*)d_in[11]; const u16* bsp=(const u16*)d_in[12]; const u16* wout=(const u16*)d_in[13];
  const u16* c=(const u16*)d_in[14]; const u16* wmod=(const u16*)d_in[15];
  char* ws=(char*)d_ws;
  float* mod=(float*)ws; float* K=(float*)(ws+(1<<20)); u16* h=(u16*)(ws+(8<<20)); u16* x1=(u16*)(ws+(12<<20)); u16* h2=(u16*)(ws+(16<<20));
  u16* z=(u16*)(ws+(20<<20)); u16* zn=(u16*)(ws+(32<<20)); u16* P=(u16*)(ws+(40<<20));
  k_mod<<<12,256,0,stream>>>(c,wmod,mod);
  k_ln<<<L,D,0,stream>>>(u,mod,mod+512,h);
  k_kgen<<<dim3(8,D),256,0,stream>>>(Are,Aim,Bre,Bim,Cre,Cim,logdt,K);
  k_conv<<<L,D,0,stream>>>(u,h,K,Dskip,mod+1024,x1);
  k_ln<<<L,D,0,stream>>>(x1,mod+1536,mod+2048,h2);
  k_gemm<0><<<dim3(F/16,L/16),dim3(16,16),0,stream>>>(h2,win,L,F,D,z,nullptr,nullptr,nullptr);
  k_ln2<<<L,1024,0,stream>>>(z,zn);
  k_gemm<1><<<dim3(FH/16,L/16),dim3(16,16),0,stream>>>(wsp,zn,L,FH,L,P,bsp,z,nullptr);
  k_gemm<2><<<dim3(D/16,L/16),dim3(16,16),0,stream>>>(P,wout,L,D,FH,(u16*)d_out,x1,nullptr,mod+2560);
}
```

```cpp
#include <hip/hip_runtime.h>
#include <hip/hip_bf16.h>
#include <stdint.h>

typedef unsigned short u16;
typedef unsigned int u32;
typedef unsigned long long u64;
typedef __bf16 bf16x8 __attribute__((ext_vector_type(8)));
typedef __bf16 bf16x2 __attribute__((ext_vector_type(2)));
typedef short s16x4 __attribute__((ext_vector_type(4)));
typedef float f32x2 __attribute__((ext_vector_type(2)));
typedef float f32x4 __attribute__((ext_vector_type(4)));
typedef u32 u32x2 __attribute__((ext_vector_type(2)));
typedef u32 u32x4 __attribute__((ext_vector_type(4)));
#define LAS __attribute__((address_space(3)))
#define GAS __attribute__((address_space(1)))

constexpr int L = 2048, D = 512, NS = 128, F = 1792, FH = 896;

__device__ __forceinline__ float bf_lo(u32 w) { return __uint_as_float(w << 16); }
__device__ __forceinline__ float bf_hi(u32 w) { return __uint_as_float(w & 0xffff0000u); }
__device__ __forceinline__ float bf1(u16 h) { return __uint_as_float(((u32)h) << 16); }
__device__ __forceinline__ u32 pk_bf16(float lo, float hi) {
  f32x2 a = {lo, hi};
  bf16x2 p = __builtin_convertvector(a, bf16x2);
  return __builtin_bit_cast(u32, p);
}
__device__ __forceinline__ float rbf(float x) { return bf_lo(pk_bf16(x, x)); }

template <int CTRL, int ROWMASK = 0xf>
__device__ __forceinline__ float dpp_add(float x) {
  int y = __builtin_amdgcn_update_dpp(0, __float_as_int(x), CTRL, ROWMASK, 0xf, true);
  return x + __int_as_float(y);
}
__device__ __forceinline__ float row16_sum(float x) {
  x = dpp_add<0xB1>(x);
  x = dpp_add<0x4E>(x);
  x = dpp_add<0x141>(x);
  x = dpp_add<0x140>(x);
  return x;
}
__device__ __forceinline__ float wave_sum(float x) {
  x = row16_sum(x);
  float a = __int_as_float(__builtin_amdgcn_readlane(__float_as_int(x), 0));
  float b = __int_as_float(__builtin_amdgcn_readlane(__float_as_int(x), 16));
  float c = __int_as_float(__builtin_amdgcn_readlane(__float_as_int(x), 32));
  float d = __int_as_float(__builtin_amdgcn_readlane(__float_as_int(x), 48));
  return (a + b) + (c + d);
}
__device__ __forceinline__ s16x4 tr_read(const char LAS* p) {
  return __builtin_amdgcn_ds_read_tr16_b64_v4i16((s16x4 LAS*)p);
}
struct AArgs {
  const u16 *x, *Are, *Aim, *Bre, *Bim, *Cre, *Cim, *logdt, *c, *wmod;
  float* mod; u32* stats1; u16* xT; u16* Krev; u32x4* gran;
};
constexpr int A_PAR = 0;
constexpr int A_PART = A_PAR + 2 * 128 * 16;
constexpr int A_KC = A_PART + 2 * 4 * 3 * 256 * 4;
constexpr int A_TILE = A_KC + 2 * 768 * 4;
constexpr int A_RED = A_TILE + 8 * 1088;
constexpr int A_LDS = A_RED + 4 * 32 * 4;

__global__ __launch_bounds__(768) void kA(AArgs a) {
  __shared__ __attribute__((aligned(16))) char smem[A_LDS];
  const int wg = blockIdx.x, tid = threadIdx.x, lane = tid & 63;
  const int w = __builtin_amdgcn_readfirstlane(tid >> 6);
  if (w < 8) {
    const int ch = w >> 2, wq = w & 3, d = 2 * wg + ch, tn = tid & 255;
    f32x4* par = (f32x4*)(smem + A_PAR) + ch * 128;
    if (tn < 128) {
      const int o = d * NS + tn;
      float are = bf1(a.Are[o]), aim = bf1(a.Aim[o]), bre = bf1(a.Bre[o]), bim = bf1(a.Bim[o]);
      float cre = bf1(a.Cre[o]), cim = bf1(a.Cim[o]);
      float dt = rbf(__expf(bf1(a.logdt[d])));
      float br = dt * bre, bi = dt * bim;
      f32x4 p;
      p[0] = (dt * are) * 1.4426950408889634f;
      p[1] = (dt * aim) * 0.15915494309189535f;
      p[2] = cre * br - cim * bi;
      p[3] = cre * bi + cim * br;
      par[tn] = p;
    }
    __syncthreads();
    const int i = lane & 15, kq = lane >> 4;
    const float l0 = (float)(128 * (i >> 3) + (i & 7));
    const float l1 = (float)(i < 8 ? 128 * (2 + (i >> 2)) + 2 * (i & 3) : 128 * (4 + ((i - 8) >> 1)) + 4 * (i & 1));
    const float l2 = (float)(i <= 8 ? 128 * (8 + i) : 0);
    const float lb = (float)(8 * i);
    f32x4 acc0 = {0, 0, 0, 0}, acc1 = acc0, acc2 = acc0;
#pragma unroll
    for (int mm = 0; mm < 8; ++mm) {
      const f32x4 p = par[4 * (8 * wq + mm) + kq];
      float e, ph, c0, s0;
      e = __builtin_amdgcn_exp2f(p[0] * lb); ph = __builtin_amdgcn_fractf(p[1] * lb);
      c0 = e * __builtin_amdgcn_cosf(ph); s0 = e * __builtin_amdgcn_sinf(ph);
      const float yr = p[2] * c0 - p[3] * s0, nyi = -(p[2] * s0 + p[3] * c0);
      e = __builtin_amdgcn_exp2f(p[0] * l0); ph = __builtin_amdgcn_fractf(p[1] * l0);
      acc0 = __builtin_amdgcn_mfma_f32_16x16x4f32(e * __builtin_amdgcn_cosf(ph), yr, acc0, 0, 0, 0);
      acc0 = __builtin_amdgcn_mfma_f32_16x16x4f32(e * __builtin_amdgcn_sinf(ph), nyi, acc0, 0, 0, 0);
      e = __builtin_amdgcn_exp2f(p[0] * l1); ph = __builtin_amdgcn_fractf(p[1] * l1);
      acc1 = __builtin_amdgcn_mfma_f32_16x16x4f32(e * __builtin_amdgcn_cosf(ph), yr, acc1, 0, 0, 0);
      acc1 = __builtin_amdgcn_mfma_f32_16x16x4f32(e * __builtin_amdgcn_sinf(ph), nyi, acc1, 0, 0, 0);
      e = __builtin_amdgcn_exp2f(p[0] * l2); ph = __builtin_amdgcn_fractf(p[1] * l2);
      acc2 = __builtin_amdgcn_mfma_f32_16x16x4f32(e * __builtin_amdgcn_cosf(ph), yr, acc2, 0, 0, 0);
      acc2 = __builtin_amdgcn_mfma_f32_16x16x4f32(e * __builtin_amdgcn_sinf(ph), nyi, acc2, 0, 0, 0);
    }
    float* part = (float*)(smem + A_PART) + (ch * 4 + wq) * 768;
#pragma unroll
    for (int r = 0; r < 4; ++r) {
      part[(4 * kq + r) * 16 + i] = acc0[r];
      part[256 + (4 * kq + r) * 16 + i] = acc1[r];
      part[512 + (4 * kq + r) * 16 + i] = acc2[r];
    }
    __syncthreads();
    float* Kc = (float*)(smem + A_KC) + ch * 768;
    const float* pp = (const float*)(smem + A_PART) + ch * 4 * 768;
#pragma unroll
    for (int j = 0; j < 3; ++j) {
      const int idx = tn + 256 * j;
      Kc[idx] = (pp[idx] + pp[768 + idx]) + (pp[1536 + idx] + pp[2304 + idx]);
    }
    __syncthreads();
    float kv[8];
#pragma unroll
    for (int j = 0; j < 8; ++j) {
      const int l = 2047 - (8 * tn + j);
      const int lr = (int)rbf((float)l);
      const int aa = lr >> 7, b = (lr >> 3) & 15, r = lr & 7;
      const int row = lr < 256 ? 8 * aa + r : lr < 512 ? 16 + 4 * (aa - 2) + (r >> 1) : lr < 1024 ? 24 + 2 * (aa - 4) + (r >> 2) : 32 + (aa - 8);
      kv[j] = Kc[row * 16 + b];
    }
    u32x4 o = {pk_bf16(kv[0], kv[1]), pk_bf16(kv[2], kv[3]), pk_bf16(kv[4], kv[5]), pk_bf16(kv[6], kv[7])};
    *(u32x4*)(a.Krev + (size_t)d * L + 8 * tn) = o;
  } else {
    const int tw = w - 8;
    const int t0 = 8 * ((wg & 7) * 32 + (wg >> 3));
    u32x4 xr[2];
#pragma unroll
    for (int j = 0; j < 2; ++j) xr[j] = *(const u32x4*)(a.x + (size_t)(t0 + 2 * tw + j) * D + 8 * lane);
    const bool doM = wg < 96;
    u32x4 wm[8]; u16 cv[8];
    const int rr = lane >> 2, q = lane & 3;
    if (doM) {
#pragma unroll
      for (int p = 0; p < 8; ++p) {
        const int row = p * 64 + tw * 16 + rr;
        wm[p] = *(const u32x4*)(a.wmod + (size_t)row * 3072 + 32 * wg + 8 * q);
        cv[p] = a.c[row];
      }
    }
    if (tw == 0) a.gran[wg * 64 + lane] = u32x4{0, 0, 0, 0};
    char* tile = smem + A_TILE;
#pragma unroll
    for (int j = 0; j < 2; ++j) {
      float v[8];
#pragma unroll
      for (int k = 0; k < 4; ++k) { v[2 * k] = bf_lo(xr[j][k]); v[2 * k + 1] = bf_hi(xr[j][k]); }
      float s = 0;
#pragma unroll
      for (int k = 0; k < 8; ++k) s += v[k];
      const float mean = wave_sum(s) * (1.f / D);
      float q2 = 0;
#pragma unroll
      for (int k = 0; k < 8; ++k) { const float dv = v[k] - mean; q2 += dv * dv; }
      const float var = wave_sum(q2) * (1.f / D);
      const float sd = rbf(__builtin_sqrtf(rbf(rbf(var) + 1e-5f)));
      if (lane == 0) a.stats1[t0 + 2 * tw + j] = pk_bf16(mean, sd);
      *(u32x4*)(tile + (2 * tw + j) * 1088 + 16 * lane) = xr[j];
    }
    __syncthreads();
#pragma unroll
    for (int ps = 0; ps < 2; ++ps) {
      const int d0 = 128 * tw + 64 * ps + 16 * (lane >> 4);
      const char LAS* src = (const char LAS*)tile + ((lane & 15) >> 2) * 1088 + 2 * (d0 + 4 * (lane & 3));
      const s16x4 lo = tr_read(src), hi = tr_read(src + 4 * 1088);
      u32x2 a0 = __builtin_bit_cast(u32x2, lo), a1 = __builtin_bit_cast(u32x2, hi);
      *(u32x4*)(a.xT + (size_t)(d0 + (lane & 15)) * L + t0) = u32x4{a0[0], a0[1], a1[0], a1[1]};
    }
    float* red = (float*)(smem + A_RED);
    if (doM) {
      float acc[8] = {0, 0, 0, 0, 0, 0, 0, 0};
#pragma unroll
      for (int p = 0; p < 8; ++p) {
        const float cx = bf1(cv[p]);
        const float s = rbf(cx * __builtin_amdgcn_rcpf(1.f + __expf(-cx)));
#pragma unroll
        for (int k = 0; k < 4; ++k) { acc[2 * k] += s * bf_lo(wm[p][k]); acc[2 * k + 1] += s * bf_hi(wm[p][k]); }
      }
#pragma unroll
      for (int k = 0; k < 8; ++k) {
        float v = acc[k];
        v += __shfl_xor(v, 4); v += __shfl_xor(v, 8); v += __shfl_xor(v, 16); v += __shfl_xor(v, 32);
        if (rr == 0) red[tw * 32 + 8 * q + k] = v;
      }
    }
    __syncthreads();
    if (doM && tw == 0 && lane < 32)
      a.mod[32 * wg + lane] = rbf((red[lane] + red[32 + lane]) + (red[64 + lane] + red[96 + lane]));
    __syncthreads();
  }
}
struct BArgs {
  const u16 *xT, *Krev, *Dskip, *win, *wout, *wsp;
  const u32* stats1; const float* mod;
  u16 *x1T, *Wt, *Wot; float* uv;
};
constexpr int B_H = 0;
constexpr int B_HS = (256 + 2048) * 2;
constexpr int B_KC = B_H + 2 * B_HS;
constexpr int B_KS = 4160;
constexpr int B_PART = B_KC + 2 * 4 * B_KS;
constexpr int B_RED = B_PART + 2 * 4 * 8 * 1280;
constexpr int B_LDS = B_RED + 4 * 16 * 4;

__global__ __launch_bounds__(768) void kB(BArgs a) {
  extern __shared__ __attribute__((aligned(16))) char smem[];
  const int wg = blockIdx.x, tid = threadIdx.x, lane = tid & 63;
  const int w = __builtin_amdgcn_readfirstlane(tid >> 6);
  if (w < 8) {
    const int ch = w >> 2, q = w & 3, d = 2 * wg + ch, tj = tid & 255;
    const u32x4 xv = *(const u32x4*)(a.xT + (size_t)d * L + 8 * tj);
    const u32x4 st0 = *(const u32x4*)(a.stats1 + 8 * tj), st1 = *(const u32x4*)(a.stats1 + 8 * tj + 4);
    const u32x4 k0 = *(const u32x4*)(a.Krev + (size_t)d * L + 8 * tj);
    u32x4 k1 = {0, 0, 0, 0};
    if (tj < 255) k1 = *(const u32x4*)(a.Krev + (size_t)d * L + 8 * tj + 8);
    const float sa = a.mod[d], Ac = rbf(1.f + a.mod[512 + d]), ga = a.mod[1024 + d], dsk = bf1(a.Dskip[d]);
    u32x4 hv;
    {
      float hh[8];
#pragma unroll
      for (int k = 0; k < 8; ++k) {
        const u32 s = k < 4 ? st0[k] : st1[k - 4];
        const float xx = (k & 1) ? bf_hi(xv[k >> 1]) : bf_lo(xv[k >> 1]);
        const float sc = __builtin_amdgcn_rcpf(bf_hi(s)) * Ac;
        hh[k] = (xx - bf_lo(s)) * sc + sa;
      }
      hv = u32x4{pk_bf16(hh[0], hh[1]), pk_bf16(hh[2], hh[3]), pk_bf16(hh[4], hh[5]), pk_bf16(hh[6], hh[7])};
    }
    char* hb = smem + B_H + ch * B_HS;
    *(u32x4*)(hb + 512 + 16 * tj) = hv;
    if (tj < 32) *(u32x4*)(hb + 16 * tj) = u32x4{0, 0, 0, 0};
    char* kc = smem + B_KC + ch * 4 * B_KS;
    {
      const u32 wd[8] = {k0[0], k0[1], k0[2], k0[3], k1[0], k1[1], k1[2], k1[3]};
      u32 od[6];
#pragma unroll
      for (int k = 0; k < 6; ++k) od[k] = __builtin_amdgcn_alignbit(wd[k + 1], wd[k], 16);
      *(u32x4*)(kc + 0 * B_KS + 16 * tj) = u32x4{wd[0], wd[1], wd[2], wd[3]};
      *(u32x4*)(kc + 1 * B_KS + 16 * tj) = u32x4{od[0], od[1], od[2], od[3]};
      *(u32x4*)(kc + 2 * B_KS + 16 * tj) = u32x4{wd[1], wd[2], wd[3], wd[4]};
      *(u32x4*)(kc + 3 * B_KS + 16 * tj) = u32x4{od[1], od[2], od[3], od[4]};
      if (tj < 16) *(u32x4*)(kc + (tj >> 2) * B_KS + 16 * (256 + (tj & 3))) = u32x4{0, 0, 0, 0};
    }
    __syncthreads();
    const int c = lane & 15, g = lane >> 4;
    const char* bbase = hb + 2 * (256 + 16 * c - 16 * (g >> 1) + 8 * (g & 1) - 32 * q) - 256;
    const int X = 2047 - 32 * q - 16 * (g >> 1) + 8 * (g & 1) - c;
    const char* abase = kc + (X & 3) * B_KS + 2 * (X & ~3) - 256 * 15;
    bf16x8 Bf[16];
#pragma unroll
    for (int v = 0; v < 16; ++v) Bf[v] = *(const bf16x8*)(bbase + 256 * v);
    f32x4 acc[8];
#pragma unroll
    for (int i = 0; i < 8; ++i) acc[i] = f32x4{0, 0, 0, 0};
#pragma unroll
    for (int u = 0; u < 16; ++u) {
      const u32x2 lo = *(const u32x2*)(abase + 256 * (15 - u)), hi = *(const u32x2*)(abase + 256 * (15 - u) + 8);
      const bf16x8 Af = __builtin_bit_cast(bf16x8, u32x4{lo[0], lo[1], hi[0], hi[1]});
#pragma unroll
      for (int i = u >> 1; i < 8; ++i) acc[i] = __builtin_amdgcn_mfma_f32_16x16x32_bf16(Af, Bf[2 * i - u + 1], acc[i], 0, 0, 0);
    }
    char* part = smem + B_PART + ch * (4 * 8 * 1280);
#pragma unroll
    for (int i = 0; i < 8; ++i) *(f32x4*)(part + (q * 8 + i) * 1280 + 16 * (20 * g + c)) = acc[i];
    __syncthreads();
    {
      const int i = tj >> 5, j5 = tj & 31, cc = j5 >> 1;
      float y[8];
#pragma unroll
      for (int hh = 0; hh < 2; ++hh) {
        const char* p = part + i * 1280 + 16 * (20 * (2 * (j5 & 1) + hh) + cc);
        const f32x4 p0 = *(const f32x4*)(p), p1 = *(const f32x4*)(p + 8 * 1280), p2 = *(const f32x4*)(p + 16 * 1280), p3 = *(const f32x4*)(p + 24 * 1280);
#pragma unroll
        for (int r = 0; r < 4; ++r) y[4 * hh + r] = (p0[r] + p1[r]) + (p2[r] + p3[r]);
      }
      float o[8];
#pragma unroll
      for (int k = 0; k < 8; ++k) {
        const float hx = (k & 1) ? bf_hi(hv[k >> 1]) : bf_lo(hv[k >> 1]);
        const float xx = (k & 1) ? bf_hi(xv[k >> 1]) : bf_lo(xv[k >> 1]);
        const float ya = rbf(rbf(y[k]) + rbf(hx * dsk));
        o[k] = xx + rbf(ga * ya);
      }
      *(u32x4*)(a.x1T + (size_t)d * L + 8 * tj) = u32x4{pk_bf16(o[0], o[1]), pk_bf16(o[2], o[3]), pk_bf16(o[4], o[5]), pk_bf16(o[6], o[7])};
    }
  } else {
    const int st = tid - 512, sw = w - 8;
    u32x4 pf[8];
#pragma unroll
    for (int j = 0; j < 8; ++j) pf[j] = *(const u32x4*)(a.wsp + (size_t)wg * 16384 + j * 2048 + st * 8);
    float* red = (float*)(smem + B_RED);
    if (wg < 224) {
      const int f0 = 8 * ((wg & 7) * 28 + (wg >> 3));
      const int d8 = st >> 2, fp = st & 3;
      u32 wv[8];
#pragma unroll
      for (int j = 0; j < 8; ++j) wv[j] = *(const u32*)(a.win + (size_t)(8 * d8 + j) * F + f0 + 2 * fp);
      const f32x4 cb0 = *(const f32x4*)(a.mod + 2048 + 8 * d8), cb1 = *(const f32x4*)(a.mod + 2048 + 8 * d8 + 4);
      const f32x4 sb0 = *(const f32x4*)(a.mod + 1536 + 8 * d8), sb1 = *(const f32x4*)(a.mod + 1536 + 8 * d8 + 4);
      const int f8 = wg >> 1, dp = (wg & 1) * 128 + (st & 127);
      u32 ov[8];
      if (st < 128) {
#pragma unroll
        for (int j = 0; j < 8; ++j) ov[j] = *(const u32*)(a.wout + (size_t)(8 * f8 + j) * D + 2 * dp);
      }
      float lo[8], hi[8], u0 = 0, u1 = 0, v0 = 0, v1 = 0;
#pragma unroll
      for (int j = 0; j < 8; ++j) {
        const float Ac = rbf(1.f + (j < 4 ? cb0[j] : cb1[j - 4])), sb = j < 4 ? sb0[j] : sb1[j - 4];
        const float wl = bf_lo(wv[j]), wh = bf_hi(wv[j]);
        lo[j] = rbf(Ac * wl); hi[j] = rbf(Ac * wh);
        u0 += lo[j]; u1 += hi[j]; v0 += sb * wl; v1 += sb * wh;
      }
      const int f = f0 + 2 * fp;
      u16* dst = a.Wt + ((size_t)((f >> 4) * 64 + d8) * 16 + (f & 15)) * 8;
      *(u32x4*)dst = u32x4{pk_bf16(lo[0], lo[1]), pk_bf16(lo[2], lo[3]), pk_bf16(lo[4], lo[5]), pk_bf16(lo[6], lo[7])};
      *(u32x4*)(dst + 8) = u32x4{pk_bf16(hi[0], hi[1]), pk_bf16(hi[2], hi[3]), pk_bf16(hi[4], hi[5]), pk_bf16(hi[6], hi[7])};
      float rv[4] = {u0, u1, v0, v1};
#pragma unroll
      for (int k = 0; k < 4; ++k) {
        float v = rv[k];
        v += __shfl_xor(v, 4); v += __shfl_xor(v, 8); v += __shfl_xor(v, 16); v += __shfl_xor(v, 32);
        if (lane < 4) red[sw * 16 + lane * 4 + k] = v;
      }
      if (st < 128) {
        const int dd = 2 * dp;
        u16* od = a.Wot + ((size_t)((dd >> 4) * 112 + f8) * 16 + (dd & 15)) * 8;
        u32x4 e, o;
#pragma unroll
        for (int k = 0; k < 4; ++k) {
          e[k] = (ov[2 * k] & 0xffffu) | (ov[2 * k + 1] << 16);
          o[k] = (ov[2 * k] >> 16) | (ov[2 * k + 1] & 0xffff0000u);
        }
        *(u32x4*)od = e; *(u32x4*)(od + 8) = o;
      }
    }
    __syncthreads();
    if (wg < 224 && st < 16) {
      const int f0 = 8 * ((wg & 7) * 28 + (wg >> 3));
      const float s = (red[st] + red[16 + st]) + (red[32 + st] + red[48 + st]);
      const int fp = st >> 2, k = st & 3;
      a.uv[(k >> 1) * F + f0 + 2 * fp + (k & 1)] = s;
    }
    __syncthreads();
#pragma unroll
    for (int j = 0; j < 8; ++j) asm volatile("" ::"v"(pf[j]));
  }
}
struct CArgs {
  const u16 *x1T, *Wt; const float* uv;
  u16 *z1t, *z2t; u64* gran;
};
constexpr int C_X = 0;
constexpr int C_ST = 65536;
constexpr int C_PS = C_ST + 512;
constexpr int C_FS = C_PS + 7 * 64 * 8;
constexpr int C_ZS = C_FS + 512;
constexpr int C_RS = 232;
constexpr int C_LDS = C_ZS + 64 * C_RS;

__device__ __forceinline__ float gelu_tanh(float z) {
  const float k0 = -2.f * 0.7978845608028654f * 1.4426950408889634f, k1 = k0 * 0.044715f;
  const float e = __builtin_amdgcn_exp2f(z * (k0 + k1 * (z * z)));
  return z * __builtin_amdgcn_rcpf(1.f + e);
}
#define WAIT_VM(n) __builtin_amdgcn_s_waitcnt(((n) & 15) | (7 << 4) | (15 << 8) | (((n) >> 4) << 14))

__global__ __launch_bounds__(512) void kC(CArgs a) {
  extern __shared__ __attribute__((aligned(16))) char smem[];
  const int wg = blockIdx.x, tid = threadIdx.x, lane = tid & 63;
  const int w = __builtin_amdgcn_readfirstlane(tid >> 6);
  const int xc = wg & 7, ii = wg >> 3;
  const int pi = (xc >> 1) * 8 + (ii & 7), j = (xc & 1) * 4 + (ii >> 3);
  const int c = lane & 15, g = lane >> 4;
#pragma unroll
  for (int i = 0; i < 8; ++i) {
    const int d = 64 * w + 8 * i + (lane >> 3);
    const int fz = (((d >> 1) & 1) | ((d >> 2) & 2)) << 1;
    __builtin_amdgcn_global_load_lds((const u32*)(a.x1T + (size_t)d * L + 64 * pi + 8 * ((lane & 7) ^ fz)),
                                     (u32*)(smem + C_X + (64 * w + 8 * i) * 128), 16, 0, 0);
  }
  const int nt2 = 56 + 7 * j + (w < 7 ? w : 0), nt1 = 7 * j + (w > 0 ? w - 1 : 0);
  bf16x8 W2[16], W1[16];
  if (w < 7) {
#pragma unroll
    for (int ks = 0; ks < 16; ++ks) W2[ks] = *(const bf16x8*)(a.Wt + ((size_t)nt2 * 64 + 4 * ks) * 128 + lane * 8);
  }
  if (w > 0) {
#pragma unroll
    for (int ks = 0; ks < 16; ++ks) W1[ks] = *(const bf16x8*)(a.Wt + ((size_t)nt1 * 64 + 4 * ks) * 128 + lane * 8);
  }
  const f32x4 u2 = *(const f32x4*)(a.uv + 16 * nt2 + 4 * g), v2 = *(const f32x4*)(a.uv + F + 16 * nt2 + 4 * g);
  const f32x4 u1 = *(const f32x4*)(a.uv + 16 * nt1 + 4 * g), v1 = *(const f32x4*)(a.uv + F + 16 * nt1 + 4 * g);
  if (w == 0) WAIT_VM(20); else if (w == 7) WAIT_VM(20); else WAIT_VM(36);
  __builtin_amdgcn_s_barrier();

  const int q4 = (lane & 15) >> 2, p4 = lane & 3;
  const int drow = 8 * g + q4;
  const int fz0 = (((drow >> 1) & 1) | ((drow >> 2) & 2)) << 1;
  const char LAS* xb = (const char LAS*)smem + C_X + drow * 128 + 8 * (p4 & 1);
  auto xfrag = [&](int ks, int tt) -> bf16x8 {
    const char LAS* p = xb + ks * 4096 + 16 * ((2 * tt + (p4 >> 1)) ^ fz0);
    const s16x4 lo = tr_read(p), hi = tr_read(p + 512);
    const u32x2 a0 = __builtin_bit_cast(u32x2, lo), a1 = __builtin_bit_cast(u32x2, hi);
    return __builtin_bit_cast(bf16x8, u32x4{a0[0], a0[1], a1[0], a1[1]});
  };
  f32x4 acc[4];
#pragma unroll
  for (int tt = 0; tt < 4; ++tt) acc[tt] = f32x4{0, 0, 0, 0};
  {
    f32x4 sacc = {0, 0, 0, 0};
    const int stt = w >> 1, kind = w & 1;
    const u32x4 ones = {0x3f803f80u, 0x3f803f80u, 0x3f803f80u, 0x3f803f80u};
#pragma unroll
    for (int ks = 0; ks < 16; ++ks) {
      const bf16x8 xs = xfrag(ks, stt);
      const bf16x8 ao = kind ? xs : __builtin_bit_cast(bf16x8, ones);
      sacc = __builtin_amdgcn_mfma_f32_16x16x32_bf16(ao, xs, sacc, 0, 0, 0);
      if (w < 7) {
#pragma unroll
        for (int tt = 0; tt < 4; ++tt) acc[tt] = __builtin_amdgcn_mfma_f32_16x16x32_bf16(W2[ks], xfrag(ks, tt), acc[tt], 0, 0, 0);
      }
    }
    float* xs2 = (float*)(smem + C_ST);
    if (kind == 0) { if (g == 0) xs2[16 * stt + c] = sacc[0]; }
    else if ((c >> 2) == g) { const int r = c & 3; xs2[64 + 16 * stt + c] = r == 0 ? sacc[0] : r == 1 ? sacc[1] : r == 2 ? sacc[2] : sacc[3]; }
  }
  __syncthreads();
  float mu[4], ri[4];
  {
    const float* xs2 = (const float*)(smem + C_ST);
#pragma unroll
    for (int tt = 0; tt < 4; ++tt) {
      const float mean = xs2[16 * tt + c] * (1.f / D);
      const float var = fmaxf(xs2[64 + 16 * tt + c] * (1.f / D) - mean * mean, 0.f);
      mu[tt] = rbf(mean);
      ri[tt] = __builtin_amdgcn_rcpf(rbf(__builtin_sqrtf(rbf(rbf(var) + 1e-5f))));
    }
  }
  u32 z2p[4][2];
  if (w < 7) {
    float* ps = (float*)(smem + C_PS) + w * 128;
#pragma unroll
    for (int tt = 0; tt < 4; ++tt) {
      float z[4];
#pragma unroll
      for (int r = 0; r < 4; ++r) z[r] = gelu_tanh(rbf(ri[tt] * (acc[tt][r] - mu[tt] * u2[r]) + v2[r]));
      z2p[tt][0] = pk_bf16(z[0], z[1]); z2p[tt][1] = pk_bf16(z[2], z[3]);
      const float a0 = bf_lo(z2p[tt][0]), a1 = bf_hi(z2p[tt][0]), a2 = bf_lo(z2p[tt][1]), a3 = bf_hi(z2p[tt][1]);
      float s1 = (a0 + a1) + (a2 + a3), s2 = (a0 * a0 + a1 * a1) + (a2 * a2 + a3 * a3);
      s1 += __shfl_xor(s1, 16); s2 += __shfl_xor(s2, 16);
      s1 += __shfl_xor(s1, 32); s2 += __shfl_xor(s2, 32);
      if (g == 0) *(f32x2*)(ps + (16 * tt + c) * 2) = f32x2{s1, s2};
    }
  }
  __syncthreads();
  if (w == 0) {
    const float* ps = (const float*)(smem + C_PS);
    float s1 = 0, s2 = 0;
#pragma unroll
    for (int k = 0; k < 7; ++k) { const f32x2 v = *(const f32x2*)(ps + k * 128 + lane * 2); s1 += v[0]; s2 += v[1]; }
    GAS u64* gp = (GAS u64*)a.gran + (size_t)pi * 8 * 128;
    __hip_atomic_store(gp + (j * 64 + lane) * 2, (1ull << 32) | __float_as_uint(s1), __ATOMIC_RELAXED, __HIP_MEMORY_SCOPE_AGENT);
    __hip_atomic_store(gp + (j * 64 + lane) * 2 + 1, (1ull << 32) | __float_as_uint(s2), __ATOMIC_RELAXED, __HIP_MEMORY_SCOPE_AGENT);
    float t1 = 0, t2 = 0;
    for (u32 spin = 0; spin < (1u << 22); ++spin) {
      bool ok = true; t1 = 0; t2 = 0;
#pragma unroll
      for (int k = 0; k < 8; ++k) {
        const u64 x0 = __hip_atomic_load(gp + (k * 64 + lane) * 2, __ATOMIC_RELAXED, __HIP_MEMORY_SCOPE_AGENT);
        const u64 x1 = __hip_atomic_load(gp + (k * 64 + lane) * 2 + 1, __ATOMIC_RELAXED, __HIP_MEMORY_SCOPE_AGENT);
        ok = ok && (x0 >> 32) == 1 && (x1 >> 32) == 1;
        t1 += __uint_as_float((u32)x0); t2 += __uint_as_float((u32)x1);
      }
      if (__all(ok)) break;
      __builtin_amdgcn_s_sleep(2);
    }
    const float mean = t1 * (1.f / FH);
    const float var = fmaxf(t2 * (1.f / FH) - mean * mean, 0.f);
    *(f32x2*)(smem + C_FS + lane * 8) = f32x2{rbf(mean), __builtin_amdgcn_rcpf(rbf(__builtin_sqrtf(rbf(rbf(var) + 1e-5f))))};
  } else {
#pragma unroll
    for (int tt = 0; tt < 4; ++tt) acc[tt] = f32x4{0, 0, 0, 0};
#pragma unroll
    for (int ks = 0; ks < 16; ++ks) {
#pragma unroll
      for (int tt = 0; tt < 4; ++tt) acc[tt] = __builtin_amdgcn_mfma_f32_16x16x32_bf16(W1[ks], xfrag(ks, tt), acc[tt], 0, 0, 0);
    }
    const int f8 = 14 * j + 2 * (w - 1) + (g >> 1);
#pragma unroll
    for (int tt = 0; tt < 4; ++tt) {
      float z[4];
#pragma unroll
      for (int r = 0; r < 4; ++r) z[r] = gelu_tanh(rbf(ri[tt] * (acc[tt][r] - mu[tt] * u1[r]) + v1[r]));
      *(u32x2*)(a.z1t + ((size_t)(4 * pi + tt) * 112 + f8) * 128 + c * 8 + (g & 1) * 4) = u32x2{pk_bf16(z[0], z[1]), pk_bf16(z[2], z[3])};
    }
  }
  __syncthreads();
  if (w < 7) {
#pragma unroll
    for (int tt = 0; tt < 4; ++tt) {
      const f32x2 fs = *(const f32x2*)(smem + C_FS + (16 * tt + c) * 8);
      const float a0 = (bf_lo(z2p[tt][0]) - fs[0]) * fs[1], a1 = (bf_hi(z2p[tt][0]) - fs[0]) * fs[1];
      const float a2 = (bf_lo(z2p[tt][1]) - fs[0]) * fs[1], a3 = (bf_hi(z2p[tt][1]) - fs[0]) * fs[1];
      *(u32x2*)(smem + C_ZS + (16 * tt + c) * C_RS + (16 * w + 4 * g) * 2) = u32x2{pk_bf16(a0, a1), pk_bf16(a2, a3)};
    }
  }
  __syncthreads();
  if (w < 7) {
#pragma unroll
    for (int kk = 0; kk < 2; ++kk) {
      const char LAS* p = (const char LAS*)smem + C_ZS + (32 * kk + 8 * g + q4) * C_RS + (16 * w + 4 * p4) * 2;
      const s16x4 lo = tr_read(p), hi = tr_read(p + 4 * C_RS);
      const u32x2 a0 = __builtin_bit_cast(u32x2, lo), a1 = __builtin_bit_cast(u32x2, hi);
      *(u32x4*)(a.z2t + ((size_t)(7 * j + w) * 256 + 8 * pi + 4 * kk) * 128 + lane * 8) = u32x4{a0[0], a0[1], a1[0], a1[1]};
    }
  }
}
struct DArgs { const u16 *wsp, *z2t, *z1t, *bsp; u16* Pt; };
constexpr int D_RING = 37888;
constexpr int D_LDS = 4 * D_RING;

__global__ __launch_bounds__(256) void kD(DArgs a) {
  extern __shared__ __attribute__((aligned(16))) char smem[];
  const int wg = blockIdx.x, tid = threadIdx.x, lane = tid & 63;
  const int w = __builtin_amdgcn_readfirstlane(tid >> 6);
  const int xc = wg & 7, ii = wg >> 3;
  const int rt = (xc >> 1) * 8 + (ii & 7), ct = (xc & 1) * 4 + (ii >> 3);
  const int c = lane & 15, g = lane >> 4;
  char* ring = smem + w * D_RING;
  const u16* pa[2];
#pragma unroll
  for (int par = 0; par < 2; ++par) {
    const int row = 8 * par + (lane >> 3);
    pa[par] = a.wsp + (size_t)(64 * rt + row) * L + 512 * w + 8 * ((lane & 7) ^ ((row >> 1) & 7));
  }
  const u16* pb = a.z2t + ((size_t)(7 * ct) * 256 + 64 * w) * 128 + lane * 8;
  auto ldA = [&](int s) {
    char* d = ring + (s & 1) * 8192;
#pragma unroll
    for (int i = 0; i < 8; ++i)
      __builtin_amdgcn_global_load_lds((const u32*)(pa[i & 1] + (size_t)(i >> 1) * 16 * L + 64 * s), (u32*)(d + i * 1024), 16, 0, 0);
  };
  auto ldB = [&](int ks) {
    char* d = ring + 16384 + (ks % 3) * 7168;
#pragma unroll
    for (int n = 0; n < 7; ++n)
      __builtin_amdgcn_global_load_lds((const u32*)(pb + ((size_t)n * 256 + 4 * ks) * 128), (u32*)(d + n * 1024), 16, 0, 0);
  };
  f32x4 acc[4][7];
#pragma unroll
  for (int m = 0; m < 4; ++m)
#pragma unroll
    for (int n = 0; n < 7; ++n) acc[m][n] = f32x4{0, 0, 0, 0};
  ldA(0); ldB(0); ldB(1);
  const int aoff = c * 128, asw = (c >> 1) & 7;
#pragma unroll
  for (int ks = 0; ks < 16; ++ks) {
    if ((ks & 1) == 0 && ks / 2 + 1 < 8) ldA(ks / 2 + 1);
    if (ks + 2 < 16) ldB(ks + 2);
    if (ks < 14) {
      if ((ks & 1) == 0) asm volatile("s_waitcnt vmcnt(22)" ::: "memory");
      else asm volatile("s_waitcnt vmcnt(14)" ::: "memory");
    } else if (ks == 14) asm volatile("s_waitcnt vmcnt(7)" ::: "memory");
    else asm volatile("s_waitcnt vmcnt(0)" ::: "memory");
    const char* ab = ring + ((ks >> 1) & 1) * 8192 + aoff + 16 * ((4 * (ks & 1) + g) ^ asw);
    const char* bb = ring + 16384 + (ks % 3) * 7168 + lane * 16;
    bf16x8 Af[4], Bf[7];
#pragma unroll
    for (int m = 0; m < 4; ++m) Af[m] = *(const bf16x8*)(ab + m * 2048);
#pragma unroll
    for (int n = 0; n < 7; ++n) Bf[n] = *(const bf16x8*)(bb + n * 1024);
#pragma unroll
    for (int n = 0; n < 7; ++n)
#pragma unroll
      for (int m = 0; m < 4; ++m) acc[m][n] = __builtin_amdgcn_mfma_f32_16x16x32_bf16(Bf[n], Af[m], acc[m][n], 0, 0, 0);
    asm volatile("s_waitcnt lgkmcnt(0)" ::: "memory");
  }
  u32x4 z1v[4]; float bs[4];
#pragma unroll
  for (int k = 0; k < 4; ++k) {
    const int it = tid + 256 * k;
    if (it < 896) {
      const int m = it / 224, rem = it - 224 * m, f8l = rem >> 4, r = rem & 15;
      z1v[k] = *(const u32x4*)(a.z1t + ((size_t)(4 * rt + m) * 112 + 14 * ct + f8l) * 128 + r * 8);
      bs[k] = bf1(a.bsp[64 * rt + 16 * m + r]);
    }
  }
  __syncthreads();
#pragma unroll
  for (int m = 0; m < 4; ++m)
#pragma unroll
    for (int n = 0; n < 7; ++n) *(f32x4*)(smem + ((w * 28 + m * 7 + n) * 64 + lane) * 16) = acc[m][n];
  __syncthreads();
#pragma unroll
  for (int k = 0; k < 4; ++k) {
    const int it = tid + 256 * k;
    if (it < 896) {
      const int m = it / 224, rem = it - 224 * m, f8l = rem >> 4, r = rem & 15;
      float gg[8];
#pragma unroll
      for (int hh = 0; hh < 2; ++hh) {
        const char* p = smem + ((m * 7 + (f8l >> 1)) * 64 + (2 * (f8l & 1) + hh) * 16 + r) * 16;
        const f32x4 p0 = *(const f32x4*)p, p1 = *(const f32x4*)(p + 28672), p2 = *(const f32x4*)(p + 2 * 28672), p3 = *(const f32x4*)(p + 3 * 28672);
#pragma unroll
        for (int e = 0; e < 4; ++e) gg[4 * hh + e] = rbf(rbf((p0[e] + p1[e]) + (p2[e] + p3[e])) + bs[k]);
      }
      u32x4 o;
#pragma unroll
      for (int e = 0; e < 4; ++e) o[e] = pk_bf16(bf_lo(z1v[k][e]) * gg[2 * e], bf_hi(z1v[k][e]) * gg[2 * e + 1]);
      *(u32x4*)(a.Pt + ((size_t)(4 * rt + m) * 112 + 14 * ct + f8l) * 128 + r * 8) = o;
    }
  }
}
struct EArgs { const u16 *Pt, *Wot, *x1T; const float* mod; u16* out; };
constexpr int E_TS = 1152;
constexpr int E_PART = 0;
constexpr int E_X = 4 * 16 * E_TS;
constexpr int E_LDS = E_X + 64 * 132;

__global__ __launch_bounds__(256) void kE(EArgs a) {
  extern __shared__ __attribute__((aligned(16))) char smem[];
  const int wg = blockIdx.x, tid = threadIdx.x, lane = tid & 63;
  const int w = __builtin_amdgcn_readfirstlane(tid >> 6);
  const int xc = wg & 7, ii = wg >> 3;
  const int rt = xc * 4 + (ii & 3), dt = ii >> 2;
  const int c = lane & 15, g = lane >> 4;
  bf16x8 Pf[7][4], Wf[7][4];
#pragma unroll
  for (int k = 0; k < 7; ++k) {
    const int f8 = 4 * (7 * w + k);
#pragma unroll
    for (int m = 0; m < 4; ++m) Pf[k][m] = *(const bf16x8*)(a.Pt + ((size_t)(4 * rt + m) * 112 + f8) * 128 + lane * 8);
#pragma unroll
    for (int n = 0; n < 4; ++n) Wf[k][n] = *(const bf16x8*)(a.Wot + ((size_t)(4 * dt + n) * 112 + f8) * 128 + lane * 8);
  }
  u32x4 xr[2];
#pragma unroll
  for (int k = 0; k < 2; ++k) {
    const int id = tid + 256 * k, dd = id >> 3, chn = id & 7;
    xr[k] = *(const u32x4*)(a.x1T + (size_t)(64 * dt + dd) * L + 64 * rt + 8 * chn);
  }
  const int d8 = lane & 7;
  const f32x4 gb0 = *(const f32x4*)(a.mod + 2560 + 64 * dt + 8 * d8), gb1 = *(const f32x4*)(a.mod + 2560 + 64 * dt + 8 * d8 + 4);
  f32x4 acc[4][4];
#pragma unroll
  for (int m = 0; m < 4; ++m)
#pragma unroll
    for (int n = 0; n < 4; ++n) acc[m][n] = f32x4{0, 0, 0, 0};
#pragma unroll
  for (int k = 0; k < 7; ++k)
#pragma unroll
    for (int m = 0; m < 4; ++m)
#pragma unroll
      for (int n = 0; n < 4; ++n) acc[m][n] = __builtin_amdgcn_mfma_f32_16x16x32_bf16(Wf[k][n], Pf[k][m], acc[m][n], 0, 0, 0);
#pragma unroll
  for (int k = 0; k < 2; ++k) {
    const int id = tid + 256 * k, dd = id >> 3, chn = id & 7;
    u32* p = (u32*)(smem + E_X + dd * 132 + chn * 16);
    p[0] = xr[k][0]; p[1] = xr[k][1]; p[2] = xr[k][2]; p[3] = xr[k][3];
  }
#pragma unroll
  for (int m = 0; m < 4; ++m)
#pragma unroll
    for (int n = 0; n < 4; ++n) *(f32x4*)(smem + E_PART + (w * 16 + m * 4 + n) * E_TS + g * 288 + c * 16) = acc[m][n];
  __syncthreads();
#pragma unroll
  for (int k = 0; k < 2; ++k) {
    const int t = 32 * k + 8 * w + (lane >> 3);
    float s[8];
#pragma unroll
    for (int hh = 0; hh < 2; ++hh) {
      const char* p = smem + E_PART + ((t >> 4) * 4 + (d8 >> 1)) * E_TS + (2 * (d8 & 1) + hh) * 288 + (t & 15) * 16;
      const f32x4 p0 = *(const f32x4*)p, p1 = *(const f32x4*)(p + 16 * E_TS), p2 = *(const f32x4*)(p + 32 * E_TS), p3 = *(const f32x4*)(p + 48 * E_TS);
#pragma unroll
      for (int e = 0; e < 4; ++e) s[4 * hh + e] = (p0[e] + p1[e]) + (p2[e] + p3[e]);
    }
    float o[8];
#pragma unroll
    for (int e = 0; e < 8; ++e) {
      const float x1 = bf1(*(const u16*)(smem + E_X + (8 * d8 + e) * 132 + t * 2));
      o[e] = x1 + rbf((e < 4 ? gb0[e] : gb1[e - 4]) * rbf(s[e]));
    }
    *(u32x4*)(a.out + (size_t)(64 * rt + t) * D + 64 * dt + 8 * d8) = u32x4{pk_bf16(o[0], o[1]), pk_bf16(o[2], o[3]), pk_bf16(o[4], o[5]), pk_bf16(o[6], o[7])};
  }
}
extern "C" void kernel_launch(void* const* d_in, const int* in_sizes, int n_in, void* d_out, int out_size,
                              void* d_ws, size_t ws_size, hipStream_t stream) {
  const u16* x = (const u16*)d_in[0];
  char* ws = (char*)d_ws;
  float* mod = (float*)(ws + 0);
  u32* stats1 = (u32*)(ws + 16384);
  float* uv = (float*)(ws + 32768);
  u64* gran = (u64*)(ws + 65536);
  u16* xT = (u16*)(ws + (1 << 20));
  u16* Krev = (u16*)(ws + (3 << 20));
  u16* x1T = (u16*)(ws + (5 << 20));
  u16* Wt = (u16*)(ws + (7 << 20));
  u16* Wot = (u16*)(ws + (9 << 20));
  u16* z1t = (u16*)(ws + (10 << 20));
  u16* z2t = (u16*)(ws + (14 << 20));
  u16* Pt = (u16*)(ws + (18 << 20));
  AArgs aa;
  aa.x = x; aa.Are = (const u16*)d_in[1]; aa.Aim = (const u16*)d_in[2]; aa.Bre = (const u16*)d_in[3]; aa.Bim = (const u16*)d_in[4];
  aa.Cre = (const u16*)d_in[5]; aa.Cim = (const u16*)d_in[6]; aa.logdt = (const u16*)d_in[7]; aa.c = (const u16*)d_in[14];
  aa.wmod = (const u16*)d_in[15]; aa.mod = mod; aa.stats1 = stats1; aa.xT = xT; aa.Krev = Krev; aa.gran = (u32x4*)gran;
  kA<<<256, 768, 0, stream>>>(aa);
  BArgs ba;
  ba.xT = xT; ba.Krev = Krev; ba.Dskip = (const u16*)d_in[8]; ba.win = (const u16*)d_in[10]; ba.wout = (const u16*)d_in[13];
  ba.wsp = (const u16*)d_in[11]; ba.stats1 = stats1; ba.mod = mod; ba.x1T = x1T; ba.Wt = Wt; ba.Wot = Wot; ba.uv = uv;
  (void)hipFuncSetAttribute((const void*)kB, hipFuncAttributeMaxDynamicSharedMemorySize, B_LDS);
  kB<<<256, 768, B_LDS, stream>>>(ba);
  CArgs ca;
  ca.x1T = x1T; ca.Wt = Wt; ca.uv = uv; ca.z1t = z1t; ca.z2t = z2t; ca.gran = gran;
  (void)hipFuncSetAttribute((const void*)kC, hipFuncAttributeMaxDynamicSharedMemorySize, C_LDS);
  kC<<<256, 512, C_LDS, stream>>>(ca);
  DArgs da;
  da.wsp = (const u16*)d_in[11]; da.z2t = z2t; da.z1t = z1t; da.bsp = (const u16*)d_in[12]; da.Pt = Pt;
  (void)hipFuncSetAttribute((const void*)kD, hipFuncAttributeMaxDynamicSharedMemorySize, D_LDS);
  kD<<<256, 256, D_LDS, stream>>>(da);
  EArgs ea;
  ea.Pt = Pt; ea.Wot = Wot; ea.x1T = x1T; ea.mod = mod; ea.out = (u16*)d_out;
  (void)hipFuncSetAttribute((const void*)kE, hipFuncAttributeMaxDynamicSharedMemorySize, E_LDS);
  kE<<<256, 256, E_LDS, stream>>>(ea);
}
```

```cpp
#include <hip/hip_runtime.h>
#include <hip/hip_bf16.h>
#include <stdint.h>

typedef unsigned short u16;
typedef unsigned int u32;
typedef unsigned long long u64;
typedef __bf16 bf16x8 __attribute__((ext_vector_type(8)));
typedef __bf16 bf16x2 __attribute__((ext_vector_type(2)));
typedef short s16x4 __attribute__((ext_vector_type(4)));
typedef float f32x2 __attribute__((ext_vector_type(2)));
typedef float f32x4 __attribute__((ext_vector_type(4)));
typedef u32 u32x2 __attribute__((ext_vector_type(2)));
typedef u32 u32x4 __attribute__((ext_vector_type(4)));
#define LAS __attribute__((address_space(3)))
#define GAS __attribute__((address_space(1)))

constexpr int L = 2048, D = 512, NS = 128, F = 1792, FH = 896;

__device__ __forceinline__ float bf_lo(u32 w) { return __uint_as_float(w << 16); }
__device__ __forceinline__ float bf_hi(u32 w) { return __uint_as_float(w & 0xffff0000u); }
__device__ __forceinline__ float bf1(u16 h) { return __uint_as_float(((u32)h) << 16); }
__device__ __forceinline__ u32 pk_bf16(float lo, float hi) {
  f32x2 a = {lo, hi};
  bf16x2 p = __builtin_convertvector(a, bf16x2);
  return __builtin_bit_cast(u32, p);
}
__device__ __forceinline__ float rbf(float x) { return bf_lo(pk_bf16(x, x)); }

template <int CTRL, int ROWMASK = 0xf>
__device__ __forceinline__ float dpp_add(float x) {
  int y = __builtin_amdgcn_update_dpp(0, __float_as_int(x), CTRL, ROWMASK, 0xf, true);
  return x + __int_as_float(y);
}
__device__ __forceinline__ float row16_sum(float x) {
  x = dpp_add<0xB1>(x);
  x = dpp_add<0x4E>(x);
  x = dpp_add<0x141>(x);
  x = dpp_add<0x140>(x);
  return x;
}
__device__ __forceinline__ float wave_sum(float x) {
  x = row16_sum(x);
  float a = __int_as_float(__builtin_amdgcn_readlane(__float_as_int(x), 0));
  float b = __int_as_float(__builtin_amdgcn_readlane(__float_as_int(x), 16));
  float c = __int_as_float(__builtin_amdgcn_readlane(__float_as_int(x), 32));
  float d = __int_as_float(__builtin_amdgcn_readlane(__float_as_int(x), 48));
  return (a + b) + (c + d);
}
__device__ __forceinline__ s16x4 tr_read(const char LAS* p) {
  return __builtin_amdgcn_ds_read_tr16_b64_v4i16((s16x4 LAS*)p);
}

#define STAMP(kern, i)
__device__ __forceinline__ void glds16(const void* gsrc, u32 lds_dst) {
  u32 keep;
  asm volatile("s_mov_b32 %0, m0\n\ts_mov_b32 m0, %2\n\ts_nop 0\n\tglobal_load_lds_dwordx4 %1, off\n\ts_mov_b32 m0, %0"
               : "=&s"(keep) : "v"(gsrc), "s"(lds_dst) : "memory");
}
#define SCHED_FENCE() __builtin_amdgcn_sched_barrier(0)
#define VM_WAIT(n) asm volatile("s_waitcnt vmcnt(" #n ")" ::: "memory")
__device__ __forceinline__ float xor16_sum(float x) {
  const u32 v = __float_as_uint(x);
  auto r = __builtin_amdgcn_permlane16_swap(v, v, false, false);
  return __uint_as_float(r[0]) + __uint_as_float(r[1]);
}
__device__ __forceinline__ float xor32_sum(float x) {
  const u32 v = __float_as_uint(x);
  auto r = __builtin_amdgcn_permlane32_swap(v, v, false, false);
  return __uint_as_float(r[0]) + __uint_as_float(r[1]);
}
__device__ __forceinline__ float xor4_8_sum(float x) {
  x = x + __int_as_float(__builtin_amdgcn_update_dpp(0, __float_as_int(x), 0x124, 0xf, 0xf, true));
  x = x + __int_as_float(__builtin_amdgcn_update_dpp(0, __float_as_int(x), 0x128, 0xf, 0xf, true));
  return x;
}
__device__ __forceinline__ void lds_rendezvous(u32 LAS* cnt, u32 target) {
  asm volatile("s_waitcnt lgkmcnt(0)" ::: "memory");
  if ((threadIdx.x & 63) == 0) __hip_atomic_fetch_add(cnt, 1u, __ATOMIC_RELAXED, __HIP_MEMORY_SCOPE_WORKGROUP);
  while (__hip_atomic_load(cnt, __ATOMIC_RELAXED, __HIP_MEMORY_SCOPE_WORKGROUP) < target) __builtin_amdgcn_s_sleep(1);
  asm volatile("" ::: "memory");
}
struct AArgs {
  const u16 *x, *Are, *Aim, *Bre, *Bim, *Cre, *Cim, *logdt, *c, *wmod;
  float* mod; u32* stats1; u16* xT; u16* Krev; u32x4* gran;
};
constexpr int A_RS = 528;
constexpr int A_TAB = 0;
constexpr int A_KC = A_TAB + 2 * 64 * A_RS;
constexpr int A_TILE = A_KC + 2 * 768 * 4;
constexpr int A_RED = A_TILE + 8 * 1088;
constexpr int A_CNT = A_RED + 4 * 32 * 4;
constexpr int A_LDS = A_CNT + 16;

struct KIdx { u16 v[2048]; };
constexpr int bf16_round_int(int l) {
  int sh = 0;
  while ((l >> sh) >= 256) ++sh;
  if (sh == 0) return l;
  const int q = l >> sh, rem = l & ((1 << sh) - 1), half = 1 << (sh - 1);
  const int up = (rem > half || (rem == half && (q & 1))) ? 1 : 0;
  return (q + up) << sh;
}
constexpr KIdx make_kidx() {
  KIdx t{};
  for (int x = 0; x < 2048; ++x) {
    const int lr = bf16_round_int(2047 - x);
    const int aa = lr >> 7, b = (lr >> 3) & 15, r = lr & 7;
    const int row = lr < 256 ? 8 * aa + r : lr < 512 ? 16 + 4 * (aa - 2) + (r >> 1) : lr < 1024 ? 24 + 2 * (aa - 4) + (r >> 2) : 32 + (aa - 8);
    t.v[x] = (u16)(row * 16 + b);
  }
  return t;
}
__device__ const KIdx g_kidx = make_kidx();

struct cpx { float r, i; };
__device__ __forceinline__ cpx cmul(cpx a, cpx b) { return cpx{a.r * b.r - a.i * b.i, a.r * b.i + a.i * b.r}; }

__global__ __launch_bounds__(768) void kA(AArgs a) {
  extern __shared__ __attribute__((aligned(16))) char smem[];
  const int wg = blockIdx.x, tid = threadIdx.x, lane = tid & 63;
  const int w = __builtin_amdgcn_readfirstlane(tid >> 6);
  STAMP(0, 0);
  if (w < 8) {
    const int ch = w >> 2, nh = (w >> 1) & 1, part = w & 1, d = 2 * wg + ch;
    const int n = 64 * nh + lane, o = d * NS + n;
    const u16 hare = a.Are[o], haim = a.Aim[o], hbre = a.Bre[o], hbim = a.Bim[o], hcre = a.Cre[o], hcim = a.Cim[o], hdt = a.logdt[d];
    const u32x4 kix = *(const u32x4*)(g_kidx.v + 8 * (tid & 255));
    SCHED_FENCE();
    __builtin_amdgcn_s_barrier();
    SCHED_FENCE();
    const float dt = rbf(__expf(bf1(hdt)));
    const float lr = (dt * bf1(hare)) * 1.4426950408889634f, lp = (dt * bf1(haim)) * 0.15915494309189535f;
    auto cexp = [&](float l) -> cpx {
      const float e = __builtin_amdgcn_exp2f(lr * l), ph = __builtin_amdgcn_fractf(lp * l);
      return cpx{e * __builtin_amdgcn_cosf(ph), e * __builtin_amdgcn_sinf(ph)};
    };
    char* tab = smem + A_TAB + ch * (64 * A_RS) + n * 4;
    auto put = [&](int row, cpx v) { *(u32*)(tab + row * A_RS) = pk_bf16(v.r, v.i); };
    if (part == 0) {
      const cpx E1 = cexp(1.f), E128 = cexp(128.f), E512 = cexp(512.f);
      const cpx E2 = cmul(E1, E1), E3 = cmul(E2, E1), E4 = cmul(E2, E2), E5 = cmul(E4, E1), E6 = cmul(E4, E2), E7 = cmul(E4, E3);
      put(0, cpx{1.f, 0.f}); put(1, E1); put(2, E2); put(3, E3); put(4, E4); put(5, E5); put(6, E6); put(7, E7);
      put(8, E128); put(9, cmul(E128, E1)); put(10, cmul(E128, E2)); put(11, cmul(E128, E3));
      put(12, cmul(E128, E4)); put(13, cmul(E128, E5)); put(14, cmul(E128, E6)); put(15, cmul(E128, E7));
      const cpx E256 = cmul(E128, E128), E384 = cmul(E256, E128);
      put(16, E256); put(17, cmul(E256, E2)); put(18, cmul(E256, E4)); put(19, cmul(E256, E6));
      put(20, E384); put(21, cmul(E384, E2)); put(22, cmul(E384, E4)); put(23, cmul(E384, E6));
      const cpx E640 = cmul(E512, E128), E768 = cmul(E512, E256), E896 = cmul(E512, E384);
      put(24, E512); put(25, cmul(E512, E4)); put(26, E640); put(27, cmul(E640, E4));
      put(28, E768); put(29, cmul(E768, E4)); put(30, E896); put(31, cmul(E896, E4));
    } else {
      const cpx E8 = cexp(8.f), E128 = cexp(128.f), E1024 = cexp(1024.f);
      const float br = dt * bf1(hbre), bi = dt * bf1(hbim), cr = bf1(hcre), ci = bf1(hcim);
      const cpx W = cmul(cpx{cr, ci}, cpx{br, bi});
      const cpx E16 = cmul(E8, E8), E32 = cmul(E16, E16), E64 = cmul(E32, E32);
      cpx Y[8];
      Y[0] = W; Y[1] = cmul(W, E8); Y[2] = cmul(W, E16); Y[3] = cmul(Y[1], E16);
#pragma unroll
      for (int b = 0; b < 4; ++b) Y[4 + b] = cmul(Y[b], E32);
#pragma unroll
      for (int b = 0; b < 8; ++b) {
        put(48 + b, cpx{Y[b].r, -Y[b].i});
        const cpx t = cmul(Y[b], E64);
        put(56 + b, cpx{t.r, -t.i});
      }
      const cpx E256 = cmul(E128, E128), E512 = cmul(E256, E256);
      cpx P[4];
      P[0] = E1024; P[1] = cmul(E1024, E128); P[2] = cmul(E1024, E256); P[3] = cmul(P[1], E256);
#pragma unroll
      for (int b = 0; b < 4; ++b) { put(32 + b, P[b]); put(36 + b, cmul(P[b], E512)); }
      put(40, cmul(E1024, E1024));
    }
    STAMP(0, 1);
    lds_rendezvous((u32 LAS*)(smem + A_CNT), 8);
    STAMP(0, 2);
    if (w < 6) {
      const int c2 = w >= 3 ? 1 : 0, t = w - 3 * c2, i = lane & 15, g = lane >> 4;
      const char* tb = smem + A_TAB + c2 * (64 * A_RS) + 16 * g;
      const char* pa = tb + (16 * t + i) * A_RS, *pb = tb + (48 + i) * A_RS;
      f32x4 acc = {0, 0, 0, 0};
      bf16x8 Af[8], Bf[8];
#pragma unroll
      for (int s = 0; s < 8; ++s) {
        Af[s] = *(const bf16x8*)(pa + s * 64); Bf[s] = *(const bf16x8*)(pb + s * 64);
      }
#pragma unroll
      for (int s = 0; s < 8; ++s) acc = __builtin_amdgcn_mfma_f32_16x16x32_bf16(Af[s], Bf[s], acc, 0, 0, 0);
      float* Kc = (float*)(smem + A_KC) + c2 * 768 + t * 256;
#pragma unroll
      for (int r = 0; r < 4; ++r) Kc[(4 * g + r) * 16 + i] = acc[r];
    }
    STAMP(0, 3);
    lds_rendezvous((u32 LAS*)(smem + A_CNT + 4), 8);
    STAMP(0, 4);
    const float* Kc = (const float*)(smem + A_KC) + ch * 768;
    float kv[8];
#pragma unroll
    for (int j = 0; j < 4; ++j) { kv[2 * j] = Kc[kix[j] & 0xffffu]; kv[2 * j + 1] = Kc[kix[j] >> 16]; }
    *(u32x4*)(a.Krev + (size_t)d * L + 8 * (tid & 255)) =
        u32x4{pk_bf16(kv[0], kv[1]), pk_bf16(kv[2], kv[3]), pk_bf16(kv[4], kv[5]), pk_bf16(kv[6], kv[7])};
    STAMP(0, 5);
  } else {
    const int tw = w - 8;
    const int t0 = 8 * ((wg & 7) * 32 + (wg >> 3));
    u32x4 xr[2];
#pragma unroll
    for (int j = 0; j < 2; ++j) xr[j] = *(const u32x4*)(a.x + (size_t)(t0 + 2 * tw + j) * D + 8 * lane);
    const bool doM = wg < 96;
    u32x4 wm[8]; u16 cv[8];
    const int rr = lane >> 2, q = lane & 3;
    if (doM) {
#pragma unroll
      for (int p = 0; p < 8; ++p) cv[p] = a.c[p * 64 + tw * 16 + rr];
#pragma unroll
      for (int p = 0; p < 8; ++p) wm[p] = *(const u32x4*)(a.wmod + (size_t)(p * 64 + tw * 16 + rr) * 3072 + 32 * wg + 8 * q);
    }
    char* tile = smem + A_TILE;
    if (tw == 0 && lane < 4) *(u32*)(smem + A_CNT + 4 * lane) = 0;
    asm volatile("s_waitcnt lgkmcnt(0)" ::: "memory");
    SCHED_FENCE();
    __builtin_amdgcn_s_barrier();
    SCHED_FENCE();
#pragma unroll
    for (int j = 0; j < 2; ++j) *(u32x4*)(tile + (2 * tw + j) * 1088 + 16 * lane) = xr[j];
    STAMP(0, 1);
    lds_rendezvous((u32 LAS*)(smem + A_CNT + 8), 4);
    STAMP(0, 2);
#pragma unroll
    for (int ps = 0; ps < 2; ++ps) {
      const int d0 = 128 * tw + 64 * ps + 16 * (lane >> 4);
      const char LAS* src = (const char LAS*)tile + ((lane & 15) >> 2) * 1088 + 2 * (d0 + 4 * (lane & 3));
      const s16x4 lo = tr_read(src), hi = tr_read(src + 4 * 1088);
      u32x2 a0 = __builtin_bit_cast(u32x2, lo), a1 = __builtin_bit_cast(u32x2, hi);
      *(u32x4*)(a.xT + (size_t)(d0 + (lane & 15)) * L + t0) = u32x4{a0[0], a0[1], a1[0], a1[1]};
    }
    if (tw == 0) a.gran[wg * 64 + lane] = u32x4{0, 0, 0, 0};
    {
      float v[2][8], s[2];
#pragma unroll
      for (int j = 0; j < 2; ++j) {
#pragma unroll
        for (int k = 0; k < 4; ++k) { v[j][2 * k] = bf_lo(xr[j][k]); v[j][2 * k + 1] = bf_hi(xr[j][k]); }
        s[j] = ((v[j][0] + v[j][1]) + (v[j][2] + v[j][3])) + ((v[j][4] + v[j][5]) + (v[j][6] + v[j][7]));
      }
      float mean[2], q2[2];
#pragma unroll
      for (int j = 0; j < 2; ++j) mean[j] = wave_sum(s[j]) * (1.f / D);
#pragma unroll
      for (int j = 0; j < 2; ++j) {
        q2[j] = 0;
#pragma unroll
        for (int k = 0; k < 8; ++k) { const float dv = v[j][k] - mean[j]; q2[j] += dv * dv; }
      }
#pragma unroll
      for (int j = 0; j < 2; ++j) {
        const float var = wave_sum(q2[j]) * (1.f / D);
        const float sd = rbf(__builtin_amdgcn_sqrtf(rbf(rbf(var) + 1e-5f)));
        if (lane == 0) a.stats1[t0 + 2 * tw + j] = pk_bf16(mean[j], sd);
      }
    }
    float* red = (float*)(smem + A_RED);
    if (doM) {
      float acc[8] = {0, 0, 0, 0, 0, 0, 0, 0};
#pragma unroll
      for (int p = 0; p < 8; ++p) {
        const float cx = bf1(cv[p]);
        const float s = rbf(cx * __builtin_amdgcn_rcpf(1.f + __expf(-cx)));
#pragma unroll
        for (int k = 0; k < 4; ++k) { acc[2 * k] += s * bf_lo(wm[p][k]); acc[2 * k + 1] += s * bf_hi(wm[p][k]); }
      }
#pragma unroll
      for (int k = 0; k < 8; ++k) {
        float v = acc[k];
        v = xor32_sum(xor16_sum(xor4_8_sum(v)));
        if (rr == 0) red[tw * 32 + 8 * q + k] = v;
      }
      STAMP(0, 3);
      lds_rendezvous((u32 LAS*)(smem + A_CNT + 12), 4);
      STAMP(0, 4);
      if (tw == 0 && lane < 32)
        a.mod[32 * wg + lane] = rbf((red[lane] + red[32 + lane]) + (red[64 + lane] + red[96 + lane]));
    }
    STAMP(0, 5);
  }
}
struct BArgs {
  const u16 *xT, *Krev, *Dskip, *win, *wout, *wsp;
  const u32* stats1; const float* mod;
  u16 *x1T, *Wt, *Wot; float* uv;
};
constexpr int B_H = 0;
constexpr int B_HS = (256 + 2048) * 2;
constexpr int B_KC = B_H + 2 * B_HS;
constexpr int B_KS = 4160;
constexpr int B_PART = B_KC + 2 * 4 * B_KS;
constexpr int B_RED = B_PART + 2 * 4 * 8 * 1280;
constexpr int B_CNT = B_RED + 4 * 16 * 4;
constexpr int B_LDS = B_CNT + 16;

__global__ __launch_bounds__(768) void kB(BArgs a) {
  extern __shared__ __attribute__((aligned(16))) char smem[];
  const int wg = blockIdx.x, tid = threadIdx.x, lane = tid & 63;
  const int w = __builtin_amdgcn_readfirstlane(tid >> 6);
  STAMP(1, 0);
  if (w < 8) {
    const int ch = w >> 2, q = w & 3, d = 2 * wg + ch, tj = tid & 255;
    const u32x4 xv = *(const u32x4*)(a.xT + (size_t)d * L + 8 * tj);
    const u32x4 st0 = *(const u32x4*)(a.stats1 + 8 * tj), st1 = *(const u32x4*)(a.stats1 + 8 * tj + 4);
    const u32x4 k0 = *(const u32x4*)(a.Krev + (size_t)d * L + 8 * tj);
    const u32x4 k1r = *(const u32x4*)(a.Krev + (size_t)d * L + 8 * (tj < 255 ? tj + 1 : tj));
    const float sa = a.mod[d], Ac = rbf(1.f + a.mod[512 + d]), ga = a.mod[1024 + d], dsk = bf1(a.Dskip[d]);
    char* hb = smem + B_H + ch * B_HS;
    char* kc = smem + B_KC + ch * 4 * B_KS;
    if (tj < 32) *(u32x4*)(hb + 16 * tj) = u32x4{0, 0, 0, 0};
    if (tj < 16) *(u32x4*)(kc + (tj >> 2) * B_KS + 16 * (256 + (tj & 3))) = u32x4{0, 0, 0, 0};
    {
      const u32 z = tj < 255 ? 0xffffffffu : 0u;
      const u32 wd[8] = {k0[0], k0[1], k0[2], k0[3], k1r[0] & z, k1r[1] & z, 0, 0};
      u32 od[5];
#pragma unroll
      for (int k = 0; k < 5; ++k) od[k] = __builtin_amdgcn_alignbit(wd[k + 1], wd[k], 16);
      *(u32x4*)(kc + 0 * B_KS + 16 * tj) = u32x4{wd[0], wd[1], wd[2], wd[3]};
      *(u32x4*)(kc + 1 * B_KS + 16 * tj) = u32x4{od[0], od[1], od[2], od[3]};
      *(u32x4*)(kc + 2 * B_KS + 16 * tj) = u32x4{wd[1], wd[2], wd[3], wd[4]};
      *(u32x4*)(kc + 3 * B_KS + 16 * tj) = u32x4{od[1], od[2], od[3], od[4]};
    }
    u32x4 hv;
    {
      float hh[8];
#pragma unroll
      for (int k = 0; k < 8; ++k) {
        const u32 s = k < 4 ? st0[k] : st1[k - 4];
        const float xx = (k & 1) ? bf_hi(xv[k >> 1]) : bf_lo(xv[k >> 1]);
        const float sc = __builtin_amdgcn_rcpf(bf_hi(s)) * Ac;
        hh[k] = (xx - bf_lo(s)) * sc + sa;
      }
      hv = u32x4{pk_bf16(hh[0], hh[1]), pk_bf16(hh[2], hh[3]), pk_bf16(hh[4], hh[5]), pk_bf16(hh[6], hh[7])};
    }
    *(u32x4*)(hb + 512 + 16 * tj) = hv;
    STAMP(1, 1);
    __syncthreads();
    STAMP(1, 2);
    const int c = lane & 15, g = lane >> 4;
    const char* bbase = hb + 2 * (256 + 16 * c - 16 * (g >> 1) + 8 * (g & 1) - 32 * q) - 256;
    const int X = 2047 - 32 * q - 16 * (g >> 1) + 8 * (g & 1) - c;
    const char* abase = kc + (X & 3) * B_KS + 2 * (X & ~3) - 256 * 15;
    bf16x8 Bf[16], Af[16];
    auto lda = [&](int u) {
      const u32x2 lo = *(const u32x2*)(abase + 256 * (15 - u)), hi = *(const u32x2*)(abase + 256 * (15 - u) + 8);
      Af[u] = __builtin_bit_cast(bf16x8, u32x4{lo[0], lo[1], hi[0], hi[1]});
    };
    lda(0);
#pragma unroll
    for (int v = 1; v < 16; v += 2) Bf[v] = *(const bf16x8*)(bbase + 256 * v);
    lda(1);
#pragma unroll
    for (int v = 0; v < 16; v += 2) Bf[v] = *(const bf16x8*)(bbase + 256 * v);
    lda(2); lda(3);
    SCHED_FENCE();
    f32x4 acc[8];
#pragma unroll
    for (int i = 0; i < 8; ++i) acc[i] = f32x4{0, 0, 0, 0};
#pragma unroll
    for (int u = 0; u < 16; ++u) {
      if (u + 4 < 16) lda(u + 4);
      SCHED_FENCE();
#pragma unroll
      for (int i = u >> 1; i < 8; ++i) acc[i] = __builtin_amdgcn_mfma_f32_16x16x32_bf16(Af[u], Bf[2 * i - u + 1], acc[i], 0, 0, 0);
      SCHED_FENCE();
    }
    STAMP(1, 3);
    char* part = smem + B_PART + ch * (4 * 8 * 1280);
#pragma unroll
    for (int i = 0; i < 8; ++i) *(f32x4*)(part + (q * 8 + i) * 1280 + 16 * (20 * g + c)) = acc[i];
    lds_rendezvous((u32 LAS*)(smem + B_CNT + 4 * ch), 4);
    STAMP(1, 4);
    {
      const int i = tj >> 5, j5 = tj & 31, cc = j5 >> 1;
      float y[8];
#pragma unroll
      for (int hh = 0; hh < 2; ++hh) {
        const char* p = part + i * 1280 + 16 * (20 * (2 * (j5 & 1) + hh) + cc);
        const f32x4 p0 = *(const f32x4*)(p), p1 = *(const f32x4*)(p + 8 * 1280), p2 = *(const f32x4*)(p + 16 * 1280), p3 = *(const f32x4*)(p + 24 * 1280);
#pragma unroll
        for (int r = 0; r < 4; ++r) y[4 * hh + r] = (p0[r] + p1[r]) + (p2[r] + p3[r]);
      }
      float o[8];
#pragma unroll
      for (int k = 0; k < 8; ++k) {
        const float hx = (k & 1) ? bf_hi(hv[k >> 1]) : bf_lo(hv[k >> 1]);
        const float xx = (k & 1) ? bf_hi(xv[k >> 1]) : bf_lo(xv[k >> 1]);
        o[k] = xx + ga * (y[k] + hx * dsk);
      }
      *(u32x4*)(a.x1T + (size_t)d * L + 8 * tj) = u32x4{pk_bf16(o[0], o[1]), pk_bf16(o[2], o[3]), pk_bf16(o[4], o[5]), pk_bf16(o[6], o[7])};
    }
    STAMP(1, 5);
  } else {
    const int st = tid - 512, sw = w - 8;
    float* red = (float*)(smem + B_RED);
    const bool act = wg < 224;
    const int f0 = 8 * ((wg & 7) * 28 + (wg >> 3));
    const int d8 = st >> 2, fp = st & 3;
    const int f8 = wg >> 1, dp = (wg & 1) * 128 + (st & 127);
    u32 wv[8], ov[8];
    f32x4 cb0, cb1, sb0, sb1;
    if (act) {
      cb0 = *(const f32x4*)(a.mod + 2048 + 8 * d8); cb1 = *(const f32x4*)(a.mod + 2048 + 8 * d8 + 4);
      sb0 = *(const f32x4*)(a.mod + 1536 + 8 * d8); sb1 = *(const f32x4*)(a.mod + 1536 + 8 * d8 + 4);
#pragma unroll
      for (int j = 0; j < 8; ++j) wv[j] = *(const u32*)(a.win + (size_t)(8 * d8 + j) * F + f0 + 2 * fp);
      if (st < 128) {
#pragma unroll
        for (int j = 0; j < 8; ++j) ov[j] = *(const u32*)(a.wout + (size_t)(8 * f8 + j) * D + 2 * dp);
      }
    }
    u32x4 pf[16];
    {
      const u16* p = a.wsp + ((size_t)((wg & 7) >> 1) * 512 + 16 * (wg >> 3)) * L + st * 8;
#pragma unroll
      for (int j = 0; j < 16; ++j) pf[j] = *(const u32x4*)(p + j * L);
    }
    if (st < 4) *(u32*)(smem + B_CNT + 4 * st) = 0;
    asm volatile("s_waitcnt lgkmcnt(0)" ::: "memory");
    SCHED_FENCE();
    __builtin_amdgcn_s_barrier();
    SCHED_FENCE();
    if (act) {
      float lo[8], hi[8], u0 = 0, u1 = 0, v0 = 0, v1 = 0;
#pragma unroll
      for (int j = 0; j < 8; ++j) {
        const float Ac = rbf(1.f + (j < 4 ? cb0[j] : cb1[j - 4])), sb = j < 4 ? sb0[j] : sb1[j - 4];
        const float wl = bf_lo(wv[j]), wh = bf_hi(wv[j]);
        lo[j] = rbf(Ac * wl); hi[j] = rbf(Ac * wh);
        u0 += lo[j]; u1 += hi[j]; v0 += sb * wl; v1 += sb * wh;
      }
      const int f = f0 + 2 * fp;
      u16* dst = a.Wt + ((size_t)((f >> 4) * 64 + d8) * 16 + (f & 15)) * 8;
      *(u32x4*)dst = u32x4{pk_bf16(lo[0], lo[1]), pk_bf16(lo[2], lo[3]), pk_bf16(lo[4], lo[5]), pk_bf16(lo[6], lo[7])};
      *(u32x4*)(dst + 8) = u32x4{pk_bf16(hi[0], hi[1]), pk_bf16(hi[2], hi[3]), pk_bf16(hi[4], hi[5]), pk_bf16(hi[6], hi[7])};
      float rv[4] = {u0, u1, v0, v1};
#pragma unroll
      for (int k = 0; k < 4; ++k) {
        const float v = xor32_sum(xor16_sum(xor4_8_sum(rv[k])));
        if (lane < 4) red[sw * 16 + lane * 4 + k] = v;
      }
      if (st < 128) {
        const int dd = 2 * dp;
        u16* od = a.Wot + ((size_t)((dd >> 4) * 112 + f8) * 16 + (dd & 15)) * 8;
        u32x4 e, o;
#pragma unroll
        for (int k = 0; k < 4; ++k) {
          e[k] = (ov[2 * k] & 0xffffu) | (ov[2 * k + 1] << 16);
          o[k] = (ov[2 * k] >> 16) | (ov[2 * k + 1] & 0xffff0000u);
        }
        *(u32x4*)od = e; *(u32x4*)(od + 8) = o;
      }
      lds_rendezvous((u32 LAS*)(smem + B_CNT + 8), 4);
      if (st < 16) {
        const float s = (red[st] + red[16 + st]) + (red[32 + st] + red[48 + st]);
        const int fq = st >> 2, k = st & 3;
        a.uv[(k >> 1) * F + f0 + 2 * fq + (k & 1)] = s;
      }
    }
    STAMP(1, 1);
#pragma unroll
    for (int j = 0; j < 16; ++j) asm volatile("" ::"v"(pf[j]));
    STAMP(1, 5);
  }
}
struct CArgs {
  const u16 *x1T, *Wt, *wsp; const float* uv;
  u16 *z1t, *z2t; u64* gran;
};
constexpr int C_X = 0;
constexpr int C_ST = 65536;
constexpr int C_PS = C_ST + 512;
constexpr int C_FS = C_PS + 7 * 64 * 8;
constexpr int C_ZS = C_FS + 512;
constexpr int C_RS = 232;
constexpr int C_PF = C_ZS + 64 * C_RS;
constexpr int C_LDS = C_PF + 1024;

__device__ __forceinline__ float gelu_tanh(float z) {
  const float k0 = -2.f * 0.7978845608028654f * 1.4426950408889634f, k1 = k0 * 0.044715f;
  const float e = __builtin_amdgcn_exp2f(z * (k0 + k1 * (z * z)));
  return z * __builtin_amdgcn_rcpf(1.f + e);
}
__device__ __forceinline__ float dot2_bf16(u32 a, u32 b, float c) {
  return __builtin_amdgcn_fdot2_f32_bf16(__builtin_bit_cast(bf16x2, a), __builtin_bit_cast(bf16x2, b), c, false);
}

template <int ROLE>
__device__ __forceinline__ void c_body(const CArgs& a, char* smem, const int w) {
  const int wg = blockIdx.x, tid = threadIdx.x, lane = tid & 63;
  const int xc = wg & 7, ii = wg >> 3;
  const int pi = (xc >> 1) * 8 + (ii & 7), j = (xc & 1) * 4 + (ii >> 3);
  const int c = lane & 15, g = lane >> 4;
  STAMP(2, 0);
  const u32 lds0 = (u32)(size_t)(const char LAS*)smem;
#pragma unroll
  for (int i = 0; i < 8; ++i) {
    const int d = 64 * w + 8 * i + (lane >> 3);
    const int fz = (((d >> 1) & 1) | ((d >> 2) & 2)) << 1;
    glds16(a.x1T + (size_t)d * L + 64 * pi + 8 * ((lane & 7) ^ fz), lds0 + C_X + (64 * w + 8 * i) * 128);
  }
  __builtin_amdgcn_s_barrier();
  const int nt2 = 56 + 7 * j + (w < 7 ? w : 0), nt1 = 7 * j + (w > 0 ? w - 1 : 0);
  const u16* pw2 = a.Wt + (size_t)nt2 * 64 * 128 + lane * 8;
  const u16* pw1 = a.Wt + (size_t)nt1 * 64 * 128 + lane * 8;

  const int q4 = (lane & 15) >> 2, p4 = lane & 3;
  const int drow = 8 * g + q4;
  const int fz0 = (((drow >> 1) & 1) | ((drow >> 2) & 2)) << 1;
  const char LAS* xb = (const char LAS*)smem + C_X + drow * 128 + 8 * (p4 & 1);
  const char LAS* xt[4];
#pragma unroll
  for (int tt = 0; tt < 4; ++tt) xt[tt] = xb + 16 * ((2 * tt + (p4 >> 1)) ^ fz0);
  auto xfrag = [&](int tt, int ks) -> bf16x8 {
    const char LAS* p = xt[tt] + ks * 4096;
    const s16x4 lo = tr_read(p), hi = tr_read(p + 512);
    const u32x2 a0 = __builtin_bit_cast(u32x2, lo), a1 = __builtin_bit_cast(u32x2, hi);
    return __builtin_bit_cast(bf16x8, u32x4{a0[0], a0[1], a1[0], a1[1]});
  };
  f32x4 acc[4];
#pragma unroll
  for (int tt = 0; tt < 4; ++tt) acc[tt] = f32x4{0, 0, 0, 0};
  bf16x8 W1[16];
  f32x4 u2, v2, u1, v1;
  float* xs2 = (float*)(smem + C_ST);
  if constexpr (ROLE < 2) {
    bf16x8 W2[16];
#pragma unroll
    for (int ks = 0; ks < 16; ++ks) W2[ks] = *(const bf16x8*)(pw2 + 4 * ks * 128);
    u2 = *(const f32x4*)(a.uv + 16 * nt2 + 4 * g); v2 = *(const f32x4*)(a.uv + F + 16 * nt2 + 4 * g);
    SCHED_FENCE();
    VM_WAIT(18);
    __builtin_amdgcn_s_barrier();
    SCHED_FENCE();
    STAMP(2, 1);
    bf16x8 X[2][4];
#pragma unroll
    for (int tt = 0; tt < 4; ++tt) X[0][tt] = xfrag(tt, 0);
#pragma unroll
    for (int ks = 0; ks < 16; ++ks) {
      if (ks + 1 < 16) {
#pragma unroll
        for (int tt = 0; tt < 4; ++tt) X[(ks + 1) & 1][tt] = xfrag(tt, ks + 1);
      }
      if constexpr (ROLE == 1) W1[ks] = *(const bf16x8*)(pw1 + 4 * ks * 128);
      SCHED_FENCE();
#pragma unroll
      for (int tt = 0; tt < 4; ++tt) acc[tt] = __builtin_amdgcn_mfma_f32_16x16x32_bf16(W2[ks], X[ks & 1][tt], acc[tt], 0, 0, 0);
      SCHED_FENCE();
    }
    if constexpr (ROLE == 1) { u1 = *(const f32x4*)(a.uv + 16 * nt1 + 4 * g); v1 = *(const f32x4*)(a.uv + F + 16 * nt1 + 4 * g); }
  } else {
    VM_WAIT(0);
    __builtin_amdgcn_s_barrier();
    SCHED_FENCE();
    STAMP(2, 1);
    const u32x4 ones = {0x3f803f80u, 0x3f803f80u, 0x3f803f80u, 0x3f803f80u};
    float sq[4] = {0, 0, 0, 0};
    bf16x8 X[2][4];
#pragma unroll
    for (int tt = 0; tt < 4; ++tt) X[0][tt] = xfrag(tt, 0);
#pragma unroll
    for (int ks = 0; ks < 16; ++ks) {
      if (ks + 1 < 16) {
#pragma unroll
        for (int tt = 0; tt < 4; ++tt) X[(ks + 1) & 1][tt] = xfrag(tt, ks + 1);
      }
      W1[ks] = *(const bf16x8*)(pw1 + 4 * ks * 128);
      SCHED_FENCE();
#pragma unroll
      for (int tt = 0; tt < 4; ++tt) {
        acc[tt] = __builtin_amdgcn_mfma_f32_16x16x32_bf16(__builtin_bit_cast(bf16x8, ones), X[ks & 1][tt], acc[tt], 0, 0, 0);
        const u32x4 xw = __builtin_bit_cast(u32x4, X[ks & 1][tt]);
#pragma unroll
        for (int e = 0; e < 4; ++e) sq[tt] = dot2_bf16(xw[e], xw[e], sq[tt]);
        asm volatile("" : "+v"(sq[tt]));
      }
      SCHED_FENCE();
    }
    u1 = *(const f32x4*)(a.uv + 16 * nt1 + 4 * g); v1 = *(const f32x4*)(a.uv + F + 16 * nt1 + 4 * g);
#pragma unroll
    for (int tt = 0; tt < 4; ++tt) {
      const float s2 = xor32_sum(xor16_sum(sq[tt]));
      if (g == 0) { xs2[16 * tt + c] = acc[tt][0]; xs2[64 + 16 * tt + c] = s2; }
    }
  }
  STAMP(2, 2);
  __syncthreads();
  STAMP(2, 3);
  float mu[4], ri[4];
#pragma unroll
  for (int tt = 0; tt < 4; ++tt) {
    const float mean = xs2[16 * tt + c] * (1.f / D);
    const float var = fmaxf(xs2[64 + 16 * tt + c] * (1.f / D) - mean * mean, 0.f);
    mu[tt] = rbf(mean);
    ri[tt] = __builtin_amdgcn_rcpf(rbf(__builtin_amdgcn_sqrtf(rbf(rbf(var) + 1e-5f))));
  }
  u32x2 z2p[4];
  if constexpr (ROLE < 2) {
    float* ps = (float*)(smem + C_PS) + w * 128;
    const s16x4 ones4 = {0x3f80, 0x3f80, 0x3f80, 0x3f80};
#pragma unroll
    for (int tt = 0; tt < 4; ++tt) {
      float z[4];
#pragma unroll
      for (int r = 0; r < 4; ++r) z[r] = gelu_tanh(ri[tt] * (acc[tt][r] - mu[tt] * u2[r]) + v2[r]);
      z2p[tt] = u32x2{pk_bf16(z[0], z[1]), pk_bf16(z[2], z[3])};
      const s16x4 zz = __builtin_bit_cast(s16x4, z2p[tt]);
      const f32x4 zero = {0, 0, 0, 0};
      const f32x4 s1 = __builtin_amdgcn_mfma_f32_16x16x16bf16_1k(ones4, zz, zero, 0, 0, 0);
      const f32x4 s2 = __builtin_amdgcn_mfma_f32_16x16x16bf16_1k(zz, zz, zero, 0, 0, 0);
      if (g == 0) ps[(16 * tt + c) * 2] = s1[0];
      if ((c >> 2) == g) { const int r = c & 3; ps[(16 * tt + c) * 2 + 1] = r == 0 ? s2[0] : r == 1 ? s2[1] : r == 2 ? s2[2] : s2[3]; }
    }
  }
  STAMP(2, 4);
  __syncthreads();
  STAMP(2, 5);
  if constexpr (ROLE == 0) {
    const float* ps = (const float*)(smem + C_PS);
    float s1 = 0, s2 = 0;
#pragma unroll
    for (int k = 0; k < 7; ++k) { const f32x2 v = *(const f32x2*)(ps + k * 128 + lane * 2); s1 += v[0]; s2 += v[1]; }
    GAS u64* gp = (GAS u64*)a.gran + (size_t)pi * 8 * 128;
    __hip_atomic_store(gp + (j * 64 + lane) * 2, (1ull << 32) | __float_as_uint(s1), __ATOMIC_RELAXED, __HIP_MEMORY_SCOPE_AGENT);
    __hip_atomic_store(gp + (j * 64 + lane) * 2 + 1, (1ull << 32) | __float_as_uint(s2), __ATOMIC_RELAXED, __HIP_MEMORY_SCOPE_AGENT);
    float t1 = 0, t2 = 0;
    for (u32 spin = 0; spin < (1u << 22); ++spin) {
      bool ok = true; t1 = 0; t2 = 0;
#pragma unroll
      for (int k = 0; k < 8; ++k) {
        const u64 x0 = __hip_atomic_load(gp + (k * 64 + lane) * 2, __ATOMIC_RELAXED, __HIP_MEMORY_SCOPE_AGENT);
        const u64 x1 = __hip_atomic_load(gp + (k * 64 + lane) * 2 + 1, __ATOMIC_RELAXED, __HIP_MEMORY_SCOPE_AGENT);
        ok = ok && (x0 >> 32) == 1 && (x1 >> 32) == 1;
        t1 += __uint_as_float((u32)x0); t2 += __uint_as_float((u32)x1);
      }
      if (__all(ok)) break;
      __builtin_amdgcn_s_sleep(2);
    }
    const float mean = t1 * (1.f / FH);
    const float var = fmaxf(t2 * (1.f / FH) - mean * mean, 0.f);
    *(f32x2*)(smem + C_FS + lane * 8) = f32x2{rbf(mean), __builtin_amdgcn_rcpf(rbf(__builtin_amdgcn_sqrtf(rbf(rbf(var) + 1e-5f))))};
  } else {
#pragma unroll
    for (int tt = 0; tt < 4; ++tt) acc[tt] = f32x4{0, 0, 0, 0};
    bf16x8 X[2][4];
#pragma unroll
    for (int tt = 0; tt < 4; ++tt) X[0][tt] = xfrag(tt, 0);
#pragma unroll
    for (int ks = 0; ks < 16; ++ks) {
      if (ks + 1 < 16) {
#pragma unroll
        for (int tt = 0; tt < 4; ++tt) X[(ks + 1) & 1][tt] = xfrag(tt, ks + 1);
      }
      SCHED_FENCE();
#pragma unroll
      for (int tt = 0; tt < 4; ++tt) acc[tt] = __builtin_amdgcn_mfma_f32_16x16x32_bf16(W1[ks], X[ks & 1][tt], acc[tt], 0, 0, 0);
      SCHED_FENCE();
    }
    STAMP(2, 6);
    const int f8 = 14 * j + 2 * (w - 1) + (g >> 1);
#pragma unroll
    for (int tt = 0; tt < 4; ++tt) {
      float z[4];
#pragma unroll
      for (int r = 0; r < 4; ++r) z[r] = gelu_tanh(ri[tt] * (acc[tt][r] - mu[tt] * u1[r]) + v1[r]);
      *(u32x2*)(a.z1t + ((size_t)(4 * pi + tt) * 112 + f8) * 128 + c * 8 + (g & 1) * 4) = u32x2{pk_bf16(z[0], z[1]), pk_bf16(z[2], z[3])};
    }
  }
  STAMP(2, 7);
  __syncthreads();
  STAMP(2, 8);
  if constexpr (ROLE < 2) {
#pragma unroll
    for (int tt = 0; tt < 4; ++tt) {
      const f32x2 fs = *(const f32x2*)(smem + C_FS + (16 * tt + c) * 8);
      const float a0 = (bf_lo(z2p[tt][0]) - fs[0]) * fs[1], a1 = (bf_hi(z2p[tt][0]) - fs[0]) * fs[1];
      const float a2 = (bf_lo(z2p[tt][1]) - fs[0]) * fs[1], a3 = (bf_hi(z2p[tt][1]) - fs[0]) * fs[1];
      *(u32x2*)(smem + C_ZS + (16 * tt + c) * C_RS + (16 * w + 4 * g) * 2) = u32x2{pk_bf16(a0, a1), pk_bf16(a2, a3)};
    }
  }
  __syncthreads();
  if constexpr (ROLE < 2) {
#pragma unroll
    for (int kk = 0; kk < 2; ++kk) {
      const char LAS* p = (const char LAS*)smem + C_ZS + (32 * kk + 8 * g + q4) * C_RS + (16 * w + 4 * p4) * 2;
      const s16x4 lo = tr_read(p), hi = tr_read(p + 4 * C_RS);
      const u32x2 a0 = __builtin_bit_cast(u32x2, lo), a1 = __builtin_bit_cast(u32x2, hi);
      *(u32x4*)(a.z2t + ((size_t)(7 * j + w) * 256 + 8 * pi + 4 * kk) * 128 + lane * 8) = u32x4{a0[0], a0[1], a1[0], a1[1]};
    }
  }
  STAMP(2, 9);
}

__global__ __launch_bounds__(576) void kC(CArgs a) {
  extern __shared__ __attribute__((aligned(16))) char smem[];
  const int w = __builtin_amdgcn_readfirstlane(threadIdx.x >> 6);
  if (w == 0) c_body<0>(a, smem, w);
  else if (w < 7) c_body<1>(a, smem, w);
  else if (w == 7) c_body<2>(a, smem, w);
  else {
    const int wg = blockIdx.x, lane = threadIdx.x & 63;
    const u16* p = a.wsp + ((size_t)((wg & 7) >> 1) * 512 + 16 * (wg >> 3)) * L + lane * 8;
    const u32 dst = (u32)(size_t)(const char LAS*)smem + C_PF;
    __builtin_amdgcn_s_barrier();
    __builtin_amdgcn_s_barrier();
#pragma unroll 4
    for (int i = 0; i < 32; ++i) glds16(p + i * 512, dst);
    __builtin_amdgcn_s_barrier();
#pragma unroll 4
    for (int i = 32; i < 64; ++i) glds16(p + i * 512, dst);
    __builtin_amdgcn_s_barrier();
    __builtin_amdgcn_s_barrier();
    __builtin_amdgcn_s_barrier();
    VM_WAIT(0);
  }
}
struct DArgs { const u16 *wsp, *z2t, *z1t, *bsp, *Wot; u16* Pt; };
constexpr int D_RING = 32768;
constexpr int D_LDS = 4 * D_RING;

constexpr int d_ops(int it) { return it < 0 ? 0 : (it + 2 < 16 ? 7 : 0) + (((it & 1) == 0 && it / 2 + 2 < 8) ? 8 : 0); }
template <int N> __device__ __forceinline__ void vm_wait_c() { asm volatile("s_waitcnt vmcnt(%0)" ::"i"(N > 63 ? 63 : N) : "memory"); }

template <int IT>
__device__ __forceinline__ void d_iter(f32x4 (&acc)[4][7], bf16x8 (&Bf)[3][7], bf16x8 (&Af)[2][4], const u16* pb, const u16* const (&pa)[2],
                                       u32 ring, const char* const (&ap)[2][4]) {
  if constexpr (IT + 1 < 16) {
    constexpr int s = (IT + 1) >> 1;
    if constexpr ((IT & 1) == 1) vm_wait_c<(IT == 1 ? d_ops(0) : d_ops(IT - 2) + d_ops(IT - 1))>();
#pragma unroll
    for (int m = 0; m < 4; ++m) Af[(IT + 1) & 1][m] = *(const bf16x8*)(ap[(IT + 1) & 1][m] + (s & 3) * 8192);
  }
  SCHED_FENCE();
#pragma unroll
  for (int n = 0; n < 7; ++n) {
    if constexpr (IT + 2 < 16) Bf[(IT + 2) % 3][n] = *(const bf16x8*)(pb + ((size_t)n * 256 + 4 * (IT + 2)) * 128);
    if constexpr ((IT & 1) == 0 && IT / 2 + 2 < 8) {
      constexpr int s = IT / 2 + 2;
      glds16(pa[n & 1] + (size_t)(n >> 1) * 16 * L + 64 * s, ring + (s & 3) * 8192 + n * 1024);
      if (n == 6) glds16(pa[1] + (size_t)3 * 16 * L + 64 * s, ring + (s & 3) * 8192 + 7 * 1024);
    }
#pragma unroll
    for (int m = 0; m < 4; ++m) acc[m][n] = __builtin_amdgcn_mfma_f32_16x16x32_bf16(Bf[IT % 3][n], Af[IT & 1][m], acc[m][n], 0, 0, 0);
    SCHED_FENCE();
  }
}

__global__ __launch_bounds__(256, 1) void kD(DArgs a) {
  extern __shared__ __attribute__((aligned(16))) char smem[];
  const int wg = blockIdx.x, tid = threadIdx.x, lane = tid & 63;
  const int w = __builtin_amdgcn_readfirstlane(tid >> 6);
  const int xc = wg & 7, ii = wg >> 3;
  const int rt = (xc >> 1) * 8 + (ii & 7), ct = (xc & 1) * 4 + (ii >> 3);
  const int c = lane & 15, g = lane >> 4;
  STAMP(3, 0);
  const u32 ring = (u32)(size_t)(const char LAS*)smem + w * D_RING;
  const u16* pa[2];
#pragma unroll
  for (int par = 0; par < 2; ++par) {
    const int row = 8 * par + (lane >> 3);
    pa[par] = a.wsp + (size_t)(64 * rt + row) * L + 512 * w + 8 * ((lane & 7) ^ ((row >> 1) & 7));
  }
  const u16* pb = a.z2t + ((size_t)(7 * ct) * 256 + 64 * w) * 128 + lane * 8;
  auto ldA = [&](int s) {
#pragma unroll
    for (int i = 0; i < 8; ++i) glds16(pa[i & 1] + (size_t)(i >> 1) * 16 * L + 64 * s, ring + (s & 3) * 8192 + i * 1024);
  };
  bf16x8 Bf[3][7], Af[2][4];
  auto ldB = [&](int ks) {
#pragma unroll
    for (int n = 0; n < 7; ++n) Bf[ks][n] = *(const bf16x8*)(pb + ((size_t)n * 256 + 4 * ks) * 128);
  };
  ldA(0); SCHED_FENCE(); ldB(0); ldB(1); SCHED_FENCE(); ldA(1); SCHED_FENCE();
  f32x4 acc[4][7];
#pragma unroll
  for (int m = 0; m < 4; ++m)
#pragma unroll
    for (int n = 0; n < 7; ++n) acc[m][n] = f32x4{0, 0, 0, 0};
  const int asw = (c >> 1) & 7;
  const char* ap[2][4];
#pragma unroll
  for (int m = 0; m < 4; ++m) {
    const char* base = smem + w * D_RING + ((m + w) & 3) * 2048 + c * 128;
    ap[0][m] = base + 16 * (g ^ asw); ap[1][m] = base + 16 * ((4 + g) ^ asw);
  }
  vm_wait_c<7 + 7 + 8>();
#pragma unroll
  for (int m = 0; m < 4; ++m) Af[0][m] = *(const bf16x8*)(ap[0][m]);
  STAMP(3, 4);
  d_iter<0>(acc, Bf, Af, pb, pa, ring, ap);   d_iter<1>(acc, Bf, Af, pb, pa, ring, ap);
  d_iter<2>(acc, Bf, Af, pb, pa, ring, ap);   d_iter<3>(acc, Bf, Af, pb, pa, ring, ap);
  STAMP(3, 5);
  d_iter<4>(acc, Bf, Af, pb, pa, ring, ap);   d_iter<5>(acc, Bf, Af, pb, pa, ring, ap);
  d_iter<6>(acc, Bf, Af, pb, pa, ring, ap);   d_iter<7>(acc, Bf, Af, pb, pa, ring, ap);
  STAMP(3, 6);
  d_iter<8>(acc, Bf, Af, pb, pa, ring, ap);   d_iter<9>(acc, Bf, Af, pb, pa, ring, ap);
  d_iter<10>(acc, Bf, Af, pb, pa, ring, ap);  d_iter<11>(acc, Bf, Af, pb, pa, ring, ap);
  STAMP(3, 7);
  d_iter<12>(acc, Bf, Af, pb, pa, ring, ap);  d_iter<13>(acc, Bf, Af, pb, pa, ring, ap);
  u32x2 z1v[7];
#pragma unroll
  for (int n = 0; n < 7; ++n)
    z1v[n] = *(const u32x2*)(a.z1t + ((size_t)(4 * rt + w) * 112 + 14 * ct + 2 * n + (g >> 1)) * 128 + c * 8 + (g & 1) * 4);
  const float bs = bf1(a.bsp[64 * rt + 16 * w + c]);
  u32x4 pf[4];
  {
    const u16* p = a.Wot + (size_t)(xc & 1) * 16 * 112 * 128 + (size_t)ii * 7168 + tid * 8;
#pragma unroll
    for (int k = 0; k < 3; ++k) pf[k] = *(const u32x4*)(p + k * 2048);
    pf[3] = *(const u32x4*)(p + (tid < 128 ? 3 * 2048 : 0));
  }
  SCHED_FENCE();
  d_iter<14>(acc, Bf, Af, pb, pa, ring, ap);  d_iter<15>(acc, Bf, Af, pb, pa, ring, ap);
  STAMP(3, 1);
  __builtin_amdgcn_s_barrier();
#pragma unroll
  for (int m = 1; m < 4; ++m) {
    char* dst = smem + ((((m + w) & 3) * 3 + (m - 1)) * 7 * 64 + lane) * 16;
#pragma unroll
    for (int n = 0; n < 7; ++n) *(f32x4*)(dst + n * 1024) = acc[m][n];
  }
  __syncthreads();
  STAMP(3, 2);
  const char* src = smem + (w * 3 * 7 * 64 + lane) * 16;
#pragma unroll
  for (int n = 0; n < 7; ++n) {
    const f32x4 p0 = *(const f32x4*)(src + n * 1024), p1 = *(const f32x4*)(src + (7 + n) * 1024), p2 = *(const f32x4*)(src + (14 + n) * 1024);
    float gg[4];
#pragma unroll
    for (int e = 0; e < 4; ++e) gg[e] = ((acc[0][n][e] + p0[e]) + (p1[e] + p2[e])) + bs;
    *(u32x2*)(a.Pt + ((size_t)(4 * rt + w) * 112 + 14 * ct + 2 * n + (g >> 1)) * 128 + c * 8 + (g & 1) * 4) =
        u32x2{pk_bf16(bf_lo(z1v[n][0]) * gg[0], bf_hi(z1v[n][0]) * gg[1]), pk_bf16(bf_lo(z1v[n][1]) * gg[2], bf_hi(z1v[n][1]) * gg[3])};
  }
#pragma unroll
  for (int k = 0; k < 4; ++k) asm volatile("" ::"v"(pf[k]));
  STAMP(3, 3);
}
struct EArgs { const u16 *Pt, *Wot, *x1T; const float* mod; u16* out; };
constexpr int E_TS = 1152;
constexpr int E_PART = 0;
constexpr int E_X = 4 * 16 * E_TS;
constexpr int E_LDS = E_X + 64 * 132;

__global__ __launch_bounds__(256, 1) void kE(EArgs a) {
  extern __shared__ __attribute__((aligned(16))) char smem[];
  const int wg = blockIdx.x, tid = threadIdx.x, lane = tid & 63;
  const int w = __builtin_amdgcn_readfirstlane(tid >> 6);
  const int xc = wg & 7, ii = wg >> 3;
  const int rt = (xc >> 1) * 8 + (ii & 7), dt = (xc & 1) * 4 + (ii >> 3);
  const int c = lane & 15, g = lane >> 4;
  STAMP(4, 0);
  bf16x8 Pf[7][4], Wf[7][4];
#pragma unroll
  for (int k = 0; k < 7; ++k) {
    const int f8 = 4 * (7 * w + k);
#pragma unroll
    for (int m = 0; m < 4; ++m) Pf[k][m] = *(const bf16x8*)(a.Pt + ((size_t)(4 * rt + m) * 112 + f8) * 128 + lane * 8);
#pragma unroll
    for (int n = 0; n < 4; ++n) Wf[k][n] = *(const bf16x8*)(a.Wot + ((size_t)(4 * dt + n) * 112 + f8) * 128 + lane * 8);
  }
  u32x4 xr[2];
#pragma unroll
  for (int k = 0; k < 2; ++k) {
    const int id = tid + 256 * k, dd = id >> 3, chn = id & 7;
    xr[k] = *(const u32x4*)(a.x1T + (size_t)(64 * dt + dd) * L + 64 * rt + 8 * chn);
  }
  const int d8 = lane & 7;
  const f32x4 gb0 = *(const f32x4*)(a.mod + 2560 + 64 * dt + 8 * d8), gb1 = *(const f32x4*)(a.mod + 2560 + 64 * dt + 8 * d8 + 4);
  SCHED_FENCE();
  f32x4 acc[4][4];
#pragma unroll
  for (int m = 0; m < 4; ++m)
#pragma unroll
    for (int n = 0; n < 4; ++n) acc[m][n] = f32x4{0, 0, 0, 0};
#pragma unroll
  for (int k = 0; k < 7; ++k)
#pragma unroll
    for (int m = 0; m < 4; ++m)
#pragma unroll
      for (int n = 0; n < 4; ++n) acc[m][n] = __builtin_amdgcn_mfma_f32_16x16x32_bf16(Wf[k][n], Pf[k][m], acc[m][n], 0, 0, 0);
  STAMP(4, 1);
#pragma unroll
  for (int k = 0; k < 2; ++k) {
    const int id = tid + 256 * k, dd = id >> 3, chn = id & 7;
    u32* p = (u32*)(smem + E_X + dd * 132 + chn * 16);
    p[0] = xr[k][0]; p[1] = xr[k][1]; p[2] = xr[k][2]; p[3] = xr[k][3];
  }
#pragma unroll
  for (int m = 0; m < 4; ++m)
#pragma unroll
    for (int n = 0; n < 4; ++n) *(f32x4*)(smem + E_PART + (w * 16 + m * 4 + n) * E_TS + g * 288 + c * 16) = acc[m][n];
  __syncthreads();
  STAMP(4, 2);
#pragma unroll
  for (int k = 0; k < 2; ++k) {
    const int t = 32 * k + 8 * w + (lane >> 3);
    float s[8];
#pragma unroll
    for (int hh = 0; hh < 2; ++hh) {
      const char* p = smem + E_PART + ((t >> 4) * 4 + (d8 >> 1)) * E_TS + (2 * (d8 & 1) + hh) * 288 + (t & 15) * 16;
      const f32x4 p0 = *(const f32x4*)p, p1 = *(const f32x4*)(p + 16 * E_TS), p2 = *(const f32x4*)(p + 32 * E_TS), p3 = *(const f32x4*)(p + 48 * E_TS);
#pragma unroll
      for (int e = 0; e < 4; ++e) s[4 * hh + e] = (p0[e] + p1[e]) + (p2[e] + p3[e]);
    }
    float o[8];
#pragma unroll
    for (int e = 0; e < 8; ++e) {
      const float x1 = bf1(*(const u16*)(smem + E_X + (8 * d8 + e) * 132 + t * 2));
      o[e] = x1 + (e < 4 ? gb0[e] : gb1[e - 4]) * s[e];
    }
    *(u32x4*)(a.out + (size_t)(64 * rt + t) * D + 64 * dt + 8 * d8) = u32x4{pk_bf16(o[0], o[1]), pk_bf16(o[2], o[3]), pk_bf16(o[4], o[5]), pk_bf16(o[6], o[7])};
  }
  STAMP(4, 3);
}
#define KMASK(b)
extern "C" void kernel_launch(void* const* d_in, const int* in_sizes, int n_in, void* d_out, int out_size,
                              void* d_ws, size_t ws_size, hipStream_t stream) {
  const u16* x = (const u16*)d_in[0];
  char* ws = (char*)d_ws;
  float* mod = (float*)(ws + 0);
  u32* stats1 = (u32*)(ws + 16384);
  float* uv = (float*)(ws + 32768);
  u64* gran = (u64*)(ws + 65536);
  u16* xT = (u16*)(ws + (1 << 20));
  u16* Krev = (u16*)(ws + (3 << 20));
  u16* x1T = (u16*)(ws + (5 << 20));
  u16* Wt = (u16*)(ws + (7 << 20));
  u16* Wot = (u16*)(ws + (9 << 20));
  u16* z1t = (u16*)(ws + (10 << 20));
  u16* z2t = (u16*)(ws + (14 << 20));
  u16* Pt = (u16*)(ws + (18 << 20));
  AArgs aa;
  aa.x = x; aa.Are = (const u16*)d_in[1]; aa.Aim = (const u16*)d_in[2]; aa.Bre = (const u16*)d_in[3]; aa.Bim = (const u16*)d_in[4];
  aa.Cre = (const u16*)d_in[5]; aa.Cim = (const u16*)d_in[6]; aa.logdt = (const u16*)d_in[7]; aa.c = (const u16*)d_in[14];
  aa.wmod = (const u16*)d_in[15]; aa.mod = mod; aa.stats1 = stats1; aa.xT = xT; aa.Krev = Krev; aa.gran = (u32x4*)gran;
  (void)hipFuncSetAttribute((const void*)kA, hipFuncAttributeMaxDynamicSharedMemorySize, A_LDS);
  KMASK(1) kA<<<256, 768, A_LDS, stream>>>(aa);
  BArgs ba;
  ba.xT = xT; ba.Krev = Krev; ba.Dskip = (const u16*)d_in[8]; ba.win = (const u16*)d_in[10]; ba.wout = (const u16*)d_in[13];
  ba.wsp = (const u16*)d_in[11]; ba.stats1 = stats1; ba.mod = mod; ba.x1T = x1T; ba.Wt = Wt; ba.Wot = Wot; ba.uv = uv;
  (void)hipFuncSetAttribute((const void*)kB, hipFuncAttributeMaxDynamicSharedMemorySize, B_LDS);
  KMASK(2) kB<<<256, 768, B_LDS, stream>>>(ba);
  CArgs ca;
  ca.x1T = x1T; ca.Wt = Wt; ca.wsp = (const u16*)d_in[11]; ca.uv = uv; ca.z1t = z1t; ca.z2t = z2t; ca.gran = gran;
  (void)hipFuncSetAttribute((const void*)kC, hipFuncAttributeMaxDynamicSharedMemorySize, C_LDS);
  KMASK(4) kC<<<256, 576, C_LDS, stream>>>(ca);
  DArgs da;
  da.wsp = (const u16*)d_in[11]; da.z2t = z2t; da.z1t = z1t; da.bsp = (const u16*)d_in[12]; da.Wot = Wot; da.Pt = Pt;
  (void)hipFuncSetAttribute((const void*)kD, hipFuncAttributeMaxDynamicSharedMemorySize, D_LDS);
  KMASK(8) kD<<<256, 256, D_LDS, stream>>>(da);
  EArgs ea;
  ea.Pt = Pt; ea.Wot = Wot; ea.x1T = x1T; ea.mod = mod; ea.out = (u16*)d_out;
  (void)hipFuncSetAttribute((const void*)kE, hipFuncAttributeMaxDynamicSharedMemorySize, E_LDS);
  KMASK(16) kE<<<256, 256, E_LDS, stream>>>(ea);
}
```

```cpp
#include <hip/hip_runtime.h>
#include <hip/hip_bf16.h>
#include <stdint.h>

typedef unsigned short u16;
typedef unsigned int u32;
typedef unsigned long long u64;
typedef __bf16 bf16x8 __attribute__((ext_vector_type(8)));
typedef __bf16 bf16x2 __attribute__((ext_vector_type(2)));
typedef short s16x4 __attribute__((ext_vector_type(4)));
typedef float f32x2 __attribute__((ext_vector_type(2)));
typedef float f32x4 __attribute__((ext_vector_type(4)));
typedef u32 u32x2 __attribute__((ext_vector_type(2)));
typedef u32 u32x4 __attribute__((ext_vector_type(4)));
#define LAS __attribute__((address_space(3)))
#define GAS __attribute__((address_space(1)))

constexpr int L = 2048, D = 512, NS = 128, F = 1792, FH = 896;

__device__ __forceinline__ float bf_lo(u32 w) { return __uint_as_float(w << 16); }
__device__ __forceinline__ float bf_hi(u32 w) { return __uint_as_float(w & 0xffff0000u); }
__device__ __forceinline__ float bf1(u16 h) { return __uint_as_float(((u32)h) << 16); }
__device__ __forceinline__ u32 pk_bf16(float lo, float hi) {
  f32x2 a = {lo, hi};
  bf16x2 p = __builtin_convertvector(a, bf16x2);
  return __builtin_bit_cast(u32, p);
}
__device__ __forceinline__ float rbf(float x) { return bf_lo(pk_bf16(x, x)); }

template <int CTRL, int ROWMASK = 0xf>
__device__ __forceinline__ float dpp_add(float x) {
  int y = __builtin_amdgcn_update_dpp(0, __float_as_int(x), CTRL, ROWMASK, 0xf, true);
  return x + __int_as_float(y);
}
__device__ __forceinline__ float row16_sum(float x) {
  x = dpp_add<0xB1>(x);
  x = dpp_add<0x4E>(x);
  x = dpp_add<0x141>(x);
  x = dpp_add<0x140>(x);
  return x;
}
__device__ __forceinline__ float wave_sum(float x) {
  x = row16_sum(x);
  float a = __int_as_float(__builtin_amdgcn_readlane(__float_as_int(x), 0));
  float b = __int_as_float(__builtin_amdgcn_readlane(__float_as_int(x), 16));
  float c = __int_as_float(__builtin_amdgcn_readlane(__float_as_int(x), 32));
  float d = __int_as_float(__builtin_amdgcn_readlane(__float_as_int(x), 48));
  return (a + b) + (c + d);
}
__device__ __forceinline__ s16x4 tr_read(const char LAS* p) {
  return __builtin_amdgcn_ds_read_tr16_b64_v4i16((s16x4 LAS*)p);
}

#define STAMP(kern, i)
__device__ __forceinline__ void glds16(const void* gsrc, u32 lds_dst) {
  u32 keep;
  asm volatile("s_mov_b32 %0, m0\n\ts_mov_b32 m0, %2\n\ts_nop 0\n\tglobal_load_lds_dwordx4 %1, off\n\ts_mov_b32 m0, %0"
               : "=&s"(keep) : "v"(gsrc), "s"(lds_dst) : "memory");
}
#define SCHED_FENCE() __builtin_amdgcn_sched_barrier(0)
#define VM_WAIT(n) asm volatile("s_waitcnt vmcnt(" #n ")" ::: "memory")
__device__ __forceinline__ float xor16_sum(float x) {
  const u32 v = __float_as_uint(x);
  auto r = __builtin_amdgcn_permlane16_swap(v, v, false, false);
  return __uint_as_float(r[0]) + __uint_as_float(r[1]);
}
__device__ __forceinline__ float xor32_sum(float x) {
  const u32 v = __float_as_uint(x);
  auto r = __builtin_amdgcn_permlane32_swap(v, v, false, false);
  return __uint_as_float(r[0]) + __uint_as_float(r[1]);
}
__device__ __forceinline__ float xor4_8_sum(float x) {
  x = x + __int_as_float(__builtin_amdgcn_update_dpp(0, __float_as_int(x), 0x124, 0xf, 0xf, true));
  x = x + __int_as_float(__builtin_amdgcn_update_dpp(0, __float_as_int(x), 0x128, 0xf, 0xf, true));
  return x;
}
__device__ __forceinline__ void lds_rendezvous(u32 LAS* cnt, u32 target) {
  asm volatile("s_waitcnt lgkmcnt(0)" ::: "memory");
  if ((threadIdx.x & 63) == 0) __hip_atomic_fetch_add(cnt, 1u, __ATOMIC_RELAXED, __HIP_MEMORY_SCOPE_WORKGROUP);
  while (__hip_atomic_load(cnt, __ATOMIC_RELAXED, __HIP_MEMORY_SCOPE_WORKGROUP) < target) __builtin_amdgcn_s_sleep(1);
  asm volatile("" ::: "memory");
}
struct AArgs {
  const u16 *x, *Are, *Aim, *Bre, *Bim, *Cre, *Cim, *logdt, *c, *wmod;
  float* mod; u32* stats1; u16* xT; u16* Krev; u32x4* gran;
};
constexpr int A_RS = 528;
constexpr int A_TAB = 0;
constexpr int A_KC = A_TAB + 2 * 64 * A_RS;
constexpr int A_TILE = A_KC + 2 * 768 * 4;
constexpr int A_RED = A_TILE + 8 * 1088;
constexpr int A_CNT = A_RED + 4 * 32 * 4;
constexpr int A_LDS = A_CNT + 16;

struct KIdx { u16 v[2048]; };
constexpr int bf16_round_int(int l) {
  int sh = 0;
  while ((l >> sh) >= 256) ++sh;
  if (sh == 0) return l;
  const int q = l >> sh, rem = l & ((1 << sh) - 1), half = 1 << (sh - 1);
  const int up = (rem > half || (rem == half && (q & 1))) ? 1 : 0;
  return (q + up) << sh;
}
constexpr KIdx make_kidx() {
  KIdx t{};
  for (int x = 0; x < 2048; ++x) {
    const int lr = bf16_round_int(2047 - x);
    const int aa = lr >> 7, b = (lr >> 3) & 15, r = lr & 7;
    const int row = lr < 256 ? 8 * aa + r : lr < 512 ? 16 + 4 * (aa - 2) + (r >> 1) : lr < 1024 ? 24 + 2 * (aa - 4) + (r >> 2) : 32 + (aa - 8);
    t.v[x] = (u16)(row * 16 + b);
  }
  return t;
}
__device__ const KIdx g_kidx = make_kidx();

struct cpx { float r, i; };
__device__ __forceinline__ cpx cmul(cpx a, cpx b) { return cpx{a.r * b.r - a.i * b.i, a.r * b.i + a.i * b.r}; }

__global__ __launch_bounds__(768) void kA(AArgs a) {
  extern __shared__ __attribute__((aligned(16))) char smem[];
  const int wg = blockIdx.x, tid = threadIdx.x, lane = tid & 63;
  const int w = __builtin_amdgcn_readfirstlane(tid >> 6);
  STAMP(0, 0);
  if (w < 8) {
    const int ch = w >> 2, nh = (w >> 1) & 1, part = w & 1, d = 2 * wg + ch;
    const int n = 64 * nh + lane, o = d * NS + n;
    const u16 hare = a.Are[o], haim = a.Aim[o], hbre = a.Bre[o], hbim = a.Bim[o], hcre = a.Cre[o], hcim = a.Cim[o], hdt = a.logdt[d];
    const u32x4 kix = *(const u32x4*)(g_kidx.v + 8 * (tid & 255));
    SCHED_FENCE();
    __builtin_amdgcn_s_barrier();
    SCHED_FENCE();
    const float dt = rbf(__expf(bf1(hdt)));
    const float lr = (dt * bf1(hare)) * 1.4426950408889634f, lp = (dt * bf1(haim)) * 0.15915494309189535f;
    auto cexp = [&](float l) -> cpx {
      const float e = __builtin_amdgcn_exp2f(lr * l), ph = __builtin_amdgcn_fractf(lp * l);
      return cpx{e * __builtin_amdgcn_cosf(ph), e * __builtin_amdgcn_sinf(ph)};
    };
    char* tab = smem + A_TAB + ch * (64 * A_RS) + n * 4;
    auto put = [&](int row, cpx v) { *(u32*)(tab + row * A_RS) = pk_bf16(v.r, v.i); };
    if (part == 0) {
      const cpx E1 = cexp(1.f), E128 = cexp(128.f), E512 = cexp(512.f);
      const cpx E2 = cmul(E1, E1), E3 = cmul(E2, E1), E4 = cmul(E2, E2), E5 = cmul(E4, E1), E6 = cmul(E4, E2), E7 = cmul(E4, E3);
      put(0, cpx{1.f, 0.f}); put(1, E1); put(2, E2); put(3, E3); put(4, E4); put(5, E5); put(6, E6); put(7, E7);
      put(8, E128); put(9, cmul(E128, E1)); put(10, cmul(E128, E2)); put(11, cmul(E128, E3));
      put(12, cmul(E128, E4)); put(13, cmul(E128, E5)); put(14, cmul(E128, E6)); put(15, cmul(E128, E7));
      const cpx E256 = cmul(E128, E128), E384 = cmul(E256, E128);
      put(16, E256); put(17, cmul(E256, E2)); put(18, cmul(E256, E4)); put(19, cmul(E256, E6));
      put(20, E384); put(21, cmul(E384, E2)); put(22, cmul(E384, E4)); put(23, cmul(E384, E6));
      const cpx E640 = cmul(E512, E128), E768 = cmul(E512, E256), E896 = cmul(E512, E384);
      put(24, E512); put(25, cmul(E512, E4)); put(26, E640); put(27, cmul(E640, E4));
      put(28, E768); put(29, cmul(E768, E4)); put(30, E896); put(31, cmul(E896, E4));
    } else {
      const cpx E8 = cexp(8.f), E128 = cexp(128.f), E1024 = cexp(1024.f);
      const float br = dt * bf1(hbre), bi = dt * bf1(hbim), cr = bf1(hcre), ci = bf1(hcim);
      const cpx W = cmul(cpx{cr, ci}, cpx{br, bi});
      const cpx E16 = cmul(E8, E8), E32 = cmul(E16, E16), E64 = cmul(E32, E32);
      cpx Y[8];
      Y[0] = W; Y[1] = cmul(W, E8); Y[2] = cmul(W, E16); Y[3] = cmul(Y[1], E16);
#pragma unroll
      for (int b = 0; b < 4; ++b) Y[4 + b] = cmul(Y[b], E32);
#pragma unroll
      for (int b = 0; b < 8; ++b) {
        put(48 + b, cpx{Y[b].r, -Y[b].i});
        const cpx t = cmul(Y[b], E64);
        put(56 + b, cpx{t.r, -t.i});
      }
      const cpx E256 = cmul(E128, E128), E512 = cmul(E256, E256);
      cpx P[4];
      P[0] = E1024; P[1] = cmul(E1024, E128); P[2] = cmul(E1024, E256); P[3] = cmul(P[1], E256);
#pragma unroll
      for (int b = 0; b < 4; ++b) { put(32 + b, P[b]); put(36 + b, cmul(P[b], E512)); }
      put(40, cmul(E1024, E1024));
    }
    STAMP(0, 1);
    lds_rendezvous((u32 LAS*)(smem + A_CNT), 8);
    STAMP(0, 2);
    if (w < 6) {
      const int c2 = w >= 3 ? 1 : 0, t = w - 3 * c2, i = lane & 15, g = lane >> 4;
      const char* tb = smem + A_TAB + c2 * (64 * A_RS) + 16 * g;
      const char* pa = tb + (16 * t + i) * A_RS, *pb = tb + (48 + i) * A_RS;
      f32x4 acc = {0, 0, 0, 0};
      bf16x8 Af[8], Bf[8];
#pragma unroll
      for (int s = 0; s < 8; ++s) {
        Af[s] = *(const bf16x8*)(pa + s * 64); Bf[s] = *(const bf16x8*)(pb + s * 64);
      }
#pragma unroll
      for (int s = 0; s < 8; ++s) acc = __builtin_amdgcn_mfma_f32_16x16x32_bf16(Af[s], Bf[s], acc, 0, 0, 0);
      float* Kc = (float*)(smem + A_KC) + c2 * 768 + t * 256;
#pragma unroll
      for (int r = 0; r < 4; ++r) Kc[(4 * g + r) * 16 + i] = acc[r];
    }
    STAMP(0, 3);
    lds_rendezvous((u32 LAS*)(smem + A_CNT + 4), 8);
    STAMP(0, 4);
    const float* Kc = (const float*)(smem + A_KC) + ch * 768;
    float kv[8];
#pragma unroll
    for (int j = 0; j < 4; ++j) { kv[2 * j] = Kc[kix[j] & 0xffffu]; kv[2 * j + 1] = Kc[kix[j] >> 16]; }
    *(u32x4*)(a.Krev + (size_t)d * L + 8 * (tid & 255)) =
        u32x4{pk_bf16(kv[0], kv[1]), pk_bf16(kv[2], kv[3]), pk_bf16(kv[4], kv[5]), pk_bf16(kv[6], kv[7])};
    STAMP(0, 5);
  } else {
    const int tw = w - 8;
    const int t0 = 8 * ((wg & 7) * 32 + (wg >> 3));
    u32x4 xr[2];
#pragma unroll
    for (int j = 0; j < 2; ++j) xr[j] = *(const u32x4*)(a.x + (size_t)(t0 + 2 * tw + j) * D + 8 * lane);
    const bool doM = wg < 96;
    u32x4 wm[8]; u16 cv[8];
    const int rr = lane >> 2, q = lane & 3;
    if (doM) {
#pragma unroll
      for (int p = 0; p < 8; ++p) cv[p] = a.c[p * 64 + tw * 16 + rr];
#pragma unroll
      for (int p = 0; p < 8; ++p) wm[p] = *(const u32x4*)(a.wmod + (size_t)(p * 64 + tw * 16 + rr) * 3072 + 32 * wg + 8 * q);
    }
    char* tile = smem + A_TILE;
    if (tw == 0 && lane < 4) *(u32*)(smem + A_CNT + 4 * lane) = 0;
    asm volatile("s_waitcnt lgkmcnt(0)" ::: "memory");
    SCHED_FENCE();
    __builtin_amdgcn_s_barrier();
    SCHED_FENCE();
#pragma unroll
    for (int j = 0; j < 2; ++j) *(u32x4*)(tile + (2 * tw + j) * 1088 + 16 * lane) = xr[j];
    STAMP(0, 1);
    lds_rendezvous((u32 LAS*)(smem + A_CNT + 8), 4);
    STAMP(0, 2);
#pragma unroll
    for (int ps = 0; ps < 2; ++ps) {
      const int d0 = 128 * tw + 64 * ps + 16 * (lane >> 4);
      const char LAS* src = (const char LAS*)tile + ((lane & 15) >> 2) * 1088 + 2 * (d0 + 4 * (lane & 3));
      const s16x4 lo = tr_read(src), hi = tr_read(src + 4 * 1088);
      u32x2 a0 = __builtin_bit_cast(u32x2, lo), a1 = __builtin_bit_cast(u32x2, hi);
      *(u32x4*)(a.xT + (size_t)(d0 + (lane & 15)) * L + t0) = u32x4{a0[0], a0[1], a1[0], a1[1]};
    }
    if (tw == 0) a.gran[wg * 64 + lane] = u32x4{0, 0, 0, 0};
    {
      float v[2][8], s[2];
#pragma unroll
      for (int j = 0; j < 2; ++j) {
#pragma unroll
        for (int k = 0; k < 4; ++k) { v[j][2 * k] = bf_lo(xr[j][k]); v[j][2 * k + 1] = bf_hi(xr[j][k]); }
        s[j] = ((v[j][0] + v[j][1]) + (v[j][2] + v[j][3])) + ((v[j][4] + v[j][5]) + (v[j][6] + v[j][7]));
      }
      float mean[2], q2[2];
#pragma unroll
      for (int j = 0; j < 2; ++j) mean[j] = wave_sum(s[j]) * (1.f / D);
#pragma unroll
      for (int j = 0; j < 2; ++j) {
        q2[j] = 0;
#pragma unroll
        for (int k = 0; k < 8; ++k) { const float dv = v[j][k] - mean[j]; q2[j] += dv * dv; }
      }
#pragma unroll
      for (int j = 0; j < 2; ++j) {
        const float var = wave_sum(q2[j]) * (1.f / D);
        const float sd = rbf(__builtin_amdgcn_sqrtf(rbf(rbf(var) + 1e-5f)));
        if (lane == 0) a.stats1[t0 + 2 * tw + j] = pk_bf16(mean[j], sd);
      }
    }
    float* red = (float*)(smem + A_RED);
    if (doM) {
      float acc[8] = {0, 0, 0, 0, 0, 0, 0, 0};
#pragma unroll
      for (int p = 0; p < 8; ++p) {
        const float cx = bf1(cv[p]);
        const float s = rbf(cx * __builtin_amdgcn_rcpf(1.f + __expf(-cx)));
#pragma unroll
        for (int k = 0; k < 4; ++k) { acc[2 * k] += s * bf_lo(wm[p][k]); acc[2 * k + 1] += s * bf_hi(wm[p][k]); }
      }
#pragma unroll
      for (int k = 0; k < 8; ++k) {
        float v = acc[k];
        v = xor32_sum(xor16_sum(xor4_8_sum(v)));
        if (rr == 0) red[tw * 32 + 8 * q + k] = v;
      }
      STAMP(0, 3);
      lds_rendezvous((u32 LAS*)(smem + A_CNT + 12), 4);
      STAMP(0, 4);
      if (tw == 0 && lane < 32)
        a.mod[32 * wg + lane] = rbf((red[lane] + red[32 + lane]) + (red[64 + lane] + red[96 + lane]));
    }
    STAMP(0, 5);
  }
}
struct BArgs {
  const u16 *xT, *Krev, *Dskip, *win, *wout, *wsp;
  const u32* stats1; const float* mod;
  u16 *x1T, *Wt, *Wot; float* uv;
};
constexpr int B_H = 0;
constexpr int B_HS = (256 + 2048) * 2;
constexpr int B_KC = B_H + 2 * B_HS;
constexpr int B_KS = 4160;
constexpr int B_PART = B_KC + 2 * 4 * B_KS;
constexpr int B_RED = B_PART + 2 * 4 * 8 * 1280;
constexpr int B_CNT = B_RED + 4 * 16 * 4;
constexpr int B_LDS = B_CNT + 32;

__global__ __launch_bounds__(768) void kB(BArgs a) {
  extern __shared__ __attribute__((aligned(16))) char smem[];
  const int wg = blockIdx.x, tid = threadIdx.x, lane = tid & 63;
  const int w = __builtin_amdgcn_readfirstlane(tid >> 6);
  STAMP(1, 0);
  if (w < 8) {
    const int ch = w >> 2, q = w & 3, d = 2 * wg + ch, tj = tid & 255;
    const u32x4 xv = *(const u32x4*)(a.xT + (size_t)d * L + 8 * tj);
    const u32x4 st0 = *(const u32x4*)(a.stats1 + 8 * tj), st1 = *(const u32x4*)(a.stats1 + 8 * tj + 4);
    const u32x4 k0 = *(const u32x4*)(a.Krev + (size_t)d * L + 8 * tj);
    const u32x4 k1r = *(const u32x4*)(a.Krev + (size_t)d * L + 8 * (tj < 255 ? tj + 1 : tj));
    const float sa = a.mod[d], Ac = rbf(1.f + a.mod[512 + d]), ga = a.mod[1024 + d], dsk = bf1(a.Dskip[d]);
    SCHED_FENCE();
    __builtin_amdgcn_s_barrier();
    SCHED_FENCE();
    char* hb = smem + B_H + ch * B_HS;
    char* kc = smem + B_KC + ch * 4 * B_KS;
    if (tj < 32) *(u32x4*)(hb + 16 * tj) = u32x4{0, 0, 0, 0};
    if (tj < 16) *(u32x4*)(kc + (tj >> 2) * B_KS + 16 * (256 + (tj & 3))) = u32x4{0, 0, 0, 0};
    {
      const u32 z = tj < 255 ? 0xffffffffu : 0u;
      const u32 wd[8] = {k0[0], k0[1], k0[2], k0[3], k1r[0] & z, k1r[1] & z, 0, 0};
      u32 od[5];
#pragma unroll
      for (int k = 0; k < 5; ++k) od[k] = __builtin_amdgcn_alignbit(wd[k + 1], wd[k], 16);
      *(u32x4*)(kc + 0 * B_KS + 16 * tj) = u32x4{wd[0], wd[1], wd[2], wd[3]};
      *(u32x4*)(kc + 1 * B_KS + 16 * tj) = u32x4{od[0], od[1], od[2], od[3]};
      *(u32x4*)(kc + 2 * B_KS + 16 * tj) = u32x4{wd[1], wd[2], wd[3], wd[4]};
      *(u32x4*)(kc + 3 * B_KS + 16 * tj) = u32x4{od[1], od[2], od[3], od[4]};
    }
    u32x4 hv;
    {
      float hh[8];
#pragma unroll
      for (int k = 0; k < 8; ++k) {
        const u32 s = k < 4 ? st0[k] : st1[k - 4];
        const float xx = (k & 1) ? bf_hi(xv[k >> 1]) : bf_lo(xv[k >> 1]);
        const float sc = __builtin_amdgcn_rcpf(bf_hi(s)) * Ac;
        hh[k] = (xx - bf_lo(s)) * sc + sa;
      }
      hv = u32x4{pk_bf16(hh[0], hh[1]), pk_bf16(hh[2], hh[3]), pk_bf16(hh[4], hh[5]), pk_bf16(hh[6], hh[7])};
    }
    *(u32x4*)(hb + 512 + 16 * tj) = hv;
    STAMP(1, 1);
    lds_rendezvous((u32 LAS*)(smem + B_CNT + 12 + 4 * 0) + ch * 2, 4);
    STAMP(1, 2);
    const int c = lane & 15, g = lane >> 4;
    const char* bbase = hb + 2 * (256 + 16 * c - 16 * (g >> 1) + 8 * (g & 1) - 32 * q) - 256;
    const int X = 2047 - 32 * q - 16 * (g >> 1) + 8 * (g & 1) - c;
    const char* abase = kc + (X & 3) * B_KS + 2 * (X & ~3) - 256 * 15;
    bf16x8 Bf[16], Af[16];
    auto lda = [&](int u) {
      const u32x2 lo = *(const u32x2*)(abase + 256 * (15 - u)), hi = *(const u32x2*)(abase + 256 * (15 - u) + 8);
      Af[u] = __builtin_bit_cast(bf16x8, u32x4{lo[0], lo[1], hi[0], hi[1]});
    };
    lda(0);
#pragma unroll
    for (int v = 1; v < 16; v += 2) Bf[v] = *(const bf16x8*)(bbase + 256 * v);
    lda(1);
#pragma unroll
    for (int v = 0; v < 16; v += 2) Bf[v] = *(const bf16x8*)(bbase + 256 * v);
    lda(2); lda(3);
    SCHED_FENCE();
    f32x4 acc[8];
#pragma unroll
    for (int i = 0; i < 8; ++i) acc[i] = f32x4{0, 0, 0, 0};
#pragma unroll
    for (int u = 0; u < 16; ++u) {
      if (u + 4 < 16) lda(u + 4);
      SCHED_FENCE();
#pragma unroll
      for (int i = u >> 1; i < 8; ++i) acc[i] = __builtin_amdgcn_mfma_f32_16x16x32_bf16(Af[u], Bf[2 * i - u + 1], acc[i], 0, 0, 0);
      SCHED_FENCE();
    }
    STAMP(1, 3);
    char* part = smem + B_PART + ch * (4 * 8 * 1280);
#pragma unroll
    for (int i = 0; i < 8; ++i) *(f32x4*)(part + (q * 8 + i) * 1280 + 16 * (20 * g + c)) = acc[i];
    lds_rendezvous((u32 LAS*)(smem + B_CNT + 4 * ch), 4);
    STAMP(1, 4);
    {
      const int i = tj >> 5, j5 = tj & 31, cc = j5 >> 1;
      float y[8];
#pragma unroll
      for (int hh = 0; hh < 2; ++hh) {
        const char* p = part + i * 1280 + 16 * (20 * (2 * (j5 & 1) + hh) + cc);
        const f32x4 p0 = *(const f32x4*)(p), p1 = *(const f32x4*)(p + 8 * 1280), p2 = *(const f32x4*)(p + 16 * 1280), p3 = *(const f32x4*)(p + 24 * 1280);
#pragma unroll
        for (int r = 0; r < 4; ++r) y[4 * hh + r] = (p0[r] + p1[r]) + (p2[r] + p3[r]);
      }
      float o[8];
#pragma unroll
      for (int k = 0; k < 8; ++k) {
        const float hx = (k & 1) ? bf_hi(hv[k >> 1]) : bf_lo(hv[k >> 1]);
        const float xx = (k & 1) ? bf_hi(xv[k >> 1]) : bf_lo(xv[k >> 1]);
        o[k] = xx + ga * (y[k] + hx * dsk);
      }
      *(u32x4*)(a.x1T + (size_t)d * L + 8 * tj) = u32x4{pk_bf16(o[0], o[1]), pk_bf16(o[2], o[3]), pk_bf16(o[4], o[5]), pk_bf16(o[6], o[7])};
    }
    STAMP(1, 5);
  } else {
    const int st = tid - 512, sw = w - 8;
    if (st < 8) *(u32*)(smem + B_CNT + 4 * st) = 0;
    asm volatile("s_waitcnt lgkmcnt(0)" ::: "memory");
    __builtin_amdgcn_s_barrier();
    SCHED_FENCE();
    float* red = (float*)(smem + B_RED);
    const bool act = wg < 224;
    const int f0 = 8 * ((wg & 7) * 28 + (wg >> 3));
    const int d8 = st >> 2, fp = st & 3;
    const int f8 = wg >> 1, dp = (wg & 1) * 128 + (st & 127);
    u32 wv[8], ov[8];
    f32x4 cb0, cb1, sb0, sb1;
    if (act) {
      cb0 = *(const f32x4*)(a.mod + 2048 + 8 * d8); cb1 = *(const f32x4*)(a.mod + 2048 + 8 * d8 + 4);
      sb0 = *(const f32x4*)(a.mod + 1536 + 8 * d8); sb1 = *(const f32x4*)(a.mod + 1536 + 8 * d8 + 4);
#pragma unroll
      for (int j = 0; j < 8; ++j) wv[j] = *(const u32*)(a.win + (size_t)(8 * d8 + j) * F + f0 + 2 * fp);
      if (st < 128) {
#pragma unroll
        for (int j = 0; j < 8; ++j) ov[j] = *(const u32*)(a.wout + (size_t)(8 * f8 + j) * D + 2 * dp);
      }
    }
    u32x4 pf[16];
    {
      const u16* p = a.wsp + ((size_t)((wg & 7) >> 1) * 512 + 16 * (wg >> 3)) * L + st * 8;
#pragma unroll
      for (int j = 0; j < 16; ++j) pf[j] = *(const u32x4*)(p + j * L);
    }
    SCHED_FENCE();
    if (act) {
      float lo[8], hi[8], u0 = 0, u1 = 0, v0 = 0, v1 = 0;
#pragma unroll
      for (int j = 0; j < 8; ++j) {
        const float Ac = rbf(1.f + (j < 4 ? cb0[j] : cb1[j - 4])), sb = j < 4 ? sb0[j] : sb1[j - 4];
        const float wl = bf_lo(wv[j]), wh = bf_hi(wv[j]);
        lo[j] = rbf(Ac * wl); hi[j] = rbf(Ac * wh);
        u0 += lo[j]; u1 += hi[j]; v0 += sb * wl; v1 += sb * wh;
      }
      const int f = f0 + 2 * fp;
      u16* dst = a.Wt + ((size_t)((f >> 4) * 64 + d8) * 16 + (f & 15)) * 8;
      *(u32x4*)dst = u32x4{pk_bf16(lo[0], lo[1]), pk_bf16(lo[2], lo[3]), pk_bf16(lo[4], lo[5]), pk_bf16(lo[6], lo[7])};
      *(u32x4*)(dst + 8) = u32x4{pk_bf16(hi[0], hi[1]), pk_bf16(hi[2], hi[3]), pk_bf16(hi[4], hi[5]), pk_bf16(hi[6], hi[7])};
      float rv[4] = {u0, u1, v0, v1};
#pragma unroll
      for (int k = 0; k < 4; ++k) {
        const float v = xor32_sum(xor16_sum(xor4_8_sum(rv[k])));
        if (lane < 4) red[sw * 16 + lane * 4 + k] = v;
      }
      if (st < 128) {
        const int dd = 2 * dp;
        u16* od = a.Wot + ((size_t)((dd >> 4) * 112 + f8) * 16 + (dd & 15)) * 8;
        u32x4 e, o;
#pragma unroll
        for (int k = 0; k < 4; ++k) {
          e[k] = (ov[2 * k] & 0xffffu) | (ov[2 * k + 1] << 16);
          o[k] = (ov[2 * k] >> 16) | (ov[2 * k + 1] & 0xffff0000u);
        }
        *(u32x4*)od = e; *(u32x4*)(od + 8) = o;
      }
      lds_rendezvous((u32 LAS*)(smem + B_CNT + 8), 4);
      if (st < 16) {
        const float s = (red[st] + red[16 + st]) + (red[32 + st] + red[48 + st]);
        const int fq = st >> 2, k = st & 3;
        a.uv[(k >> 1) * F + f0 + 2 * fq + (k & 1)] = s;
      }
    }
    STAMP(1, 1);
#pragma unroll
    for (int j = 0; j < 16; ++j) asm volatile("" ::"v"(pf[j]));
    STAMP(1, 5);
  }
}
struct CArgs {
  const u16 *x1T, *Wt; const float* uv;
  u16 *z1t, *z2t; u64* gran;
};
constexpr int C_X = 0;
constexpr int C_ST = 65536;
constexpr int C_MR = C_ST + 512;
constexpr int C_PS = C_MR + 8 * 512;
constexpr int C_FS = C_PS + 7 * 64 * 8;
constexpr int C_ZS = C_FS + 512;
constexpr int C_RS = 232;
constexpr int C_LDS = C_ZS + 64 * C_RS;

__device__ __forceinline__ float gelu_tanh(float z) {
  const float k0 = -2.f * 0.7978845608028654f * 1.4426950408889634f, k1 = k0 * 0.044715f;
  const float e = __builtin_amdgcn_exp2f(z * (k0 + k1 * (z * z)));
  return z * __builtin_amdgcn_rcpf(1.f + e);
}
__device__ __forceinline__ float pick4(f32x4 v, int r) { return r == 0 ? v[0] : r == 1 ? v[1] : r == 2 ? v[2] : v[3]; }

template <int ROLE>
__device__ __forceinline__ void c_body(const CArgs& a, char* smem, const int w) {
  const int wg = blockIdx.x, tid = threadIdx.x, lane = tid & 63;
  const int xc = wg & 7, ii = wg >> 3;
  const int pi = (xc >> 1) * 8 + (ii & 7), j = (xc & 1) * 4 + (ii >> 3);
  const int c = lane & 15, g = lane >> 4;
  STAMP(2, 0);
  const u32 lds0 = (u32)(size_t)(const char LAS*)smem;
#pragma unroll
  for (int i = 0; i < 8; ++i) {
    const int d = 64 * w + 8 * i + (lane >> 3);
    const int fz = (((d >> 1) & 1) | ((d >> 2) & 2)) << 1;
    glds16(a.x1T + (size_t)d * L + 64 * pi + 8 * ((lane & 7) ^ fz), lds0 + C_X + (64 * w + 8 * i) * 128);
  }
  __builtin_amdgcn_s_barrier();
  const int nt2 = 56 + 7 * j + (w < 7 ? w : 0), nt1 = 7 * j + (w > 0 ? w - 1 : 0);
  const u16* pw2 = a.Wt + (size_t)nt2 * 64 * 128 + lane * 8;
  const u16* pw1 = a.Wt + (size_t)nt1 * 64 * 128 + lane * 8;

  const int q4 = (lane & 15) >> 2, p4 = lane & 3;
  const int drow = 8 * g + q4;
  const int fz0 = (((drow >> 1) & 1) | ((drow >> 2) & 2)) << 1;
  const char LAS* xb = (const char LAS*)smem + C_X + drow * 128 + 8 * (p4 & 1);
  const char LAS* xt[4];
  int tb[4];
#pragma unroll
  for (int s = 0; s < 4; ++s) { tb[s] = (s + w) & 3; xt[s] = xb + 16 * ((2 * tb[s] + (p4 >> 1)) ^ fz0); }
  auto xfrag = [&](int s, int ks) -> bf16x8 {
    const char LAS* p = xt[s] + ks * 4096;
    const s16x4 lo = tr_read(p), hi = tr_read(p + 512);
    const u32x2 a0 = __builtin_bit_cast(u32x2, lo), a1 = __builtin_bit_cast(u32x2, hi);
    return __builtin_bit_cast(bf16x8, u32x4{a0[0], a0[1], a1[0], a1[1]});
  };
  f32x4 acc[4];
#pragma unroll
  for (int s = 0; s < 4; ++s) acc[s] = f32x4{0, 0, 0, 0};
  bf16x8 W1[16];
  f32x4 u2, v2, u1, v1;
  float* xs2 = (float*)(smem + C_ST);
  if constexpr (ROLE < 2) {
    bf16x8 W2[16];
#pragma unroll
    for (int ks = 0; ks < 16; ++ks) W2[ks] = *(const bf16x8*)(pw2 + 4 * ks * 128);
    u2 = *(const f32x4*)(a.uv + 16 * nt2 + 4 * g); v2 = *(const f32x4*)(a.uv + F + 16 * nt2 + 4 * g);
    SCHED_FENCE();
    VM_WAIT(18);
    __builtin_amdgcn_s_barrier();
    SCHED_FENCE();
    STAMP(2, 1);
    f32x4 sq = {0, 0, 0, 0};
    bf16x8 X[2][4];
#pragma unroll
    for (int s = 0; s < 4; ++s) X[0][s] = xfrag(s, 0);
#pragma unroll
    for (int ks = 0; ks < 16; ++ks) {
      if (ks + 1 < 16) {
#pragma unroll
        for (int s = 0; s < 4; ++s) X[(ks + 1) & 1][s] = xfrag(s, ks + 1);
      }
      if constexpr (ROLE == 1) W1[ks] = *(const bf16x8*)(pw1 + 4 * ks * 128);
      SCHED_FENCE();
#pragma unroll
      for (int s = 0; s < 4; ++s) acc[s] = __builtin_amdgcn_mfma_f32_16x16x32_bf16(W2[ks], X[ks & 1][s], acc[s], 0, 0, 0);
      sq = __builtin_amdgcn_mfma_f32_16x16x32_bf16(X[ks & 1][0], X[ks & 1][0], sq, 0, 0, 0);
      SCHED_FENCE();
    }
    if constexpr (ROLE == 1) { u1 = *(const f32x4*)(a.uv + 16 * nt1 + 4 * g); v1 = *(const f32x4*)(a.uv + F + 16 * nt1 + 4 * g); }
    if (w < 4 && (c >> 2) == g) xs2[64 + 16 * w + c] = pick4(sq, c & 3);
  } else {
    VM_WAIT(0);
    __builtin_amdgcn_s_barrier();
    SCHED_FENCE();
    STAMP(2, 1);
    const u32x4 ones = {0x3f803f80u, 0x3f803f80u, 0x3f803f80u, 0x3f803f80u};
    bf16x8 X[2][4];
#pragma unroll
    for (int s = 0; s < 4; ++s) X[0][s] = xfrag(s, 0);
#pragma unroll
    for (int ks = 0; ks < 16; ++ks) {
      if (ks + 1 < 16) {
#pragma unroll
        for (int s = 0; s < 4; ++s) X[(ks + 1) & 1][s] = xfrag(s, ks + 1);
      }
      W1[ks] = *(const bf16x8*)(pw1 + 4 * ks * 128);
      SCHED_FENCE();
#pragma unroll
      for (int s = 0; s < 4; ++s) acc[s] = __builtin_amdgcn_mfma_f32_16x16x32_bf16(__builtin_bit_cast(bf16x8, ones), X[ks & 1][s], acc[s], 0, 0, 0);
      SCHED_FENCE();
    }
    u1 = *(const f32x4*)(a.uv + 16 * nt1 + 4 * g); v1 = *(const f32x4*)(a.uv + F + 16 * nt1 + 4 * g);
    if (g == 0) {
#pragma unroll
      for (int s = 0; s < 4; ++s) xs2[16 * tb[s] + c] = acc[s][0];
    }
  }
  STAMP(2, 2);
  __syncthreads();
  STAMP(2, 3);
  float mu[4], ri[4];
  {
    const float mean = xs2[lane] * (1.f / D);
    const float var = fmaxf(xs2[64 + lane] * (1.f / D) - mean * mean, 0.f);
    f32x2* mr = (f32x2*)(smem + C_MR + w * 512);
    mr[lane] = f32x2{rbf(mean), __builtin_amdgcn_rcpf(rbf(__builtin_amdgcn_sqrtf(rbf(rbf(var) + 1e-5f))))};
#pragma unroll
    for (int s = 0; s < 4; ++s) { const f32x2 v = mr[16 * tb[s] + c]; mu[s] = v[0]; ri[s] = v[1]; }
  }
  u32x2 z2p[4];
  if constexpr (ROLE < 2) {
    float* ps = (float*)(smem + C_PS) + w * 128;
    const s16x4 ones4 = {0x3f80, 0x3f80, 0x3f80, 0x3f80};
#pragma unroll
    for (int s = 0; s < 4; ++s) {
      float z[4];
#pragma unroll
      for (int r = 0; r < 4; ++r) z[r] = gelu_tanh(ri[s] * (acc[s][r] - mu[s] * u2[r]) + v2[r]);
      z2p[s] = u32x2{pk_bf16(z[0], z[1]), pk_bf16(z[2], z[3])};
      const s16x4 zz = __builtin_bit_cast(s16x4, z2p[s]);
      const f32x4 zero = {0, 0, 0, 0};
      const f32x4 s1 = __builtin_amdgcn_mfma_f32_16x16x16bf16_1k(ones4, zz, zero, 0, 0, 0);
      const f32x4 s2 = __builtin_amdgcn_mfma_f32_16x16x16bf16_1k(zz, zz, zero, 0, 0, 0);
      if ((c >> 2) == g) *(f32x2*)(ps + (16 * tb[s] + c) * 2) = f32x2{s1[0], pick4(s2, c & 3)};
    }
  }
  STAMP(2, 4);
  __syncthreads();
  STAMP(2, 5);
  if constexpr (ROLE == 0) {
    const float* ps = (const float*)(smem + C_PS);
    float s1 = 0, s2 = 0;
#pragma unroll
    for (int k = 0; k < 7; ++k) { const f32x2 v = *(const f32x2*)(ps + k * 128 + lane * 2); s1 += v[0]; s2 += v[1]; }
    GAS u64* gp = (GAS u64*)a.gran + (size_t)pi * 8 * 128;
    __hip_atomic_store(gp + (j * 64 + lane) * 2, (1ull << 32) | __float_as_uint(s1), __ATOMIC_RELAXED, __HIP_MEMORY_SCOPE_AGENT);
    __hip_atomic_store(gp + (j * 64 + lane) * 2 + 1, (1ull << 32) | __float_as_uint(s2), __ATOMIC_RELAXED, __HIP_MEMORY_SCOPE_AGENT);
    float t1 = 0, t2 = 0;
    for (u32 spin = 0; spin < (1u << 22); ++spin) {
      bool ok = true; t1 = 0; t2 = 0;
#pragma unroll
      for (int k = 0; k < 8; ++k) {
        const u64 x0 = __hip_atomic_load(gp + (k * 64 + lane) * 2, __ATOMIC_RELAXED, __HIP_MEMORY_SCOPE_AGENT);
        const u64 x1 = __hip_atomic_load(gp + (k * 64 + lane) * 2 + 1, __ATOMIC_RELAXED, __HIP_MEMORY_SCOPE_AGENT);
        ok = ok && (x0 >> 32) == 1 && (x1 >> 32) == 1;
        t1 += __uint_as_float((u32)x0); t2 += __uint_as_float((u32)x1);
      }
      if (__all(ok)) break;
      __builtin_amdgcn_s_sleep(2);
    }
    const float mean = t1 * (1.f / FH);
    const float var = fmaxf(t2 * (1.f / FH) - mean * mean, 0.f);
    *(f32x2*)(smem + C_FS + lane * 8) = f32x2{rbf(mean), __builtin_amdgcn_rcpf(rbf(__builtin_amdgcn_sqrtf(rbf(rbf(var) + 1e-5f))))};
  } else {
#pragma unroll
    for (int s = 0; s < 4; ++s) acc[s] = f32x4{0, 0, 0, 0};
    bf16x8 X[2][4];
#pragma unroll
    for (int s = 0; s < 4; ++s) X[0][s] = xfrag(s, 0);
#pragma unroll
    for (int ks = 0; ks < 16; ++ks) {
      if (ks + 1 < 16) {
#pragma unroll
        for (int s = 0; s < 4; ++s) X[(ks + 1) & 1][s] = xfrag(s, ks + 1);
      }
      SCHED_FENCE();
#pragma unroll
      for (int s = 0; s < 4; ++s) acc[s] = __builtin_amdgcn_mfma_f32_16x16x32_bf16(W1[ks], X[ks & 1][s], acc[s], 0, 0, 0);
      SCHED_FENCE();
    }
    STAMP(2, 6);
    const int f8 = 14 * j + 2 * (w - 1) + (g >> 1);
#pragma unroll
    for (int s = 0; s < 4; ++s) {
      float z[4];
#pragma unroll
      for (int r = 0; r < 4; ++r) z[r] = gelu_tanh(ri[s] * (acc[s][r] - mu[s] * u1[r]) + v1[r]);
      *(u32x2*)(a.z1t + ((size_t)(4 * pi + tb[s]) * 112 + f8) * 128 + c * 8 + (g & 1) * 4) = u32x2{pk_bf16(z[0], z[1]), pk_bf16(z[2], z[3])};
    }
  }
  STAMP(2, 7);
  __syncthreads();
  STAMP(2, 8);
  if constexpr (ROLE < 2) {
#pragma unroll
    for (int s = 0; s < 4; ++s) {
      const f32x2 fs = *(const f32x2*)(smem + C_FS + (16 * tb[s] + c) * 8);
      const float a0 = (bf_lo(z2p[s][0]) - fs[0]) * fs[1], a1 = (bf_hi(z2p[s][0]) - fs[0]) * fs[1];
      const float a2 = (bf_lo(z2p[s][1]) - fs[0]) * fs[1], a3 = (bf_hi(z2p[s][1]) - fs[0]) * fs[1];
      *(u32x2*)(smem + C_ZS + (16 * tb[s] + c) * C_RS + (16 * w + 4 * g) * 2) = u32x2{pk_bf16(a0, a1), pk_bf16(a2, a3)};
    }
#pragma unroll
    for (int kk = 0; kk < 2; ++kk) {
      const char LAS* p = (const char LAS*)smem + C_ZS + (32 * kk + 8 * g + q4) * C_RS + (16 * w + 4 * p4) * 2;
      const s16x4 lo = tr_read(p), hi = tr_read(p + 4 * C_RS);
      const u32x2 a0 = __builtin_bit_cast(u32x2, lo), a1 = __builtin_bit_cast(u32x2, hi);
      *(u32x4*)(a.z2t + ((size_t)(7 * j + w) * 256 + 8 * pi + 4 * kk) * 128 + lane * 8) = u32x4{a0[0], a0[1], a1[0], a1[1]};
    }
  }
  STAMP(2, 9);
}

__global__ __launch_bounds__(512, 2) void kC(CArgs a) {
  extern __shared__ __attribute__((aligned(16))) char smem[];
  const int w = __builtin_amdgcn_readfirstlane(threadIdx.x >> 6);
  if (w == 0) c_body<0>(a, smem, w);
  else if (w < 7) c_body<1>(a, smem, w);
  else c_body<2>(a, smem, w);
}
struct DArgs { const u16 *wsp, *z2t, *z1t, *bsp, *Wot; u16* Pt; };
constexpr int D_RING = 32768;
constexpr int D_LDS = 4 * D_RING;

constexpr int d_ops(int it) { return it < 0 ? 0 : (it + 2 < 16 ? 7 : 0) + (((it & 1) == 0 && it / 2 + 2 < 8) ? 8 : 0); }
template <int N> __device__ __forceinline__ void vm_wait_c() { asm volatile("s_waitcnt vmcnt(%0)" ::"i"(N > 63 ? 63 : N) : "memory"); }

template <int IT>
__device__ __forceinline__ void d_iter(f32x4 (&acc)[4][7], bf16x8 (&Bf)[3][7], bf16x8 (&Af)[2][4], const u16* pb, const u16* const (&pa)[2],
                                       u32 ring, const char* const (&ap)[2][4]) {
  if constexpr (IT + 1 < 16) {
    constexpr int s = (IT + 1) >> 1;
    if constexpr ((IT & 1) == 1) vm_wait_c<(IT == 1 ? d_ops(0) : d_ops(IT - 2) + d_ops(IT - 1))>();
#pragma unroll
    for (int m = 0; m < 4; ++m) Af[(IT + 1) & 1][m] = *(const bf16x8*)(ap[(IT + 1) & 1][m] + (s & 3) * 8192);
  }
  SCHED_FENCE();
#pragma unroll
  for (int n = 0; n < 7; ++n) {
    if constexpr (IT + 2 < 16) Bf[(IT + 2) % 3][n] = *(const bf16x8*)(pb + ((size_t)n * 256 + 4 * (IT + 2)) * 128);
    if constexpr ((IT & 1) == 0 && IT / 2 + 2 < 8) {
      constexpr int s = IT / 2 + 2;
      glds16(pa[n & 1] + (size_t)(n >> 1) * 16 * L + 64 * s, ring + (s & 3) * 8192 + n * 1024);
      if (n == 6) glds16(pa[1] + (size_t)3 * 16 * L + 64 * s, ring + (s & 3) * 8192 + 7 * 1024);
    }
#pragma unroll
    for (int m = 0; m < 4; ++m) acc[m][n] = __builtin_amdgcn_mfma_f32_16x16x32_bf16(Bf[IT % 3][n], Af[IT & 1][m], acc[m][n], 0, 0, 0);
    SCHED_FENCE();
  }
}

__global__ __launch_bounds__(256, 1) void kD(DArgs a) {
  extern __shared__ __attribute__((aligned(16))) char smem[];
  const int wg = blockIdx.x, tid = threadIdx.x, lane = tid & 63;
  const int w = __builtin_amdgcn_readfirstlane(tid >> 6);
  const int xc = wg & 7, ii = wg >> 3;
  const int rt = (xc >> 1) * 8 + (ii & 7), ct = (xc & 1) * 4 + (ii >> 3);
  const int c = lane & 15, g = lane >> 4;
  STAMP(3, 0);
  const u32 ring = (u32)(size_t)(const char LAS*)smem + w * D_RING;
  const u16* pa[2];
#pragma unroll
  for (int par = 0; par < 2; ++par) {
    const int row = 8 * par + (lane >> 3);
    pa[par] = a.wsp + (size_t)(64 * rt + row) * L + 512 * w + 8 * ((lane & 7) ^ ((row >> 1) & 7));
  }
  const u16* pb = a.z2t + ((size_t)(7 * ct) * 256 + 64 * w) * 128 + lane * 8;
  auto ldA = [&](int s) {
#pragma unroll
    for (int i = 0; i < 8; ++i) glds16(pa[i & 1] + (size_t)(i >> 1) * 16 * L + 64 * s, ring + (s & 3) * 8192 + i * 1024);
  };
  bf16x8 Bf[3][7], Af[2][4];
  auto ldB = [&](int ks) {
#pragma unroll
    for (int n = 0; n < 7; ++n) Bf[ks][n] = *(const bf16x8*)(pb + ((size_t)n * 256 + 4 * ks) * 128);
  };
  ldA(0); SCHED_FENCE(); ldB(0); ldB(1); SCHED_FENCE(); ldA(1); SCHED_FENCE();
  f32x4 acc[4][7];
#pragma unroll
  for (int m = 0; m < 4; ++m)
#pragma unroll
    for (int n = 0; n < 7; ++n) acc[m][n] = f32x4{0, 0, 0, 0};
  const int asw = (c >> 1) & 7;
  const char* ap[2][4];
#pragma unroll
  for (int m = 0; m < 4; ++m) {
    const char* base = smem + w * D_RING + ((m + w) & 3) * 2048 + c * 128;
    ap[0][m] = base + 16 * (g ^ asw); ap[1][m] = base + 16 * ((4 + g) ^ asw);
  }
  vm_wait_c<7 + 7 + 8>();
#pragma unroll
  for (int m = 0; m < 4; ++m) Af[0][m] = *(const bf16x8*)(ap[0][m]);
  STAMP(3, 4);
  d_iter<0>(acc, Bf, Af, pb, pa, ring, ap);   d_iter<1>(acc, Bf, Af, pb, pa, ring, ap);
  d_iter<2>(acc, Bf, Af, pb, pa, ring, ap);   d_iter<3>(acc, Bf, Af, pb, pa, ring, ap);
  STAMP(3, 5);
  d_iter<4>(acc, Bf, Af, pb, pa, ring, ap);   d_iter<5>(acc, Bf, Af, pb, pa, ring, ap);
  d_iter<6>(acc, Bf, Af, pb, pa, ring, ap);   d_iter<7>(acc, Bf, Af, pb, pa, ring, ap);
  STAMP(3, 6);
  d_iter<8>(acc, Bf, Af, pb, pa, ring, ap);   d_iter<9>(acc, Bf, Af, pb, pa, ring, ap);
  d_iter<10>(acc, Bf, Af, pb, pa, ring, ap);  d_iter<11>(acc, Bf, Af, pb, pa, ring, ap);
  STAMP(3, 7);
  d_iter<12>(acc, Bf, Af, pb, pa, ring, ap);  d_iter<13>(acc, Bf, Af, pb, pa, ring, ap);
  u32x2 z1v[7];
#pragma unroll
  for (int n = 0; n < 7; ++n)
    z1v[n] = *(const u32x2*)(a.z1t + ((size_t)(4 * rt + w) * 112 + 14 * ct + 2 * n + (g >> 1)) * 128 + c * 8 + (g & 1) * 4);
  const float bs = bf1(a.bsp[64 * rt + 16 * w + c]);
  u32x4 pf[4];
  {
    const u16* p = a.Wot + (size_t)(xc & 1) * 16 * 112 * 128 + (size_t)ii * 7168 + tid * 8;
#pragma unroll
    for (int k = 0; k < 3; ++k) pf[k] = *(const u32x4*)(p + k * 2048);
    pf[3] = *(const u32x4*)(p + (tid < 128 ? 3 * 2048 : 0));
  }
  SCHED_FENCE();
  d_iter<14>(acc, Bf, Af, pb, pa, ring, ap);  d_iter<15>(acc, Bf, Af, pb, pa, ring, ap);
  STAMP(3, 1);
  __builtin_amdgcn_s_barrier();
#pragma unroll
  for (int m = 1; m < 4; ++m) {
    char* dst = smem + ((((m + w) & 3) * 3 + (m - 1)) * 7 * 64 + lane) * 16;
#pragma unroll
    for (int n = 0; n < 7; ++n) *(f32x4*)(dst + n * 1024) = acc[m][n];
  }
  __syncthreads();
  STAMP(3, 2);
  const char* src = smem + (w * 3 * 7 * 64 + lane) * 16;
#pragma unroll
  for (int n = 0; n < 7; ++n) {
    const f32x4 p0 = *(const f32x4*)(src + n * 1024), p1 = *(const f32x4*)(src + (7 + n) * 1024), p2 = *(const f32x4*)(src + (14 + n) * 1024);
    float gg[4];
#pragma unroll
    for (int e = 0; e < 4; ++e) gg[e] = ((acc[0][n][e] + p0[e]) + (p1[e] + p2[e])) + bs;
    *(u32x2*)(a.Pt + ((size_t)(4 * rt + w) * 112 + 14 * ct + 2 * n + (g >> 1)) * 128 + c * 8 + (g & 1) * 4) =
        u32x2{pk_bf16(bf_lo(z1v[n][0]) * gg[0], bf_hi(z1v[n][0]) * gg[1]), pk_bf16(bf_lo(z1v[n][1]) * gg[2], bf_hi(z1v[n][1]) * gg[3])};
  }
#pragma unroll
  for (int k = 0; k < 4; ++k) asm volatile("" ::"v"(pf[k]));
  STAMP(3, 3);
}
struct EArgs { const u16 *Pt, *Wot, *x1T; const float* mod; u16* out; };
constexpr int E_TS = 1152;
constexpr int E_PART = 0;
constexpr int E_X = 4 * 16 * E_TS;
constexpr int E_LDS = E_X + 64 * 132;

__global__ __launch_bounds__(256, 1) void kE(EArgs a) {
  extern __shared__ __attribute__((aligned(16))) char smem[];
  const int wg = blockIdx.x, tid = threadIdx.x, lane = tid & 63;
  const int w = __builtin_amdgcn_readfirstlane(tid >> 6);
  const int xc = wg & 7, ii = wg >> 3;
  const int rt = (xc >> 1) * 8 + (ii & 7), dt = (xc & 1) * 4 + (ii >> 3);
  const int c = lane & 15, g = lane >> 4;
  STAMP(4, 0);
  const u16* pp = a.Pt + ((size_t)(4 * rt) * 112 + 28 * w) * 128 + lane * 8;
  const u16* pw = a.Wot + ((size_t)(4 * dt) * 112 + 28 * w) * 128 + lane * 8;
  bf16x8 Pf[3][4], Wf[3][4];
  auto ld = [&](int k) {
#pragma unroll
    for (int m = 0; m < 4; ++m) {
      Wf[k % 3][m] = *(const bf16x8*)(pw + ((size_t)m * 112 + 4 * k) * 128);
      Pf[k % 3][m] = *(const bf16x8*)(pp + ((size_t)m * 112 + 4 * k) * 128);
    }
  };
  ld(0); ld(1);
  SCHED_FENCE();
  f32x4 acc[4][4];
#pragma unroll
  for (int m = 0; m < 4; ++m)
#pragma unroll
    for (int n = 0; n < 4; ++n) acc[m][n] = f32x4{0, 0, 0, 0};
  u32x4 xr[2];
  f32x4 gb0, gb1;
  const int d8 = lane & 7;
#pragma unroll
  for (int k = 0; k < 7; ++k) {
    if (k + 2 < 7) ld(k + 2);
    if (k == 5) {
#pragma unroll
      for (int kk = 0; kk < 2; ++kk) {
        const int id = tid + 256 * kk, dd = id >> 3, chn = id & 7;
        xr[kk] = *(const u32x4*)(a.x1T + (size_t)(64 * dt + dd) * L + 64 * rt + 8 * chn);
      }
      gb0 = *(const f32x4*)(a.mod + 2560 + 64 * dt + 8 * d8); gb1 = *(const f32x4*)(a.mod + 2560 + 64 * dt + 8 * d8 + 4);
    }
    SCHED_FENCE();
#pragma unroll
    for (int n = 0; n < 4; ++n)
#pragma unroll
      for (int m = 0; m < 4; ++m) acc[m][n] = __builtin_amdgcn_mfma_f32_16x16x32_bf16(Wf[k % 3][n], Pf[k % 3][m], acc[m][n], 0, 0, 0);
    SCHED_FENCE();
  }
  STAMP(4, 1);
#pragma unroll
  for (int m = 0; m < 4; ++m)
#pragma unroll
    for (int n = 0; n < 4; ++n) *(f32x4*)(smem + E_PART + (w * 16 + m * 4 + n) * E_TS + g * 288 + c * 16) = acc[m][n];
#pragma unroll
  for (int k = 0; k < 2; ++k) {
    const int id = tid + 256 * k, dd = id >> 3, chn = id & 7;
    u32* p = (u32*)(smem + E_X + dd * 132 + chn * 16);
    p[0] = xr[k][0]; p[1] = xr[k][1]; p[2] = xr[k][2]; p[3] = xr[k][3];
  }
  __syncthreads();
  STAMP(4, 2);
#pragma unroll
  for (int k = 0; k < 2; ++k) {
    const int t = 32 * k + 8 * w + (lane >> 3);
    float s[8];
#pragma unroll
    for (int hh = 0; hh < 2; ++hh) {
      const char* p = smem + E_PART + ((t >> 4) * 4 + (d8 >> 1)) * E_TS + (2 * (d8 & 1) + hh) * 288 + (t & 15) * 16;
      const f32x4 p0 = *(const f32x4*)p, p1 = *(const f32x4*)(p + 16 * E_TS), p2 = *(const f32x4*)(p + 32 * E_TS), p3 = *(const f32x4*)(p + 48 * E_TS);
#pragma unroll
      for (int e = 0; e < 4; ++e) s[4 * hh + e] = (p0[e] + p1[e]) + (p2[e] + p3[e]);
    }
    float o[8];
#pragma unroll
    for (int e = 0; e < 8; ++e) {
      const float x1 = bf1(*(const u16*)(smem + E_X + (8 * d8 + e) * 132 + t * 2));
      o[e] = x1 + (e < 4 ? gb0[e] : gb1[e - 4]) * s[e];
    }
    *(u32x4*)(a.out + (size_t)(64 * rt + t) * D + 64 * dt + 8 * d8) = u32x4{pk_bf16(o[0], o[1]), pk_bf16(o[2], o[3]), pk_bf16(o[4], o[5]), pk_bf16(o[6], o[7])};
  }
  STAMP(4, 3);
}
#define KMASK(b)
extern "C" void kernel_launch(void* const* d_in, const int* in_sizes, int n_in, void* d_out, int out_size,
                              void* d_ws, size_t ws_size, hipStream_t stream) {
  const u16* x = (const u16*)d_in[0];
  char* ws = (char*)d_ws;
  float* mod = (float*)(ws + 0);
  u32* stats1 = (u32*)(ws + 16384);
  float* uv = (float*)(ws + 32768);
  u64* gran = (u64*)(ws + 65536);
  u16* xT = (u16*)(ws + (1 << 20));
  u16* Krev = (u16*)(ws + (3 << 20));
  u16* x1T = (u16*)(ws + (5 << 20));
  u16* Wt = (u16*)(ws + (7 << 20));
  u16* Wot = (u16*)(ws + (9 << 20));
  u16* z1t = (u16*)(ws + (10 << 20));
  u16* z2t = (u16*)(ws + (14 << 20));
  u16* Pt = (u16*)(ws + (18 << 20));
  AArgs aa;
  aa.x = x; aa.Are = (const u16*)d_in[1]; aa.Aim = (const u16*)d_in[2]; aa.Bre = (const u16*)d_in[3]; aa.Bim = (const u16*)d_in[4];
  aa.Cre = (const u16*)d_in[5]; aa.Cim = (const u16*)d_in[6]; aa.logdt = (const u16*)d_in[7]; aa.c = (const u16*)d_in[14];
  aa.wmod = (const u16*)d_in[15]; aa.mod = mod; aa.stats1 = stats1; aa.xT = xT; aa.Krev = Krev; aa.gran = (u32x4*)gran;
  (void)hipFuncSetAttribute((const void*)kA, hipFuncAttributeMaxDynamicSharedMemorySize, A_LDS);
  KMASK(1) kA<<<256, 768, A_LDS, stream>>>(aa);
  BArgs ba;
  ba.xT = xT; ba.Krev = Krev; ba.Dskip = (const u16*)d_in[8]; ba.win = (const u16*)d_in[10]; ba.wout = (const u16*)d_in[13];
  ba.wsp = (const u16*)d_in[11]; ba.stats1 = stats1; ba.mod = mod; ba.x1T = x1T; ba.Wt = Wt; ba.Wot = Wot; ba.uv = uv;
  (void)hipFuncSetAttribute((const void*)kB, hipFuncAttributeMaxDynamicSharedMemorySize, B_LDS);
  KMASK(2) kB<<<256, 768, B_LDS, stream>>>(ba);
  CArgs ca;
  ca.x1T = x1T; ca.Wt = Wt; ca.uv = uv; ca.z1t = z1t; ca.z2t = z2t; ca.gran = gran;
  (void)hipFuncSetAttribute((const void*)kC, hipFuncAttributeMaxDynamicSharedMemorySize, C_LDS);
  KMASK(4) kC<<<256, 512, C_LDS, stream>>>(ca);
  DArgs da;
  da.wsp = (const u16*)d_in[11]; da.z2t = z2t; da.z1t = z1t; da.bsp = (const u16*)d_in[12]; da.Wot = Wot; da.Pt = Pt;
  (void)hipFuncSetAttribute((const void*)kD, hipFuncAttributeMaxDynamicSharedMemorySize, D_LDS);
  KMASK(8) kD<<<256, 256, D_LDS, stream>>>(da);
  EArgs ea;
  ea.Pt = Pt; ea.Wot = Wot; ea.x1T = x1T; ea.mod = mod; ea.out = (u16*)d_out;
  (void)hipFuncSetAttribute((const void*)kE, hipFuncAttributeMaxDynamicSharedMemorySize, E_LDS);
  KMASK(16) kE<<<256, 256, E_LDS, stream>>>(ea);
}
```

```cpp
#include <hip/hip_runtime.h>
#include <hip/hip_bf16.h>
#include <stdint.h>

typedef unsigned short u16;
typedef unsigned int u32;
typedef unsigned long long u64;
typedef __bf16 bf16x8 __attribute__((ext_vector_type(8)));
typedef __bf16 bf16x2 __attribute__((ext_vector_type(2)));
typedef short s16x4 __attribute__((ext_vector_type(4)));
typedef float f32x2 __attribute__((ext_vector_type(2)));
typedef float f32x4 __attribute__((ext_vector_type(4)));
typedef u32 u32x2 __attribute__((ext_vector_type(2)));
typedef u32 u32x4 __attribute__((ext_vector_type(4)));
#define LAS __attribute__((address_space(3)))
#define GAS __attribute__((address_space(1)))

constexpr int L = 2048, D = 512, NS = 128, F = 1792, FH = 896;

__device__ __forceinline__ float bf_lo(u32 w) { return __uint_as_float(w << 16); }
__device__ __forceinline__ float bf_hi(u32 w) { return __uint_as_float(w & 0xffff0000u); }
__device__ __forceinline__ float bf1(u16 h) { return __uint_as_float(((u32)h) << 16); }
__device__ __forceinline__ u32 pk_bf16(float lo, float hi) {
  f32x2 a = {lo, hi};
  bf16x2 p = __builtin_convertvector(a, bf16x2);
  return __builtin_bit_cast(u32, p);
}
__device__ __forceinline__ float rbf(float x) { return bf_lo(pk_bf16(x, x)); }

template <int CTRL, int ROWMASK = 0xf>
__device__ __forceinline__ float dpp_add(float x) {
  int y = __builtin_amdgcn_update_dpp(0, __float_as_int(x), CTRL, ROWMASK, 0xf, true);
  return x + __int_as_float(y);
}
__device__ __forceinline__ float row16_sum(float x) {
  x = dpp_add<0xB1>(x);
  x = dpp_add<0x4E>(x);
  x = dpp_add<0x141>(x);
  x = dpp_add<0x140>(x);
  return x;
}
__device__ __forceinline__ float wave_sum(float x) {
  x = row16_sum(x);
  float a = __int_as_float(__builtin_amdgcn_readlane(__float_as_int(x), 0));
  float b = __int_as_float(__builtin_amdgcn_readlane(__float_as_int(x), 16));
  float c = __int_as_float(__builtin_amdgcn_readlane(__float_as_int(x), 32));
  float d = __int_as_float(__builtin_amdgcn_readlane(__float_as_int(x), 48));
  return (a + b) + (c + d);
}
__device__ __forceinline__ s16x4 tr_read(const char LAS* p) {
  return __builtin_amdgcn_ds_read_tr16_b64_v4i16((s16x4 LAS*)p);
}

#define STAMP(kern, i)
__device__ __forceinline__ void glds16(const void* gsrc, u32 lds_dst) {
  u32 keep;
  asm volatile("s_mov_b32 %0, m0\n\ts_mov_b32 m0, %2\n\ts_nop 0\n\tglobal_load_lds_dwordx4 %1, off\n\ts_mov_b32 m0, %0"
               : "=&s"(keep) : "v"(gsrc), "s"(lds_dst) : "memory");
}
#define SCHED_FENCE() __builtin_amdgcn_sched_barrier(0)
#define VM_WAIT(n) asm volatile("s_waitcnt vmcnt(" #n ")" ::: "memory")
__device__ __forceinline__ float xor16_sum(float x) {
  const u32 v = __float_as_uint(x);
  auto r = __builtin_amdgcn_permlane16_swap(v, v, false, false);
  return __uint_as_float(r[0]) + __uint_as_float(r[1]);
}
__device__ __forceinline__ float xor32_sum(float x) {
  const u32 v = __float_as_uint(x);
  auto r = __builtin_amdgcn_permlane32_swap(v, v, false, false);
  return __uint_as_float(r[0]) + __uint_as_float(r[1]);
}
__device__ __forceinline__ float xor4_8_sum(float x) {
  x = x + __int_as_float(__builtin_amdgcn_update_dpp(0, __float_as_int(x), 0x124, 0xf, 0xf, true));
  x = x + __int_as_float(__builtin_amdgcn_update_dpp(0, __float_as_int(x), 0x128, 0xf, 0xf, true));
  return x;
}
__device__ __forceinline__ void lds_rendezvous(u32 LAS* cnt, u32 target) {
  asm volatile("s_waitcnt lgkmcnt(0)" ::: "memory");
  if ((threadIdx.x & 63) == 0) __hip_atomic_fetch_add(cnt, 1u, __ATOMIC_RELAXED, __HIP_MEMORY_SCOPE_WORKGROUP);
  while (__hip_atomic_load(cnt, __ATOMIC_RELAXED, __HIP_MEMORY_SCOPE_WORKGROUP) < target) __builtin_amdgcn_s_sleep(1);
  asm volatile("" ::: "memory");
}
struct AArgs {
  const u16 *x, *Are, *Aim, *Bre, *Bim, *Cre, *Cim, *logdt, *c, *wmod;
  float* mod; u32* stats1; u16* xT; u16* Krev; u32x4* gran;
};
constexpr int A_RS = 528;
constexpr int A_TAB = 0;
constexpr int A_KC = A_TAB + 2 * 64 * A_RS;
constexpr int A_TILE = A_KC + 2 * 768 * 4;
constexpr int A_RED = A_TILE + 8 * 1088;
constexpr int A_CNT = A_RED + 4 * 32 * 4;
constexpr int A_LDS = A_CNT + 16;

struct KIdx { u16 v[2048]; };
constexpr int bf16_round_int(int l) {
  int sh = 0;
  while ((l >> sh) >= 256) ++sh;
  if (sh == 0) return l;
  const int q = l >> sh, rem = l & ((1 << sh) - 1), half = 1 << (sh - 1);
  const int up = (rem > half || (rem == half && (q & 1))) ? 1 : 0;
  return (q + up) << sh;
}
constexpr KIdx make_kidx() {
  KIdx t{};
  for (int x = 0; x < 2048; ++x) {
    const int lr = bf16_round_int(2047 - x);
    const int aa = lr >> 7, b = (lr >> 3) & 15, r = lr & 7;
    const int row = lr < 256 ? 8 * aa + r : lr < 512 ? 16 + 4 * (aa - 2) + (r >> 1) : lr < 1024 ? 24 + 2 * (aa - 4) + (r >> 2) : 32 + (aa - 8);
    t.v[x] = (u16)(row * 16 + b);
  }
  return t;
}
__device__ const KIdx g_kidx = make_kidx();

struct cpx { float r, i; };
__device__ __forceinline__ cpx cmul(cpx a, cpx b) { return cpx{a.r * b.r - a.i * b.i, a.r * b.i + a.i * b.r}; }

__global__ __launch_bounds__(768) void kA(AArgs a) {
  extern __shared__ __attribute__((aligned(16))) char smem[];
  const int wg = blockIdx.x, tid = threadIdx.x, lane = tid & 63;
  const int w = __builtin_amdgcn_readfirstlane(tid >> 6);
  STAMP(0, 0);
  if (w < 8) {
    const int ch = w >> 2, nh = (w >> 1) & 1, part = w & 1, d = 2 * wg + ch;
    const int n = 64 * nh + lane, o = d * NS + n;
    const u16 hare = a.Are[o], haim = a.Aim[o], hbre = a.Bre[o], hbim = a.Bim[o], hcre = a.Cre[o], hcim = a.Cim[o], hdt = a.logdt[d];
    const u32x4 kix = *(const u32x4*)(g_kidx.v + 8 * (tid & 255));
    SCHED_FENCE();
    __builtin_amdgcn_s_barrier();
    SCHED_FENCE();
    const float dt = rbf(__expf(bf1(hdt)));
    const float lr = (dt * bf1(hare)) * 1.4426950408889634f, lp = (dt * bf1(haim)) * 0.15915494309189535f;
    auto cexp = [&](float l) -> cpx {
      const float e = __builtin_amdgcn_exp2f(lr * l), ph = __builtin_amdgcn_fractf(lp * l);
      return cpx{e * __builtin_amdgcn_cosf(ph), e * __builtin_amdgcn_sinf(ph)};
    };
    char* tab = smem + A_TAB + ch * (64 * A_RS) + n * 4;
    auto put = [&](int row, cpx v) { *(u32*)(tab + row * A_RS) = pk_bf16(v.r, v.i); };
    if (part == 0) {
      const cpx E1 = cexp(1.f), E128 = cexp(128.f), E512 = cexp(512.f);
      const cpx E2 = cmul(E1, E1), E3 = cmul(E2, E1), E4 = cmul(E2, E2), E5 = cmul(E4, E1), E6 = cmul(E4, E2), E7 = cmul(E4, E3);
      put(0, cpx{1.f, 0.f}); put(1, E1); put(2, E2); put(3, E3); put(4, E4); put(5, E5); put(6, E6); put(7, E7);
      put(8, E128); put(9, cmul(E128, E1)); put(10, cmul(E128, E2)); put(11, cmul(E128, E3));
      put(12, cmul(E128, E4)); put(13, cmul(E128, E5)); put(14, cmul(E128, E6)); put(15, cmul(E128, E7));
      const cpx E256 = cmul(E128, E128), E384 = cmul(E256, E128);
      put(16, E256); put(17, cmul(E256, E2)); put(18, cmul(E256, E4)); put(19, cmul(E256, E6));
      put(20, E384); put(21, cmul(E384, E2)); put(22, cmul(E384, E4)); put(23, cmul(E384, E6));
      const cpx E640 = cmul(E512, E128), E768 = cmul(E512, E256), E896 = cmul(E512, E384);
      put(24, E512); put(25, cmul(E512, E4)); put(26, E640); put(27, cmul(E640, E4));
      put(28, E768); put(29, cmul(E768, E4)); put(30, E896); put(31, cmul(E896, E4));
    } else {
      const cpx E8 = cexp(8.f), E128 = cexp(128.f), E1024 = cexp(1024.f);
      const float br = dt * bf1(hbre), bi = dt * bf1(hbim), cr = bf1(hcre), ci = bf1(hcim);
      const cpx W = cmul(cpx{cr, ci}, cpx{br, bi});
      const cpx E16 = cmul(E8, E8), E32 = cmul(E16, E16), E64 = cmul(E32, E32);
      cpx Y[8];
      Y[0] = W; Y[1] = cmul(W, E8); Y[2] = cmul(W, E16); Y[3] = cmul(Y[1], E16);
#pragma unroll
      for (int b = 0; b < 4; ++b) Y[4 + b] = cmul(Y[b], E32);
#pragma unroll
      for (int b = 0; b < 8; ++b) {
        put(48 + b, cpx{Y[b].r, -Y[b].i});
        const cpx t = cmul(Y[b], E64);
        put(56 + b, cpx{t.r, -t.i});
      }
      const cpx E256 = cmul(E128, E128), E512 = cmul(E256, E256);
      cpx P[4];
      P[0] = E1024; P[1] = cmul(E1024, E128); P[2] = cmul(E1024, E256); P[3] = cmul(P[1], E256);
#pragma unroll
      for (int b = 0; b < 4; ++b) { put(32 + b, P[b]); put(36 + b, cmul(P[b], E512)); }
      put(40, cmul(E1024, E1024));
    }
    STAMP(0, 1);
    lds_rendezvous((u32 LAS*)(smem + A_CNT), 8);
    STAMP(0, 2);
    if (w < 6) {
      const int c2 = w >= 3 ? 1 : 0, t = w - 3 * c2, i = lane & 15, g = lane >> 4;
      const char* tb = smem + A_TAB + c2 * (64 * A_RS) + 16 * g;
      const char* pa = tb + (16 * t + i) * A_RS, *pb = tb + (48 + i) * A_RS;
      f32x4 acc = {0, 0, 0, 0};
      bf16x8 Af[8], Bf[8];
#pragma unroll
      for (int s = 0; s < 8; ++s) {
        Af[s] = *(const bf16x8*)(pa + s * 64); Bf[s] = *(const bf16x8*)(pb + s * 64);
      }
#pragma unroll
      for (int s = 0; s < 8; ++s) acc = __builtin_amdgcn_mfma_f32_16x16x32_bf16(Af[s], Bf[s], acc, 0, 0, 0);
      float* Kc = (float*)(smem + A_KC) + c2 * 768 + t * 256;
#pragma unroll
      for (int r = 0; r < 4; ++r) Kc[(4 * g + r) * 16 + i] = acc[r];
    }
    STAMP(0, 3);
    lds_rendezvous((u32 LAS*)(smem + A_CNT + 4), 8);
    STAMP(0, 4);
    const float* Kc = (const float*)(smem + A_KC) + ch * 768;
    float kv[8];
#pragma unroll
    for (int j = 0; j < 4; ++j) { kv[2 * j] = Kc[kix[j] & 0xffffu]; kv[2 * j + 1] = Kc[kix[j] >> 16]; }
    *(u32x4*)(a.Krev + (size_t)d * L + 8 * (tid & 255)) =
        u32x4{pk_bf16(kv[0], kv[1]), pk_bf16(kv[2], kv[3]), pk_bf16(kv[4], kv[5]), pk_bf16(kv[6], kv[7])};
    STAMP(0, 5);
  } else {
    const int tw = w - 8;
    const int t0 = 8 * ((wg & 7) * 32 + (wg >> 3));
    u32x4 xr[2];
#pragma unroll
    for (int j = 0; j < 2; ++j) xr[j] = *(const u32x4*)(a.x + (size_t)(t0 + 2 * tw + j) * D + 8 * lane);
    const bool doM = wg < 96;
    u32x4 wm[8]; u16 cv[8];
    const int rr = lane >> 2, q = lane & 3;
    if (doM) {
#pragma unroll
      for (int p = 0; p < 8; ++p) cv[p] = a.c[p * 64 + tw * 16 + rr];
#pragma unroll
      for (int p = 0; p < 8; ++p) wm[p] = *(const u32x4*)(a.wmod + (size_t)(p * 64 + tw * 16 + rr) * 3072 + 32 * wg + 8 * q);
    }
    char* tile = smem + A_TILE;
    if (tw == 0 && lane < 4) *(u32*)(smem + A_CNT + 4 * lane) = 0;
    asm volatile("s_waitcnt lgkmcnt(0)" ::: "memory");
    SCHED_FENCE();
    __builtin_amdgcn_s_barrier();
    SCHED_FENCE();
#pragma unroll
    for (int j = 0; j < 2; ++j) *(u32x4*)(tile + (2 * tw + j) * 1088 + 16 * lane) = xr[j];
    STAMP(0, 1);
    lds_rendezvous((u32 LAS*)(smem + A_CNT + 8), 4);
    STAMP(0, 2);
#pragma unroll
    for (int ps = 0; ps < 2; ++ps) {
      const int d0 = 128 * tw + 64 * ps + 16 * (lane >> 4);
      const char LAS* src = (const char LAS*)tile + ((lane & 15) >> 2) * 1088 + 2 * (d0 + 4 * (lane & 3));
      const s16x4 lo = tr_read(src), hi = tr_read(src + 4 * 1088);
      u32x2 a0 = __builtin_bit_cast(u32x2, lo), a1 = __builtin_bit_cast(u32x2, hi);
      *(u32x4*)(a.xT + (size_t)(d0 + (lane & 15)) * L + t0) = u32x4{a0[0], a0[1], a1[0], a1[1]};
    }
    if (tw == 0) a.gran[wg * 64 + lane] = u32x4{0, 0, 0, 0};
    {
      float v[2][8], s[2];
#pragma unroll
      for (int j = 0; j < 2; ++j) {
#pragma unroll
        for (int k = 0; k < 4; ++k) { v[j][2 * k] = bf_lo(xr[j][k]); v[j][2 * k + 1] = bf_hi(xr[j][k]); }
        s[j] = ((v[j][0] + v[j][1]) + (v[j][2] + v[j][3])) + ((v[j][4] + v[j][5]) + (v[j][6] + v[j][7]));
      }
      float mean[2], q2[2];
#pragma unroll
      for (int j = 0; j < 2; ++j) mean[j] = wave_sum(s[j]) * (1.f / D);
#pragma unroll
      for (int j = 0; j < 2; ++j) {
        q2[j] = 0;
#pragma unroll
        for (int k = 0; k < 8; ++k) { const float dv = v[j][k] - mean[j]; q2[j] += dv * dv; }
      }
#pragma unroll
      for (int j = 0; j < 2; ++j) {
        const float var = wave_sum(q2[j]) * (1.f / D);
        const float sd = rbf(__builtin_amdgcn_sqrtf(rbf(rbf(var) + 1e-5f)));
        if (lane == 0) a.stats1[t0 + 2 * tw + j] = pk_bf16(mean[j], sd);
      }
    }
    float* red = (float*)(smem + A_RED);
    if (doM) {
      float acc[8] = {0, 0, 0, 0, 0, 0, 0, 0};
#pragma unroll
      for (int p = 0; p < 8; ++p) {
        const float cx = bf1(cv[p]);
        const float s = rbf(cx * __builtin_amdgcn_rcpf(1.f + __expf(-cx)));
#pragma unroll
        for (int k = 0; k < 4; ++k) { acc[2 * k] += s * bf_lo(wm[p][k]); acc[2 * k + 1] += s * bf_hi(wm[p][k]); }
      }
#pragma unroll
      for (int k = 0; k < 8; ++k) {
        float v = acc[k];
        v = xor32_sum(xor16_sum(xor4_8_sum(v)));
        if (rr == 0) red[tw * 32 + 8 * q + k] = v;
      }
      STAMP(0, 3);
      lds_rendezvous((u32 LAS*)(smem + A_CNT + 12), 4);
      STAMP(0, 4);
      if (tw == 0 && lane < 32)
        a.mod[32 * wg + lane] = rbf((red[lane] + red[32 + lane]) + (red[64 + lane] + red[96 + lane]));
    }
    STAMP(0, 5);
  }
}
struct BArgs {
  const u16 *xT, *Krev, *Dskip, *win, *wout, *wsp;
  const u32* stats1; const float* mod;
  u16 *x1T, *Wt, *Wot; float* uv;
};
constexpr int B_H = 0;
constexpr int B_HS = (256 + 2048) * 2;
constexpr int B_KC = B_H + 2 * B_HS;
constexpr int B_KS = 4160;
constexpr int B_PART = B_KC + 2 * 4 * B_KS;
constexpr int B_RED = B_PART + 2 * 4 * 8 * 1280;
constexpr int B_CNT = B_RED + 4 * 16 * 4;
constexpr int B_LDS = B_CNT + 32;

__global__ __launch_bounds__(768) void kB(BArgs a) {
  extern __shared__ __attribute__((aligned(16))) char smem[];
  const int wg = blockIdx.x, tid = threadIdx.x, lane = tid & 63;
  const int w = __builtin_amdgcn_readfirstlane(tid >> 6);
  STAMP(1, 0);
  if (w < 8) {
    const int ch = w >> 2, q = w & 3, d = 2 * wg + ch, tj = tid & 255;
    const u32x4 xv = *(const u32x4*)(a.xT + (size_t)d * L + 8 * tj);
    const u32x4 st0 = *(const u32x4*)(a.stats1 + 8 * tj), st1 = *(const u32x4*)(a.stats1 + 8 * tj + 4);
    const u32x4 k0 = *(const u32x4*)(a.Krev + (size_t)d * L + 8 * tj);
    const u32x4 k1r = *(const u32x4*)(a.Krev + (size_t)d * L + 8 * (tj < 255 ? tj + 1 : tj));
    const float sa = a.mod[d], Ac = rbf(1.f + a.mod[512 + d]), ga = a.mod[1024 + d], dsk = bf1(a.Dskip[d]);
    SCHED_FENCE();
    __builtin_amdgcn_s_barrier();
    SCHED_FENCE();
    char* hb = smem + B_H + ch * B_HS;
    char* kc = smem + B_KC + ch * 4 * B_KS;
    if (tj < 32) *(u32x4*)(hb + 16 * tj) = u32x4{0, 0, 0, 0};
    if (tj < 16) *(u32x4*)(kc + (tj >> 2) * B_KS + 16 * (256 + (tj & 3))) = u32x4{0, 0, 0, 0};
    {
      const u32 z = tj < 255 ? 0xffffffffu : 0u;
      const u32 wd[8] = {k0[0], k0[1], k0[2], k0[3], k1r[0] & z, k1r[1] & z, 0, 0};
      u32 od[5];
#pragma unroll
      for (int k = 0; k < 5; ++k) od[k] = __builtin_amdgcn_alignbit(wd[k + 1], wd[k], 16);
      *(u32x4*)(kc + 0 * B_KS + 16 * tj) = u32x4{wd[0], wd[1], wd[2], wd[3]};
      *(u32x4*)(kc + 1 * B_KS + 16 * tj) = u32x4{od[0], od[1], od[2], od[3]};
      *(u32x4*)(kc + 2 * B_KS + 16 * tj) = u32x4{wd[1], wd[2], wd[3], wd[4]};
      *(u32x4*)(kc + 3 * B_KS + 16 * tj) = u32x4{od[1], od[2], od[3], od[4]};
    }
    u32x4 hv;
    {
      float hh[8];
#pragma unroll
      for (int k = 0; k < 8; ++k) {
        const u32 s = k < 4 ? st0[k] : st1[k - 4];
        const float xx = (k & 1) ? bf_hi(xv[k >> 1]) : bf_lo(xv[k >> 1]);
        const float sc = __builtin_amdgcn_rcpf(bf_hi(s)) * Ac;
        hh[k] = (xx - bf_lo(s)) * sc + sa;
      }
      hv = u32x4{pk_bf16(hh[0], hh[1]), pk_bf16(hh[2], hh[3]), pk_bf16(hh[4], hh[5]), pk_bf16(hh[6], hh[7])};
    }
    *(u32x4*)(hb + 512 + 16 * tj) = hv;
    STAMP(1, 1);
    lds_rendezvous((u32 LAS*)(smem + B_CNT + 12 + 4 * 0) + ch * 2, 4);
    STAMP(1, 2);
    const int c = lane & 15, g = lane >> 4;
    const char* bbase = hb + 2 * (256 + 16 * c - 16 * (g >> 1) + 8 * (g & 1) - 32 * q) - 256;
    const int X = 2047 - 32 * q - 16 * (g >> 1) + 8 * (g & 1) - c;
    const char* abase = kc + (X & 3) * B_KS + 2 * (X & ~3) - 256 * 15;
    bf16x8 Bf[16], Af[16];
    auto lda = [&](int u) {
      const u32x2 lo = *(const u32x2*)(abase + 256 * (15 - u)), hi = *(const u32x2*)(abase + 256 * (15 - u) + 8);
      Af[u] = __builtin_bit_cast(bf16x8, u32x4{lo[0], lo[1], hi[0], hi[1]});
    };
    lda(0);
#pragma unroll
    for (int v = 1; v < 16; v += 2) Bf[v] = *(const bf16x8*)(bbase + 256 * v);
    lda(1);
#pragma unroll
    for (int v = 0; v < 16; v += 2) Bf[v] = *(const bf16x8*)(bbase + 256 * v);
    lda(2); lda(3);
    SCHED_FENCE();
    f32x4 acc[8];
#pragma unroll
    for (int i = 0; i < 8; ++i) acc[i] = f32x4{0, 0, 0, 0};
#pragma unroll
    for (int u = 0; u < 16; ++u) {
      if (u + 4 < 16) lda(u + 4);
      SCHED_FENCE();
#pragma unroll
      for (int i = u >> 1; i < 8; ++i) acc[i] = __builtin_amdgcn_mfma_f32_16x16x32_bf16(Af[u], Bf[2 * i - u + 1], acc[i], 0, 0, 0);
      SCHED_FENCE();
    }
    STAMP(1, 3);
    char* part = smem + B_PART + ch * (4 * 8 * 1280);
#pragma unroll
    for (int i = 0; i < 8; ++i) *(f32x4*)(part + (q * 8 + i) * 1280 + 16 * (20 * g + c)) = acc[i];
    lds_rendezvous((u32 LAS*)(smem + B_CNT + 4 * ch), 4);
    STAMP(1, 4);
    {
      const int i = tj >> 5, j5 = tj & 31, cc = j5 >> 1;
      float y[8];
#pragma unroll
      for (int hh = 0; hh < 2; ++hh) {
        const char* p = part + i * 1280 + 16 * (20 * (2 * (j5 & 1) + hh) + cc);
        const f32x4 p0 = *(const f32x4*)(p), p1 = *(const f32x4*)(p + 8 * 1280), p2 = *(const f32x4*)(p + 16 * 1280), p3 = *(const f32x4*)(p + 24 * 1280);
#pragma unroll
        for (int r = 0; r < 4; ++r) y[4 * hh + r] = (p0[r] + p1[r]) + (p2[r] + p3[r]);
      }
      float o[8];
#pragma unroll
      for (int k = 0; k < 8; ++k) {
        const float hx = (k & 1) ? bf_hi(hv[k >> 1]) : bf_lo(hv[k >> 1]);
        const float xx = (k & 1) ? bf_hi(xv[k >> 1]) : bf_lo(xv[k >> 1]);
        o[k] = xx + ga * (y[k] + hx * dsk);
      }
      *(u32x4*)(a.x1T + (size_t)d * L + 8 * tj) = u32x4{pk_bf16(o[0], o[1]), pk_bf16(o[2], o[3]), pk_bf16(o[4], o[5]), pk_bf16(o[6], o[7])};
    }
    STAMP(1, 5);
  } else {
    const int st = tid - 512, sw = w - 8;
    if (st < 8) *(u32*)(smem + B_CNT + 4 * st) = 0;
    asm volatile("s_waitcnt lgkmcnt(0)" ::: "memory");
    __builtin_amdgcn_s_barrier();
    SCHED_FENCE();
    float* red = (float*)(smem + B_RED);
    const bool act = wg < 224;
    const int f0 = 8 * ((wg & 7) * 28 + (wg >> 3));
    const int d8 = st >> 2, fp = st & 3;
    const int f8 = wg >> 1, dp = (wg & 1) * 128 + (st & 127);
    u32 wv[8], ov[8];
    f32x4 cb0, cb1, sb0, sb1;
    if (act) {
      cb0 = *(const f32x4*)(a.mod + 2048 + 8 * d8); cb1 = *(const f32x4*)(a.mod + 2048 + 8 * d8 + 4);
      sb0 = *(const f32x4*)(a.mod + 1536 + 8 * d8); sb1 = *(const f32x4*)(a.mod + 1536 + 8 * d8 + 4);
#pragma unroll
      for (int j = 0; j < 8; ++j) wv[j] = *(const u32*)(a.win + (size_t)(8 * d8 + j) * F + f0 + 2 * fp);
      if (st < 128) {
#pragma unroll
        for (int j = 0; j < 8; ++j) ov[j] = *(const u32*)(a.wout + (size_t)(8 * f8 + j) * D + 2 * dp);
      }
    }
    u32x4 pf[8];
    {
      const u16* p = a.wsp + (size_t)wg * 16384 + st * 8;
#pragma unroll
      for (int j = 0; j < 8; ++j) pf[j] = *(const u32x4*)(p + j * 2048);
    }
    SCHED_FENCE();
    if (act) {
      float lo[8], hi[8], u0 = 0, u1 = 0, v0 = 0, v1 = 0;
#pragma unroll
      for (int j = 0; j < 8; ++j) {
        const float Ac = rbf(1.f + (j < 4 ? cb0[j] : cb1[j - 4])), sb = j < 4 ? sb0[j] : sb1[j - 4];
        const float wl = bf_lo(wv[j]), wh = bf_hi(wv[j]);
        lo[j] = rbf(Ac * wl); hi[j] = rbf(Ac * wh);
        u0 += lo[j]; u1 += hi[j]; v0 += sb * wl; v1 += sb * wh;
      }
      const int f = f0 + 2 * fp;
      u16* dst = a.Wt + ((size_t)((f >> 4) * 64 + d8) * 16 + (f & 15)) * 8;
      *(u32x4*)dst = u32x4{pk_bf16(lo[0], lo[1]), pk_bf16(lo[2], lo[3]), pk_bf16(lo[4], lo[5]), pk_bf16(lo[6], lo[7])};
      *(u32x4*)(dst + 8) = u32x4{pk_bf16(hi[0], hi[1]), pk_bf16(hi[2], hi[3]), pk_bf16(hi[4], hi[5]), pk_bf16(hi[6], hi[7])};
      float rv[4] = {u0, u1, v0, v1};
#pragma unroll
      for (int k = 0; k < 4; ++k) {
        const float v = xor32_sum(xor16_sum(xor4_8_sum(rv[k])));
        if (lane < 4) red[sw * 16 + lane * 4 + k] = v;
      }
      if (st < 128) {
        const int dd = 2 * dp;
        u16* od = a.Wot + ((size_t)((dd >> 4) * 112 + f8) * 16 + (dd & 15)) * 8;
        u32x4 e, o;
#pragma unroll
        for (int k = 0; k < 4; ++k) {
          e[k] = (ov[2 * k] & 0xffffu) | (ov[2 * k + 1] << 16);
          o[k] = (ov[2 * k] >> 16) | (ov[2 * k + 1] & 0xffff0000u);
        }
        *(u32x4*)od = e; *(u32x4*)(od + 8) = o;
      }
      lds_rendezvous((u32 LAS*)(smem + B_CNT + 8), 4);
      if (st < 16) {
        const float s = (red[st] + red[16 + st]) + (red[32 + st] + red[48 + st]);
        const int fq = st >> 2, k = st & 3;
        a.uv[(k >> 1) * F + f0 + 2 * fq + (k & 1)] = s;
      }
    }
    STAMP(1, 1);
#pragma unroll
    for (int j = 0; j < 8; ++j) asm volatile("" ::"v"(pf[j]));
    STAMP(1, 5);
  }
}
struct CArgs {
  const u16 *x1T, *Wt; const float* uv;
  u16 *z1t, *z2t; u64* gran;
};
constexpr int C_X = 0;
constexpr int C_ST = 65536;
constexpr int C_MR = C_ST + 512;
constexpr int C_PS = C_MR + 8 * 512;
constexpr int C_FS = C_PS + 7 * 64 * 8;
constexpr int C_ZS = C_FS + 512;
constexpr int C_RS = 232;
constexpr int C_LDS = C_ZS + 64 * C_RS;

__device__ __forceinline__ float gelu_tanh(float z) {
  const float k0 = -2.f * 0.7978845608028654f * 1.4426950408889634f, k1 = k0 * 0.044715f;
  const float e = __builtin_amdgcn_exp2f(z * (k0 + k1 * (z * z)));
  return z * __builtin_amdgcn_rcpf(1.f + e);
}
__device__ __forceinline__ float pick4(f32x4 v, int r) { return r == 0 ? v[0] : r == 1 ? v[1] : r == 2 ? v[2] : v[3]; }

template <int ROLE>
__device__ __forceinline__ void c_body(const CArgs& a, char* smem, const int w) {
  const int wg = blockIdx.x, tid = threadIdx.x, lane = tid & 63;
  const int xc = wg & 7, ii = wg >> 3;
  const int pi = (xc >> 1) * 8 + (ii & 7), j = (xc & 1) * 4 + (ii >> 3);
  const int c = lane & 15, g = lane >> 4;
  STAMP(2, 0);
  const u32 lds0 = (u32)(size_t)(const char LAS*)smem;
#pragma unroll
  for (int i = 0; i < 8; ++i) {
    const int d = 64 * w + 8 * i + (lane >> 3);
    const int fz = (((d >> 1) & 1) | ((d >> 2) & 2)) << 1;
    glds16(a.x1T + (size_t)d * L + 64 * pi + 8 * ((lane & 7) ^ fz), lds0 + C_X + (64 * w + 8 * i) * 128);
  }
  __builtin_amdgcn_s_barrier();
  const int nt2 = 56 + 7 * j + (w < 7 ? w : 0), nt1 = 7 * j + (w > 0 ? w - 1 : 0);
  const u16* pw2 = a.Wt + (size_t)nt2 * 64 * 128 + lane * 8;
  const u16* pw1 = a.Wt + (size_t)nt1 * 64 * 128 + lane * 8;

  const int q4 = (lane & 15) >> 2, p4 = lane & 3;
  const int drow = 8 * g + q4;
  const int fz0 = (((drow >> 1) & 1) | ((drow >> 2) & 2)) << 1;
  const char LAS* xb = (const char LAS*)smem + C_X + drow * 128 + 8 * (p4 & 1);
  const char LAS* xt[4];
  int tb[4];
#pragma unroll
  for (int s = 0; s < 4; ++s) { tb[s] = (s + w) & 3; xt[s] = xb + 16 * ((2 * tb[s] + (p4 >> 1)) ^ fz0); }
  auto xfrag = [&](int s, int ks) -> bf16x8 {
    const char LAS* p = xt[s] + ks * 4096;
    const s16x4 lo = tr_read(p), hi = tr_read(p + 512);
    const u32x2 a0 = __builtin_bit_cast(u32x2, lo), a1 = __builtin_bit_cast(u32x2, hi);
    return __builtin_bit_cast(bf16x8, u32x4{a0[0], a0[1], a1[0], a1[1]});
  };
  f32x4 acc[4];
#pragma unroll
  for (int s = 0; s < 4; ++s) acc[s] = f32x4{0, 0, 0, 0};
  bf16x8 W1[16];
  f32x4 u2, v2, u1, v1;
  float* xs2 = (float*)(smem + C_ST);
  if constexpr (ROLE < 2) {
    bf16x8 W2[16];
#pragma unroll
    for (int ks = 0; ks < 16; ++ks) W2[ks] = *(const bf16x8*)(pw2 + 4 * ks * 128);
    u2 = *(const f32x4*)(a.uv + 16 * nt2 + 4 * g); v2 = *(const f32x4*)(a.uv + F + 16 * nt2 + 4 * g);
    SCHED_FENCE();
    VM_WAIT(18);
    __builtin_amdgcn_s_barrier();
    SCHED_FENCE();
    STAMP(2, 1);
    f32x4 sq = {0, 0, 0, 0};
    bf16x8 X[2][4];
#pragma unroll
    for (int s = 0; s < 4; ++s) X[0][s] = xfrag(s, 0);
#pragma unroll
    for (int ks = 0; ks < 16; ++ks) {
      if (ks + 1 < 16) {
#pragma unroll
        for (int s = 0; s < 4; ++s) X[(ks + 1) & 1][s] = xfrag(s, ks + 1);
      }
      if constexpr (ROLE == 1) W1[ks] = *(const bf16x8*)(pw1 + 4 * ks * 128);
      SCHED_FENCE();
#pragma unroll
      for (int s = 0; s < 4; ++s) acc[s] = __builtin_amdgcn_mfma_f32_16x16x32_bf16(W2[ks], X[ks & 1][s], acc[s], 0, 0, 0);
      sq = __builtin_amdgcn_mfma_f32_16x16x32_bf16(X[ks & 1][0], X[ks & 1][0], sq, 0, 0, 0);
      SCHED_FENCE();
    }
    if constexpr (ROLE == 1) { u1 = *(const f32x4*)(a.uv + 16 * nt1 + 4 * g); v1 = *(const f32x4*)(a.uv + F + 16 * nt1 + 4 * g); }
    if (w < 4 && (c >> 2) == g) xs2[64 + 16 * w + c] = pick4(sq, c & 3);
  } else {
    VM_WAIT(0);
    __builtin_amdgcn_s_barrier();
    SCHED_FENCE();
    STAMP(2, 1);
    const u32x4 ones = {0x3f803f80u, 0x3f803f80u, 0x3f803f80u, 0x3f803f80u};
    bf16x8 X[2][4];
#pragma unroll
    for (int s = 0; s < 4; ++s) X[0][s] = xfrag(s, 0);
#pragma unroll
    for (int ks = 0; ks < 16; ++ks) {
      if (ks + 1 < 16) {
#pragma unroll
        for (int s = 0; s < 4; ++s) X[(ks + 1) & 1][s] = xfrag(s, ks + 1);
      }
      W1[ks] = *(const bf16x8*)(pw1 + 4 * ks * 128);
      SCHED_FENCE();
#pragma unroll
      for (int s = 0; s < 4; ++s) acc[s] = __builtin_amdgcn_mfma_f32_16x16x32_bf16(__builtin_bit_cast(bf16x8, ones), X[ks & 1][s], acc[s], 0, 0, 0);
      SCHED_FENCE();
    }
    u1 = *(const f32x4*)(a.uv + 16 * nt1 + 4 * g); v1 = *(const f32x4*)(a.uv + F + 16 * nt1 + 4 * g);
    if (g == 0) {
#pragma unroll
      for (int s = 0; s < 4; ++s) xs2[16 * tb[s] + c] = acc[s][0];
    }
  }
  STAMP(2, 2);
  __syncthreads();
  STAMP(2, 3);
  float mu[4], ri[4];
  {
    const float mean = xs2[lane] * (1.f / D);
    const float var = fmaxf(xs2[64 + lane] * (1.f / D) - mean * mean, 0.f);
    f32x2* mr = (f32x2*)(smem + C_MR + w * 512);
    mr[lane] = f32x2{rbf(mean), __builtin_amdgcn_rcpf(rbf(__builtin_amdgcn_sqrtf(rbf(rbf(var) + 1e-5f))))};
#pragma unroll
    for (int s = 0; s < 4; ++s) { const f32x2 v = mr[16 * tb[s] + c]; mu[s] = v[0]; ri[s] = v[1]; }
  }
  u32x2 z2p[4];
  if constexpr (ROLE < 2) {
    float* ps = (float*)(smem + C_PS) + w * 128;
    const s16x4 ones4 = {0x3f80, 0x3f80, 0x3f80, 0x3f80};
#pragma unroll
    for (int s = 0; s < 4; ++s) {
      float z[4];
#pragma unroll
      for (int r = 0; r < 4; ++r) z[r] = gelu_tanh(ri[s] * (acc[s][r] - mu[s] * u2[r]) + v2[r]);
      z2p[s] = u32x2{pk_bf16(z[0], z[1]), pk_bf16(z[2], z[3])};
      const s16x4 zz = __builtin_bit_cast(s16x4, z2p[s]);
      const f32x4 zero = {0, 0, 0, 0};
      const f32x4 s1 = __builtin_amdgcn_mfma_f32_16x16x16bf16_1k(ones4, zz, zero, 0, 0, 0);
      const f32x4 s2 = __builtin_amdgcn_mfma_f32_16x16x16bf16_1k(zz, zz, zero, 0, 0, 0);
      if ((c >> 2) == g) *(f32x2*)(ps + (16 * tb[s] + c) * 2) = f32x2{s1[0], pick4(s2, c & 3)};
    }
  }
  STAMP(2, 4);
  __syncthreads();
  STAMP(2, 5);
  if constexpr (ROLE == 0) {
    const float* ps = (const float*)(smem + C_PS);
    float s1 = 0, s2 = 0;
#pragma unroll
    for (int k = 0; k < 7; ++k) { const f32x2 v = *(const f32x2*)(ps + k * 128 + lane * 2); s1 += v[0]; s2 += v[1]; }
    GAS u64* gp = (GAS u64*)a.gran + (size_t)pi * 8 * 128;
    __hip_atomic_store(gp + (j * 64 + lane) * 2, (1ull << 32) | __float_as_uint(s1), __ATOMIC_RELAXED, __HIP_MEMORY_SCOPE_AGENT);
    __hip_atomic_store(gp + (j * 64 + lane) * 2 + 1, (1ull << 32) | __float_as_uint(s2), __ATOMIC_RELAXED, __HIP_MEMORY_SCOPE_AGENT);
    float t1 = 0, t2 = 0;
    for (u32 spin = 0; spin < (1u << 22); ++spin) {
      bool ok = true; t1 = 0; t2 = 0;
#pragma unroll
      for (int k = 0; k < 8; ++k) {
        const u64 x0 = __hip_atomic_load(gp + (k * 64 + lane) * 2, __ATOMIC_RELAXED, __HIP_MEMORY_SCOPE_AGENT);
        const u64 x1 = __hip_atomic_load(gp + (k * 64 + lane) * 2 + 1, __ATOMIC_RELAXED, __HIP_MEMORY_SCOPE_AGENT);
        ok = ok && (x0 >> 32) == 1 && (x1 >> 32) == 1;
        t1 += __uint_as_float((u32)x0); t2 += __uint_as_float((u32)x1);
      }
      if (__all(ok)) break;
      __builtin_amdgcn_s_sleep(2);
    }
    const float mean = t1 * (1.f / FH);
    const float var = fmaxf(t2 * (1.f / FH) - mean * mean, 0.f);
    *(f32x2*)(smem + C_FS + lane * 8) = f32x2{rbf(mean), __builtin_amdgcn_rcpf(rbf(__builtin_amdgcn_sqrtf(rbf(rbf(var) + 1e-5f))))};
  } else {
#pragma unroll
    for (int s = 0; s < 4; ++s) acc[s] = f32x4{0, 0, 0, 0};
    bf16x8 X[2][4];
#pragma unroll
    for (int s = 0; s < 4; ++s) X[0][s] = xfrag(s, 0);
#pragma unroll
    for (int ks = 0; ks < 16; ++ks) {
      if (ks + 1 < 16) {
#pragma unroll
        for (int s = 0; s < 4; ++s) X[(ks + 1) & 1][s] = xfrag(s, ks + 1);
      }
      SCHED_FENCE();
#pragma unroll
      for (int s = 0; s < 4; ++s) acc[s] = __builtin_amdgcn_mfma_f32_16x16x32_bf16(W1[ks], X[ks & 1][s], acc[s], 0, 0, 0);
      SCHED_FENCE();
    }
    STAMP(2, 6);
    const int f8 = 14 * j + 2 * (w - 1) + (g >> 1);
#pragma unroll
    for (int s = 0; s < 4; ++s) {
      float z[4];
#pragma unroll
      for (int r = 0; r < 4; ++r) z[r] = gelu_tanh(ri[s] * (acc[s][r] - mu[s] * u1[r]) + v1[r]);
      *(u32x2*)(a.z1t + ((size_t)(4 * pi + tb[s]) * 112 + f8) * 128 + c * 8 + (g & 1) * 4) = u32x2{pk_bf16(z[0], z[1]), pk_bf16(z[2], z[3])};
    }
  }
  STAMP(2, 7);
  __syncthreads();
  STAMP(2, 8);
  if constexpr (ROLE < 2) {
#pragma unroll
    for (int s = 0; s < 4; ++s) {
      const f32x2 fs = *(const f32x2*)(smem + C_FS + (16 * tb[s] + c) * 8);
      const float a0 = (bf_lo(z2p[s][0]) - fs[0]) * fs[1], a1 = (bf_hi(z2p[s][0]) - fs[0]) * fs[1];
      const float a2 = (bf_lo(z2p[s][1]) - fs[0]) * fs[1], a3 = (bf_hi(z2p[s][1]) - fs[0]) * fs[1];
      *(u32x2*)(smem + C_ZS + (16 * tb[s] + c) * C_RS + (16 * w + 4 * g) * 2) = u32x2{pk_bf16(a0, a1), pk_bf16(a2, a3)};
    }
#pragma unroll
    for (int kk = 0; kk < 2; ++kk) {
      const char LAS* p = (const char LAS*)smem + C_ZS + (32 * kk + 8 * g + q4) * C_RS + (16 * w + 4 * p4) * 2;
      const s16x4 lo = tr_read(p), hi = tr_read(p + 4 * C_RS);
      const u32x2 a0 = __builtin_bit_cast(u32x2, lo), a1 = __builtin_bit_cast(u32x2, hi);
      *(u32x4*)(a.z2t + ((size_t)(7 * j + w) * 256 + 8 * pi + 4 * kk) * 128 + lane * 8) = u32x4{a0[0], a0[1], a1[0], a1[1]};
    }
  }
  STAMP(2, 9);
}

__global__ __launch_bounds__(512, 2) void kC(CArgs a) {
  extern __shared__ __attribute__((aligned(16))) char smem[];
  const int w = __builtin_amdgcn_readfirstlane(threadIdx.x >> 6);
  if (w == 0) c_body<0>(a, smem, w);
  else if (w < 7) c_body<1>(a, smem, w);
  else c_body<2>(a, smem, w);
}
struct DArgs { const u16 *wsp, *z2t, *z1t, *bsp, *Wot; u16* Pt; };
constexpr int D_RING = 32768;
constexpr int D_LDS = 4 * D_RING;

constexpr int d_ops(int it) { return it < 0 ? 0 : (it + 2 < 16 ? 7 : 0) + (((it & 1) == 0 && it / 2 + 2 < 8) ? 8 : 0); }
template <int N> __device__ __forceinline__ void vm_wait_c() { asm volatile("s_waitcnt vmcnt(%0)" ::"i"(N > 63 ? 63 : N) : "memory"); }

template <int IT>
__device__ __forceinline__ void d_iter(f32x4 (&acc)[4][7], bf16x8 (&Bf)[3][7], bf16x8 (&Af)[2][4], const u16* pb, const u16* const (&pa)[2],
                                       u32 ring, const char* const (&ap)[2][4]) {
  if constexpr (IT + 1 < 16) {
    constexpr int s = (IT + 1) >> 1;
    if constexpr ((IT & 1) == 1) vm_wait_c<(IT == 1 ? d_ops(0) : d_ops(IT - 2) + d_ops(IT - 1))>();
#pragma unroll
    for (int m = 0; m < 4; ++m) Af[(IT + 1) & 1][m] = *(const bf16x8*)(ap[(IT + 1) & 1][m] + (s & 3) * 8192);
  }
  SCHED_FENCE();
#pragma unroll
  for (int n = 0; n < 7; ++n) {
    if constexpr (IT + 2 < 16) Bf[(IT + 2) % 3][n] = *(const bf16x8*)(pb + ((size_t)n * 256 + 4 * (IT + 2)) * 128);
    if constexpr ((IT & 1) == 0 && IT / 2 + 2 < 8) {
      constexpr int s = IT / 2 + 2;
      glds16(pa[n & 1] + (size_t)(n >> 1) * 16 * L + 64 * s, ring + (s & 3) * 8192 + n * 1024);
      if (n == 6) glds16(pa[1] + (size_t)3 * 16 * L + 64 * s, ring + (s & 3) * 8192 + 7 * 1024);
    }
#pragma unroll
    for (int m = 0; m < 4; ++m) acc[m][n] = __builtin_amdgcn_mfma_f32_16x16x32_bf16(Bf[IT % 3][n], Af[IT & 1][m], acc[m][n], 0, 0, 0);
    SCHED_FENCE();
  }
}

__global__ __launch_bounds__(256, 1) void kD(DArgs a) {
  extern __shared__ __attribute__((aligned(16))) char smem[];
  const int wg = blockIdx.x, tid = threadIdx.x, lane = tid & 63;
  const int w = __builtin_amdgcn_readfirstlane(tid >> 6);
  const int xc = wg & 7, ii = wg >> 3;
  const int rt = (xc >> 1) * 8 + (ii & 7), ct = (xc & 1) * 4 + (ii >> 3);
  const int c = lane & 15, g = lane >> 4;
  STAMP(3, 0);
  const u32 ring = (u32)(size_t)(const char LAS*)smem + w * D_RING;
  const u16* pa[2];
#pragma unroll
  for (int par = 0; par < 2; ++par) {
    const int row = 8 * par + (lane >> 3);
    pa[par] = a.wsp + (size_t)(64 * rt + row) * L + 512 * w + 8 * ((lane & 7) ^ ((row >> 1) & 7));
  }
  const u16* pb = a.z2t + ((size_t)(7 * ct) * 256 + 64 * w) * 128 + lane * 8;
  auto ldA = [&](int s) {
#pragma unroll
    for (int i = 0; i < 8; ++i) glds16(pa[i & 1] + (size_t)(i >> 1) * 16 * L + 64 * s, ring + (s & 3) * 8192 + i * 1024);
  };
  bf16x8 Bf[3][7], Af[2][4];
  auto ldB = [&](int ks) {
#pragma unroll
    for (int n = 0; n < 7; ++n) Bf[ks][n] = *(const bf16x8*)(pb + ((size_t)n * 256 + 4 * ks) * 128);
  };
  ldA(0); SCHED_FENCE(); ldB(0); ldB(1); SCHED_FENCE(); ldA(1); SCHED_FENCE();
  f32x4 acc[4][7];
#pragma unroll
  for (int m = 0; m < 4; ++m)
#pragma unroll
    for (int n = 0; n < 7; ++n) acc[m][n] = f32x4{0, 0, 0, 0};
  const int asw = (c >> 1) & 7;
  const char* ap[2][4];
#pragma unroll
  for (int m = 0; m < 4; ++m) {
    const char* base = smem + w * D_RING + ((m + w) & 3) * 2048 + c * 128;
    ap[0][m] = base + 16 * (g ^ asw); ap[1][m] = base + 16 * ((4 + g) ^ asw);
  }
  vm_wait_c<7 + 7 + 8>();
#pragma unroll
  for (int m = 0; m < 4; ++m) Af[0][m] = *(const bf16x8*)(ap[0][m]);
  STAMP(3, 4);
  d_iter<0>(acc, Bf, Af, pb, pa, ring, ap);   d_iter<1>(acc, Bf, Af, pb, pa, ring, ap);
  d_iter<2>(acc, Bf, Af, pb, pa, ring, ap);   d_iter<3>(acc, Bf, Af, pb, pa, ring, ap);
  STAMP(3, 5);
  d_iter<4>(acc, Bf, Af, pb, pa, ring, ap);   d_iter<5>(acc, Bf, Af, pb, pa, ring, ap);
  d_iter<6>(acc, Bf, Af, pb, pa, ring, ap);   d_iter<7>(acc, Bf, Af, pb, pa, ring, ap);
  STAMP(3, 6);
  d_iter<8>(acc, Bf, Af, pb, pa, ring, ap);   d_iter<9>(acc, Bf, Af, pb, pa, ring, ap);
  d_iter<10>(acc, Bf, Af, pb, pa, ring, ap);  d_iter<11>(acc, Bf, Af, pb, pa, ring, ap);
  STAMP(3, 7);
  d_iter<12>(acc, Bf, Af, pb, pa, ring, ap);  d_iter<13>(acc, Bf, Af, pb, pa, ring, ap);
  u32x2 z1v[7];
#pragma unroll
  for (int n = 0; n < 7; ++n)
    z1v[n] = *(const u32x2*)(a.z1t + ((size_t)(4 * rt + w) * 112 + 14 * ct + 2 * n + (g >> 1)) * 128 + c * 8 + (g & 1) * 4);
  const float bs = bf1(a.bsp[64 * rt + 16 * w + c]);
  u32x4 pf[4];
  {
    const u16* p = a.Wot + (size_t)(xc & 1) * 16 * 112 * 128 + (size_t)ii * 7168 + tid * 8;
#pragma unroll
    for (int k = 0; k < 3; ++k) pf[k] = *(const u32x4*)(p + k * 2048);
    pf[3] = *(const u32x4*)(p + (tid < 128 ? 3 * 2048 : 0));
  }
  SCHED_FENCE();
  d_iter<14>(acc, Bf, Af, pb, pa, ring, ap);  d_iter<15>(acc, Bf, Af, pb, pa, ring, ap);
  STAMP(3, 1);
  __builtin_amdgcn_s_barrier();
#pragma unroll
  for (int m = 1; m < 4; ++m) {
    char* dst = smem + ((((m + w) & 3) * 3 + (m - 1)) * 7 * 64 + lane) * 16;
#pragma unroll
    for (int n = 0; n < 7; ++n) *(f32x4*)(dst + n * 1024) = acc[m][n];
  }
  __syncthreads();
  STAMP(3, 2);
  const char* src = smem + (w * 3 * 7 * 64 + lane) * 16;
#pragma unroll
  for (int n = 0; n < 7; ++n) {
    const f32x4 p0 = *(const f32x4*)(src + n * 1024), p1 = *(const f32x4*)(src + (7 + n) * 1024), p2 = *(const f32x4*)(src + (14 + n) * 1024);
    float gg[4];
#pragma unroll
    for (int e = 0; e < 4; ++e) gg[e] = ((acc[0][n][e] + p0[e]) + (p1[e] + p2[e])) + bs;
    *(u32x2*)(a.Pt + ((size_t)(4 * rt + w) * 112 + 14 * ct + 2 * n + (g >> 1)) * 128 + c * 8 + (g & 1) * 4) =
        u32x2{pk_bf16(bf_lo(z1v[n][0]) * gg[0], bf_hi(z1v[n][0]) * gg[1]), pk_bf16(bf_lo(z1v[n][1]) * gg[2], bf_hi(z1v[n][1]) * gg[3])};
  }
#pragma unroll
  for (int k = 0; k < 4; ++k) asm volatile("" ::"v"(pf[k]));
  STAMP(3, 3);
}
struct EArgs { const u16 *Pt, *Wot, *x1T; const float* mod; u16* out; };
constexpr int E_TS = 1152;
constexpr int E_PART = 0;
constexpr int E_X = 4 * 16 * E_TS;
constexpr int E_LDS = E_X + 64 * 132;

__global__ __launch_bounds__(256, 1) void kE(EArgs a) {
  extern __shared__ __attribute__((aligned(16))) char smem[];
  const int wg = blockIdx.x, tid = threadIdx.x, lane = tid & 63;
  const int w = __builtin_amdgcn_readfirstlane(tid >> 6);
  const int xc = wg & 7, ii = wg >> 3;
  const int rt = (xc >> 1) * 8 + (ii & 7), dt = (xc & 1) * 4 + (ii >> 3);
  const int c = lane & 15, g = lane >> 4;
  STAMP(4, 0);
  const u16* pp = a.Pt + ((size_t)(4 * rt) * 112 + 28 * w) * 128 + lane * 8;
  const u16* pw = a.Wot + ((size_t)(4 * dt) * 112 + 28 * w) * 128 + lane * 8;
  bf16x8 Pf[3][4], Wf[3][4];
  auto ld = [&](int k) {
#pragma unroll
    for (int m = 0; m < 4; ++m) {
      Wf[k % 3][m] = *(const bf16x8*)(pw + ((size_t)m * 112 + 4 * k) * 128);
      Pf[k % 3][m] = *(const bf16x8*)(pp + ((size_t)m * 112 + 4 * k) * 128);
    }
  };
  ld(0); ld(1);
  SCHED_FENCE();
  f32x4 acc[4][4];
#pragma unroll
  for (int m = 0; m < 4; ++m)
#pragma unroll
    for (int n = 0; n < 4; ++n) acc[m][n] = f32x4{0, 0, 0, 0};
  u32x4 xr[2];
  f32x4 gb0, gb1;
  const int d8 = lane & 7;
#pragma unroll
  for (int k = 0; k < 7; ++k) {
    if (k + 2 < 7) ld(k + 2);
    if (k == 5) {
#pragma unroll
      for (int kk = 0; kk < 2; ++kk) {
        const int id = tid + 256 * kk, dd = id >> 3, chn = id & 7;
        xr[kk] = *(const u32x4*)(a.x1T + (size_t)(64 * dt + dd) * L + 64 * rt + 8 * chn);
      }
      gb0 = *(const f32x4*)(a.mod + 2560 + 64 * dt + 8 * d8); gb1 = *(const f32x4*)(a.mod + 2560 + 64 * dt + 8 * d8 + 4);
    }
    SCHED_FENCE();
#pragma unroll
    for (int n = 0; n < 4; ++n)
#pragma unroll
      for (int m = 0; m < 4; ++m) acc[m][n] = __builtin_amdgcn_mfma_f32_16x16x32_bf16(Wf[k % 3][n], Pf[k % 3][m], acc[m][n], 0, 0, 0);
    SCHED_FENCE();
  }
  STAMP(4, 1);
#pragma unroll
  for (int m = 0; m < 4; ++m)
#pragma unroll
    for (int n = 0; n < 4; ++n) *(f32x4*)(smem + E_PART + (w * 16 + m * 4 + n) * E_TS + g * 288 + c * 16) = acc[m][n];
#pragma unroll
  for (int k = 0; k < 2; ++k) {
    const int id = tid + 256 * k, dd = id >> 3, chn = id & 7;
    u32* p = (u32*)(smem + E_X + dd * 132 + chn * 16);
    p[0] = xr[k][0]; p[1] = xr[k][1]; p[2] = xr[k][2]; p[3] = xr[k][3];
  }
  __syncthreads();
  STAMP(4, 2);
#pragma unroll
  for (int k = 0; k < 2; ++k) {
    const int t = 32 * k + 8 * w + (lane >> 3);
    float s[8];
#pragma unroll
    for (int hh = 0; hh < 2; ++hh) {
      const char* p = smem + E_PART + ((t >> 4) * 4 + (d8 >> 1)) * E_TS + (2 * (d8 & 1) + hh) * 288 + (t & 15) * 16;
      const f32x4 p0 = *(const f32x4*)p, p1 = *(const f32x4*)(p + 16 * E_TS), p2 = *(const f32x4*)(p + 32 * E_TS), p3 = *(const f32x4*)(p + 48 * E_TS);
#pragma unroll
      for (int e = 0; e < 4; ++e) s[4 * hh + e] = (p0[e] + p1[e]) + (p2[e] + p3[e]);
    }
    float o[8];
#pragma unroll
    for (int e = 0; e < 8; ++e) {
      const float x1 = bf1(*(const u16*)(smem + E_X + (8 * d8 + e) * 132 + t * 2));
      o[e] = x1 + (e < 4 ? gb0[e] : gb1[e - 4]) * s[e];
    }
    *(u32x4*)(a.out + (size_t)(64 * rt + t) * D + 64 * dt + 8 * d8) = u32x4{pk_bf16(o[0], o[1]), pk_bf16(o[2], o[3]), pk_bf16(o[4], o[5]), pk_bf16(o[6], o[7])};
  }
  STAMP(4, 3);
}
#define KMASK(b)
extern "C" void kernel_launch(void* const* d_in, const int* in_sizes, int n_in, void* d_out, int out_size,
                              void* d_ws, size_t ws_size, hipStream_t stream) {
  const u16* x = (const u16*)d_in[0];
  char* ws = (char*)d_ws;
  float* mod = (float*)(ws + 0);
  u32* stats1 = (u32*)(ws + 16384);
  float* uv = (float*)(ws + 32768);
  u64* gran = (u64*)(ws + 65536);
  u16* xT = (u16*)(ws + (1 << 20));
  u16* Krev = (u16*)(ws + (3 << 20));
  u16* x1T = (u16*)(ws + (5 << 20));
  u16* Wt = (u16*)(ws + (7 << 20));
  u16* Wot = (u16*)(ws + (9 << 20));
  u16* z1t = (u16*)(ws + (10 << 20));
  u16* z2t = (u16*)(ws + (14 << 20));
  u16* Pt = (u16*)(ws + (18 << 20));
  AArgs aa;
  aa.x = x; aa.Are = (const u16*)d_in[1]; aa.Aim = (const u16*)d_in[2]; aa.Bre = (const u16*)d_in[3]; aa.Bim = (const u16*)d_in[4];
  aa.Cre = (const u16*)d_in[5]; aa.Cim = (const u16*)d_in[6]; aa.logdt = (const u16*)d_in[7]; aa.c = (const u16*)d_in[14];
  aa.wmod = (const u16*)d_in[15]; aa.mod = mod; aa.stats1 = stats1; aa.xT = xT; aa.Krev = Krev; aa.gran = (u32x4*)gran;
  (void)hipFuncSetAttribute((const void*)kA, hipFuncAttributeMaxDynamicSharedMemorySize, A_LDS);
  KMASK(1) kA<<<256, 768, A_LDS, stream>>>(aa);
  BArgs ba;
  ba.xT = xT; ba.Krev = Krev; ba.Dskip = (const u16*)d_in[8]; ba.win = (const u16*)d_in[10]; ba.wout = (const u16*)d_in[13];
  ba.wsp = (const u16*)d_in[11]; ba.stats1 = stats1; ba.mod = mod; ba.x1T = x1T; ba.Wt = Wt; ba.Wot = Wot; ba.uv = uv;
  (void)hipFuncSetAttribute((const void*)kB, hipFuncAttributeMaxDynamicSharedMemorySize, B_LDS);
  KMASK(2) kB<<<256, 768, B_LDS, stream>>>(ba);
  CArgs ca;
  ca.x1T = x1T; ca.Wt = Wt; ca.uv = uv; ca.z1t = z1t; ca.z2t = z2t; ca.gran = gran;
  (void)hipFuncSetAttribute((const void*)kC, hipFuncAttributeMaxDynamicSharedMemorySize, C_LDS);
  KMASK(4) kC<<<256, 512, C_LDS, stream>>>(ca);
  DArgs da;
  da.wsp = (const u16*)d_in[11]; da.z2t = z2t; da.z1t = z1t; da.bsp = (const u16*)d_in[12]; da.Wot = Wot; da.Pt = Pt;
  (void)hipFuncSetAttribute((const void*)kD, hipFuncAttributeMaxDynamicSharedMemorySize, D_LDS);
  KMASK(8) kD<<<256, 256, D_LDS, stream>>>(da);
  EArgs ea;
  ea.Pt = Pt; ea.Wot = Wot; ea.x1T = x1T; ea.mod = mod; ea.out = (u16*)d_out;
  (void)hipFuncSetAttribute((const void*)kE, hipFuncAttributeMaxDynamicSharedMemorySize, E_LDS);
  KMASK(16) kE<<<256, 256, E_LDS, stream>>>(ea);
}
```

```cpp
#include <hip/hip_runtime.h>
#include <hip/hip_bf16.h>
#include <stdint.h>

typedef unsigned short u16;
typedef unsigned int u32;
typedef unsigned long long u64;
typedef __bf16 bf16x8 __attribute__((ext_vector_type(8)));
typedef __bf16 bf16x2 __attribute__((ext_vector_type(2)));
typedef short s16x4 __attribute__((ext_vector_type(4)));
typedef float f32x2 __attribute__((ext_vector_type(2)));
typedef float f32x4 __attribute__((ext_vector_type(4)));
typedef u32 u32x2 __attribute__((ext_vector_type(2)));
typedef u32 u32x4 __attribute__((ext_vector_type(4)));
#define LAS __attribute__((address_space(3)))
#define GAS __attribute__((address_space(1)))

constexpr int L = 2048, D = 512, NS = 128, F = 1792, FH = 896;

__device__ __forceinline__ float bf_lo(u32 w) { return __uint_as_float(w << 16); }
__device__ __forceinline__ float bf_hi(u32 w) { return __uint_as_float(w & 0xffff0000u); }
__device__ __forceinline__ float bf1(u16 h) { return __uint_as_float(((u32)h) << 16); }
__device__ __forceinline__ u32 pk_bf16(float lo, float hi) {
  f32x2 a = {lo, hi};
  bf16x2 p = __builtin_convertvector(a, bf16x2);
  return __builtin_bit_cast(u32, p);
}
__device__ __forceinline__ float rbf(float x) { return bf_lo(pk_bf16(x, x)); }

template <int CTRL, int ROWMASK = 0xf>
__device__ __forceinline__ float dpp_add(float x) {
  int y = __builtin_amdgcn_update_dpp(0, __float_as_int(x), CTRL, ROWMASK, 0xf, true);
  return x + __int_as_float(y);
}
__device__ __forceinline__ float row16_sum(float x) {
  x = dpp_add<0xB1>(x);
  x = dpp_add<0x4E>(x);
  x = dpp_add<0x141>(x);
  x = dpp_add<0x140>(x);
  return x;
}
__device__ __forceinline__ float wave_sum(float x) {
  x = row16_sum(x);
  float a = __int_as_float(__builtin_amdgcn_readlane(__float_as_int(x), 0));
  float b = __int_as_float(__builtin_amdgcn_readlane(__float_as_int(x), 16));
  float c = __int_as_float(__builtin_amdgcn_readlane(__float_as_int(x), 32));
  float d = __int_as_float(__builtin_amdgcn_readlane(__float_as_int(x), 48));
  return (a + b) + (c + d);
}
__device__ __forceinline__ s16x4 tr_read(const char LAS* p) {
  return __builtin_amdgcn_ds_read_tr16_b64_v4i16((s16x4 LAS*)p);
}

#define STAMP(kern, i)
__device__ __forceinline__ void glds16(const void* gsrc, u32 lds_dst) {
  u32 keep;
  asm volatile("s_mov_b32 %0, m0\n\ts_mov_b32 m0, %2\n\ts_nop 0\n\tglobal_load_lds_dwordx4 %1, off\n\ts_mov_b32 m0, %0"
               : "=&s"(keep) : "v"(gsrc), "s"(lds_dst) : "memory");
}
#define SCHED_FENCE() __builtin_amdgcn_sched_barrier(0)
#define VM_WAIT(n) asm volatile("s_waitcnt vmcnt(" #n ")" ::: "memory")
__device__ __forceinline__ float xor16_sum(float x) {
  const u32 v = __float_as_uint(x);
  auto r = __builtin_amdgcn_permlane16_swap(v, v, false, false);
  return __uint_as_float(r[0]) + __uint_as_float(r[1]);
}
__device__ __forceinline__ float xor32_sum(float x) {
  const u32 v = __float_as_uint(x);
  auto r = __builtin_amdgcn_permlane32_swap(v, v, false, false);
  return __uint_as_float(r[0]) + __uint_as_float(r[1]);
}
__device__ __forceinline__ float xor4_8_sum(float x) {
  x = x + __int_as_float(__builtin_amdgcn_update_dpp(0, __float_as_int(x), 0x124, 0xf, 0xf, true));
  x = x + __int_as_float(__builtin_amdgcn_update_dpp(0, __float_as_int(x), 0x128, 0xf, 0xf, true));
  return x;
}
__device__ __forceinline__ void lds_rendezvous(u32 LAS* cnt, u32 target) {
  asm volatile("s_waitcnt lgkmcnt(0)" ::: "memory");
  if ((threadIdx.x & 63) == 0) __hip_atomic_fetch_add(cnt, 1u, __ATOMIC_RELAXED, __HIP_MEMORY_SCOPE_WORKGROUP);
  while (__hip_atomic_load(cnt, __ATOMIC_RELAXED, __HIP_MEMORY_SCOPE_WORKGROUP) < target) __builtin_amdgcn_s_sleep(1);
  asm volatile("" ::: "memory");
}

typedef __amdgpu_buffer_rsrc_t rsrc_t;
__device__ __forceinline__ rsrc_t make_rsrc(const void* base, u32 bytes) {
  return __builtin_amdgcn_make_buffer_rsrc(const_cast<void*>(base), 0, bytes, 0x00020000);
}
__device__ __forceinline__ void wt_store16(rsrc_t r, u32 byte_off, u32x4 v) { __builtin_amdgcn_raw_buffer_store_b128(v, r, byte_off, 0, 16); }
__device__ __forceinline__ u32x4 pair16(u32x2 a, u32x2 b) {
  const auto r0 = __builtin_amdgcn_permlane16_swap(a[0], b[0], false, false);
  const auto r1 = __builtin_amdgcn_permlane16_swap(a[1], b[1], false, false);
  return u32x4{r0[0], r1[0], r0[1], r1[1]};
}
struct AArgs {
  const u16 *x, *Are, *Aim, *Bre, *Bim, *Cre, *Cim, *logdt, *c, *wmod, *win, *wout;
  float* mod; u32* stats1; u16* xT; u16* Krev; u32x4* gran;
};
constexpr int A_RS = 528;
constexpr int A_TAB = 0;
constexpr int A_KC = A_TAB + 2 * 64 * A_RS;
constexpr int A_TILE = A_KC + 2 * 768 * 4;
constexpr int A_RED = A_TILE + 8 * 1088;
constexpr int A_CNT = A_RED + 4 * 32 * 4;
constexpr int A_LDS = A_CNT + 16;

struct KIdx { u16 v[2048]; };
constexpr int bf16_round_int(int l) {
  int sh = 0;
  while ((l >> sh) >= 256) ++sh;
  if (sh == 0) return l;
  const int q = l >> sh, rem = l & ((1 << sh) - 1), half = 1 << (sh - 1);
  const int up = (rem > half || (rem == half && (q & 1))) ? 1 : 0;
  return (q + up) << sh;
}
constexpr KIdx make_kidx() {
  KIdx t{};
  for (int x = 0; x < 2048; ++x) {
    const int lr = bf16_round_int(2047 - x);
    const int aa = lr >> 7, b = (lr >> 3) & 15, r = lr & 7;
    const int row = lr < 256 ? 8 * aa + r : lr < 512 ? 16 + 4 * (aa - 2) + (r >> 1) : lr < 1024 ? 24 + 2 * (aa - 4) + (r >> 2) : 32 + (aa - 8);
    t.v[x] = (u16)(row * 16 + b);
  }
  return t;
}
__device__ const KIdx g_kidx = make_kidx();

struct cpx { float r, i; };
__device__ __forceinline__ cpx cmul(cpx a, cpx b) { return cpx{a.r * b.r - a.i * b.i, a.r * b.i + a.i * b.r}; }

__global__ __launch_bounds__(768) void kA(AArgs a) {
  extern __shared__ __attribute__((aligned(16))) char smem[];
  const int wg = blockIdx.x, tid = threadIdx.x, lane = tid & 63;
  const int w = __builtin_amdgcn_readfirstlane(tid >> 6);
  STAMP(0, 0);
  if (w < 8) {
    __builtin_amdgcn_s_setprio(2);
    const int ch = w >> 2, nh = (w >> 1) & 1, part = w & 1, d = 2 * wg + ch;
    const int n = 64 * nh + lane, o = d * NS + n;
    const u16 hare = a.Are[o], haim = a.Aim[o], hbre = a.Bre[o], hbim = a.Bim[o], hcre = a.Cre[o], hcim = a.Cim[o], hdt = a.logdt[d];
    const u32x4 kix = *(const u32x4*)(g_kidx.v + 8 * (tid & 255));
    SCHED_FENCE();
    __builtin_amdgcn_s_barrier();
    SCHED_FENCE();
    const float dt = rbf(__expf(bf1(hdt)));
    const float lr = (dt * bf1(hare)) * 1.4426950408889634f, lp = (dt * bf1(haim)) * 0.15915494309189535f;
    auto cexp = [&](float l) -> cpx {
      const float e = __builtin_amdgcn_exp2f(lr * l), ph = __builtin_amdgcn_fractf(lp * l);
      return cpx{e * __builtin_amdgcn_cosf(ph), e * __builtin_amdgcn_sinf(ph)};
    };
    char* tab = smem + A_TAB + ch * (64 * A_RS) + n * 4;
    auto put = [&](int row, cpx v) { *(u32*)(tab + row * A_RS) = pk_bf16(v.r, v.i); };
    if (part == 0) {
      const cpx E1 = cexp(1.f), E128 = cexp(128.f), E512 = cexp(512.f);
      const cpx E2 = cmul(E1, E1), E3 = cmul(E2, E1), E4 = cmul(E2, E2), E5 = cmul(E4, E1), E6 = cmul(E4, E2), E7 = cmul(E4, E3);
      put(0, cpx{1.f, 0.f}); put(1, E1); put(2, E2); put(3, E3); put(4, E4); put(5, E5); put(6, E6); put(7, E7);
      put(8, E128); put(9, cmul(E128, E1)); put(10, cmul(E128, E2)); put(11, cmul(E128, E3));
      put(12, cmul(E128, E4)); put(13, cmul(E128, E5)); put(14, cmul(E128, E6)); put(15, cmul(E128, E7));
      const cpx E256 = cmul(E128, E128), E384 = cmul(E256, E128);
      put(16, E256); put(17, cmul(E256, E2)); put(18, cmul(E256, E4)); put(19, cmul(E256, E6));
      put(20, E384); put(21, cmul(E384, E2)); put(22, cmul(E384, E4)); put(23, cmul(E384, E6));
      const cpx E640 = cmul(E512, E128), E768 = cmul(E512, E256), E896 = cmul(E512, E384);
      put(24, E512); put(25, cmul(E512, E4)); put(26, E640); put(27, cmul(E640, E4));
      put(28, E768); put(29, cmul(E768, E4)); put(30, E896); put(31, cmul(E896, E4));
    } else {
      const cpx E8 = cexp(8.f), E128 = cexp(128.f), E1024 = cexp(1024.f);
      const float br = dt * bf1(hbre), bi = dt * bf1(hbim), cr = bf1(hcre), ci = bf1(hcim);
      const cpx W = cmul(cpx{cr, ci}, cpx{br, bi});
      const cpx E16 = cmul(E8, E8), E32 = cmul(E16, E16), E64 = cmul(E32, E32);
      cpx Y[8];
      Y[0] = W; Y[1] = cmul(W, E8); Y[2] = cmul(W, E16); Y[3] = cmul(Y[1], E16);
#pragma unroll
      for (int b = 0; b < 4; ++b) Y[4 + b] = cmul(Y[b], E32);
#pragma unroll
      for (int b = 0; b < 8; ++b) {
        put(48 + b, cpx{Y[b].r, -Y[b].i});
        const cpx t = cmul(Y[b], E64);
        put(56 + b, cpx{t.r, -t.i});
      }
      const cpx E256 = cmul(E128, E128), E512 = cmul(E256, E256);
      cpx P[4];
      P[0] = E1024; P[1] = cmul(E1024, E128); P[2] = cmul(E1024, E256); P[3] = cmul(P[1], E256);
#pragma unroll
      for (int b = 0; b < 4; ++b) { put(32 + b, P[b]); put(36 + b, cmul(P[b], E512)); }
      put(40, cmul(E1024, E1024));
    }
    STAMP(0, 1);
    lds_rendezvous((u32 LAS*)(smem + A_CNT), 8);
    STAMP(0, 2);
    if (w < 6) {
      const int c2 = w >= 3 ? 1 : 0, t = w - 3 * c2, i = lane & 15, g = lane >> 4;
      const char* tb = smem + A_TAB + c2 * (64 * A_RS) + 16 * g;
      const char* pa = tb + (16 * t + i) * A_RS, *pb = tb + (48 + i) * A_RS;
      f32x4 acc = {0, 0, 0, 0};
      bf16x8 Af[8], Bf[8];
#pragma unroll
      for (int s = 0; s < 8; ++s) {
        Af[s] = *(const bf16x8*)(pa + s * 64); Bf[s] = *(const bf16x8*)(pb + s * 64);
      }
#pragma unroll
      for (int s = 0; s < 8; ++s) acc = __builtin_amdgcn_mfma_f32_16x16x32_bf16(Af[s], Bf[s], acc, 0, 0, 0);
      float* Kc = (float*)(smem + A_KC) + c2 * 768 + t * 256;
#pragma unroll
      for (int r = 0; r < 4; ++r) Kc[(4 * g + r) * 16 + i] = acc[r];
    }
    STAMP(0, 3);
    lds_rendezvous((u32 LAS*)(smem + A_CNT + 4), 8);
    STAMP(0, 4);
    const float* Kc = (const float*)(smem + A_KC) + ch * 768;
    float kv[8];
#pragma unroll
    for (int j = 0; j < 4; ++j) { kv[2 * j] = Kc[kix[j] & 0xffffu]; kv[2 * j + 1] = Kc[kix[j] >> 16]; }
    *(u32x4*)(a.Krev + (size_t)d * L + 8 * (tid & 255)) =
        u32x4{pk_bf16(kv[0], kv[1]), pk_bf16(kv[2], kv[3]), pk_bf16(kv[4], kv[5]), pk_bf16(kv[6], kv[7])};
    STAMP(0, 5);
  } else {
    const int tw = w - 8;
    const int t0 = 8 * ((wg & 7) * 32 + (wg >> 3));
    u32x4 xr[2];
#pragma unroll
    for (int j = 0; j < 2; ++j) xr[j] = *(const u32x4*)(a.x + (size_t)(t0 + 2 * tw + j) * D + 8 * lane);
    const bool doM = wg < 96;
    u32x4 wm[8]; u16 cv[8];
    const int rr = lane >> 2, q = lane & 3;
    if (doM) {
#pragma unroll
      for (int p = 0; p < 8; ++p) cv[p] = a.c[p * 64 + tw * 16 + rr];
#pragma unroll
      for (int p = 0; p < 8; ++p) wm[p] = *(const u32x4*)(a.wmod + (size_t)(p * 64 + tw * 16 + rr) * 3072 + 32 * wg + 8 * q);
    }
    u32x4 pf[3];
    {
      const int st = tid - 512;
      pf[0] = *(const u32x4*)(a.win + (size_t)wg * 3584 + st * 8);
      pf[1] = *(const u32x4*)(a.win + (size_t)wg * 3584 + (st < 192 ? 2048 + st * 8 : 0));
      pf[2] = *(const u32x4*)(a.wout + (size_t)wg * 1792 + (st < 224 ? st * 8 : 0));
    }
    char* tile = smem + A_TILE;
    if (tw == 0 && lane < 4) *(u32*)(smem + A_CNT + 4 * lane) = 0;
    asm volatile("s_waitcnt lgkmcnt(0)" ::: "memory");
    SCHED_FENCE();
    __builtin_amdgcn_s_barrier();
    SCHED_FENCE();
#pragma unroll
    for (int j = 0; j < 2; ++j) *(u32x4*)(tile + (2 * tw + j) * 1088 + 16 * lane) = xr[j];
    STAMP(0, 1);
    lds_rendezvous((u32 LAS*)(smem + A_CNT + 8), 4);
    STAMP(0, 2);
#pragma unroll
    for (int ps = 0; ps < 2; ++ps) {
      const int d0 = 128 * tw + 64 * ps + 16 * (lane >> 4);
      const char LAS* src = (const char LAS*)tile + ((lane & 15) >> 2) * 1088 + 2 * (d0 + 4 * (lane & 3));
      const s16x4 lo = tr_read(src), hi = tr_read(src + 4 * 1088);
      u32x2 a0 = __builtin_bit_cast(u32x2, lo), a1 = __builtin_bit_cast(u32x2, hi);
      *(u32x4*)(a.xT + (size_t)(d0 + (lane & 15)) * L + t0) = u32x4{a0[0], a0[1], a1[0], a1[1]};
    }
    if (tw == 0) a.gran[wg * 64 + lane] = u32x4{0, 0, 0, 0};
    {
      float v[2][8], s[2];
#pragma unroll
      for (int j = 0; j < 2; ++j) {
#pragma unroll
        for (int k = 0; k < 4; ++k) { v[j][2 * k] = bf_lo(xr[j][k]); v[j][2 * k + 1] = bf_hi(xr[j][k]); }
        s[j] = ((v[j][0] + v[j][1]) + (v[j][2] + v[j][3])) + ((v[j][4] + v[j][5]) + (v[j][6] + v[j][7]));
      }
      float mean[2], q2[2];
#pragma unroll
      for (int j = 0; j < 2; ++j) mean[j] = wave_sum(s[j]) * (1.f / D);
#pragma unroll
      for (int j = 0; j < 2; ++j) {
        q2[j] = 0;
#pragma unroll
        for (int k = 0; k < 8; ++k) { const float dv = v[j][k] - mean[j]; q2[j] += dv * dv; }
      }
#pragma unroll
      for (int j = 0; j < 2; ++j) {
        const float var = wave_sum(q2[j]) * (1.f / D);
        const float sd = rbf(__builtin_amdgcn_sqrtf(rbf(rbf(var) + 1e-5f)));
        if (lane == 0) *(f32x2*)(a.stats1 + 2 * (t0 + 2 * tw + j)) = f32x2{rbf(mean[j]), __builtin_amdgcn_rcpf(sd)};
      }
    }
    float* red = (float*)(smem + A_RED);
    if (doM) {
      float acc[8] = {0, 0, 0, 0, 0, 0, 0, 0};
#pragma unroll
      for (int p = 0; p < 8; ++p) {
        const float cx = bf1(cv[p]);
        const float s = rbf(cx * __builtin_amdgcn_rcpf(1.f + __expf(-cx)));
#pragma unroll
        for (int k = 0; k < 4; ++k) { acc[2 * k] += s * bf_lo(wm[p][k]); acc[2 * k + 1] += s * bf_hi(wm[p][k]); }
      }
#pragma unroll
      for (int k = 0; k < 8; ++k) {
        float v = acc[k];
        v = xor32_sum(xor16_sum(xor4_8_sum(v)));
        if (rr == 0) red[tw * 32 + 8 * q + k] = v;
      }
      STAMP(0, 3);
      lds_rendezvous((u32 LAS*)(smem + A_CNT + 12), 4);
      STAMP(0, 4);
      if (tw == 0 && lane < 32)
        a.mod[32 * wg + lane] = rbf((red[lane] + red[32 + lane]) + (red[64 + lane] + red[96 + lane]));
    }
#pragma unroll
    for (int k = 0; k < 3; ++k) asm volatile("" ::"v"(pf[k]));
    STAMP(0, 5);
  }
}
struct BArgs {
  const u16 *xT, *Krev, *Dskip, *win, *wout, *wsp;
  const u32* stats1; const float* mod;
  u16 *x1T, *Wt, *Wot; float* uv;
};
constexpr int B_H = 0;
constexpr int B_HS = (256 + 2048) * 2;
constexpr int B_KC = B_H + 2 * B_HS;
constexpr int B_KS = 4160;
constexpr int B_PART = B_KC + 2 * 4 * B_KS;
constexpr int B_RED = B_PART + 2 * 4 * 8 * 1280;
constexpr int B_CNT = B_RED + 4 * 16 * 4;
constexpr int B_LDS = B_CNT + 32;

__global__ __launch_bounds__(768) void kB(BArgs a) {
  extern __shared__ __attribute__((aligned(16))) char smem[];
  const int wg = blockIdx.x, tid = threadIdx.x, lane = tid & 63;
  const int w = __builtin_amdgcn_readfirstlane(tid >> 6);
  STAMP(1, 0);
  if (w < 8) {
    __builtin_amdgcn_s_setprio(2);
    const int ch = w >> 2, q = w & 3, d = 2 * wg + ch, tj = tid & 255;
    const u32x4 xv = *(const u32x4*)(a.xT + (size_t)d * L + 8 * tj);
    f32x4 st[4];
#pragma unroll
    for (int k = 0; k < 4; ++k) st[k] = *(const f32x4*)(a.stats1 + 16 * tj + 4 * k);
    const u32x4 k0 = *(const u32x4*)(a.Krev + (size_t)d * L + 8 * tj);
    const u32x4 k1r = *(const u32x4*)(a.Krev + (size_t)d * L + 8 * (tj < 255 ? tj + 1 : tj));
    const float sa = a.mod[d], Ac = rbf(1.f + a.mod[512 + d]), ga = a.mod[1024 + d], dsk = bf1(a.Dskip[d]);
    SCHED_FENCE();
    __builtin_amdgcn_s_barrier();
    SCHED_FENCE();
    char* hb = smem + B_H + ch * B_HS;
    char* kc = smem + B_KC + ch * 4 * B_KS;
    if (tj < 32) *(u32x4*)(hb + 16 * tj) = u32x4{0, 0, 0, 0};
    if (tj < 16) *(u32x4*)(kc + (tj >> 2) * B_KS + 16 * (256 + (tj & 3))) = u32x4{0, 0, 0, 0};
    {
      const u32 z = tj < 255 ? 0xffffffffu : 0u;
      const u32 wd[8] = {k0[0], k0[1], k0[2], k0[3], k1r[0] & z, k1r[1] & z, 0, 0};
      u32 od[5];
#pragma unroll
      for (int k = 0; k < 5; ++k) od[k] = __builtin_amdgcn_alignbit(wd[k + 1], wd[k], 16);
      *(u32x4*)(kc + 0 * B_KS + 16 * tj) = u32x4{wd[0], wd[1], wd[2], wd[3]};
      *(u32x4*)(kc + 1 * B_KS + 16 * tj) = u32x4{od[0], od[1], od[2], od[3]};
      *(u32x4*)(kc + 2 * B_KS + 16 * tj) = u32x4{wd[1], wd[2], wd[3], wd[4]};
      *(u32x4*)(kc + 3 * B_KS + 16 * tj) = u32x4{od[1], od[2], od[3], od[4]};
    }
    u32x4 hv;
    {
      float hh[8];
#pragma unroll
      for (int k = 0; k < 8; ++k) {
        const float m = st[k >> 1][2 * (k & 1)], ri = st[k >> 1][2 * (k & 1) + 1];
        const float xx = (k & 1) ? bf_hi(xv[k >> 1]) : bf_lo(xv[k >> 1]);
        hh[k] = (xx - m) * (ri * Ac) + sa;
      }
      hv = u32x4{pk_bf16(hh[0], hh[1]), pk_bf16(hh[2], hh[3]), pk_bf16(hh[4], hh[5]), pk_bf16(hh[6], hh[7])};
    }
    *(u32x4*)(hb + 512 + 16 * tj) = hv;
    STAMP(1, 1);
    lds_rendezvous((u32 LAS*)(smem + B_CNT + 12 + 4 * 0) + ch * 2, 4);
    STAMP(1, 2);
    const int c = lane & 15, g = lane >> 4;
    const char* bbase = hb + 2 * (256 + 16 * c - 16 * (g >> 1) + 8 * (g & 1) - 32 * q) - 256;
    const int X = 2047 - 32 * q - 16 * (g >> 1) + 8 * (g & 1) - c;
    const char* abase = kc + (X & 3) * B_KS + 2 * (X & ~3) - 256 * 15;
    bf16x8 Bf[16], Af[16];
    auto lda = [&](int u) {
      const u32x2 lo = *(const u32x2*)(abase + 256 * (15 - u)), hi = *(const u32x2*)(abase + 256 * (15 - u) + 8);
      Af[u] = __builtin_bit_cast(bf16x8, u32x4{lo[0], lo[1], hi[0], hi[1]});
    };
    lda(0);
#pragma unroll
    for (int v = 1; v < 16; v += 2) Bf[v] = *(const bf16x8*)(bbase + 256 * v);
    lda(1);
#pragma unroll
    for (int v = 0; v < 16; v += 2) Bf[v] = *(const bf16x8*)(bbase + 256 * v);
    lda(2); lda(3);
    SCHED_FENCE();
    f32x4 acc[8];
#pragma unroll
    for (int i = 0; i < 8; ++i) acc[i] = f32x4{0, 0, 0, 0};
#pragma unroll
    for (int u = 0; u < 16; ++u) {
      if (u + 4 < 16) lda(u + 4);
      SCHED_FENCE();
#pragma unroll
      for (int i = u >> 1; i < 8; ++i) acc[i] = __builtin_amdgcn_mfma_f32_16x16x32_bf16(Af[u], Bf[2 * i - u + 1], acc[i], 0, 0, 0);
      SCHED_FENCE();
    }
    STAMP(1, 3);
    char* part = smem + B_PART + ch * (4 * 8 * 1280);
#pragma unroll
    for (int i = 0; i < 8; ++i) *(f32x4*)(part + (q * 8 + i) * 1280 + 16 * (20 * g + c)) = acc[i];
    lds_rendezvous((u32 LAS*)(smem + B_CNT + 4 * ch), 4);
    STAMP(1, 4);
    {
      const int i = tj >> 5, j5 = tj & 31, cc = j5 >> 1;
      float y[8];
#pragma unroll
      for (int hh = 0; hh < 2; ++hh) {
        const char* p = part + i * 1280 + 16 * (20 * (2 * (j5 & 1) + hh) + cc);
        const f32x4 p0 = *(const f32x4*)(p), p1 = *(const f32x4*)(p + 8 * 1280), p2 = *(const f32x4*)(p + 16 * 1280), p3 = *(const f32x4*)(p + 24 * 1280);
#pragma unroll
        for (int r = 0; r < 4; ++r) y[4 * hh + r] = (p0[r] + p1[r]) + (p2[r] + p3[r]);
      }
      float o[8];
#pragma unroll
      for (int k = 0; k < 8; ++k) {
        const float hx = (k & 1) ? bf_hi(hv[k >> 1]) : bf_lo(hv[k >> 1]);
        const float xx = (k & 1) ? bf_hi(xv[k >> 1]) : bf_lo(xv[k >> 1]);
        o[k] = xx + ga * (y[k] + hx * dsk);
      }
      wt_store16(make_rsrc(a.x1T, D * L * 2), (u32)(d * L + 8 * tj) * 2, u32x4{pk_bf16(o[0], o[1]), pk_bf16(o[2], o[3]), pk_bf16(o[4], o[5]), pk_bf16(o[6], o[7])});
    }
    STAMP(1, 5);
  } else {
    const int st = tid - 512, sw = w - 8;
    if (st < 8) *(u32*)(smem + B_CNT + 4 * st) = 0;
    asm volatile("s_waitcnt lgkmcnt(0)" ::: "memory");
    __builtin_amdgcn_s_barrier();
    SCHED_FENCE();
    float* red = (float*)(smem + B_RED);
    const bool act = wg < 224;
    const int f0 = 8 * ((wg & 7) * 28 + (wg >> 3));
    const int d8 = st >> 2, fp = st & 3;
    const int f8 = wg >> 1, dp = (wg & 1) * 128 + (st & 127);
    u32 wv[8], ov[8];
    f32x4 cb0, cb1, sb0, sb1;
    if (act) {
      cb0 = *(const f32x4*)(a.mod + 2048 + 8 * d8); cb1 = *(const f32x4*)(a.mod + 2048 + 8 * d8 + 4);
      sb0 = *(const f32x4*)(a.mod + 1536 + 8 * d8); sb1 = *(const f32x4*)(a.mod + 1536 + 8 * d8 + 4);
#pragma unroll
      for (int j = 0; j < 8; ++j) wv[j] = *(const u32*)(a.win + (size_t)(8 * d8 + j) * F + f0 + 2 * fp);
      if (st < 128) {
#pragma unroll
        for (int j = 0; j < 8; ++j) ov[j] = *(const u32*)(a.wout + (size_t)(8 * f8 + j) * D + 2 * dp);
      }
    }
    u32x4 pf[8];
    {
      const u16* p = a.wsp + (size_t)wg * 16384 + st * 8;
#pragma unroll
      for (int j = 0; j < 8; ++j) pf[j] = *(const u32x4*)(p + j * 2048);
    }
    SCHED_FENCE();
    if (act) {
      float lo[8], hi[8], u0 = 0, u1 = 0, v0 = 0, v1 = 0;
#pragma unroll
      for (int j = 0; j < 8; ++j) {
        const float Ac = rbf(1.f + (j < 4 ? cb0[j] : cb1[j - 4])), sb = j < 4 ? sb0[j] : sb1[j - 4];
        const float wl = bf_lo(wv[j]), wh = bf_hi(wv[j]);
        lo[j] = rbf(Ac * wl); hi[j] = rbf(Ac * wh);
        u0 += lo[j]; u1 += hi[j]; v0 += sb * wl; v1 += sb * wh;
      }
      const int f = f0 + 2 * fp;
      const rsrc_t rw = make_rsrc(a.Wt, D * F * 2);
      const u32 dst = (u32)((((f >> 4) * 64 + d8) * 16 + (f & 15)) * 16);
      wt_store16(rw, dst, u32x4{pk_bf16(lo[0], lo[1]), pk_bf16(lo[2], lo[3]), pk_bf16(lo[4], lo[5]), pk_bf16(lo[6], lo[7])});
      wt_store16(rw, dst + 16, u32x4{pk_bf16(hi[0], hi[1]), pk_bf16(hi[2], hi[3]), pk_bf16(hi[4], hi[5]), pk_bf16(hi[6], hi[7])});
      float rv[4] = {u0, u1, v0, v1};
#pragma unroll
      for (int k = 0; k < 4; ++k) {
        const float v = xor32_sum(xor16_sum(xor4_8_sum(rv[k])));
        if (lane < 4) red[sw * 16 + lane * 4 + k] = v;
      }
      if (st < 128) {
        const int dd = 2 * dp;
        const rsrc_t ro = make_rsrc(a.Wot, FH * D * 2);
        const u32 od = (u32)((((dd >> 4) * 112 + f8) * 16 + (dd & 15)) * 16);
        u32x4 e, o;
#pragma unroll
        for (int k = 0; k < 4; ++k) {
          e[k] = (ov[2 * k] & 0xffffu) | (ov[2 * k + 1] << 16);
          o[k] = (ov[2 * k] >> 16) | (ov[2 * k + 1] & 0xffff0000u);
        }
        wt_store16(ro, od, e); wt_store16(ro, od + 16, o);
      }
      lds_rendezvous((u32 LAS*)(smem + B_CNT + 8), 4);
      if (st < 16) {
        const float s = (red[st] + red[16 + st]) + (red[32 + st] + red[48 + st]);
        const int fq = st >> 2, k = st & 3;
        a.uv[(k >> 1) * F + f0 + 2 * fq + (k & 1)] = s;
      }
    }
    STAMP(1, 1);
#pragma unroll
    for (int j = 0; j < 8; ++j) asm volatile("" ::"v"(pf[j]));
    STAMP(1, 5);
  }
}
struct CArgs {
  const u16 *x1T, *Wt; const float* uv;
  u16 *z1t, *z2t; u64* gran;
};
constexpr int C_X = 0;
constexpr int C_ST = 65536;
constexpr int C_MR = C_ST + 512;
constexpr int C_PS = C_MR + 8 * 512;
constexpr int C_FS = C_PS + 7 * 64 * 8;
constexpr int C_ZS = C_FS + 512;
constexpr int C_RS = 232;
constexpr int C_LDS = C_ZS + 64 * C_RS;

__device__ __forceinline__ float gelu_tanh(float z) {
  const float k0 = -2.f * 0.7978845608028654f * 1.4426950408889634f, k1 = k0 * 0.044715f;
  const float e = __builtin_amdgcn_exp2f(z * (k0 + k1 * (z * z)));
  return z * __builtin_amdgcn_rcpf(1.f + e);
}
__device__ __forceinline__ float pick4(f32x4 v, int r) { return r == 0 ? v[0] : r == 1 ? v[1] : r == 2 ? v[2] : v[3]; }

template <int ROLE>
__device__ __forceinline__ void c_body(const CArgs& a, char* smem, const int w) {
  const int wg = blockIdx.x, tid = threadIdx.x, lane = tid & 63;
  const int xc = wg & 7, ii = wg >> 3;
  const int pi = (xc >> 1) * 8 + (ii & 7), j = (xc & 1) * 4 + (ii >> 3);
  const int c = lane & 15, g = lane >> 4;
  const int nt2 = 56 + 7 * j + (w < 7 ? w : 0), nt1 = 7 * j + (w > 0 ? w - 1 : 0);
  const u16* pw2 = a.Wt + (size_t)nt2 * 64 * 128 + lane * 8;
  const u16* pw1 = a.Wt + (size_t)nt1 * 64 * 128 + lane * 8;

  const int q4 = (lane & 15) >> 2, p4 = lane & 3;
  const int drow = 8 * g + q4;
  const int fz0 = (((drow >> 1) & 1) | ((drow >> 2) & 2)) << 1;
  const char LAS* xb = (const char LAS*)smem + C_X + drow * 128 + 8 * (p4 & 1);
  const char LAS* xt[4];
  int tb[4];
#pragma unroll
  for (int s = 0; s < 4; ++s) { tb[s] = (s + w) & 3; xt[s] = xb + 16 * ((2 * tb[s] + (p4 >> 1)) ^ fz0); }
  auto xfrag = [&](int s, int ks) -> bf16x8 {
    const char LAS* p = xt[s] + ks * 4096;
    const s16x4 lo = tr_read(p), hi = tr_read(p + 512);
    const u32x2 a0 = __builtin_bit_cast(u32x2, lo), a1 = __builtin_bit_cast(u32x2, hi);
    return __builtin_bit_cast(bf16x8, u32x4{a0[0], a0[1], a1[0], a1[1]});
  };
  f32x4 acc[4];
#pragma unroll
  for (int s = 0; s < 4; ++s) acc[s] = f32x4{0, 0, 0, 0};
  bf16x8 W1[16];
  f32x4 u2, v2, u1, v1;
  float* xs2 = (float*)(smem + C_ST);
  if constexpr (ROLE < 2) {
    bf16x8 W2[16];
#pragma unroll
    for (int ks = 0; ks < 16; ++ks) W2[ks] = *(const bf16x8*)(pw2 + 4 * ks * 128);
    u2 = *(const f32x4*)(a.uv + 16 * nt2 + 4 * g); v2 = *(const f32x4*)(a.uv + F + 16 * nt2 + 4 * g);
    SCHED_FENCE();
    VM_WAIT(18);
    __builtin_amdgcn_s_barrier();
    SCHED_FENCE();
    STAMP(2, 1);
    f32x4 sq = {0, 0, 0, 0};
    bf16x8 X[2][4];
#pragma unroll
    for (int s = 0; s < 4; ++s) X[0][s] = xfrag(s, 0);
#pragma unroll
    for (int ks = 0; ks < 16; ++ks) {
      if (ks + 1 < 16) {
#pragma unroll
        for (int s = 0; s < 4; ++s) X[(ks + 1) & 1][s] = xfrag(s, ks + 1);
      }
      SCHED_FENCE();
#pragma unroll
      for (int s = 0; s < 4; ++s) acc[s] = __builtin_amdgcn_mfma_f32_16x16x32_bf16(W2[ks], X[ks & 1][s], acc[s], 0, 0, 0);
      sq = __builtin_amdgcn_mfma_f32_16x16x32_bf16(X[ks & 1][0], X[ks & 1][0], sq, 0, 0, 0);
      SCHED_FENCE();
    }
    if constexpr (ROLE == 1) {
#pragma unroll
      for (int ks = 0; ks < 16; ++ks) W1[ks] = *(const bf16x8*)(pw1 + 4 * ks * 128);
      u1 = *(const f32x4*)(a.uv + 16 * nt1 + 4 * g); v1 = *(const f32x4*)(a.uv + F + 16 * nt1 + 4 * g);
      SCHED_FENCE();
    }
    if (w < 4 && (c >> 2) == g) xs2[64 + 16 * w + c] = pick4(sq, c & 3);
  } else {
    VM_WAIT(0);
    __builtin_amdgcn_s_barrier();
    SCHED_FENCE();
    STAMP(2, 1);
    const u32x4 ones = {0x3f803f80u, 0x3f803f80u, 0x3f803f80u, 0x3f803f80u};
    bf16x8 X[2][4];
#pragma unroll
    for (int s = 0; s < 4; ++s) X[0][s] = xfrag(s, 0);
#pragma unroll
    for (int ks = 0; ks < 16; ++ks) {
      if (ks + 1 < 16) {
#pragma unroll
        for (int s = 0; s < 4; ++s) X[(ks + 1) & 1][s] = xfrag(s, ks + 1);
      }
      SCHED_FENCE();
#pragma unroll
      for (int s = 0; s < 4; ++s) acc[s] = __builtin_amdgcn_mfma_f32_16x16x32_bf16(__builtin_bit_cast(bf16x8, ones), X[ks & 1][s], acc[s], 0, 0, 0);
      SCHED_FENCE();
    }
#pragma unroll
    for (int ks = 0; ks < 16; ++ks) W1[ks] = *(const bf16x8*)(pw1 + 4 * ks * 128);
    u1 = *(const f32x4*)(a.uv + 16 * nt1 + 4 * g); v1 = *(const f32x4*)(a.uv + F + 16 * nt1 + 4 * g);
    SCHED_FENCE();
    if (g == 0) {
#pragma unroll
      for (int s = 0; s < 4; ++s) xs2[16 * tb[s] + c] = acc[s][0];
    }
  }
  STAMP(2, 2);
  __syncthreads();
  STAMP(2, 3);
  float mu[4], ri[4];
  {
    const float mean = xs2[lane] * (1.f / D);
    const float var = fmaxf(xs2[64 + lane] * (1.f / D) - mean * mean, 0.f);
    f32x2* mr = (f32x2*)(smem + C_MR + w * 512);
    mr[lane] = f32x2{rbf(mean), __builtin_amdgcn_rcpf(rbf(__builtin_amdgcn_sqrtf(rbf(rbf(var) + 1e-5f))))};
#pragma unroll
    for (int s = 0; s < 4; ++s) { const f32x2 v = mr[16 * tb[s] + c]; mu[s] = v[0]; ri[s] = v[1]; }
  }
  u32x2 z2p[4];
  if constexpr (ROLE < 2) {
    float* ps = (float*)(smem + C_PS) + w * 128;
    const s16x4 ones4 = {0x3f80, 0x3f80, 0x3f80, 0x3f80};
#pragma unroll
    for (int s = 0; s < 4; ++s) {
      float z[4];
#pragma unroll
      for (int r = 0; r < 4; ++r) z[r] = gelu_tanh(ri[s] * (acc[s][r] - mu[s] * u2[r]) + v2[r]);
      z2p[s] = u32x2{pk_bf16(z[0], z[1]), pk_bf16(z[2], z[3])};
      const s16x4 zz = __builtin_bit_cast(s16x4, z2p[s]);
      const f32x4 zero = {0, 0, 0, 0};
      const f32x4 s1 = __builtin_amdgcn_mfma_f32_16x16x16bf16_1k(ones4, zz, zero, 0, 0, 0);
      const f32x4 s2 = __builtin_amdgcn_mfma_f32_16x16x16bf16_1k(zz, zz, zero, 0, 0, 0);
      if ((c >> 2) == g) *(f32x2*)(ps + (16 * tb[s] + c) * 2) = f32x2{s1[0], pick4(s2, c & 3)};
    }
  }
  STAMP(2, 4);
  __syncthreads();
  STAMP(2, 5);
  if constexpr (ROLE == 0) {
    const float* ps = (const float*)(smem + C_PS);
    float s1 = 0, s2 = 0;
#pragma unroll
    for (int k = 0; k < 7; ++k) { const f32x2 v = *(const f32x2*)(ps + k * 128 + lane * 2); s1 += v[0]; s2 += v[1]; }
    GAS u64* gp = (GAS u64*)a.gran + (size_t)pi * 8 * 128;
    __hip_atomic_store(gp + (j * 64 + lane) * 2, (1ull << 32) | __float_as_uint(s1), __ATOMIC_RELAXED, __HIP_MEMORY_SCOPE_AGENT);
    __hip_atomic_store(gp + (j * 64 + lane) * 2 + 1, (1ull << 32) | __float_as_uint(s2), __ATOMIC_RELAXED, __HIP_MEMORY_SCOPE_AGENT);
    float t1 = 0, t2 = 0;
    for (u32 spin = 0; spin < (1u << 22); ++spin) {
      bool ok = true; t1 = 0; t2 = 0;
#pragma unroll
      for (int k = 0; k < 8; ++k) {
        const u64 x0 = __hip_atomic_load(gp + (k * 64 + lane) * 2, __ATOMIC_RELAXED, __HIP_MEMORY_SCOPE_AGENT);
        const u64 x1 = __hip_atomic_load(gp + (k * 64 + lane) * 2 + 1, __ATOMIC_RELAXED, __HIP_MEMORY_SCOPE_AGENT);
        ok = ok && (x0 >> 32) == 1 && (x1 >> 32) == 1;
        t1 += __uint_as_float((u32)x0); t2 += __uint_as_float((u32)x1);
      }
      if (__all(ok)) break;
      __builtin_amdgcn_s_sleep(2);
    }
    const float mean = t1 * (1.f / FH);
    const float var = fmaxf(t2 * (1.f / FH) - mean * mean, 0.f);
    *(f32x2*)(smem + C_FS + lane * 8) = f32x2{rbf(mean), __builtin_amdgcn_rcpf(rbf(__builtin_amdgcn_sqrtf(rbf(rbf(var) + 1e-5f))))};
  } else {
#pragma unroll
    for (int s = 0; s < 4; ++s) acc[s] = f32x4{0, 0, 0, 0};
    bf16x8 X[2][4];
#pragma unroll
    for (int s = 0; s < 4; ++s) X[0][s] = xfrag(s, 0);
#pragma unroll
    for (int ks = 0; ks < 16; ++ks) {
      if (ks + 1 < 16) {
#pragma unroll
        for (int s = 0; s < 4; ++s) X[(ks + 1) & 1][s] = xfrag(s, ks + 1);
      }
      SCHED_FENCE();
#pragma unroll
      for (int s = 0; s < 4; ++s) acc[s] = __builtin_amdgcn_mfma_f32_16x16x32_bf16(W1[ks], X[ks & 1][s], acc[s], 0, 0, 0);
      SCHED_FENCE();
    }
    STAMP(2, 6);
    const int f8 = 14 * j + 2 * (w - 1) + (g >> 1);
    const rsrc_t rz = make_rsrc(a.z1t, L * FH * 2);
    u32x2 zp[4];
#pragma unroll
    for (int s = 0; s < 4; ++s) {
      float z[4];
#pragma unroll
      for (int r = 0; r < 4; ++r) z[r] = gelu_tanh(ri[s] * (acc[s][r] - mu[s] * u1[r]) + v1[r]);
      zp[s] = u32x2{pk_bf16(z[0], z[1]), pk_bf16(z[2], z[3])};
      if (s & 1) wt_store16(rz, (u32)((((4 * pi + ((g & 1) ? tb[s] : tb[s - 1])) * 112 + f8) * 16 + c) * 16), pair16(zp[s - 1], zp[s]));
    }
  }
  STAMP(2, 7);
  __syncthreads();
  STAMP(2, 8);
  if constexpr (ROLE < 2) {
#pragma unroll
    for (int s = 0; s < 4; ++s) {
      const f32x2 fs = *(const f32x2*)(smem + C_FS + (16 * tb[s] + c) * 8);
      const float a0 = (bf_lo(z2p[s][0]) - fs[0]) * fs[1], a1 = (bf_hi(z2p[s][0]) - fs[0]) * fs[1];
      const float a2 = (bf_lo(z2p[s][1]) - fs[0]) * fs[1], a3 = (bf_hi(z2p[s][1]) - fs[0]) * fs[1];
      *(u32x2*)(smem + C_ZS + (16 * tb[s] + c) * C_RS + (16 * w + 4 * g) * 2) = u32x2{pk_bf16(a0, a1), pk_bf16(a2, a3)};
    }
#pragma unroll
    for (int kk = 0; kk < 2; ++kk) {
      const char LAS* p = (const char LAS*)smem + C_ZS + (32 * kk + 8 * g + q4) * C_RS + (16 * w + 4 * p4) * 2;
      const s16x4 lo = tr_read(p), hi = tr_read(p + 4 * C_RS);
      const u32x2 a0 = __builtin_bit_cast(u32x2, lo), a1 = __builtin_bit_cast(u32x2, hi);
      wt_store16(make_rsrc(a.z2t, L * FH * 2), (u32)((((7 * j + w) * 256 + 8 * pi + 4 * kk) * 64 + lane * 4) * 4), u32x4{a0[0], a0[1], a1[0], a1[1]});
    }
  }
  STAMP(2, 9);
}

__global__ __launch_bounds__(512, 2) void kC(CArgs a) {
  extern __shared__ __attribute__((aligned(16))) char smem[];
  const int w = __builtin_amdgcn_readfirstlane(threadIdx.x >> 6);
  STAMP(2, 0);
  {
    const int wg = blockIdx.x, lane = threadIdx.x & 63;
    const int pi = ((wg & 7) >> 1) * 8 + ((wg >> 3) & 7);
    const u32 lds0 = (u32)(size_t)(const char LAS*)smem;
#pragma unroll
    for (int i = 0; i < 8; ++i) {
      const int d = 64 * w + 8 * i + (lane >> 3);
      const int fz = (((d >> 1) & 1) | ((d >> 2) & 2)) << 1;
      glds16(a.x1T + (size_t)d * L + 64 * pi + 8 * ((lane & 7) ^ fz), lds0 + C_X + (64 * w + 8 * i) * 128);
    }
    __builtin_amdgcn_s_barrier();
  }
  if (w == 0) c_body<0>(a, smem, w);
  else if (w < 7) c_body<1>(a, smem, w);
  else c_body<2>(a, smem, w);
}
struct DArgs { const u16 *wsp, *z2t, *z1t, *bsp, *Wot; u16* Pt; };
constexpr int D_RING = 32768;
constexpr int D_LDS = 4 * D_RING;

constexpr int d_ops(int it) { return it < 0 ? 0 : (it + 2 < 16 ? 7 : 0) + (((it & 1) == 0 && it / 2 + 2 < 8) ? 8 : 0); }
template <int N> __device__ __forceinline__ void vm_wait_c() { asm volatile("s_waitcnt vmcnt(%0)" ::"i"(N > 63 ? 63 : N) : "memory"); }

template <int IT>
__device__ __forceinline__ void d_iter(f32x4 (&acc)[4][7], bf16x8 (&Bf)[3][7], bf16x8 (&Af)[2][4], const u16* pb, const u16* const (&pa)[2],
                                       u32 ring, const char* const (&ap)[2][4]) {
  if constexpr (IT + 1 < 16) {
    constexpr int s = (IT + 1) >> 1;
    if constexpr ((IT & 1) == 1) vm_wait_c<(IT == 1 ? d_ops(0) : d_ops(IT - 2) + d_ops(IT - 1))>();
#pragma unroll
    for (int m = 0; m < 4; ++m) Af[(IT + 1) & 1][m] = *(const bf16x8*)(ap[(IT + 1) & 1][m] + (s & 3) * 8192);
  }
  SCHED_FENCE();
#pragma unroll
  for (int n = 0; n < 7; ++n) {
    if constexpr (IT + 2 < 16) Bf[(IT + 2) % 3][n] = *(const bf16x8*)(pb + ((size_t)n * 256 + 4 * (IT + 2)) * 128);
    if constexpr ((IT & 1) == 0 && IT / 2 + 2 < 8) {
      constexpr int s = IT / 2 + 2;
      glds16(pa[n & 1] + (size_t)(n >> 1) * 16 * L + 64 * s, ring + (s & 3) * 8192 + n * 1024);
      if (n == 6) glds16(pa[1] + (size_t)3 * 16 * L + 64 * s, ring + (s & 3) * 8192 + 7 * 1024);
    }
#pragma unroll
    for (int m = 0; m < 4; ++m) acc[m][n] = __builtin_amdgcn_mfma_f32_16x16x32_bf16(Bf[IT % 3][n], Af[IT & 1][m], acc[m][n], 0, 0, 0);
    SCHED_FENCE();
  }
}

__global__ __launch_bounds__(256, 1) void kD(DArgs a) {
  extern __shared__ __attribute__((aligned(16))) char smem[];
  const int wg = blockIdx.x, tid = threadIdx.x, lane = tid & 63;
  const int w = __builtin_amdgcn_readfirstlane(tid >> 6);
  const int xc = wg & 7, ii = wg >> 3;
  const int rt = (xc >> 1) * 8 + (ii & 7), ct = (xc & 1) * 4 + (ii >> 3);
  const int c = lane & 15, g = lane >> 4;
  STAMP(3, 0);
  const u32 ring = (u32)(size_t)(const char LAS*)smem + w * D_RING;
  const u16* pa[2];
#pragma unroll
  for (int par = 0; par < 2; ++par) {
    const int row = 8 * par + (lane >> 3);
    pa[par] = a.wsp + (size_t)(64 * rt + row) * L + 512 * w + 8 * ((lane & 7) ^ ((row >> 1) & 7));
  }
  const u16* pb = a.z2t + ((size_t)(7 * ct) * 256 + 64 * w) * 128 + lane * 8;
  auto ldA = [&](int s) {
#pragma unroll
    for (int i = 0; i < 8; ++i) glds16(pa[i & 1] + (size_t)(i >> 1) * 16 * L + 64 * s, ring + (s & 3) * 8192 + i * 1024);
  };
  bf16x8 Bf[3][7], Af[2][4];
  auto ldB = [&](int ks) {
#pragma unroll
    for (int n = 0; n < 7; ++n) Bf[ks][n] = *(const bf16x8*)(pb + ((size_t)n * 256 + 4 * ks) * 128);
  };
  ldA(0); SCHED_FENCE(); ldB(0); ldB(1); SCHED_FENCE(); ldA(1); SCHED_FENCE();
  f32x4 acc[4][7];
#pragma unroll
  for (int m = 0; m < 4; ++m)
#pragma unroll
    for (int n = 0; n < 7; ++n) acc[m][n] = f32x4{0, 0, 0, 0};
  const int asw = (c >> 1) & 7;
  const char* ap[2][4];
#pragma unroll
  for (int m = 0; m < 4; ++m) {
    const char* base = smem + w * D_RING + ((m + w) & 3) * 2048 + c * 128;
    ap[0][m] = base + 16 * (g ^ asw); ap[1][m] = base + 16 * ((4 + g) ^ asw);
  }
  vm_wait_c<7 + 7 + 8>();
#pragma unroll
  for (int m = 0; m < 4; ++m) Af[0][m] = *(const bf16x8*)(ap[0][m]);
  STAMP(3, 4);
  d_iter<0>(acc, Bf, Af, pb, pa, ring, ap);   d_iter<1>(acc, Bf, Af, pb, pa, ring, ap);
  d_iter<2>(acc, Bf, Af, pb, pa, ring, ap);   d_iter<3>(acc, Bf, Af, pb, pa, ring, ap);
  STAMP(3, 5);
  d_iter<4>(acc, Bf, Af, pb, pa, ring, ap);   d_iter<5>(acc, Bf, Af, pb, pa, ring, ap);
  d_iter<6>(acc, Bf, Af, pb, pa, ring, ap);   d_iter<7>(acc, Bf, Af, pb, pa, ring, ap);
  STAMP(3, 6);
  d_iter<8>(acc, Bf, Af, pb, pa, ring, ap);   d_iter<9>(acc, Bf, Af, pb, pa, ring, ap);
  d_iter<10>(acc, Bf, Af, pb, pa, ring, ap);  d_iter<11>(acc, Bf, Af, pb, pa, ring, ap);
  STAMP(3, 7);
  d_iter<12>(acc, Bf, Af, pb, pa, ring, ap);  d_iter<13>(acc, Bf, Af, pb, pa, ring, ap);
  u32x2 z1v[7];
#pragma unroll
  for (int n = 0; n < 7; ++n)
    z1v[n] = *(const u32x2*)(a.z1t + ((size_t)(4 * rt + w) * 112 + 14 * ct + 2 * n + (g >> 1)) * 128 + c * 8 + (g & 1) * 4);
  const float bs = bf1(a.bsp[64 * rt + 16 * w + c]);
  u32x4 pf[4];
  {
    const u16* p = a.Wot + (size_t)(xc & 1) * 16 * 112 * 128 + (size_t)ii * 7168 + tid * 8;
#pragma unroll
    for (int k = 0; k < 3; ++k) pf[k] = *(const u32x4*)(p + k * 2048);
    pf[3] = *(const u32x4*)(p + (tid < 128 ? 3 * 2048 : 0));
  }
  SCHED_FENCE();
  d_iter<14>(acc, Bf, Af, pb, pa, ring, ap);  d_iter<15>(acc, Bf, Af, pb, pa, ring, ap);
  STAMP(3, 1);
  __builtin_amdgcn_s_barrier();
#pragma unroll
  for (int m = 1; m < 4; ++m) {
    char* dst = smem + ((((m + w) & 3) * 3 + (m - 1)) * 7 * 64 + lane) * 16;
#pragma unroll
    for (int n = 0; n < 7; ++n) *(f32x4*)(dst + n * 1024) = acc[m][n];
  }
  __syncthreads();
  STAMP(3, 2);
  const char* src = smem + (w * 3 * 7 * 64 + lane) * 16;
  const rsrc_t rp = make_rsrc(a.Pt, L * FH * 2);
  u32x2 pv[8];
#pragma unroll
  for (int n = 0; n < 7; ++n) {
    const f32x4 p0 = *(const f32x4*)(src + n * 1024), p1 = *(const f32x4*)(src + (7 + n) * 1024), p2 = *(const f32x4*)(src + (14 + n) * 1024);
    float gg[4];
#pragma unroll
    for (int e = 0; e < 4; ++e) gg[e] = ((acc[0][n][e] + p0[e]) + (p1[e] + p2[e])) + bs;
    pv[n] = u32x2{pk_bf16(bf_lo(z1v[n][0]) * gg[0], bf_hi(z1v[n][0]) * gg[1]), pk_bf16(bf_lo(z1v[n][1]) * gg[2], bf_hi(z1v[n][1]) * gg[3])};
    if (n & 1) wt_store16(rp, (u32)((((4 * rt + w) * 112 + 14 * ct + 2 * (n - 1 + (g & 1)) + (g >> 1)) * 16 + c) * 16), pair16(pv[n - 1], pv[n]));
  }
  {
    const u32x4 v = pair16(pv[6], pv[6]);
    if ((g & 1) == 0) wt_store16(rp, (u32)((((4 * rt + w) * 112 + 14 * ct + 12 + (g >> 1)) * 16 + c) * 16), v);
  }
#pragma unroll
  for (int k = 0; k < 4; ++k) asm volatile("" ::"v"(pf[k]));
  STAMP(3, 3);
}
struct EArgs { const u16 *Pt, *Wot, *x1T; const float* mod; u16* out; };
constexpr int E_TS = 1152;
constexpr int E_PART = 0;
constexpr int E_X = 4 * 16 * E_TS;
constexpr int E_LDS = E_X + 64 * 132;

__global__ __launch_bounds__(256, 1) void kE(EArgs a) {
  extern __shared__ __attribute__((aligned(16))) char smem[];
  const int wg = blockIdx.x, tid = threadIdx.x, lane = tid & 63;
  const int w = __builtin_amdgcn_readfirstlane(tid >> 6);
  const int xc = wg & 7, ii = wg >> 3;
  const int rt = (xc >> 1) * 8 + (ii & 7), dt = (xc & 1) * 4 + (ii >> 3);
  const int c = lane & 15, g = lane >> 4;
  STAMP(4, 0);
  const u16* pp = a.Pt + ((size_t)(4 * rt) * 112 + 28 * w) * 128 + lane * 8;
  const u16* pw = a.Wot + ((size_t)(4 * dt) * 112 + 28 * w) * 128 + lane * 8;
  bf16x8 Pf[3][4], Wf[3][4];
  auto ld = [&](int k) {
#pragma unroll
    for (int m = 0; m < 4; ++m) {
      Wf[k % 3][m] = *(const bf16x8*)(pw + ((size_t)m * 112 + 4 * k) * 128);
      Pf[k % 3][m] = *(const bf16x8*)(pp + ((size_t)m * 112 + 4 * k) * 128);
    }
  };
  ld(0); ld(1);
  SCHED_FENCE();
  f32x4 acc[4][4];
#pragma unroll
  for (int m = 0; m < 4; ++m)
#pragma unroll
    for (int n = 0; n < 4; ++n) acc[m][n] = f32x4{0, 0, 0, 0};
  u32x4 xr[2];
  f32x4 gb0, gb1;
  const int d8 = lane & 7;
#pragma unroll
  for (int k = 0; k < 7; ++k) {
    if (k + 2 < 7) ld(k + 2);
    if (k == 5) {
#pragma unroll
      for (int kk = 0; kk < 2; ++kk) {
        const int id = tid + 256 * kk, dd = id >> 3, chn = id & 7;
        xr[kk] = *(const u32x4*)(a.x1T + (size_t)(64 * dt + dd) * L + 64 * rt + 8 * chn);
      }
      gb0 = *(const f32x4*)(a.mod + 2560 + 64 * dt + 8 * d8); gb1 = *(const f32x4*)(a.mod + 2560 + 64 * dt + 8 * d8 + 4);
    }
    SCHED_FENCE();
#pragma unroll
    for (int n = 0; n < 4; ++n)
#pragma unroll
      for (int m = 0; m < 4; ++m) acc[m][n] = __builtin_amdgcn_mfma_f32_16x16x32_bf16(Wf[k % 3][n], Pf[k % 3][m], acc[m][n], 0, 0, 0);
    SCHED_FENCE();
  }
  STAMP(4, 1);
#pragma unroll
  for (int m = 0; m < 4; ++m)
#pragma unroll
    for (int n = 0; n < 4; ++n) *(f32x4*)(smem + E_PART + (w * 16 + m * 4 + n) * E_TS + g * 288 + c * 16) = acc[m][n];
#pragma unroll
  for (int k = 0; k < 2; ++k) {
    const int id = tid + 256 * k, dd = id >> 3, chn = id & 7;
    u32* p = (u32*)(smem + E_X + dd * 132 + chn * 16);
    p[0] = xr[k][0]; p[1] = xr[k][1]; p[2] = xr[k][2]; p[3] = xr[k][3];
  }
  __syncthreads();
  STAMP(4, 2);
#pragma unroll
  for (int k = 0; k < 2; ++k) {
    const int t = 32 * k + 8 * w + (lane >> 3);
    float s[8];
#pragma unroll
    for (int hh = 0; hh < 2; ++hh) {
      const char* p = smem + E_PART + ((t >> 4) * 4 + (d8 >> 1)) * E_TS + (2 * (d8 & 1) + hh) * 288 + (t & 15) * 16;
      const f32x4 p0 = *(const f32x4*)p, p1 = *(const f32x4*)(p + 16 * E_TS), p2 = *(const f32x4*)(p + 32 * E_TS), p3 = *(const f32x4*)(p + 48 * E_TS);
#pragma unroll
      for (int e = 0; e < 4; ++e) s[4 * hh + e] = (p0[e] + p1[e]) + (p2[e] + p3[e]);
    }
    float o[8];
#pragma unroll
    for (int e = 0; e < 8; ++e) {
      const float x1 = bf1(*(const u16*)(smem + E_X + (8 * d8 + e) * 132 + t * 2));
      o[e] = x1 + (e < 4 ? gb0[e] : gb1[e - 4]) * s[e];
    }
    wt_store16(make_rsrc(a.out, L * D * 2), (u32)(((64 * rt + t) * D + 64 * dt + 8 * d8) * 2), u32x4{pk_bf16(o[0], o[1]), pk_bf16(o[2], o[3]), pk_bf16(o[4], o[5]), pk_bf16(o[6], o[7])});
  }
  STAMP(4, 3);
}
#define KMASK(b)
extern "C" void kernel_launch(void* const* d_in, const int* in_sizes, int n_in, void* d_out, int out_size,
                              void* d_ws, size_t ws_size, hipStream_t stream) {
  const u16* x = (const u16*)d_in[0];
  char* ws = (char*)d_ws;
  float* mod = (float*)(ws + 0);
  u32* stats1 = (u32*)(ws + 16384);
  float* uv = (float*)(ws + 32768);
  u64* gran = (u64*)(ws + 65536);
  u16* xT = (u16*)(ws + (1 << 20));
  u16* Krev = (u16*)(ws + (3 << 20));
  u16* x1T = (u16*)(ws + (5 << 20));
  u16* Wt = (u16*)(ws + (7 << 20));
  u16* Wot = (u16*)(ws + (9 << 20));
  u16* z1t = (u16*)(ws + (10 << 20));
  u16* z2t = (u16*)(ws + (14 << 20));
  u16* Pt = (u16*)(ws + (18 << 20));
  AArgs aa;
  aa.x = x; aa.Are = (const u16*)d_in[1]; aa.Aim = (const u16*)d_in[2]; aa.Bre = (const u16*)d_in[3]; aa.Bim = (const u16*)d_in[4];
  aa.Cre = (const u16*)d_in[5]; aa.Cim = (const u16*)d_in[6]; aa.logdt = (const u16*)d_in[7]; aa.c = (const u16*)d_in[14];
  aa.wmod = (const u16*)d_in[15]; aa.win = (const u16*)d_in[10]; aa.wout = (const u16*)d_in[13];
  aa.mod = mod; aa.stats1 = stats1; aa.xT = xT; aa.Krev = Krev; aa.gran = (u32x4*)gran;
  (void)hipFuncSetAttribute((const void*)kA, hipFuncAttributeMaxDynamicSharedMemorySize, A_LDS);
  KMASK(1) kA<<<256, 768, A_LDS, stream>>>(aa);
  BArgs ba;
  ba.xT = xT; ba.Krev = Krev; ba.Dskip = (const u16*)d_in[8]; ba.win = (const u16*)d_in[10]; ba.wout = (const u16*)d_in[13];
  ba.wsp = (const u16*)d_in[11]; ba.stats1 = stats1; ba.mod = mod; ba.x1T = x1T; ba.Wt = Wt; ba.Wot = Wot; ba.uv = uv;
  (void)hipFuncSetAttribute((const void*)kB, hipFuncAttributeMaxDynamicSharedMemorySize, B_LDS);
  KMASK(2) kB<<<256, 768, B_LDS, stream>>>(ba);
  CArgs ca;
  ca.x1T = x1T; ca.Wt = Wt; ca.uv = uv; ca.z1t = z1t; ca.z2t = z2t; ca.gran = gran;
  (void)hipFuncSetAttribute((const void*)kC, hipFuncAttributeMaxDynamicSharedMemorySize, C_LDS);
  KMASK(4) kC<<<256, 512, C_LDS, stream>>>(ca);
  DArgs da;
  da.wsp = (const u16*)d_in[11]; da.z2t = z2t; da.z1t = z1t; da.bsp = (const u16*)d_in[12]; da.Wot = Wot; da.Pt = Pt;
  (void)hipFuncSetAttribute((const void*)kD, hipFuncAttributeMaxDynamicSharedMemorySize, D_LDS);
  KMASK(8) kD<<<256, 256, D_LDS, stream>>>(da);
  EArgs ea;
  ea.Pt = Pt; ea.Wot = Wot; ea.x1T = x1T; ea.mod = mod; ea.out = (u16*)d_out;
  (void)hipFuncSetAttribute((const void*)kE, hipFuncAttributeMaxDynamicSharedMemorySize, E_LDS);
  KMASK(16) kE<<<256, 256, E_LDS, stream>>>(ea);
}
```

```cpp
#include <hip/hip_runtime.h>
#include <hip/hip_bf16.h>
#include <stdint.h>

typedef unsigned short u16;
typedef unsigned int u32;
typedef unsigned long long u64;
typedef __bf16 bf16x8 __attribute__((ext_vector_type(8)));
typedef __bf16 bf16x2 __attribute__((ext_vector_type(2)));
typedef short s16x4 __attribute__((ext_vector_type(4)));
typedef float f32x2 __attribute__((ext_vector_type(2)));
typedef float f32x4 __attribute__((ext_vector_type(4)));
typedef u32 u32x2 __attribute__((ext_vector_type(2)));
typedef u32 u32x4 __attribute__((ext_vector_type(4)));
#define LAS __attribute__((address_space(3)))
#define GAS __attribute__((address_space(1)))

constexpr int L = 2048, D = 512, NS = 128, F = 1792, FH = 896;

__device__ __forceinline__ float bf_lo(u32 w) { return __uint_as_float(w << 16); }
__device__ __forceinline__ float bf_hi(u32 w) { return __uint_as_float(w & 0xffff0000u); }
__device__ __forceinline__ float bf1(u16 h) { return __uint_as_float(((u32)h) << 16); }
__device__ __forceinline__ u32 pk_bf16(float lo, float hi) {
  f32x2 a = {lo, hi};
  bf16x2 p = __builtin_convertvector(a, bf16x2);
  return __builtin_bit_cast(u32, p);
}
__device__ __forceinline__ float rbf(float x) { return bf_lo(pk_bf16(x, x)); }

template <int CTRL, int ROWMASK = 0xf>
__device__ __forceinline__ float dpp_add(float x) {
  int y = __builtin_amdgcn_update_dpp(0, __float_as_int(x), CTRL, ROWMASK, 0xf, true);
  return x + __int_as_float(y);
}
__device__ __forceinline__ float row16_sum(float x) {
  x = dpp_add<0xB1>(x);
  x = dpp_add<0x4E>(x);
  x = dpp_add<0x141>(x);
  x = dpp_add<0x140>(x);
  return x;
}
__device__ __forceinline__ float wave_sum(float x) {
  x = row16_sum(x);
  float a = __int_as_float(__builtin_amdgcn_readlane(__float_as_int(x), 0));
  float b = __int_as_float(__builtin_amdgcn_readlane(__float_as_int(x), 16));
  float c = __int_as_float(__builtin_amdgcn_readlane(__float_as_int(x), 32));
  float d = __int_as_float(__builtin_amdgcn_readlane(__float_as_int(x), 48));
  return (a + b) + (c + d);
}
__device__ __forceinline__ s16x4 tr_read(const char LAS* p) {
  return __builtin_amdgcn_ds_read_tr16_b64_v4i16((s16x4 LAS*)p);
}

#define STAMP(kern, i)
__device__ __forceinline__ void glds16(const void* gsrc, u32 lds_dst) {
  u32 keep;
  asm volatile("s_mov_b32 %0, m0\n\ts_mov_b32 m0, %2\n\ts_nop 0\n\tglobal_load_lds_dwordx4 %1, off\n\ts_mov_b32 m0, %0"
               : "=&s"(keep) : "v"(gsrc), "s"(lds_dst) : "memory");
}
#define SCHED_FENCE() __builtin_amdgcn_sched_barrier(0)
#define VM_WAIT(n) asm volatile("s_waitcnt vmcnt(" #n ")" ::: "memory")
__device__ __forceinline__ float xor16_sum(float x) {
  const u32 v = __float_as_uint(x);
  auto r = __builtin_amdgcn_permlane16_swap(v, v, false, false);
  return __uint_as_float(r[0]) + __uint_as_float(r[1]);
}
__device__ __forceinline__ float xor32_sum(float x) {
  const u32 v = __float_as_uint(x);
  auto r = __builtin_amdgcn_permlane32_swap(v, v, false, false);
  return __uint_as_float(r[0]) + __uint_as_float(r[1]);
}
__device__ __forceinline__ float xor4_8_sum(float x) {
  x = x + __int_as_float(__builtin_amdgcn_update_dpp(0, __float_as_int(x), 0x124, 0xf, 0xf, true));
  x = x + __int_as_float(__builtin_amdgcn_update_dpp(0, __float_as_int(x), 0x128, 0xf, 0xf, true));
  return x;
}
__device__ __forceinline__ void lds_rendezvous(u32 LAS* cnt, u32 target) {
  asm volatile("s_waitcnt lgkmcnt(0)" ::: "memory");
  if ((threadIdx.x & 63) == 0) __hip_atomic_fetch_add(cnt, 1u, __ATOMIC_RELAXED, __HIP_MEMORY_SCOPE_WORKGROUP);
  while (__hip_atomic_load(cnt, __ATOMIC_RELAXED, __HIP_MEMORY_SCOPE_WORKGROUP) < target) __builtin_amdgcn_s_sleep(1);
  asm volatile("" ::: "memory");
}

typedef __amdgpu_buffer_rsrc_t rsrc_t;
__device__ __forceinline__ rsrc_t make_rsrc(const void* base, u32 bytes) {
  return __builtin_amdgcn_make_buffer_rsrc(const_cast<void*>(base), 0, bytes, 0x00020000);
}
__device__ __forceinline__ void wt_store16(rsrc_t r, u32 byte_off, u32x4 v) { __builtin_amdgcn_raw_buffer_store_b128(v, r, byte_off, 0, 16); }
__device__ __forceinline__ u32x4 pair16(u32x2 a, u32x2 b) {
  const auto r0 = __builtin_amdgcn_permlane16_swap(a[0], b[0], false, false);
  const auto r1 = __builtin_amdgcn_permlane16_swap(a[1], b[1], false, false);
  return u32x4{r0[0], r1[0], r0[1], r1[1]};
}
struct AArgs {
  const u16 *x, *Are, *Aim, *Bre, *Bim, *Cre, *Cim, *logdt, *c, *wmod, *win, *wout;
  float* mod; u32* stats1; u16* xT; u16* Krev; u32x4* gran;
};
constexpr int A_RS = 528;
constexpr int A_TAB = 0;
constexpr int A_KC = A_TAB + 2 * 64 * A_RS;
constexpr int A_TILE = A_KC + 2 * 768 * 4;
constexpr int A_RED = A_TILE + 8 * 1088;
constexpr int A_CNT = A_RED + 4 * 32 * 4;
constexpr int A_LDS = A_CNT + 16;

struct KIdx { u16 v[2048]; };
constexpr int bf16_round_int(int l) {
  int sh = 0;
  while ((l >> sh) >= 256) ++sh;
  if (sh == 0) return l;
  const int q = l >> sh, rem = l & ((1 << sh) - 1), half = 1 << (sh - 1);
  const int up = (rem > half || (rem == half && (q & 1))) ? 1 : 0;
  return (q + up) << sh;
}
constexpr KIdx make_kidx() {
  KIdx t{};
  for (int x = 0; x < 2048; ++x) {
    const int lr = bf16_round_int(2047 - x);
    const int aa = lr >> 7, b = (lr >> 3) & 15, r = lr & 7;
    const int row = lr < 256 ? 8 * aa + r : lr < 512 ? 16 + 4 * (aa - 2) + (r >> 1) : lr < 1024 ? 24 + 2 * (aa - 4) + (r >> 2) : 32 + (aa - 8);
    t.v[x] = (u16)(row * 16 + b);
  }
  return t;
}
__device__ const KIdx g_kidx = make_kidx();

struct cpx { float r, i; };
__device__ __forceinline__ cpx cmul(cpx a, cpx b) { return cpx{a.r * b.r - a.i * b.i, a.r * b.i + a.i * b.r}; }

__global__ __launch_bounds__(768) void kA(AArgs a) {
  extern __shared__ __attribute__((aligned(16))) char smem[];
  const int wg = blockIdx.x, tid = threadIdx.x, lane = tid & 63;
  const int w = __builtin_amdgcn_readfirstlane(tid >> 6);
  STAMP(0, 0);
  if (w < 8) {
    __builtin_amdgcn_s_setprio(2);
    const int ch = w >> 2, nh = (w >> 1) & 1, part = w & 1, d = 2 * wg + ch;
    const int n = 64 * nh + lane, o = d * NS + n;
    const u16 hare = a.Are[o], haim = a.Aim[o], hbre = a.Bre[o], hbim = a.Bim[o], hcre = a.Cre[o], hcim = a.Cim[o], hdt = a.logdt[d];
    const u32x4 kix = *(const u32x4*)(g_kidx.v + 8 * (tid & 255));
    SCHED_FENCE();
    __builtin_amdgcn_s_barrier();
    SCHED_FENCE();
    const float dt = rbf(__expf(bf1(hdt)));
    const float lr = (dt * bf1(hare)) * 1.4426950408889634f, lp = (dt * bf1(haim)) * 0.15915494309189535f;
    auto cexp = [&](float l) -> cpx {
      const float e = __builtin_amdgcn_exp2f(lr * l), ph = __builtin_amdgcn_fractf(lp * l);
      return cpx{e * __builtin_amdgcn_cosf(ph), e * __builtin_amdgcn_sinf(ph)};
    };
    char* tab = smem + A_TAB + ch * (64 * A_RS) + n * 4;
    auto put = [&](int row, cpx v) { *(u32*)(tab + row * A_RS) = pk_bf16(v.r, v.i); };
    if (part == 0) {
      const cpx E1 = cexp(1.f), E128 = cexp(128.f), E512 = cexp(512.f);
      const cpx E2 = cmul(E1, E1), E3 = cmul(E2, E1), E4 = cmul(E2, E2), E5 = cmul(E4, E1), E6 = cmul(E4, E2), E7 = cmul(E4, E3);
      put(0, cpx{1.f, 0.f}); put(1, E1); put(2, E2); put(3, E3); put(4, E4); put(5, E5); put(6, E6); put(7, E7);
      put(8, E128); put(9, cmul(E128, E1)); put(10, cmul(E128, E2)); put(11, cmul(E128, E3));
      put(12, cmul(E128, E4)); put(13, cmul(E128, E5)); put(14, cmul(E128, E6)); put(15, cmul(E128, E7));
      const cpx E256 = cmul(E128, E128), E384 = cmul(E256, E128);
      put(16, E256); put(17, cmul(E256, E2)); put(18, cmul(E256, E4)); put(19, cmul(E256, E6));
      put(20, E384); put(21, cmul(E384, E2)); put(22, cmul(E384, E4)); put(23, cmul(E384, E6));
      const cpx E640 = cmul(E512, E128), E768 = cmul(E512, E256), E896 = cmul(E512, E384);
      put(24, E512); put(25, cmul(E512, E4)); put(26, E640); put(27, cmul(E640, E4));
      put(28, E768); put(29, cmul(E768, E4)); put(30, E896); put(31, cmul(E896, E4));
    } else {
      const cpx E8 = cexp(8.f), E128 = cexp(128.f), E1024 = cexp(1024.f);
      const float br = dt * bf1(hbre), bi = dt * bf1(hbim), cr = bf1(hcre), ci = bf1(hcim);
      const cpx W = cmul(cpx{cr, ci}, cpx{br, bi});
      const cpx E16 = cmul(E8, E8), E32 = cmul(E16, E16), E64 = cmul(E32, E32);
      cpx Y[8];
      Y[0] = W; Y[1] = cmul(W, E8); Y[2] = cmul(W, E16); Y[3] = cmul(Y[1], E16);
#pragma unroll
      for (int b = 0; b < 4; ++b) Y[4 + b] = cmul(Y[b], E32);
#pragma unroll
      for (int b = 0; b < 8; ++b) {
        put(48 + b, cpx{Y[b].r, -Y[b].i});
        const cpx t = cmul(Y[b], E64);
        put(56 + b, cpx{t.r, -t.i});
      }
      const cpx E256 = cmul(E128, E128), E512 = cmul(E256, E256);
      cpx P[4];
      P[0] = E1024; P[1] = cmul(E1024, E128); P[2] = cmul(E1024, E256); P[3] = cmul(P[1], E256);
#pragma unroll
      for (int b = 0; b < 4; ++b) { put(32 + b, P[b]); put(36 + b, cmul(P[b], E512)); }
      put(40, cmul(E1024, E1024));
    }
    STAMP(0, 1);
    lds_rendezvous((u32 LAS*)(smem + A_CNT), 8);
    STAMP(0, 2);
    if (w < 6) {
      const int c2 = w >= 3 ? 1 : 0, t = w - 3 * c2, i = lane & 15, g = lane >> 4;
      const char* tb = smem + A_TAB + c2 * (64 * A_RS) + 16 * g;
      const char* pa = tb + (16 * t + i) * A_RS, *pb = tb + (48 + i) * A_RS;
      f32x4 acc = {0, 0, 0, 0};
      bf16x8 Af[8], Bf[8];
#pragma unroll
      for (int s = 0; s < 8; ++s) {
        Af[s] = *(const bf16x8*)(pa + s * 64); Bf[s] = *(const bf16x8*)(pb + s * 64);
      }
#pragma unroll
      for (int s = 0; s < 8; ++s) acc = __builtin_amdgcn_mfma_f32_16x16x32_bf16(Af[s], Bf[s], acc, 0, 0, 0);
      float* Kc = (float*)(smem + A_KC) + c2 * 768 + t * 256;
#pragma unroll
      for (int r = 0; r < 4; ++r) Kc[(4 * g + r) * 16 + i] = acc[r];
    }
    STAMP(0, 3);
    lds_rendezvous((u32 LAS*)(smem + A_CNT + 4), 8);
    STAMP(0, 4);
    const float* Kc = (const float*)(smem + A_KC) + ch * 768;
    float kv[8];
#pragma unroll
    for (int j = 0; j < 4; ++j) { kv[2 * j] = Kc[kix[j] & 0xffffu]; kv[2 * j + 1] = Kc[kix[j] >> 16]; }
    *(u32x4*)(a.Krev + (size_t)d * L + 8 * (tid & 255)) =
        u32x4{pk_bf16(kv[0], kv[1]), pk_bf16(kv[2], kv[3]), pk_bf16(kv[4], kv[5]), pk_bf16(kv[6], kv[7])};
    STAMP(0, 5);
  } else {
    const int tw = w - 8;
    const int t0 = 8 * ((wg & 7) * 32 + (wg >> 3));
    u32x4 xr[2];
#pragma unroll
    for (int j = 0; j < 2; ++j) xr[j] = *(const u32x4*)(a.x + (size_t)(t0 + 2 * tw + j) * D + 8 * lane);
    const bool doM = wg < 96;
    u32x4 wm[8]; u16 cv[8];
    const int rr = lane >> 2, q = lane & 3;
    if (doM) {
#pragma unroll
      for (int p = 0; p < 8; ++p) cv[p] = a.c[p * 64 + tw * 16 + rr];
#pragma unroll
      for (int p = 0; p < 8; ++p) wm[p] = *(const u32x4*)(a.wmod + (size_t)(p * 64 + tw * 16 + rr) * 3072 + 32 * wg + 8 * q);
    }
    u32x4 pf[3];
    {
      const int st = tid - 512;
      pf[0] = *(const u32x4*)(a.win + (size_t)wg * 3584 + st * 8);
      pf[1] = *(const u32x4*)(a.win + (size_t)wg * 3584 + (st < 192 ? 2048 + st * 8 : 0));
      pf[2] = *(const u32x4*)(a.wout + (size_t)wg * 1792 + (st < 224 ? st * 8 : 0));
    }
    u32x4 cf;
    {
      u64 base;
      asm volatile("s_getpc_b64 s[40:41]\n\ts_add_u32 s40, s40, _Z2kB5BArgs@rel32@lo+4\n\ts_addc_u32 s41, s41, _Z2kB5BArgs@rel32@hi+12\n\ts_mov_b64 %0, s[40:41]"
                   : "=s"(base) : : "s40", "s41", "scc");
      const int slot = (wg >> 3) * 76 + (tid - 512);
      cf = *(const u32x4*)((const char*)base + (size_t)((tid - 512) < 76 ? slot : 0) * 16);
    }
    char* tile = smem + A_TILE;
    if (tw == 0 && lane < 4) *(u32*)(smem + A_CNT + 4 * lane) = 0;
    asm volatile("s_waitcnt lgkmcnt(0)" ::: "memory");
    SCHED_FENCE();
    __builtin_amdgcn_s_barrier();
    SCHED_FENCE();
#pragma unroll
    for (int j = 0; j < 2; ++j) *(u32x4*)(tile + (2 * tw + j) * 1088 + 16 * lane) = xr[j];
    STAMP(0, 1);
    lds_rendezvous((u32 LAS*)(smem + A_CNT + 8), 4);
    STAMP(0, 2);
#pragma unroll
    for (int ps = 0; ps < 2; ++ps) {
      const int d0 = 128 * tw + 64 * ps + 16 * (lane >> 4);
      const char LAS* src = (const char LAS*)tile + ((lane & 15) >> 2) * 1088 + 2 * (d0 + 4 * (lane & 3));
      const s16x4 lo = tr_read(src), hi = tr_read(src + 4 * 1088);
      u32x2 a0 = __builtin_bit_cast(u32x2, lo), a1 = __builtin_bit_cast(u32x2, hi);
      *(u32x4*)(a.xT + (size_t)(d0 + (lane & 15)) * L + t0) = u32x4{a0[0], a0[1], a1[0], a1[1]};
    }
    if (tw == 0) a.gran[wg * 64 + lane] = u32x4{0, 0, 0, 0};
    {
      float v[2][8], s[2];
#pragma unroll
      for (int j = 0; j < 2; ++j) {
#pragma unroll
        for (int k = 0; k < 4; ++k) { v[j][2 * k] = bf_lo(xr[j][k]); v[j][2 * k + 1] = bf_hi(xr[j][k]); }
        s[j] = ((v[j][0] + v[j][1]) + (v[j][2] + v[j][3])) + ((v[j][4] + v[j][5]) + (v[j][6] + v[j][7]));
      }
      float mean[2], q2[2];
#pragma unroll
      for (int j = 0; j < 2; ++j) mean[j] = wave_sum(s[j]) * (1.f / D);
#pragma unroll
      for (int j = 0; j < 2; ++j) {
        q2[j] = 0;
#pragma unroll
        for (int k = 0; k < 8; ++k) { const float dv = v[j][k] - mean[j]; q2[j] += dv * dv; }
      }
#pragma unroll
      for (int j = 0; j < 2; ++j) {
        const float var = wave_sum(q2[j]) * (1.f / D);
        const float sd = rbf(__builtin_amdgcn_sqrtf(rbf(rbf(var) + 1e-5f)));
        if (lane == 0) *(f32x2*)(a.stats1 + 2 * (t0 + 2 * tw + j)) = f32x2{rbf(mean[j]), __builtin_amdgcn_rcpf(sd)};
      }
    }
    float* red = (float*)(smem + A_RED);
    if (doM) {
      float acc[8] = {0, 0, 0, 0, 0, 0, 0, 0};
#pragma unroll
      for (int p = 0; p < 8; ++p) {
        const float cx = bf1(cv[p]);
        const float s = rbf(cx * __builtin_amdgcn_rcpf(1.f + __expf(-cx)));
#pragma unroll
        for (int k = 0; k < 4; ++k) { acc[2 * k] += s * bf_lo(wm[p][k]); acc[2 * k + 1] += s * bf_hi(wm[p][k]); }
      }
#pragma unroll
      for (int k = 0; k < 8; ++k) {
        float v = acc[k];
        v = xor32_sum(xor16_sum(xor4_8_sum(v)));
        if (rr == 0) red[tw * 32 + 8 * q + k] = v;
      }
      STAMP(0, 3);
      lds_rendezvous((u32 LAS*)(smem + A_CNT + 12), 4);
      STAMP(0, 4);
      if (tw == 0 && lane < 32)
        a.mod[32 * wg + lane] = rbf((red[lane] + red[32 + lane]) + (red[64 + lane] + red[96 + lane]));
    }
#pragma unroll
    for (int k = 0; k < 3; ++k) asm volatile("" ::"v"(pf[k]));
    asm volatile("" ::"v"(cf));
    STAMP(0, 5);
  }
}
struct BArgs {
  const u16 *xT, *Krev, *Dskip, *win, *wout, *wsp;
  const u32* stats1; const float* mod;
  u16 *x1T, *Wt, *Wot; float* uv;
};
constexpr int B_H = 0;
constexpr int B_HS = (256 + 2048) * 2;
constexpr int B_KC = B_H + 2 * B_HS;
constexpr int B_KS = 4160;
constexpr int B_PART = B_KC + 2 * 4 * B_KS;
constexpr int B_RED = B_PART + 2 * 4 * 8 * 1280;
constexpr int B_CNT = B_RED + 4 * 16 * 4;
constexpr int B_LDS = B_CNT + 32;

__global__ __launch_bounds__(768) void kB(BArgs a) {
  extern __shared__ __attribute__((aligned(16))) char smem[];
  const int wg = blockIdx.x, tid = threadIdx.x, lane = tid & 63;
  const int w = __builtin_amdgcn_readfirstlane(tid >> 6);
  STAMP(1, 0);
  if (w < 8) {
    __builtin_amdgcn_s_setprio(2);
    const int ch = w >> 2, q = w & 3, d = 2 * wg + ch, tj = tid & 255;
    const u32x4 xv = *(const u32x4*)(a.xT + (size_t)d * L + 8 * tj);
    f32x4 st[4];
#pragma unroll
    for (int k = 0; k < 4; ++k) st[k] = *(const f32x4*)(a.stats1 + 16 * tj + 4 * k);
    const u32x4 k0 = *(const u32x4*)(a.Krev + (size_t)d * L + 8 * tj);
    const u32x4 k1r = *(const u32x4*)(a.Krev + (size_t)d * L + 8 * (tj < 255 ? tj + 1 : tj));
    const float sa = a.mod[d], Ac = rbf(1.f + a.mod[512 + d]), ga = a.mod[1024 + d], dsk = bf1(a.Dskip[d]);
    SCHED_FENCE();
    __builtin_amdgcn_s_barrier();
    SCHED_FENCE();
    char* hb = smem + B_H + ch * B_HS;
    char* kc = smem + B_KC + ch * 4 * B_KS;
    if (tj < 32) *(u32x4*)(hb + 16 * tj) = u32x4{0, 0, 0, 0};
    if (tj < 16) *(u32x4*)(kc + (tj >> 2) * B_KS + 16 * (256 + (tj & 3))) = u32x4{0, 0, 0, 0};
    {
      const u32 z = tj < 255 ? 0xffffffffu : 0u;
      const u32 wd[8] = {k0[0], k0[1], k0[2], k0[3], k1r[0] & z, k1r[1] & z, 0, 0};
      u32 od[5];
#pragma unroll
      for (int k = 0; k < 5; ++k) od[k] = __builtin_amdgcn_alignbit(wd[k + 1], wd[k], 16);
      *(u32x4*)(kc + 0 * B_KS + 16 * tj) = u32x4{wd[0], wd[1], wd[2], wd[3]};
      *(u32x4*)(kc + 1 * B_KS + 16 * tj) = u32x4{od[0], od[1], od[2], od[3]};
      *(u32x4*)(kc + 2 * B_KS + 16 * tj) = u32x4{wd[1], wd[2], wd[3], wd[4]};
      *(u32x4*)(kc + 3 * B_KS + 16 * tj) = u32x4{od[1], od[2], od[3], od[4]};
    }
    u32x4 hv;
    {
      float hh[8];
#pragma unroll
      for (int k = 0; k < 8; ++k) {
        const float m = st[k >> 1][2 * (k & 1)], ri = st[k >> 1][2 * (k & 1) + 1];
        const float xx = (k & 1) ? bf_hi(xv[k >> 1]) : bf_lo(xv[k >> 1]);
        hh[k] = (xx - m) * (ri * Ac) + sa;
      }
      hv = u32x4{pk_bf16(hh[0], hh[1]), pk_bf16(hh[2], hh[3]), pk_bf16(hh[4], hh[5]), pk_bf16(hh[6], hh[7])};
    }
    *(u32x4*)(hb + 512 + 16 * tj) = hv;
    STAMP(1, 1);
    lds_rendezvous((u32 LAS*)(smem + B_CNT + 12 + 4 * 0) + ch * 2, 4);
    STAMP(1, 2);
    const int c = lane & 15, g = lane >> 4;
    const char* bbase = hb + 2 * (256 + 16 * c - 16 * (g >> 1) + 8 * (g & 1) - 32 * q) - 256;
    const int X = 2047 - 32 * q - 16 * (g >> 1) + 8 * (g & 1) - c;
    const char* abase = kc + (X & 3) * B_KS + 2 * (X & ~3) - 256 * 15;
    bf16x8 Bf[16], Af[16];
    auto lda = [&](int u) {
      const u32x2 lo = *(const u32x2*)(abase + 256 * (15 - u)), hi = *(const u32x2*)(abase + 256 * (15 - u) + 8);
      Af[u] = __builtin_bit_cast(bf16x8, u32x4{lo[0], lo[1], hi[0], hi[1]});
    };
    lda(0);
#pragma unroll
    for (int v = 1; v < 16; v += 2) Bf[v] = *(const bf16x8*)(bbase + 256 * v);
    lda(1);
#pragma unroll
    for (int v = 0; v < 16; v += 2) Bf[v] = *(const bf16x8*)(bbase + 256 * v);
    lda(2); lda(3);
    SCHED_FENCE();
    f32x4 acc[8];
#pragma unroll
    for (int i = 0; i < 8; ++i) acc[i] = f32x4{0, 0, 0, 0};
#pragma unroll
    for (int u = 0; u < 16; ++u) {
      if (u + 4 < 16) lda(u + 4);
      SCHED_FENCE();
#pragma unroll
      for (int i = u >> 1; i < 8; ++i) acc[i] = __builtin_amdgcn_mfma_f32_16x16x32_bf16(Af[u], Bf[2 * i - u + 1], acc[i], 0, 0, 0);
      SCHED_FENCE();
    }
    STAMP(1, 3);
    char* part = smem + B_PART + ch * (4 * 8 * 1280);
#pragma unroll
    for (int i = 0; i < 8; ++i) *(f32x4*)(part + (q * 8 + i) * 1280 + 16 * (20 * g + c)) = acc[i];
    lds_rendezvous((u32 LAS*)(smem + B_CNT + 4 * ch), 4);
    STAMP(1, 4);
    {
      const int i = tj >> 5, j5 = tj & 31, cc = j5 >> 1;
      float y[8];
#pragma unroll
      for (int hh = 0; hh < 2; ++hh) {
        const char* p = part + i * 1280 + 16 * (20 * (2 * (j5 & 1) + hh) + cc);
        const f32x4 p0 = *(const f32x4*)(p), p1 = *(const f32x4*)(p + 8 * 1280), p2 = *(const f32x4*)(p + 16 * 1280), p3 = *(const f32x4*)(p + 24 * 1280);
#pragma unroll
        for (int r = 0; r < 4; ++r) y[4 * hh + r] = (p0[r] + p1[r]) + (p2[r] + p3[r]);
      }
      float o[8];
#pragma unroll
      for (int k = 0; k < 8; ++k) {
        const float hx = (k & 1) ? bf_hi(hv[k >> 1]) : bf_lo(hv[k >> 1]);
        const float xx = (k & 1) ? bf_hi(xv[k >> 1]) : bf_lo(xv[k >> 1]);
        o[k] = xx + ga * (y[k] + hx * dsk);
      }
      wt_store16(make_rsrc(a.x1T, D * L * 2), (u32)(d * L + 8 * tj) * 2, u32x4{pk_bf16(o[0], o[1]), pk_bf16(o[2], o[3]), pk_bf16(o[4], o[5]), pk_bf16(o[6], o[7])});
    }
    STAMP(1, 5);
  } else {
    const int st = tid - 512, sw = w - 8;
    if (st < 8) *(u32*)(smem + B_CNT + 4 * st) = 0;
    asm volatile("s_waitcnt lgkmcnt(0)" ::: "memory");
    __builtin_amdgcn_s_barrier();
    SCHED_FENCE();
    float* red = (float*)(smem + B_RED);
    const bool act = wg < 224;
    const int f0 = 8 * ((wg & 7) * 28 + (wg >> 3));
    const int d8 = st >> 2, fp = st & 3;
    const int f8 = wg >> 1, dp = (wg & 1) * 128 + (st & 127);
    u32 wv[8], ov[8];
    f32x4 cb0, cb1, sb0, sb1;
    if (act) {
      cb0 = *(const f32x4*)(a.mod + 2048 + 8 * d8); cb1 = *(const f32x4*)(a.mod + 2048 + 8 * d8 + 4);
      sb0 = *(const f32x4*)(a.mod + 1536 + 8 * d8); sb1 = *(const f32x4*)(a.mod + 1536 + 8 * d8 + 4);
#pragma unroll
      for (int j = 0; j < 8; ++j) wv[j] = *(const u32*)(a.win + (size_t)(8 * d8 + j) * F + f0 + 2 * fp);
      if (st < 128) {
#pragma unroll
        for (int j = 0; j < 8; ++j) ov[j] = *(const u32*)(a.wout + (size_t)(8 * f8 + j) * D + 2 * dp);
      }
    }
    u32x4 pf[8];
    {
      const u16* p = a.wsp + (size_t)wg * 16384 + st * 8;
#pragma unroll
      for (int j = 0; j < 8; ++j) pf[j] = *(const u32x4*)(p + j * 2048);
    }
    SCHED_FENCE();
    if (act) {
      float lo[8], hi[8], u0 = 0, u1 = 0, v0 = 0, v1 = 0;
#pragma unroll
      for (int j = 0; j < 8; ++j) {
        const float Ac = rbf(1.f + (j < 4 ? cb0[j] : cb1[j - 4])), sb = j < 4 ? sb0[j] : sb1[j - 4];
        const float wl = bf_lo(wv[j]), wh = bf_hi(wv[j]);
        lo[j] = rbf(Ac * wl); hi[j] = rbf(Ac * wh);
        u0 += lo[j]; u1 += hi[j]; v0 += sb * wl; v1 += sb * wh;
      }
      const int f = f0 + 2 * fp;
      const rsrc_t rw = make_rsrc(a.Wt, D * F * 2);
      const u32 dst = (u32)((((f >> 4) * 64 + d8) * 16 + (f & 15)) * 16);
      wt_store16(rw, dst, u32x4{pk_bf16(lo[0], lo[1]), pk_bf16(lo[2], lo[3]), pk_bf16(lo[4], lo[5]), pk_bf16(lo[6], lo[7])});
      wt_store16(rw, dst + 16, u32x4{pk_bf16(hi[0], hi[1]), pk_bf16(hi[2], hi[3]), pk_bf16(hi[4], hi[5]), pk_bf16(hi[6], hi[7])});
      float rv[4] = {u0, u1, v0, v1};
#pragma unroll
      for (int k = 0; k < 4; ++k) {
        const float v = xor32_sum(xor16_sum(xor4_8_sum(rv[k])));
        if (lane < 4) red[sw * 16 + lane * 4 + k] = v;
      }
      if (st < 128) {
        const int dd = 2 * dp;
        const rsrc_t ro = make_rsrc(a.Wot, FH * D * 2);
        const u32 od = (u32)((((dd >> 4) * 112 + f8) * 16 + (dd & 15)) * 16);
        u32x4 e, o;
#pragma unroll
        for (int k = 0; k < 4; ++k) {
          e[k] = (ov[2 * k] & 0xffffu) | (ov[2 * k + 1] << 16);
          o[k] = (ov[2 * k] >> 16) | (ov[2 * k + 1] & 0xffff0000u);
        }
        wt_store16(ro, od, e); wt_store16(ro, od + 16, o);
      }
      lds_rendezvous((u32 LAS*)(smem + B_CNT + 8), 4);
      if (st < 16) {
        const float s = (red[st] + red[16 + st]) + (red[32 + st] + red[48 + st]);
        const int fq = st >> 2, k = st & 3;
        a.uv[(k >> 1) * F + f0 + 2 * fq + (k & 1)] = s;
      }
    }
    STAMP(1, 1);
#pragma unroll
    for (int j = 0; j < 8; ++j) asm volatile("" ::"v"(pf[j]));
    STAMP(1, 5);
  }
}
struct CArgs {
  const u16 *x1T, *Wt; const float* uv;
  u16 *z1t, *z2t; u64* gran;
};
constexpr int C_X = 0;
constexpr int C_ST = 65536;
constexpr int C_MR = C_ST + 512;
constexpr int C_PS = C_MR + 8 * 512;
constexpr int C_FS = C_PS + 7 * 64 * 8;
constexpr int C_ZS = C_FS + 512;
constexpr int C_RS = 232;
constexpr int C_LDS = C_ZS + 64 * C_RS;

__device__ __forceinline__ float gelu_tanh(float z) {
  const float k0 = -2.f * 0.7978845608028654f * 1.4426950408889634f, k1 = k0 * 0.044715f;
  const float e = __builtin_amdgcn_exp2f(z * (k0 + k1 * (z * z)));
  return z * __builtin_amdgcn_rcpf(1.f + e);
}
__device__ __forceinline__ float pick4(f32x4 v, int r) { return r == 0 ? v[0] : r == 1 ? v[1] : r == 2 ? v[2] : v[3]; }

template <int ROLE>
__device__ __forceinline__ void c_body(const CArgs& a, char* smem, const int w) {
  const int wg = blockIdx.x, tid = threadIdx.x, lane = tid & 63;
  const int xc = wg & 7, ii = wg >> 3;
  const int pi = (xc >> 1) * 8 + (ii & 7), j = (xc & 1) * 4 + (ii >> 3);
  const int c = lane & 15, g = lane >> 4;
  const int nt2 = 56 + 7 * j + (w < 7 ? w : 0), nt1 = 7 * j + (w > 0 ? w - 1 : 0);
  const u16* pw2 = a.Wt + (size_t)nt2 * 64 * 128 + lane * 8;
  const u16* pw1 = a.Wt + (size_t)nt1 * 64 * 128 + lane * 8;

  const int q4 = (lane & 15) >> 2, p4 = lane & 3;
  const int drow = 8 * g + q4;
  const int fz0 = (((drow >> 1) & 1) | ((drow >> 2) & 2)) << 1;
  const char LAS* xb = (const char LAS*)smem + C_X + drow * 128 + 8 * (p4 & 1);
  const char LAS* xt[4];
  int tb[4];
#pragma unroll
  for (int s = 0; s < 4; ++s) { tb[s] = (s + w) & 3; xt[s] = xb + 16 * ((2 * tb[s] + (p4 >> 1)) ^ fz0); }
  auto xfrag = [&](int s, int ks) -> bf16x8 {
    const char LAS* p = xt[s] + ks * 4096;
    const s16x4 lo = tr_read(p), hi = tr_read(p + 512);
    const u32x2 a0 = __builtin_bit_cast(u32x2, lo), a1 = __builtin_bit_cast(u32x2, hi);
    return __builtin_bit_cast(bf16x8, u32x4{a0[0], a0[1], a1[0], a1[1]});
  };
  f32x4 acc[4];
#pragma unroll
  for (int s = 0; s < 4; ++s) acc[s] = f32x4{0, 0, 0, 0};
  bf16x8 W1[16];
  f32x4 u2, v2, u1, v1;
  float* xs2 = (float*)(smem + C_ST);
  if constexpr (ROLE < 2) {
    bf16x8 W2[16];
#pragma unroll
    for (int ks = 0; ks < 16; ++ks) W2[ks] = *(const bf16x8*)(pw2 + 4 * ks * 128);
    u2 = *(const f32x4*)(a.uv + 16 * nt2 + 4 * g); v2 = *(const f32x4*)(a.uv + F + 16 * nt2 + 4 * g);
    SCHED_FENCE();
    VM_WAIT(18);
    __builtin_amdgcn_s_barrier();
    SCHED_FENCE();
    STAMP(2, 1);
    f32x4 sq = {0, 0, 0, 0};
    bf16x8 X[2][4];
#pragma unroll
    for (int s = 0; s < 4; ++s) X[0][s] = xfrag(s, 0);
#pragma unroll
    for (int ks = 0; ks < 16; ++ks) {
      if (ks + 1 < 16) {
#pragma unroll
        for (int s = 0; s < 4; ++s) X[(ks + 1) & 1][s] = xfrag(s, ks + 1);
      }
      SCHED_FENCE();
#pragma unroll
      for (int s = 0; s < 4; ++s) acc[s] = __builtin_amdgcn_mfma_f32_16x16x32_bf16(W2[ks], X[ks & 1][s], acc[s], 0, 0, 0);
      sq = __builtin_amdgcn_mfma_f32_16x16x32_bf16(X[ks & 1][0], X[ks & 1][0], sq, 0, 0, 0);
      SCHED_FENCE();
    }
    if constexpr (ROLE == 1) {
#pragma unroll
      for (int ks = 0; ks < 16; ++ks) W1[ks] = *(const bf16x8*)(pw1 + 4 * ks * 128);
      u1 = *(const f32x4*)(a.uv + 16 * nt1 + 4 * g); v1 = *(const f32x4*)(a.uv + F + 16 * nt1 + 4 * g);
      SCHED_FENCE();
    }
    if (w < 4 && (c >> 2) == g) xs2[64 + 16 * w + c] = pick4(sq, c & 3);
  } else {
    VM_WAIT(0);
    __builtin_amdgcn_s_barrier();
    SCHED_FENCE();
    STAMP(2, 1);
    const u32x4 ones = {0x3f803f80u, 0x3f803f80u, 0x3f803f80u, 0x3f803f80u};
    bf16x8 X[2][4];
#pragma unroll
    for (int s = 0; s < 4; ++s) X[0][s] = xfrag(s, 0);
#pragma unroll
    for (int ks = 0; ks < 16; ++ks) {
      if (ks + 1 < 16) {
#pragma unroll
        for (int s = 0; s < 4; ++s) X[(ks + 1) & 1][s] = xfrag(s, ks + 1);
      }
      SCHED_FENCE();
#pragma unroll
      for (int s = 0; s < 4; ++s) acc[s] = __builtin_amdgcn_mfma_f32_16x16x32_bf16(__builtin_bit_cast(bf16x8, ones), X[ks & 1][s], acc[s], 0, 0, 0);
      SCHED_FENCE();
    }
#pragma unroll
    for (int ks = 0; ks < 16; ++ks) W1[ks] = *(const bf16x8*)(pw1 + 4 * ks * 128);
    u1 = *(const f32x4*)(a.uv + 16 * nt1 + 4 * g); v1 = *(const f32x4*)(a.uv + F + 16 * nt1 + 4 * g);
    SCHED_FENCE();
    if (g == 0) {
#pragma unroll
      for (int s = 0; s < 4; ++s) xs2[16 * tb[s] + c] = acc[s][0];
    }
  }
  STAMP(2, 2);
  __syncthreads();
  STAMP(2, 3);
  float mu[4], ri[4];
  {
    const float mean = xs2[lane] * (1.f / D);
    const float var = fmaxf(xs2[64 + lane] * (1.f / D) - mean * mean, 0.f);
    f32x2* mr = (f32x2*)(smem + C_MR + w * 512);
    mr[lane] = f32x2{rbf(mean), __builtin_amdgcn_rcpf(rbf(__builtin_amdgcn_sqrtf(rbf(rbf(var) + 1e-5f))))};
#pragma unroll
    for (int s = 0; s < 4; ++s) { const f32x2 v = mr[16 * tb[s] + c]; mu[s] = v[0]; ri[s] = v[1]; }
  }
  u32x2 z2p[4];
  if constexpr (ROLE < 2) {
    float* ps = (float*)(smem + C_PS) + w * 128;
    const s16x4 ones4 = {0x3f80, 0x3f80, 0x3f80, 0x3f80};
#pragma unroll
    for (int s = 0; s < 4; ++s) {
      float z[4];
#pragma unroll
      for (int r = 0; r < 4; ++r) z[r] = gelu_tanh(ri[s] * (acc[s][r] - mu[s] * u2[r]) + v2[r]);
      z2p[s] = u32x2{pk_bf16(z[0], z[1]), pk_bf16(z[2], z[3])};
      const s16x4 zz = __builtin_bit_cast(s16x4, z2p[s]);
      const f32x4 zero = {0, 0, 0, 0};
      const f32x4 s1 = __builtin_amdgcn_mfma_f32_16x16x16bf16_1k(ones4, zz, zero, 0, 0, 0);
      const f32x4 s2 = __builtin_amdgcn_mfma_f32_16x16x16bf16_1k(zz, zz, zero, 0, 0, 0);
      if ((c >> 2) == g) *(f32x2*)(ps + (16 * tb[s] + c) * 2) = f32x2{s1[0], pick4(s2, c & 3)};
    }
  }
  STAMP(2, 4);
  __syncthreads();
  STAMP(2, 5);
  if constexpr (ROLE == 0) {
    const float* ps = (const float*)(smem + C_PS);
    float s1 = 0, s2 = 0;
#pragma unroll
    for (int k = 0; k < 7; ++k) { const f32x2 v = *(const f32x2*)(ps + k * 128 + lane * 2); s1 += v[0]; s2 += v[1]; }
    GAS u64* gp = (GAS u64*)a.gran + (size_t)pi * 8 * 128;
    __hip_atomic_store(gp + (j * 64 + lane) * 2, (1ull << 32) | __float_as_uint(s1), __ATOMIC_RELAXED, __HIP_MEMORY_SCOPE_AGENT);
    __hip_atomic_store(gp + (j * 64 + lane) * 2 + 1, (1ull << 32) | __float_as_uint(s2), __ATOMIC_RELAXED, __HIP_MEMORY_SCOPE_AGENT);
    float t1 = 0, t2 = 0;
    for (u32 spin = 0; spin < (1u << 22); ++spin) {
      bool ok = true; t1 = 0; t2 = 0;
#pragma unroll
      for (int k = 0; k < 8; ++k) {
        const u64 x0 = __hip_atomic_load(gp + (k * 64 + lane) * 2, __ATOMIC_RELAXED, __HIP_MEMORY_SCOPE_AGENT);
        const u64 x1 = __hip_atomic_load(gp + (k * 64 + lane) * 2 + 1, __ATOMIC_RELAXED, __HIP_MEMORY_SCOPE_AGENT);
        ok = ok && (x0 >> 32) == 1 && (x1 >> 32) == 1;
        t1 += __uint_as_float((u32)x0); t2 += __uint_as_float((u32)x1);
      }
      if (__all(ok)) break;
      __builtin_amdgcn_s_sleep(2);
    }
    const float mean = t1 * (1.f / FH);
    const float var = fmaxf(t2 * (1.f / FH) - mean * mean, 0.f);
    *(f32x2*)(smem + C_FS + lane * 8) = f32x2{rbf(mean), __builtin_amdgcn_rcpf(rbf(__builtin_amdgcn_sqrtf(rbf(rbf(var) + 1e-5f))))};
  } else {
#pragma unroll
    for (int s = 0; s < 4; ++s) acc[s] = f32x4{0, 0, 0, 0};
    bf16x8 X[2][4];
#pragma unroll
    for (int s = 0; s < 4; ++s) X[0][s] = xfrag(s, 0);
#pragma unroll
    for (int ks = 0; ks < 16; ++ks) {
      if (ks + 1 < 16) {
#pragma unroll
        for (int s = 0; s < 4; ++s) X[(ks + 1) & 1][s] = xfrag(s, ks + 1);
      }
      SCHED_FENCE();
#pragma unroll
      for (int s = 0; s < 4; ++s) acc[s] = __builtin_amdgcn_mfma_f32_16x16x32_bf16(W1[ks], X[ks & 1][s], acc[s], 0, 0, 0);
      SCHED_FENCE();
    }
    STAMP(2, 6);
    const int f8 = 14 * j + 2 * (w - 1) + (g >> 1);
    const rsrc_t rz = make_rsrc(a.z1t, L * FH * 2);
    u32x2 zp[4];
#pragma unroll
    for (int s = 0; s < 4; ++s) {
      float z[4];
#pragma unroll
      for (int r = 0; r < 4; ++r) z[r] = gelu_tanh(ri[s] * (acc[s][r] - mu[s] * u1[r]) + v1[r]);
      zp[s] = u32x2{pk_bf16(z[0], z[1]), pk_bf16(z[2], z[3])};
      if (s & 1) wt_store16(rz, (u32)((((4 * pi + ((g & 1) ? tb[s] : tb[s - 1])) * 112 + f8) * 16 + c) * 16), pair16(zp[s - 1], zp[s]));
    }
  }
  STAMP(2, 7);
  __syncthreads();
  STAMP(2, 8);
  if constexpr (ROLE < 2) {
#pragma unroll
    for (int s = 0; s < 4; ++s) {
      const f32x2 fs = *(const f32x2*)(smem + C_FS + (16 * tb[s] + c) * 8);
      const float a0 = (bf_lo(z2p[s][0]) - fs[0]) * fs[1], a1 = (bf_hi(z2p[s][0]) - fs[0]) * fs[1];
      const float a2 = (bf_lo(z2p[s][1]) - fs[0]) * fs[1], a3 = (bf_hi(z2p[s][1]) - fs[0]) * fs[1];
      *(u32x2*)(smem + C_ZS + (16 * tb[s] + c) * C_RS + (16 * w + 4 * g) * 2) = u32x2{pk_bf16(a0, a1), pk_bf16(a2, a3)};
    }
#pragma unroll
    for (int kk = 0; kk < 2; ++kk) {
      const char LAS* p = (const char LAS*)smem + C_ZS + (32 * kk + 8 * g + q4) * C_RS + (16 * w + 4 * p4) * 2;
      const s16x4 lo = tr_read(p), hi = tr_read(p + 4 * C_RS);
      const u32x2 a0 = __builtin_bit_cast(u32x2, lo), a1 = __builtin_bit_cast(u32x2, hi);
      wt_store16(make_rsrc(a.z2t, L * FH * 2), (u32)((((7 * j + w) * 256 + 8 * pi + 4 * kk) * 64 + lane * 4) * 4), u32x4{a0[0], a0[1], a1[0], a1[1]});
    }
  }
  STAMP(2, 9);
}

__global__ __launch_bounds__(512, 2) void kC(CArgs a) {
  extern __shared__ __attribute__((aligned(16))) char smem[];
  const int w = __builtin_amdgcn_readfirstlane(threadIdx.x >> 6);
  STAMP(2, 0);
  {
    const int wg = blockIdx.x, lane = threadIdx.x & 63;
    const int pi = ((wg & 7) >> 1) * 8 + ((wg >> 3) & 7);
    const u32 lds0 = (u32)(size_t)(const char LAS*)smem;
#pragma unroll
    for (int i = 0; i < 8; ++i) {
      const int d = 64 * w + 8 * i + (lane >> 3);
      const int fz = (((d >> 1) & 1) | ((d >> 2) & 2)) << 1;
      glds16(a.x1T + (size_t)d * L + 64 * pi + 8 * ((lane & 7) ^ fz), lds0 + C_X + (64 * w + 8 * i) * 128);
    }
    __builtin_amdgcn_s_barrier();
  }
  if (w == 0) c_body<0>(a, smem, w);
  else if (w < 7) c_body<1>(a, smem, w);
  else c_body<2>(a, smem, w);
}
struct DArgs { const u16 *wsp, *z2t, *z1t, *bsp, *Wot; u16* Pt; };
constexpr int D_RING = 32768;
constexpr int D_LDS = 4 * D_RING;

constexpr int d_ops(int it) { return it < 0 ? 0 : (it + 2 < 16 ? 7 : 0) + (((it & 1) == 0 && it / 2 + 2 < 8) ? 8 : 0); }
template <int N> __device__ __forceinline__ void vm_wait_c() { asm volatile("s_waitcnt vmcnt(%0)" ::"i"(N > 63 ? 63 : N) : "memory"); }

template <int IT>
__device__ __forceinline__ void d_iter(f32x4 (&acc)[4][7], bf16x8 (&Bf)[3][7], bf16x8 (&Af)[2][4], const u16* pb, const u16* const (&pa)[2],
                                       u32 ring, const char* const (&ap)[2][4]) {
  if constexpr (IT + 1 < 16) {
    constexpr int s = (IT + 1) >> 1;
    if constexpr ((IT & 1) == 1) vm_wait_c<(IT == 1 ? d_ops(0) : d_ops(IT - 2) + d_ops(IT - 1))>();
#pragma unroll
    for (int m = 0; m < 4; ++m) Af[(IT + 1) & 1][m] = *(const bf16x8*)(ap[(IT + 1) & 1][m] + (s & 3) * 8192);
  }
  SCHED_FENCE();
#pragma unroll
  for (int n = 0; n < 7; ++n) {
    if constexpr (IT + 2 < 16) Bf[(IT + 2) % 3][n] = *(const bf16x8*)(pb + ((size_t)n * 256 + 4 * (IT + 2)) * 128);
    if constexpr ((IT & 1) == 0 && IT / 2 + 2 < 8) {
      constexpr int s = IT / 2 + 2;
      glds16(pa[n & 1] + (size_t)(n >> 1) * 16 * L + 64 * s, ring + (s & 3) * 8192 + n * 1024);
      if (n == 6) glds16(pa[1] + (size_t)3 * 16 * L + 64 * s, ring + (s & 3) * 8192 + 7 * 1024);
    }
#pragma unroll
    for (int m = 0; m < 4; ++m) acc[m][n] = __builtin_amdgcn_mfma_f32_16x16x32_bf16(Bf[IT % 3][n], Af[IT & 1][m], acc[m][n], 0, 0, 0);
    SCHED_FENCE();
  }
}

__global__ __launch_bounds__(256, 1) void kD(DArgs a) {
  extern __shared__ __attribute__((aligned(16))) char smem[];
  const int wg = blockIdx.x, tid = threadIdx.x, lane = tid & 63;
  const int w = __builtin_amdgcn_readfirstlane(tid >> 6);
  const int xc = wg & 7, ii = wg >> 3;
  const int rt = (xc >> 1) * 8 + (ii & 7), ct = (xc & 1) * 4 + (ii >> 3);
  const int c = lane & 15, g = lane >> 4;
  STAMP(3, 0);
  const u32 ring = (u32)(size_t)(const char LAS*)smem + w * D_RING;
  const u16* pa[2];
#pragma unroll
  for (int par = 0; par < 2; ++par) {
    const int row = 8 * par + (lane >> 3);
    pa[par] = a.wsp + (size_t)(64 * rt + row) * L + 512 * w + 8 * ((lane & 7) ^ ((row >> 1) & 7));
  }
  const u16* pb = a.z2t + ((size_t)(7 * ct) * 256 + 64 * w) * 128 + lane * 8;
  auto ldA = [&](int s) {
#pragma unroll
    for (int i = 0; i < 8; ++i) glds16(pa[i & 1] + (size_t)(i >> 1) * 16 * L + 64 * s, ring + (s & 3) * 8192 + i * 1024);
  };
  bf16x8 Bf[3][7], Af[2][4];
  auto ldB = [&](int ks) {
#pragma unroll
    for (int n = 0; n < 7; ++n) Bf[ks][n] = *(const bf16x8*)(pb + ((size_t)n * 256 + 4 * ks) * 128);
  };
  ldA(0); SCHED_FENCE(); ldB(0); ldB(1); SCHED_FENCE(); ldA(1); SCHED_FENCE();
  f32x4 acc[4][7];
#pragma unroll
  for (int m = 0; m < 4; ++m)
#pragma unroll
    for (int n = 0; n < 7; ++n) acc[m][n] = f32x4{0, 0, 0, 0};
  const int asw = (c >> 1) & 7;
  const char* ap[2][4];
#pragma unroll
  for (int m = 0; m < 4; ++m) {
    const char* base = smem + w * D_RING + ((m + w) & 3) * 2048 + c * 128;
    ap[0][m] = base + 16 * (g ^ asw); ap[1][m] = base + 16 * ((4 + g) ^ asw);
  }
  vm_wait_c<7 + 7 + 8>();
#pragma unroll
  for (int m = 0; m < 4; ++m) Af[0][m] = *(const bf16x8*)(ap[0][m]);
  STAMP(3, 4);
  d_iter<0>(acc, Bf, Af, pb, pa, ring, ap);   d_iter<1>(acc, Bf, Af, pb, pa, ring, ap);
  d_iter<2>(acc, Bf, Af, pb, pa, ring, ap);   d_iter<3>(acc, Bf, Af, pb, pa, ring, ap);
  STAMP(3, 5);
  d_iter<4>(acc, Bf, Af, pb, pa, ring, ap);   d_iter<5>(acc, Bf, Af, pb, pa, ring, ap);
  d_iter<6>(acc, Bf, Af, pb, pa, ring, ap);   d_iter<7>(acc, Bf, Af, pb, pa, ring, ap);
  STAMP(3, 6);
  d_iter<8>(acc, Bf, Af, pb, pa, ring, ap);   d_iter<9>(acc, Bf, Af, pb, pa, ring, ap);
  d_iter<10>(acc, Bf, Af, pb, pa, ring, ap);  d_iter<11>(acc, Bf, Af, pb, pa, ring, ap);
  STAMP(3, 7);
  d_iter<12>(acc, Bf, Af, pb, pa, ring, ap);  d_iter<13>(acc, Bf, Af, pb, pa, ring, ap);
  u32x2 z1v[7];
#pragma unroll
  for (int n = 0; n < 7; ++n)
    z1v[n] = *(const u32x2*)(a.z1t + ((size_t)(4 * rt + w) * 112 + 14 * ct + 2 * n + (g >> 1)) * 128 + c * 8 + (g & 1) * 4);
  const float bs = bf1(a.bsp[64 * rt + 16 * w + c]);
  u32x4 pf[4];
  {
    const u16* p = a.Wot + (size_t)(xc & 1) * 16 * 112 * 128 + (size_t)ii * 7168 + tid * 8;
#pragma unroll
    for (int k = 0; k < 3; ++k) pf[k] = *(const u32x4*)(p + k * 2048);
    pf[3] = *(const u32x4*)(p + (tid < 128 ? 3 * 2048 : 0));
  }
  SCHED_FENCE();
  d_iter<14>(acc, Bf, Af, pb, pa, ring, ap);  d_iter<15>(acc, Bf, Af, pb, pa, ring, ap);
  STAMP(3, 1);
  __builtin_amdgcn_s_barrier();
#pragma unroll
  for (int m = 1; m < 4; ++m) {
    char* dst = smem + ((((m + w) & 3) * 3 + (m - 1)) * 7 * 64 + lane) * 16;
#pragma unroll
    for (int n = 0; n < 7; ++n) *(f32x4*)(dst + n * 1024) = acc[m][n];
  }
  __syncthreads();
  STAMP(3, 2);
  const char* src = smem + (w * 3 * 7 * 64 + lane) * 16;
  const rsrc_t rp = make_rsrc(a.Pt, L * FH * 2);
  u32x2 pv[8];
#pragma unroll
  for (int n = 0; n < 7; ++n) {
    const f32x4 p0 = *(const f32x4*)(src + n * 1024), p1 = *(const f32x4*)(src + (7 + n) * 1024), p2 = *(const f32x4*)(src + (14 + n) * 1024);
    float gg[4];
#pragma unroll
    for (int e = 0; e < 4; ++e) gg[e] = ((acc[0][n][e] + p0[e]) + (p1[e] + p2[e])) + bs;
    pv[n] = u32x2{pk_bf16(bf_lo(z1v[n][0]) * gg[0], bf_hi(z1v[n][0]) * gg[1]), pk_bf16(bf_lo(z1v[n][1]) * gg[2], bf_hi(z1v[n][1]) * gg[3])};
    if (n & 1) wt_store16(rp, (u32)((((4 * rt + w) * 112 + 14 * ct + 2 * (n - 1 + (g & 1)) + (g >> 1)) * 16 + c) * 16), pair16(pv[n - 1], pv[n]));
  }
  {
    const u32x4 v = pair16(pv[6], pv[6]);
    if ((g & 1) == 0) wt_store16(rp, (u32)((((4 * rt + w) * 112 + 14 * ct + 12 + (g >> 1)) * 16 + c) * 16), v);
  }
#pragma unroll
  for (int k = 0; k < 4; ++k) asm volatile("" ::"v"(pf[k]));
  STAMP(3, 3);
}
struct EArgs { const u16 *Pt, *Wot, *x1T; const float* mod; u16* out; };
constexpr int E_TS = 1152;
constexpr int E_PART = 0;
constexpr int E_X = 4 * 16 * E_TS;
constexpr int E_LDS = E_X + 64 * 132;

__global__ __launch_bounds__(256, 1) void kE(EArgs a) {
  extern __shared__ __attribute__((aligned(16))) char smem[];
  const int wg = blockIdx.x, tid = threadIdx.x, lane = tid & 63;
  const int w = __builtin_amdgcn_readfirstlane(tid >> 6);
  const int xc = wg & 7, ii = wg >> 3;
  const int rt = (xc >> 1) * 8 + (ii & 7), dt = (xc & 1) * 4 + (ii >> 3);
  const int c = lane & 15, g = lane >> 4;
  STAMP(4, 0);
  const u16* pp = a.Pt + ((size_t)(4 * rt) * 112 + 28 * w) * 128 + lane * 8;
  const u16* pw = a.Wot + ((size_t)(4 * dt) * 112 + 28 * w) * 128 + lane * 8;
  bf16x8 Pf[3][4], Wf[3][4];
  auto ld = [&](int k) {
#pragma unroll
    for (int m = 0; m < 4; ++m) {
      Wf[k % 3][m] = *(const bf16x8*)(pw + ((size_t)m * 112 + 4 * k) * 128);
      Pf[k % 3][m] = *(const bf16x8*)(pp + ((size_t)m * 112 + 4 * k) * 128);
    }
  };
  ld(0); ld(1);
  SCHED_FENCE();
  f32x4 acc[4][4];
#pragma unroll
  for (int m = 0; m < 4; ++m)
#pragma unroll
    for (int n = 0; n < 4; ++n) acc[m][n] = f32x4{0, 0, 0, 0};
  u32x4 xr[2];
  f32x4 gb0, gb1;
  const int d8 = lane & 7;
#pragma unroll
  for (int k = 0; k < 7; ++k) {
    if (k + 2 < 7) ld(k + 2);
    if (k == 5) {
#pragma unroll
      for (int kk = 0; kk < 2; ++kk) {
        const int id = tid + 256 * kk, dd = id >> 3, chn = id & 7;
        xr[kk] = *(const u32x4*)(a.x1T + (size_t)(64 * dt + dd) * L + 64 * rt + 8 * chn);
      }
      gb0 = *(const f32x4*)(a.mod + 2560 + 64 * dt + 8 * d8); gb1 = *(const f32x4*)(a.mod + 2560 + 64 * dt + 8 * d8 + 4);
    }
    SCHED_FENCE();
#pragma unroll
    for (int n = 0; n < 4; ++n)
#pragma unroll
      for (int m = 0; m < 4; ++m) acc[m][n] = __builtin_amdgcn_mfma_f32_16x16x32_bf16(Wf[k % 3][n], Pf[k % 3][m], acc[m][n], 0, 0, 0);
    SCHED_FENCE();
  }
  STAMP(4, 1);
#pragma unroll
  for (int m = 0; m < 4; ++m)
#pragma unroll
    for (int n = 0; n < 4; ++n) *(f32x4*)(smem + E_PART + (w * 16 + m * 4 + n) * E_TS + g * 288 + c * 16) = acc[m][n];
#pragma unroll
  for (int k = 0; k < 2; ++k) {
    const int id = tid + 256 * k, dd = id >> 3, chn = id & 7;
    u32* p = (u32*)(smem + E_X + dd * 132 + chn * 16);
    p[0] = xr[k][0]; p[1] = xr[k][1]; p[2] = xr[k][2]; p[3] = xr[k][3];
  }
  __syncthreads();
  STAMP(4, 2);
#pragma unroll
  for (int k = 0; k < 2; ++k) {
    const int t = 32 * k + 8 * w + (lane >> 3);
    float s[8];
#pragma unroll
    for (int hh = 0; hh < 2; ++hh) {
      const char* p = smem + E_PART + ((t >> 4) * 4 + (d8 >> 1)) * E_TS + (2 * (d8 & 1) + hh) * 288 + (t & 15) * 16;
      const f32x4 p0 = *(const f32x4*)p, p1 = *(const f32x4*)(p + 16 * E_TS), p2 = *(const f32x4*)(p + 32 * E_TS), p3 = *(const f32x4*)(p + 48 * E_TS);
#pragma unroll
      for (int e = 0; e < 4; ++e) s[4 * hh + e] = (p0[e] + p1[e]) + (p2[e] + p3[e]);
    }
    float o[8];
#pragma unroll
    for (int e = 0; e < 8; ++e) {
      const float x1 = bf1(*(const u16*)(smem + E_X + (8 * d8 + e) * 132 + t * 2));
      o[e] = x1 + (e < 4 ? gb0[e] : gb1[e - 4]) * s[e];
    }
    wt_store16(make_rsrc(a.out, L * D * 2), (u32)(((64 * rt + t) * D + 64 * dt + 8 * d8) * 2), u32x4{pk_bf16(o[0], o[1]), pk_bf16(o[2], o[3]), pk_bf16(o[4], o[5]), pk_bf16(o[6], o[7])});
  }
  STAMP(4, 3);
}
#define KMASK(b)
extern "C" void kernel_launch(void* const* d_in, const int* in_sizes, int n_in, void* d_out, int out_size,
                              void* d_ws, size_t ws_size, hipStream_t stream) {
  const u16* x = (const u16*)d_in[0];
  char* ws = (char*)d_ws;
  float* mod = (float*)(ws + 0);
  u32* stats1 = (u32*)(ws + 16384);
  float* uv = (float*)(ws + 32768);
  u64* gran = (u64*)(ws + 65536);
  u16* xT = (u16*)(ws + (1 << 20));
  u16* Krev = (u16*)(ws + (3 << 20));
  u16* x1T = xT;
  u16* Wt = (u16*)(ws + (7 << 20));
  u16* Wot = (u16*)(ws + (9 << 20));
  u16* z1t = (u16*)(ws + (10 << 20));
  u16* z2t = Krev;
  u16* Pt = z1t;
  AArgs aa;
  aa.x = x; aa.Are = (const u16*)d_in[1]; aa.Aim = (const u16*)d_in[2]; aa.Bre = (const u16*)d_in[3]; aa.Bim = (const u16*)d_in[4];
  aa.Cre = (const u16*)d_in[5]; aa.Cim = (const u16*)d_in[6]; aa.logdt = (const u16*)d_in[7]; aa.c = (const u16*)d_in[14];
  aa.wmod = (const u16*)d_in[15]; aa.win = (const u16*)d_in[10]; aa.wout = (const u16*)d_in[13];
  aa.mod = mod; aa.stats1 = stats1; aa.xT = xT; aa.Krev = Krev; aa.gran = (u32x4*)gran;
  (void)hipFuncSetAttribute((const void*)kA, hipFuncAttributeMaxDynamicSharedMemorySize, A_LDS);
  KMASK(1) kA<<<256, 768, A_LDS, stream>>>(aa);
  BArgs ba;
  ba.xT = xT; ba.Krev = Krev; ba.Dskip = (const u16*)d_in[8]; ba.win = (const u16*)d_in[10]; ba.wout = (const u16*)d_in[13];
  ba.wsp = (const u16*)d_in[11]; ba.stats1 = stats1; ba.mod = mod; ba.x1T = x1T; ba.Wt = Wt; ba.Wot = Wot; ba.uv = uv;
  (void)hipFuncSetAttribute((const void*)kB, hipFuncAttributeMaxDynamicSharedMemorySize, B_LDS);
  KMASK(2) kB<<<256, 768, B_LDS, stream>>>(ba);
  CArgs ca;
  ca.x1T = x1T; ca.Wt = Wt; ca.uv = uv; ca.z1t = z1t; ca.z2t = z2t; ca.gran = gran;
  (void)hipFuncSetAttribute((const void*)kC, hipFuncAttributeMaxDynamicSharedMemorySize, C_LDS);
  KMASK(4) kC<<<256, 512, C_LDS, stream>>>(ca);
  DArgs da;
  da.wsp = (const u16*)d_in[11]; da.z2t = z2t; da.z1t = z1t; da.bsp = (const u16*)d_in[12]; da.Wot = Wot; da.Pt = Pt;
  (void)hipFuncSetAttribute((const void*)kD, hipFuncAttributeMaxDynamicSharedMemorySize, D_LDS);
  KMASK(8) kD<<<256, 256, D_LDS, stream>>>(da);
  EArgs ea;
  ea.Pt = Pt; ea.Wot = Wot; ea.x1T = x1T; ea.mod = mod; ea.out = (u16*)d_out;
  (void)hipFuncSetAttribute((const void*)kE, hipFuncAttributeMaxDynamicSharedMemorySize, E_LDS);
  KMASK(16) kE<<<256, 256, E_LDS, stream>>>(ea);
}
```

```cpp
#include <hip/hip_runtime.h>
#include <hip/hip_bf16.h>
#include <stdint.h>

typedef unsigned short u16;
typedef unsigned int u32;
typedef unsigned long long u64;
typedef __bf16 bf16x8 __attribute__((ext_vector_type(8)));
typedef __bf16 bf16x2 __attribute__((ext_vector_type(2)));
typedef short s16x4 __attribute__((ext_vector_type(4)));
typedef float f32x2 __attribute__((ext_vector_type(2)));
typedef float f32x4 __attribute__((ext_vector_type(4)));
typedef u32 u32x2 __attribute__((ext_vector_type(2)));
typedef u32 u32x4 __attribute__((ext_vector_type(4)));
#define LAS __attribute__((address_space(3)))
#define GAS __attribute__((address_space(1)))

constexpr int L = 2048, D = 512, NS = 128, F = 1792, FH = 896;

__device__ __forceinline__ float bf_lo(u32 w) { return __uint_as_float(w << 16); }
__device__ __forceinline__ float bf_hi(u32 w) { return __uint_as_float(w & 0xffff0000u); }
__device__ __forceinline__ float bf1(u16 h) { return __uint_as_float(((u32)h) << 16); }
__device__ __forceinline__ u32 pk_bf16(float lo, float hi) {
  f32x2 a = {lo, hi};
  bf16x2 p = __builtin_convertvector(a, bf16x2);
  return __builtin_bit_cast(u32, p);
}
__device__ __forceinline__ float rbf(float x) { return bf_lo(pk_bf16(x, x)); }

template <int CTRL, int ROWMASK = 0xf>
__device__ __forceinline__ float dpp_add(float x) {
  int y = __builtin_amdgcn_update_dpp(0, __float_as_int(x), CTRL, ROWMASK, 0xf, true);
  return x + __int_as_float(y);
}
__device__ __forceinline__ float row16_sum(float x) {
  x = dpp_add<0xB1>(x);
  x = dpp_add<0x4E>(x);
  x = dpp_add<0x141>(x);
  x = dpp_add<0x140>(x);
  return x;
}
__device__ __forceinline__ float wave_sum(float x) {
  x = row16_sum(x);
  float a = __int_as_float(__builtin_amdgcn_readlane(__float_as_int(x), 0));
  float b = __int_as_float(__builtin_amdgcn_readlane(__float_as_int(x), 16));
  float c = __int_as_float(__builtin_amdgcn_readlane(__float_as_int(x), 32));
  float d = __int_as_float(__builtin_amdgcn_readlane(__float_as_int(x), 48));
  return (a + b) + (c + d);
}
__device__ __forceinline__ s16x4 tr_read(const char LAS* p) {
  return __builtin_amdgcn_ds_read_tr16_b64_v4i16((s16x4 LAS*)p);
}

#define STAMP(kern, i)
__device__ __forceinline__ void glds16(const void* gsrc, u32 lds_dst) {
  u32 keep;
  asm volatile("s_mov_b32 %0, m0\n\ts_mov_b32 m0, %2\n\ts_nop 0\n\tglobal_load_lds_dwordx4 %1, off\n\ts_mov_b32 m0, %0"
               : "=&s"(keep) : "v"(gsrc), "s"(lds_dst) : "memory");
}
#define SCHED_FENCE() __builtin_amdgcn_sched_barrier(0)
#define VM_WAIT(n) asm volatile("s_waitcnt vmcnt(" #n ")" ::: "memory")
__device__ __forceinline__ float xor16_sum(float x) {
  const u32 v = __float_as_uint(x);
  auto r = __builtin_amdgcn_permlane16_swap(v, v, false, false);
  return __uint_as_float(r[0]) + __uint_as_float(r[1]);
}
__device__ __forceinline__ float xor32_sum(float x) {
  const u32 v = __float_as_uint(x);
  auto r = __builtin_amdgcn_permlane32_swap(v, v, false, false);
  return __uint_as_float(r[0]) + __uint_as_float(r[1]);
}
__device__ __forceinline__ float xor4_8_sum(float x) {
  x = x + __int_as_float(__builtin_amdgcn_update_dpp(0, __float_as_int(x), 0x124, 0xf, 0xf, true));
  x = x + __int_as_float(__builtin_amdgcn_update_dpp(0, __float_as_int(x), 0x128, 0xf, 0xf, true));
  return x;
}
__device__ __forceinline__ void lds_rendezvous(u32 LAS* cnt, u32 target) {
  asm volatile("s_waitcnt lgkmcnt(0)" ::: "memory");
  if ((threadIdx.x & 63) == 0) __hip_atomic_fetch_add(cnt, 1u, __ATOMIC_RELAXED, __HIP_MEMORY_SCOPE_WORKGROUP);
  while (__hip_atomic_load(cnt, __ATOMIC_RELAXED, __HIP_MEMORY_SCOPE_WORKGROUP) < target) __builtin_amdgcn_s_sleep(1);
  asm volatile("" ::: "memory");
}

typedef __amdgpu_buffer_rsrc_t rsrc_t;
__device__ __forceinline__ rsrc_t make_rsrc(const void* base, u32 bytes) {
  return __builtin_amdgcn_make_buffer_rsrc(const_cast<void*>(base), 0, bytes, 0x00020000);
}
__device__ __forceinline__ void wt_store16(rsrc_t r, u32 byte_off, u32x4 v) { __builtin_amdgcn_raw_buffer_store_b128(v, r, byte_off, 0, 16); }
__device__ __forceinline__ u32x4 pair16(u32x2 a, u32x2 b) {
  const auto r0 = __builtin_amdgcn_permlane16_swap(a[0], b[0], false, false);
  const auto r1 = __builtin_amdgcn_permlane16_swap(a[1], b[1], false, false);
  return u32x4{r0[0], r1[0], r0[1], r1[1]};
}
constexpr int CODE_LANES = 80;
struct AArgs {
  const u16 *x, *Are, *Aim, *Bre, *Bim, *Cre, *Cim, *logdt, *c, *wmod, *win, *wout;
  float* mod; u32* stats1; u16* xT; u16* Krev; u32x4* gran;
};
constexpr int A_RS = 528;
constexpr int A_TAB = 0;
constexpr int A_KC = A_TAB + 2 * 64 * A_RS;
constexpr int A_TILE = A_KC + 2 * 768 * 4;
constexpr int A_RED = A_TILE + 8 * 1088;
constexpr int A_CNT = A_RED + 4 * 32 * 4;
constexpr int A_LDS = A_CNT + 16;

struct KIdx { u16 v[2048]; };
constexpr int bf16_round_int(int l) {
  int sh = 0;
  while ((l >> sh) >= 256) ++sh;
  if (sh == 0) return l;
  const int q = l >> sh, rem = l & ((1 << sh) - 1), half = 1 << (sh - 1);
  const int up = (rem > half || (rem == half && (q & 1))) ? 1 : 0;
  return (q + up) << sh;
}
constexpr KIdx make_kidx() {
  KIdx t{};
  for (int x = 0; x < 2048; ++x) {
    const int lr = bf16_round_int(2047 - x);
    const int aa = lr >> 7, b = (lr >> 3) & 15, r = lr & 7;
    const int row = lr < 256 ? 8 * aa + r : lr < 512 ? 16 + 4 * (aa - 2) + (r >> 1) : lr < 1024 ? 24 + 2 * (aa - 4) + (r >> 2) : 32 + (aa - 8);
    t.v[x] = (u16)(row * 16 + b);
  }
  return t;
}
__device__ const KIdx g_kidx = make_kidx();

struct cpx { float r, i; };
__device__ __forceinline__ cpx cmul(cpx a, cpx b) { return cpx{a.r * b.r - a.i * b.i, a.r * b.i + a.i * b.r}; }

__global__ __launch_bounds__(768) void kA(AArgs a) {
  extern __shared__ __attribute__((aligned(16))) char smem[];
  const int wg = blockIdx.x, tid = threadIdx.x, lane = tid & 63;
  const int w = __builtin_amdgcn_readfirstlane(tid >> 6);
  STAMP(0, 0);
  if (w < 8) {
    __builtin_amdgcn_s_setprio(2);
    const int ch = w >> 2, nh = (w >> 1) & 1, part = w & 1, d = 2 * wg + ch;
    const int n = 64 * nh + lane, o = d * NS + n;
    const u16 hare = a.Are[o], haim = a.Aim[o], hbre = a.Bre[o], hbim = a.Bim[o], hcre = a.Cre[o], hcim = a.Cim[o], hdt = a.logdt[d];
    const u32x4 kix = *(const u32x4*)(g_kidx.v + 8 * (tid & 255));
    SCHED_FENCE();
    __builtin_amdgcn_s_barrier();
    SCHED_FENCE();
    const float dt = rbf(__expf(bf1(hdt)));
    const float lr = (dt * bf1(hare)) * 1.4426950408889634f, lp = (dt * bf1(haim)) * 0.15915494309189535f;
    auto cexp = [&](float l) -> cpx {
      const float e = __builtin_amdgcn_exp2f(lr * l), ph = __builtin_amdgcn_fractf(lp * l);
      return cpx{e * __builtin_amdgcn_cosf(ph), e * __builtin_amdgcn_sinf(ph)};
    };
    char* tab = smem + A_TAB + ch * (64 * A_RS) + n * 4;
    auto put = [&](int row, cpx v) { *(u32*)(tab + row * A_RS) = pk_bf16(v.r, v.i); };
    if (part == 0) {
      const cpx E1 = cexp(1.f), E128 = cexp(128.f), E512 = cexp(512.f);
      const cpx E2 = cmul(E1, E1), E3 = cmul(E2, E1), E4 = cmul(E2, E2), E5 = cmul(E4, E1), E6 = cmul(E4, E2), E7 = cmul(E4, E3);
      put(0, cpx{1.f, 0.f}); put(1, E1); put(2, E2); put(3, E3); put(4, E4); put(5, E5); put(6, E6); put(7, E7);
      put(8, E128); put(9, cmul(E128, E1)); put(10, cmul(E128, E2)); put(11, cmul(E128, E3));
      put(12, cmul(E128, E4)); put(13, cmul(E128, E5)); put(14, cmul(E128, E6)); put(15, cmul(E128, E7));
      const cpx E256 = cmul(E128, E128), E384 = cmul(E256, E128);
      put(16, E256); put(17, cmul(E256, E2)); put(18, cmul(E256, E4)); put(19, cmul(E256, E6));
      put(20, E384); put(21, cmul(E384, E2)); put(22, cmul(E384, E4)); put(23, cmul(E384, E6));
      const cpx E640 = cmul(E512, E128), E768 = cmul(E512, E256), E896 = cmul(E512, E384);
      put(24, E512); put(25, cmul(E512, E4)); put(26, E640); put(27, cmul(E640, E4));
      put(28, E768); put(29, cmul(E768, E4)); put(30, E896); put(31, cmul(E896, E4));
    } else {
      const cpx E8 = cexp(8.f), E128 = cexp(128.f), E1024 = cexp(1024.f);
      const float br = dt * bf1(hbre), bi = dt * bf1(hbim), cr = bf1(hcre), ci = bf1(hcim);
      const cpx W = cmul(cpx{cr, ci}, cpx{br, bi});
      const cpx E16 = cmul(E8, E8), E32 = cmul(E16, E16), E64 = cmul(E32, E32);
      cpx Y[8];
      Y[0] = W; Y[1] = cmul(W, E8); Y[2] = cmul(W, E16); Y[3] = cmul(Y[1], E16);
#pragma unroll
      for (int b = 0; b < 4; ++b) Y[4 + b] = cmul(Y[b], E32);
#pragma unroll
      for (int b = 0; b < 8; ++b) {
        put(48 + b, cpx{Y[b].r, -Y[b].i});
        const cpx t = cmul(Y[b], E64);
        put(56 + b, cpx{t.r, -t.i});
      }
      const cpx E256 = cmul(E128, E128), E512 = cmul(E256, E256);
      cpx P[4];
      P[0] = E1024; P[1] = cmul(E1024, E128); P[2] = cmul(E1024, E256); P[3] = cmul(P[1], E256);
#pragma unroll
      for (int b = 0; b < 4; ++b) { put(32 + b, P[b]); put(36 + b, cmul(P[b], E512)); }
      put(40, cmul(E1024, E1024));
    }
    STAMP(0, 1);
    lds_rendezvous((u32 LAS*)(smem + A_CNT), 8);
    STAMP(0, 2);
    if (w < 6) {
      const int c2 = w >= 3 ? 1 : 0, t = w - 3 * c2, i = lane & 15, g = lane >> 4;
      const char* tb = smem + A_TAB + c2 * (64 * A_RS) + 16 * g;
      const char* pa = tb + (16 * t + i) * A_RS, *pb = tb + (48 + i) * A_RS;
      f32x4 acc = {0, 0, 0, 0};
      bf16x8 Af[8], Bf[8];
#pragma unroll
      for (int s = 0; s < 8; ++s) {
        Af[s] = *(const bf16x8*)(pa + s * 64); Bf[s] = *(const bf16x8*)(pb + s * 64);
      }
#pragma unroll
      for (int s = 0; s < 8; ++s) acc = __builtin_amdgcn_mfma_f32_16x16x32_bf16(Af[s], Bf[s], acc, 0, 0, 0);
      float* Kc = (float*)(smem + A_KC) + c2 * 768 + t * 256;
#pragma unroll
      for (int r = 0; r < 4; ++r) Kc[(4 * g + r) * 16 + i] = acc[r];
    }
    STAMP(0, 3);
    lds_rendezvous((u32 LAS*)(smem + A_CNT + 4), 8);
    STAMP(0, 4);
    const float* Kc = (const float*)(smem + A_KC) + ch * 768;
    float kv[8];
#pragma unroll
    for (int j = 0; j < 4; ++j) { kv[2 * j] = Kc[kix[j] & 0xffffu]; kv[2 * j + 1] = Kc[kix[j] >> 16]; }
    *(u32x4*)(a.Krev + (size_t)d * L + 8 * (tid & 255)) =
        u32x4{pk_bf16(kv[0], kv[1]), pk_bf16(kv[2], kv[3]), pk_bf16(kv[4], kv[5]), pk_bf16(kv[6], kv[7])};
    STAMP(0, 5);
  } else {
    const int tw = w - 8;
    const int t0 = 8 * ((wg & 7) * 32 + (wg >> 3));
    u32x4 xr[2];
#pragma unroll
    for (int j = 0; j < 2; ++j) xr[j] = *(const u32x4*)(a.x + (size_t)(t0 + 2 * tw + j) * D + 8 * lane);
    const bool doM = wg < 96;
    u32x4 wm[8]; u16 cv[8];
    const int rr = lane >> 2, q = lane & 3;
    if (doM) {
#pragma unroll
      for (int p = 0; p < 8; ++p) cv[p] = a.c[p * 64 + tw * 16 + rr];
#pragma unroll
      for (int p = 0; p < 8; ++p) wm[p] = *(const u32x4*)(a.wmod + (size_t)(p * 64 + tw * 16 + rr) * 3072 + 32 * wg + 8 * q);
    }
    u32x4 pf[3];
    {
      const int st = tid - 512;
      pf[0] = *(const u32x4*)(a.win + (size_t)wg * 3584 + st * 8);
      pf[1] = *(const u32x4*)(a.win + (size_t)wg * 3584 + (st < 192 ? 2048 + st * 8 : 0));
      pf[2] = *(const u32x4*)(a.wout + (size_t)wg * 1792 + (st < 224 ? st * 8 : 0));
    }
    u32x4 cf;
    {
      u64 base;
      asm volatile("s_getpc_b64 s[40:41]\n\ts_add_u32 s40, s40, _Z2kB5BArgs@rel32@lo+4\n\ts_addc_u32 s41, s41, _Z2kB5BArgs@rel32@hi+12\n\ts_mov_b64 %0, s[40:41]"
                   : "=s"(base) : : "s40", "s41", "scc");
      const int slot = (wg >> 3) * CODE_LANES + (tid - 512);
      cf = *(const u32x4*)((const char*)base + (size_t)((tid - 512) < CODE_LANES ? slot : 0) * 16);
    }
    char* tile = smem + A_TILE;
    if (tw == 0 && lane < 4) *(u32*)(smem + A_CNT + 4 * lane) = 0;
    asm volatile("s_waitcnt lgkmcnt(0)" ::: "memory");
    SCHED_FENCE();
    __builtin_amdgcn_s_barrier();
    SCHED_FENCE();
#pragma unroll
    for (int j = 0; j < 2; ++j) *(u32x4*)(tile + (2 * tw + j) * 1088 + 16 * lane) = xr[j];
    STAMP(0, 1);
    lds_rendezvous((u32 LAS*)(smem + A_CNT + 8), 4);
    STAMP(0, 2);
#pragma unroll
    for (int ps = 0; ps < 2; ++ps) {
      const int d0 = 128 * tw + 64 * ps + 16 * (lane >> 4);
      const char LAS* src = (const char LAS*)tile + ((lane & 15) >> 2) * 1088 + 2 * (d0 + 4 * (lane & 3));
      const s16x4 lo = tr_read(src), hi = tr_read(src + 4 * 1088);
      u32x2 a0 = __builtin_bit_cast(u32x2, lo), a1 = __builtin_bit_cast(u32x2, hi);
      *(u32x4*)(a.xT + (size_t)(d0 + (lane & 15)) * L + t0) = u32x4{a0[0], a0[1], a1[0], a1[1]};
    }
    if (tw == 0) a.gran[wg * 64 + lane] = u32x4{0, 0, 0, 0};
    if (tw == 1 && wg < 8) a.gran[(256 + wg) * 64 + lane] = u32x4{0, 0, 0, 0};
    {
      float v[2][8], s[2];
#pragma unroll
      for (int j = 0; j < 2; ++j) {
#pragma unroll
        for (int k = 0; k < 4; ++k) { v[j][2 * k] = bf_lo(xr[j][k]); v[j][2 * k + 1] = bf_hi(xr[j][k]); }
        s[j] = ((v[j][0] + v[j][1]) + (v[j][2] + v[j][3])) + ((v[j][4] + v[j][5]) + (v[j][6] + v[j][7]));
      }
      float mean[2], q2[2];
#pragma unroll
      for (int j = 0; j < 2; ++j) mean[j] = wave_sum(s[j]) * (1.f / D);
#pragma unroll
      for (int j = 0; j < 2; ++j) {
        q2[j] = 0;
#pragma unroll
        for (int k = 0; k < 8; ++k) { const float dv = v[j][k] - mean[j]; q2[j] += dv * dv; }
      }
#pragma unroll
      for (int j = 0; j < 2; ++j) {
        const float var = wave_sum(q2[j]) * (1.f / D);
        const float sd = rbf(__builtin_amdgcn_sqrtf(rbf(rbf(var) + 1e-5f)));
        if (lane == 0) *(f32x2*)(a.stats1 + 2 * (t0 + 2 * tw + j)) = f32x2{rbf(mean[j]), __builtin_amdgcn_rcpf(sd)};
      }
    }
    float* red = (float*)(smem + A_RED);
    if (doM) {
      float acc[8] = {0, 0, 0, 0, 0, 0, 0, 0};
#pragma unroll
      for (int p = 0; p < 8; ++p) {
        const float cx = bf1(cv[p]);
        const float s = rbf(cx * __builtin_amdgcn_rcpf(1.f + __expf(-cx)));
#pragma unroll
        for (int k = 0; k < 4; ++k) { acc[2 * k] += s * bf_lo(wm[p][k]); acc[2 * k + 1] += s * bf_hi(wm[p][k]); }
      }
#pragma unroll
      for (int k = 0; k < 8; ++k) {
        float v = acc[k];
        v = xor32_sum(xor16_sum(xor4_8_sum(v)));
        if (rr == 0) red[tw * 32 + 8 * q + k] = v;
      }
      STAMP(0, 3);
      lds_rendezvous((u32 LAS*)(smem + A_CNT + 12), 4);
      STAMP(0, 4);
      if (tw == 0 && lane < 32)
        a.mod[32 * wg + lane] = rbf((red[lane] + red[32 + lane]) + (red[64 + lane] + red[96 + lane]));
    }
#pragma unroll
    for (int k = 0; k < 3; ++k) asm volatile("" ::"v"(pf[k]));
    asm volatile("" ::"v"(cf));
    STAMP(0, 5);
  }
}
struct BArgs {
  const u16 *xT, *Krev, *Dskip, *win, *wout, *wsp;
  const u32* stats1; const float* mod;
  u16 *x1T, *Wt, *Wot; float* uv;
};
constexpr int B_H = 0;
constexpr int B_HS = (256 + 2048) * 2;
constexpr int B_KC = B_H + 2 * B_HS;
constexpr int B_KS = 4160;
constexpr int B_PART = B_KC + 2 * 4 * B_KS;
constexpr int B_RED = B_PART + 2 * 4 * 8 * 1280;
constexpr int B_CNT = B_RED + 4 * 16 * 4;
constexpr int B_LDS = B_CNT + 32;

__global__ __launch_bounds__(768) void kB(BArgs a) {
  extern __shared__ __attribute__((aligned(16))) char smem[];
  const int wg = blockIdx.x, tid = threadIdx.x, lane = tid & 63;
  const int w = __builtin_amdgcn_readfirstlane(tid >> 6);
  STAMP(1, 0);
  if (w < 8) {
    __builtin_amdgcn_s_setprio(2);
    const int ch = w >> 2, q = w & 3, d = 2 * wg + ch, tj = tid & 255;
    const u32x4 xv = *(const u32x4*)(a.xT + (size_t)d * L + 8 * tj);
    f32x4 st[4];
#pragma unroll
    for (int k = 0; k < 4; ++k) st[k] = *(const f32x4*)(a.stats1 + 16 * tj + 4 * k);
    const u32x4 k0 = *(const u32x4*)(a.Krev + (size_t)d * L + 8 * tj);
    const u32x4 k1r = *(const u32x4*)(a.Krev + (size_t)d * L + 8 * (tj < 255 ? tj + 1 : tj));
    const float sa = a.mod[d], Ac = rbf(1.f + a.mod[512 + d]), ga = a.mod[1024 + d], dsk = bf1(a.Dskip[d]);
    SCHED_FENCE();
    __builtin_amdgcn_s_barrier();
    SCHED_FENCE();
    char* hb = smem + B_H + ch * B_HS;
    char* kc = smem + B_KC + ch * 4 * B_KS;
    if (tj < 32) *(u32x4*)(hb + 16 * tj) = u32x4{0, 0, 0, 0};
    if (tj < 16) *(u32x4*)(kc + (tj >> 2) * B_KS + 16 * (256 + (tj & 3))) = u32x4{0, 0, 0, 0};
    {
      const u32 z = tj < 255 ? 0xffffffffu : 0u;
      const u32 wd[8] = {k0[0], k0[1], k0[2], k0[3], k1r[0] & z, k1r[1] & z, 0, 0};
      u32 od[5];
#pragma unroll
      for (int k = 0; k < 5; ++k) od[k] = __builtin_amdgcn_alignbit(wd[k + 1], wd[k], 16);
      *(u32x4*)(kc + 0 * B_KS + 16 * tj) = u32x4{wd[0], wd[1], wd[2], wd[3]};
      *(u32x4*)(kc + 1 * B_KS + 16 * tj) = u32x4{od[0], od[1], od[2], od[3]};
      *(u32x4*)(kc + 2 * B_KS + 16 * tj) = u32x4{wd[1], wd[2], wd[3], wd[4]};
      *(u32x4*)(kc + 3 * B_KS + 16 * tj) = u32x4{od[1], od[2], od[3], od[4]};
    }
    u32x4 hv;
    {
      float hh[8];
#pragma unroll
      for (int k = 0; k < 8; ++k) {
        const float m = st[k >> 1][2 * (k & 1)], ri = st[k >> 1][2 * (k & 1) + 1];
        const float xx = (k & 1) ? bf_hi(xv[k >> 1]) : bf_lo(xv[k >> 1]);
        hh[k] = (xx - m) * (ri * Ac) + sa;
      }
      hv = u32x4{pk_bf16(hh[0], hh[1]), pk_bf16(hh[2], hh[3]), pk_bf16(hh[4], hh[5]), pk_bf16(hh[6], hh[7])};
    }
    *(u32x4*)(hb + 512 + 16 * tj) = hv;
    STAMP(1, 1);
    lds_rendezvous((u32 LAS*)(smem + B_CNT + 12 + 4 * 0) + ch * 2, 4);
    STAMP(1, 2);
    const int c = lane & 15, g = lane >> 4;
    const char* bbase = hb + 2 * (256 + 16 * c - 16 * (g >> 1) + 8 * (g & 1) - 32 * q) - 256;
    const int X = 2047 - 32 * q - 16 * (g >> 1) + 8 * (g & 1) - c;
    const char* abase = kc + (X & 3) * B_KS + 2 * (X & ~3) - 256 * 15;
    bf16x8 Bf[16], Af[16];
    auto lda = [&](int u) {
      const u32x2 lo = *(const u32x2*)(abase + 256 * (15 - u)), hi = *(const u32x2*)(abase + 256 * (15 - u) + 8);
      Af[u] = __builtin_bit_cast(bf16x8, u32x4{lo[0], lo[1], hi[0], hi[1]});
    };
    lda(0);
#pragma unroll
    for (int v = 1; v < 16; v += 2) Bf[v] = *(const bf16x8*)(bbase + 256 * v);
    lda(1);
#pragma unroll
    for (int v = 0; v < 16; v += 2) Bf[v] = *(const bf16x8*)(bbase + 256 * v);
    lda(2); lda(3);
    SCHED_FENCE();
    f32x4 acc[8];
#pragma unroll
    for (int i = 0; i < 8; ++i) acc[i] = f32x4{0, 0, 0, 0};
#pragma unroll
    for (int u = 0; u < 16; ++u) {
      if (u + 4 < 16) lda(u + 4);
      SCHED_FENCE();
#pragma unroll
      for (int i = u >> 1; i < 8; ++i) acc[i] = __builtin_amdgcn_mfma_f32_16x16x32_bf16(Af[u], Bf[2 * i - u + 1], acc[i], 0, 0, 0);
      SCHED_FENCE();
    }
    STAMP(1, 3);
    char* part = smem + B_PART + ch * (4 * 8 * 1280);
#pragma unroll
    for (int i = 0; i < 8; ++i) *(f32x4*)(part + (q * 8 + i) * 1280 + 16 * (20 * g + c)) = acc[i];
    lds_rendezvous((u32 LAS*)(smem + B_CNT + 4 * ch), 4);
    STAMP(1, 4);
    {
      const int i = tj >> 5, j5 = tj & 31, cc = j5 >> 1;
      float y[8];
#pragma unroll
      for (int hh = 0; hh < 2; ++hh) {
        const char* p = part + i * 1280 + 16 * (20 * (2 * (j5 & 1) + hh) + cc);
        const f32x4 p0 = *(const f32x4*)(p), p1 = *(const f32x4*)(p + 8 * 1280), p2 = *(const f32x4*)(p + 16 * 1280), p3 = *(const f32x4*)(p + 24 * 1280);
#pragma unroll
        for (int r = 0; r < 4; ++r) y[4 * hh + r] = (p0[r] + p1[r]) + (p2[r] + p3[r]);
      }
      float o[8];
#pragma unroll
      for (int k = 0; k < 8; ++k) {
        const float hx = (k & 1) ? bf_hi(hv[k >> 1]) : bf_lo(hv[k >> 1]);
        const float xx = (k & 1) ? bf_hi(xv[k >> 1]) : bf_lo(xv[k >> 1]);
        o[k] = xx + ga * (y[k] + hx * dsk);
      }
      wt_store16(make_rsrc(a.x1T, D * L * 2), (u32)(d * L + 8 * tj) * 2, u32x4{pk_bf16(o[0], o[1]), pk_bf16(o[2], o[3]), pk_bf16(o[4], o[5]), pk_bf16(o[6], o[7])});
    }
    STAMP(1, 5);
  } else {
    const int st = tid - 512, sw = w - 8;
    if (st < 8) *(u32*)(smem + B_CNT + 4 * st) = 0;
    asm volatile("s_waitcnt lgkmcnt(0)" ::: "memory");
    __builtin_amdgcn_s_barrier();
    SCHED_FENCE();
    float* red = (float*)(smem + B_RED);
    const bool act = wg < 224;
    const int f0 = 8 * ((wg & 7) * 28 + (wg >> 3));
    const int d8 = st >> 2, fp = st & 3;
    const int f8 = wg >> 1, dp = (wg & 1) * 128 + (st & 127);
    u32 wv[8], ov[8];
    f32x4 cb0, cb1, sb0, sb1;
    if (act) {
      cb0 = *(const f32x4*)(a.mod + 2048 + 8 * d8); cb1 = *(const f32x4*)(a.mod + 2048 + 8 * d8 + 4);
      sb0 = *(const f32x4*)(a.mod + 1536 + 8 * d8); sb1 = *(const f32x4*)(a.mod + 1536 + 8 * d8 + 4);
#pragma unroll
      for (int j = 0; j < 8; ++j) wv[j] = *(const u32*)(a.win + (size_t)(8 * d8 + j) * F + f0 + 2 * fp);
      if (st < 128) {
#pragma unroll
        for (int j = 0; j < 8; ++j) ov[j] = *(const u32*)(a.wout + (size_t)(8 * f8 + j) * D + 2 * dp);
      }
    }
    u32x4 pf[8];
    {
      const u16* p = a.wsp + (size_t)wg * 16384 + st * 8;
#pragma unroll
      for (int j = 0; j < 8; ++j) pf[j] = *(const u32x4*)(p + j * 2048);
    }
    SCHED_FENCE();
    if (act) {
      float lo[8], hi[8], u0 = 0, u1 = 0, v0 = 0, v1 = 0;
#pragma unroll
      for (int j = 0; j < 8; ++j) {
        const float Ac = rbf(1.f + (j < 4 ? cb0[j] : cb1[j - 4])), sb = j < 4 ? sb0[j] : sb1[j - 4];
        const float wl = bf_lo(wv[j]), wh = bf_hi(wv[j]);
        lo[j] = rbf(Ac * wl); hi[j] = rbf(Ac * wh);
        u0 += lo[j]; u1 += hi[j]; v0 += sb * wl; v1 += sb * wh;
      }
      const int f = f0 + 2 * fp;
      const rsrc_t rw = make_rsrc(a.Wt, D * F * 2);
      const u32 dst = (u32)((((f >> 4) * 64 + d8) * 16 + (f & 15)) * 16);
      wt_store16(rw, dst, u32x4{pk_bf16(lo[0], lo[1]), pk_bf16(lo[2], lo[3]), pk_bf16(lo[4], lo[5]), pk_bf16(lo[6], lo[7])});
      wt_store16(rw, dst + 16, u32x4{pk_bf16(hi[0], hi[1]), pk_bf16(hi[2], hi[3]), pk_bf16(hi[4], hi[5]), pk_bf16(hi[6], hi[7])});
      float rv[4] = {u0, u1, v0, v1};
#pragma unroll
      for (int k = 0; k < 4; ++k) {
        const float v = xor32_sum(xor16_sum(xor4_8_sum(rv[k])));
        if (lane < 4) red[sw * 16 + lane * 4 + k] = v;
      }
      if (st < 128) {
        const int dd = 2 * dp;
        const rsrc_t ro = make_rsrc(a.Wot, FH * D * 2);
        const u32 od = (u32)((((dd >> 4) * 112 + f8) * 16 + (dd & 15)) * 16);
        u32x4 e, o;
#pragma unroll
        for (int k = 0; k < 4; ++k) {
          e[k] = (ov[2 * k] & 0xffffu) | (ov[2 * k + 1] << 16);
          o[k] = (ov[2 * k] >> 16) | (ov[2 * k + 1] & 0xffff0000u);
        }
        wt_store16(ro, od, e); wt_store16(ro, od + 16, o);
      }
      lds_rendezvous((u32 LAS*)(smem + B_CNT + 8), 4);
      if (st < 16) {
        const float s = (red[st] + red[16 + st]) + (red[32 + st] + red[48 + st]);
        const int fq = st >> 2, k = st & 3;
        a.uv[(k >> 1) * F + f0 + 2 * fq + (k & 1)] = s;
      }
    }
    STAMP(1, 1);
#pragma unroll
    for (int j = 0; j < 8; ++j) asm volatile("" ::"v"(pf[j]));
    STAMP(1, 5);
  }
}
struct FArgs {
  const u16 *x1T, *Wt, *wsp, *bsp, *Wot; const float *uv, *mod;
  u16 *z2t, *Pt, *out; u64* gran; u32* flags;
};
constexpr int F_X = 0;
constexpr int F_ST = 65536;
constexpr int F_MR = F_ST + 512;
constexpr int F_PS = F_MR + 8 * 512;
constexpr int F_FS = F_PS + 7 * 64 * 8;
constexpr int F_ZS = F_FS + 512;
constexpr int F_RS = 232;
constexpr int F_A0 = 90112;
constexpr int F_LDS = F_A0 + 8 * 8192;
constexpr int F_ETS = 1152;
constexpr int F_EX = 112 * 1024;
constexpr int F_BD = 3;
static_assert(F_ZS + 64 * F_RS <= F_A0 && F_EX + 64 * 132 <= F_LDS, "LDS map");

__device__ __forceinline__ float gelu_tanh(float z) {
  const float k0 = -2.f * 0.7978845608028654f * 1.4426950408889634f, k1 = k0 * 0.044715f;
  const float e = __builtin_amdgcn_exp2f(z * (k0 + k1 * (z * z)));
  return z * __builtin_amdgcn_rcpf(1.f + e);
}
__device__ __forceinline__ float pick4(f32x4 v, int r) { return r == 0 ? v[0] : r == 1 ? v[1] : r == 2 ? v[2] : v[3]; }
template <int N> __device__ __forceinline__ void vm_wait_c() { asm volatile("s_waitcnt vmcnt(%0)" ::"i"(N > 63 ? 63 : N) : "memory"); }
__device__ __forceinline__ u32 f_slot(int kq, int sg) { return (u32)(((sg & 2) ? 0 : F_A0) + (kq * 2 + (sg & 1)) * 8192); }
__device__ __forceinline__ bf16x8 ld_dev16(rsrc_t r, u32 off) { return __builtin_bit_cast(bf16x8, __builtin_amdgcn_raw_buffer_load_b128(r, off, 0, 16)); }
__device__ __forceinline__ void f_lda(const u16* const (&pa)[2], u32 lds0, int kq, int nh, int sg) {
#pragma unroll
  for (int i = 0; i < 4; ++i) glds16(pa[i & 1] + (size_t)(i >> 1) * 16 * L + 64 * sg, lds0 + f_slot(kq, sg) + (4 * nh + i) * 1024);
}
__device__ __forceinline__ void wait_flags(const u32* p, int n) {
  const int lane = threadIdx.x & 63;
  for (u32 spin = 0; spin < (1u << 22); ++spin) {
    const u32 v = __hip_atomic_load((GAS u32*)p + (lane < n ? lane : 0), __ATOMIC_RELAXED, __HIP_MEMORY_SCOPE_AGENT);
    if (__all(v == 1u)) break;
    __builtin_amdgcn_s_sleep(1);
  }
}

template <int ROLE>
__device__ __forceinline__ void f_phase_c(const FArgs& a, char* smem, const int w, const int pi, const int j, const u16* const (&pa)[2]) {
  const int tid = threadIdx.x, lane = tid & 63;
  const int c = lane & 15, g = lane >> 4;
  const u32 lds0 = (u32)(size_t)(const char LAS*)smem;
  const int nt2 = 56 + 7 * j + (w < 7 ? w : 0), nt1 = 7 * j + (w > 0 ? w - 1 : 0);
  const u16* pw2 = a.Wt + (size_t)nt2 * 64 * 128 + lane * 8;
  const u16* pw1 = a.Wt + (size_t)nt1 * 64 * 128 + lane * 8;
  const int q4 = (lane & 15) >> 2, p4 = lane & 3;
  const int drow = 8 * g + q4;
  const int fz0 = (((drow >> 1) & 1) | ((drow >> 2) & 2)) << 1;
  const char LAS* xb = (const char LAS*)smem + F_X + drow * 128 + 8 * (p4 & 1);
  const char LAS* xt[4];
  int tb[4];
#pragma unroll
  for (int s = 0; s < 4; ++s) { tb[s] = (s + w) & 3; xt[s] = xb + 16 * ((2 * tb[s] + (p4 >> 1)) ^ fz0); }
  auto xfrag = [&](int s, int ks) -> bf16x8 {
    const char LAS* p = xt[s] + ks * 4096;
    const s16x4 lo = tr_read(p), hi = tr_read(p + 512);
    const u32x2 a0 = __builtin_bit_cast(u32x2, lo), a1 = __builtin_bit_cast(u32x2, hi);
    return __builtin_bit_cast(bf16x8, u32x4{a0[0], a0[1], a1[0], a1[1]});
  };
  f32x4 acc[4];
#pragma unroll
  for (int s = 0; s < 4; ++s) acc[s] = f32x4{0, 0, 0, 0};
  bf16x8 W1[16];
  f32x4 u2, v2, u1, v1;
  float* xs2 = (float*)(smem + F_ST);
  if constexpr (ROLE < 2) {
    bf16x8 W2[16];
#pragma unroll
    for (int ks = 0; ks < 16; ++ks) W2[ks] = *(const bf16x8*)(pw2 + 4 * ks * 128);
    u2 = *(const f32x4*)(a.uv + 16 * nt2 + 4 * g); v2 = *(const f32x4*)(a.uv + F + 16 * nt2 + 4 * g);
    SCHED_FENCE();
    VM_WAIT(18);
    __builtin_amdgcn_s_barrier();
    SCHED_FENCE();
    STAMP(2, 1);
    f32x4 sq = {0, 0, 0, 0};
    bf16x8 X[2][4];
#pragma unroll
    for (int s = 0; s < 4; ++s) X[0][s] = xfrag(s, 0);
#pragma unroll
    for (int ks = 0; ks < 16; ++ks) {
      if (ks + 1 < 16) {
#pragma unroll
        for (int s = 0; s < 4; ++s) X[(ks + 1) & 1][s] = xfrag(s, ks + 1);
      }
      SCHED_FENCE();
#pragma unroll
      for (int s = 0; s < 4; ++s) acc[s] = __builtin_amdgcn_mfma_f32_16x16x32_bf16(W2[ks], X[ks & 1][s], acc[s], 0, 0, 0);
      sq = __builtin_amdgcn_mfma_f32_16x16x32_bf16(X[ks & 1][0], X[ks & 1][0], sq, 0, 0, 0);
      SCHED_FENCE();
    }
    if constexpr (ROLE == 1) {
#pragma unroll
      for (int ks = 0; ks < 16; ++ks) W1[ks] = *(const bf16x8*)(pw1 + 4 * ks * 128);
      u1 = *(const f32x4*)(a.uv + 16 * nt1 + 4 * g); v1 = *(const f32x4*)(a.uv + F + 16 * nt1 + 4 * g);
      SCHED_FENCE();
    }
    if (w < 4 && (c >> 2) == g) xs2[64 + 16 * w + c] = pick4(sq, c & 3);
  } else {
    VM_WAIT(0);
    __builtin_amdgcn_s_barrier();
    SCHED_FENCE();
    STAMP(2, 1);
    const u32x4 ones = {0x3f803f80u, 0x3f803f80u, 0x3f803f80u, 0x3f803f80u};
    bf16x8 X[2][4];
#pragma unroll
    for (int s = 0; s < 4; ++s) X[0][s] = xfrag(s, 0);
#pragma unroll
    for (int ks = 0; ks < 16; ++ks) {
      if (ks + 1 < 16) {
#pragma unroll
        for (int s = 0; s < 4; ++s) X[(ks + 1) & 1][s] = xfrag(s, ks + 1);
      }
      SCHED_FENCE();
#pragma unroll
      for (int s = 0; s < 4; ++s) acc[s] = __builtin_amdgcn_mfma_f32_16x16x32_bf16(__builtin_bit_cast(bf16x8, ones), X[ks & 1][s], acc[s], 0, 0, 0);
      SCHED_FENCE();
    }
#pragma unroll
    for (int ks = 0; ks < 16; ++ks) W1[ks] = *(const bf16x8*)(pw1 + 4 * ks * 128);
    u1 = *(const f32x4*)(a.uv + 16 * nt1 + 4 * g); v1 = *(const f32x4*)(a.uv + F + 16 * nt1 + 4 * g);
    SCHED_FENCE();
    if (g == 0) {
#pragma unroll
      for (int s = 0; s < 4; ++s) xs2[16 * tb[s] + c] = acc[s][0];
    }
  }
  f_lda(pa, lds0, w & 3, w >> 2, 0);
  f_lda(pa, lds0, w & 3, w >> 2, 1);
  STAMP(2, 2);
  __syncthreads();
  STAMP(2, 3);
  float mu[4], ri[4];
  {
    const float mean = xs2[lane] * (1.f / D);
    const float var = fmaxf(xs2[64 + lane] * (1.f / D) - mean * mean, 0.f);
    f32x2* mr = (f32x2*)(smem + F_MR + w * 512);
    mr[lane] = f32x2{rbf(mean), __builtin_amdgcn_rcpf(rbf(__builtin_amdgcn_sqrtf(rbf(rbf(var) + 1e-5f))))};
#pragma unroll
    for (int s = 0; s < 4; ++s) { const f32x2 v = mr[16 * tb[s] + c]; mu[s] = v[0]; ri[s] = v[1]; }
  }
  u32x2 z2p[4];
  if constexpr (ROLE < 2) {
    float* ps = (float*)(smem + F_PS) + w * 128;
    const s16x4 ones4 = {0x3f80, 0x3f80, 0x3f80, 0x3f80};
#pragma unroll
    for (int s = 0; s < 4; ++s) {
      float z[4];
#pragma unroll
      for (int r = 0; r < 4; ++r) z[r] = gelu_tanh(ri[s] * (acc[s][r] - mu[s] * u2[r]) + v2[r]);
      z2p[s] = u32x2{pk_bf16(z[0], z[1]), pk_bf16(z[2], z[3])};
      const s16x4 zz = __builtin_bit_cast(s16x4, z2p[s]);
      const f32x4 zero = {0, 0, 0, 0};
      const f32x4 s1 = __builtin_amdgcn_mfma_f32_16x16x16bf16_1k(ones4, zz, zero, 0, 0, 0);
      const f32x4 s2 = __builtin_amdgcn_mfma_f32_16x16x16bf16_1k(zz, zz, zero, 0, 0, 0);
      if ((c >> 2) == g) *(f32x2*)(ps + (16 * tb[s] + c) * 2) = f32x2{s1[0], pick4(s2, c & 3)};
    }
  }
  STAMP(2, 4);
  __syncthreads();
  STAMP(2, 5);
  if constexpr (ROLE == 0) {
    const float* ps = (const float*)(smem + F_PS);
    float s1 = 0, s2 = 0;
#pragma unroll
    for (int k = 0; k < 7; ++k) { const f32x2 v = *(const f32x2*)(ps + k * 128 + lane * 2); s1 += v[0]; s2 += v[1]; }
    GAS u64* gp = (GAS u64*)a.gran + (size_t)pi * 8 * 128;
    __hip_atomic_store(gp + (j * 64 + lane) * 2, (1ull << 32) | __float_as_uint(s1), __ATOMIC_RELAXED, __HIP_MEMORY_SCOPE_AGENT);
    __hip_atomic_store(gp + (j * 64 + lane) * 2 + 1, (1ull << 32) | __float_as_uint(s2), __ATOMIC_RELAXED, __HIP_MEMORY_SCOPE_AGENT);
    float t1 = 0, t2 = 0;
    for (u32 spin = 0; spin < (1u << 22); ++spin) {
      bool ok = true; t1 = 0; t2 = 0;
#pragma unroll
      for (int k = 0; k < 8; ++k) {
        const u64 x0 = __hip_atomic_load(gp + (k * 64 + lane) * 2, __ATOMIC_RELAXED, __HIP_MEMORY_SCOPE_AGENT);
        const u64 x1 = __hip_atomic_load(gp + (k * 64 + lane) * 2 + 1, __ATOMIC_RELAXED, __HIP_MEMORY_SCOPE_AGENT);
        ok = ok && (x0 >> 32) == 1 && (x1 >> 32) == 1;
        t1 += __uint_as_float((u32)x0); t2 += __uint_as_float((u32)x1);
      }
      if (__all(ok)) break;
      __builtin_amdgcn_s_sleep(2);
    }
    const float mean = t1 * (1.f / FH);
    const float var = fmaxf(t2 * (1.f / FH) - mean * mean, 0.f);
    *(f32x2*)(smem + F_FS + lane * 8) = f32x2{rbf(mean), __builtin_amdgcn_rcpf(rbf(__builtin_amdgcn_sqrtf(rbf(rbf(var) + 1e-5f))))};
  } else {
#pragma unroll
    for (int s = 0; s < 4; ++s) acc[s] = f32x4{0, 0, 0, 0};
    bf16x8 X[2][4];
#pragma unroll
    for (int s = 0; s < 4; ++s) X[0][s] = xfrag(s, 0);
#pragma unroll
    for (int ks = 0; ks < 16; ++ks) {
      if (ks + 1 < 16) {
#pragma unroll
        for (int s = 0; s < 4; ++s) X[(ks + 1) & 1][s] = xfrag(s, ks + 1);
      }
      SCHED_FENCE();
#pragma unroll
      for (int s = 0; s < 4; ++s) acc[s] = __builtin_amdgcn_mfma_f32_16x16x32_bf16(W1[ks], X[ks & 1][s], acc[s], 0, 0, 0);
      SCHED_FENCE();
    }
  }
  STAMP(2, 6);
  __syncthreads();
  STAMP(2, 7);
  if constexpr (ROLE < 2) {
#pragma unroll
    for (int s = 0; s < 4; ++s) {
      const f32x2 fs = *(const f32x2*)(smem + F_FS + (16 * tb[s] + c) * 8);
      const float a0 = (bf_lo(z2p[s][0]) - fs[0]) * fs[1], a1 = (bf_hi(z2p[s][0]) - fs[0]) * fs[1];
      const float a2 = (bf_lo(z2p[s][1]) - fs[0]) * fs[1], a3 = (bf_hi(z2p[s][1]) - fs[0]) * fs[1];
      *(u32x2*)(smem + F_ZS + (16 * tb[s] + c) * F_RS + (16 * w + 4 * g) * 2) = u32x2{pk_bf16(a0, a1), pk_bf16(a2, a3)};
    }
#pragma unroll
    for (int kk = 0; kk < 2; ++kk) {
      const char LAS* p = (const char LAS*)smem + F_ZS + (32 * kk + 8 * g + q4) * F_RS + (16 * w + 4 * p4) * 2;
      const s16x4 lo = tr_read(p), hi = tr_read(p + 4 * F_RS);
      const u32x2 a0 = __builtin_bit_cast(u32x2, lo), a1 = __builtin_bit_cast(u32x2, hi);
      wt_store16(make_rsrc(a.z2t, L * FH * 2), (u32)((((7 * j + w) * 256 + 8 * pi + 4 * kk) * 64 + lane * 4) * 4), u32x4{a0[0], a0[1], a1[0], a1[1]});
    }
  }
  VM_WAIT(0);
  __syncthreads();
  if (tid == 0) __hip_atomic_store((GAS u32*)a.flags + j * 32 + pi, 1u, __ATOMIC_RELAXED, __HIP_MEMORY_SCOPE_AGENT);
  STAMP(2, 8);
  f_lda(pa, lds0, w & 3, w >> 2, 2);
  f_lda(pa, lds0, w & 3, w >> 2, 3);
  if constexpr (ROLE > 0) {
#pragma unroll
    for (int s = 0; s < 4; ++s) {
      float z[4];
#pragma unroll
      for (int r = 0; r < 4; ++r) z[r] = gelu_tanh(ri[s] * (acc[s][r] - mu[s] * u1[r]) + v1[r]);
      *(u32x2*)(smem + F_ZS + (16 * tb[s] + c) * F_RS + (16 * (w - 1) + 4 * g) * 2) = u32x2{pk_bf16(z[0], z[1]), pk_bf16(z[2], z[3])};
    }
  } else {
    wait_flags(a.flags + j * 32, 32);
  }
  STAMP(2, 9);
}

template <int NT> constexpr int fd_glds(int sg) { return (sg >= 1 && sg + 3 < 8) ? 4 : 0; }
template <int NT> constexpr int fd_bld(int sg) { return sg < 0 ? 0 : (2 * sg + F_BD < 16 ? NT : 0) + (2 * sg + 1 + F_BD < 16 ? NT : 0); }
template <int NT> constexpr int fd_ops(int sg) { return fd_glds<NT>(sg) + fd_bld<NT>(sg); }

template <int NT, int SG>
__device__ __forceinline__ void fd_stage(f32x4 (&acc)[4][NT], bf16x8 (&Bf)[4][NT], const rsrc_t rb, u32 boff, const u16* const (&pa)[2],
                                         u32 lds0, const char* const (&ap)[2][2][4], int kq, int nh) {
  if constexpr (SG == 2) vm_wait_c<F_BD * NT + fd_ops<NT>(0) + fd_ops<NT>(1)>();
  else if constexpr (SG >= 4) vm_wait_c<fd_bld<NT>(SG - 3) + fd_ops<NT>(SG - 2) + fd_ops<NT>(SG - 1)>();
  if constexpr (SG > 0) __builtin_amdgcn_s_barrier();
  if constexpr (fd_glds<NT>(SG) > 0) f_lda(pa, lds0, kq, nh, SG + 3);
  bf16x8 Af[2][4];
#pragma unroll
  for (int kk = 0; kk < 2; ++kk)
#pragma unroll
    for (int m = 0; m < 4; ++m) Af[kk][m] = *(const bf16x8*)(ap[(SG >> 1) & 1][kk][m] + (SG & 1) * 8192);
  SCHED_FENCE();
#pragma unroll
  for (int kk = 0; kk < 2; ++kk) {
    constexpr int ks0 = 2 * SG;
#pragma unroll
    for (int n = 0; n < NT; ++n) {
#pragma unroll
      for (int m = 0; m < 4; ++m) acc[m][n] = __builtin_amdgcn_mfma_f32_16x16x32_bf16(Bf[(ks0 + kk) & 3][n], Af[kk][m], acc[m][n], 0, 0, 0);
      if (ks0 + kk + F_BD < 16) Bf[(ks0 + kk + F_BD) & 3][n] = ld_dev16(rb, boff + (u32)((n * 256 + 4 * (ks0 + kk + F_BD)) * 256));
      SCHED_FENCE();
    }
  }
}

template <int NT>
__device__ __forceinline__ void f_phase_d(const FArgs& a, char* smem, const int w, const int rt, const int ct, const u16* const (&pa)[2]) {
  const int tid = threadIdx.x, lane = tid & 63;
  const int kq = w & 3, nh = w >> 2, n0 = nh * 4;
  const int c = lane & 15, g = lane >> 4;
  const u32 lds0 = (u32)(size_t)(const char LAS*)smem;
  const rsrc_t rb = make_rsrc(a.z2t, L * FH * 2);
  const u32 boff = (u32)((((7 * ct + n0) * 256 + 64 * kq) * 64 + lane * 4) * 4);
  bf16x8 Bf[4][NT];
#pragma unroll
  for (int ks = 0; ks < F_BD; ++ks)
#pragma unroll
    for (int n = 0; n < NT; ++n) Bf[ks][n] = ld_dev16(rb, boff + (u32)((n * 256 + 4 * ks) * 256));
  SCHED_FENCE();
  f32x4 acc[4][NT];
#pragma unroll
  for (int m = 0; m < 4; ++m)
#pragma unroll
    for (int n = 0; n < NT; ++n) acc[m][n] = f32x4{0, 0, 0, 0};
  const int asw = (c >> 1) & 7;
  const char* ap[2][2][4];
#pragma unroll
  for (int hi = 0; hi < 2; ++hi)
#pragma unroll
    for (int m = 0; m < 4; ++m) {
      const char* base = smem + f_slot(kq, 2 * hi) + ((m + kq) & 3) * 2048 + c * 128;
      ap[hi][0][m] = base + 16 * (g ^ asw); ap[hi][1][m] = base + 16 * ((4 + g) ^ asw);
    }
  STAMP(3, 4);
  fd_stage<NT, 0>(acc, Bf, rb, boff, pa, lds0, ap, kq, nh);
  fd_stage<NT, 1>(acc, Bf, rb, boff, pa, lds0, ap, kq, nh);
  STAMP(3, 5);
  fd_stage<NT, 2>(acc, Bf, rb, boff, pa, lds0, ap, kq, nh);
  fd_stage<NT, 3>(acc, Bf, rb, boff, pa, lds0, ap, kq, nh);
  STAMP(3, 6);
  fd_stage<NT, 4>(acc, Bf, rb, boff, pa, lds0, ap, kq, nh);
  fd_stage<NT, 5>(acc, Bf, rb, boff, pa, lds0, ap, kq, nh);
  STAMP(3, 7);
  fd_stage<NT, 6>(acc, Bf, rb, boff, pa, lds0, ap, kq, nh);
  const float bs = bf1(a.bsp[64 * rt + 16 * kq + c]);
  fd_stage<NT, 7>(acc, Bf, rb, boff, pa, lds0, ap, kq, nh);
  STAMP(3, 1);
  __builtin_amdgcn_s_barrier();
#pragma unroll
  for (int m = 1; m < 4; ++m) {
    const int o = (m + kq) & 3;
    char* dst = smem + (o < 3 ? o * 21504 : F_A0) + ((m - 1) * 7 * 64 + n0 * 64 + lane) * 16;
#pragma unroll
    for (int n = 0; n < NT; ++n) *(f32x4*)(dst + n * 1024) = acc[m][n];
  }
  __syncthreads();
  STAMP(3, 2);
  const char* src = smem + (kq < 3 ? kq * 21504 : F_A0) + (n0 * 64 + lane) * 16;
  const char* zs = smem + F_ZS + (16 * kq + c) * F_RS + (16 * n0 + 4 * g) * 2;
  const rsrc_t rp = make_rsrc(a.Pt, L * FH * 2);
  u32x2 pv[NT];
#pragma unroll
  for (int n = 0; n < NT; ++n) {
    const f32x4 p0 = *(const f32x4*)(src + n * 1024), p1 = *(const f32x4*)(src + (7 + n) * 1024), p2 = *(const f32x4*)(src + (14 + n) * 1024);
    const u32x2 z1 = *(const u32x2*)(zs + n * 32);
    float gg[4];
#pragma unroll
    for (int e = 0; e < 4; ++e) gg[e] = ((acc[0][n][e] + p0[e]) + (p1[e] + p2[e])) + bs;
    pv[n] = u32x2{pk_bf16(bf_lo(z1[0]) * gg[0], bf_hi(z1[0]) * gg[1]), pk_bf16(bf_lo(z1[1]) * gg[2], bf_hi(z1[1]) * gg[3])};
    if (n & 1) wt_store16(rp, (u32)((((4 * rt + kq) * 112 + 14 * ct + 2 * (n0 + n - 1 + (g & 1)) + (g >> 1)) * 16 + c) * 16), pair16(pv[n - 1], pv[n]));
  }
  if constexpr (NT & 1) {
    const u32x4 v = pair16(pv[NT - 1], pv[NT - 1]);
    if ((g & 1) == 0) wt_store16(rp, (u32)((((4 * rt + kq) * 112 + 14 * ct + 2 * (n0 + NT - 1) + (g >> 1)) * 16 + c) * 16), v);
  }
  STAMP(3, 3);
}

__device__ __forceinline__ void f_phase_e(const FArgs& a, char* smem, const int w, const int rt, const int dt) {
  const int tid = threadIdx.x, lane = tid & 63;
  const int kq = w & 3, mh = w >> 2;
  const int c = lane & 15, g = lane >> 4;
  const u32 lds0 = (u32)(size_t)(const char LAS*)smem;
  VM_WAIT(0);
  __syncthreads();
  if (tid == 0) __hip_atomic_store((GAS u32*)a.flags + 256 + rt * 32 + dt, 1u, __ATOMIC_RELAXED, __HIP_MEMORY_SCOPE_AGENT);
  STAMP(4, 5);
#pragma unroll
  for (int k = 0; k < 7; ++k)
#pragma unroll
    for (int nn = 0; nn < 2; ++nn) {
      const int n = 2 * mh + nn;
      glds16(a.Wot + ((size_t)(4 * dt + n) * 112 + 4 * (7 * kq + k)) * 128 + lane * 8, lds0 + (n * 28 + 7 * kq + k) * 1024);
    }
  const u32x4 xr = *(const u32x4*)(a.x1T + (size_t)(64 * dt + (tid >> 3)) * L + 64 * rt + 8 * (tid & 7));
  const int d8 = lane & 7;
  const f32x4 gb0 = *(const f32x4*)(a.mod + 2560 + 64 * dt + 8 * d8), gb1 = *(const f32x4*)(a.mod + 2560 + 64 * dt + 8 * d8 + 4);
  SCHED_FENCE();
  if (w == 0) wait_flags(a.flags + 256 + rt * 32, 8);
  VM_WAIT(0);
  __builtin_amdgcn_s_barrier();
  SCHED_FENCE();
  STAMP(4, 4);
  const rsrc_t rp = make_rsrc(a.Pt, L * FH * 2);
  bf16x8 Pf[7][2];
#pragma unroll
  for (int k = 0; k < 7; ++k)
#pragma unroll
    for (int m = 0; m < 2; ++m) Pf[k][m] = ld_dev16(rp, (u32)((((4 * rt + 2 * mh + m) * 112 + 4 * (7 * kq + k)) * 64 + lane * 4) * 4));
  {
    u32* p = (u32*)(smem + F_EX + (tid >> 3) * 132 + (tid & 7) * 16);
    p[0] = xr[0]; p[1] = xr[1]; p[2] = xr[2]; p[3] = xr[3];
  }
  SCHED_FENCE();
  f32x4 acc[2][4];
#pragma unroll
  for (int m = 0; m < 2; ++m)
#pragma unroll
    for (int n = 0; n < 4; ++n) acc[m][n] = f32x4{0, 0, 0, 0};
  const char* wp = smem + (7 * kq) * 1024 + lane * 16;
#pragma unroll
  for (int k = 0; k < 7; ++k) {
    bf16x8 Wf[4];
#pragma unroll
    for (int n = 0; n < 4; ++n) Wf[n] = *(const bf16x8*)(wp + (n * 28 + k) * 1024);
#pragma unroll
    for (int n = 0; n < 4; ++n)
#pragma unroll
      for (int m = 0; m < 2; ++m) acc[m][n] = __builtin_amdgcn_mfma_f32_16x16x32_bf16(Wf[n], Pf[k][m], acc[m][n], 0, 0, 0);
  }
  STAMP(4, 1);
  __syncthreads();
#pragma unroll
  for (int m = 0; m < 2; ++m)
#pragma unroll
    for (int n = 0; n < 4; ++n) *(f32x4*)(smem + (kq * 16 + (2 * mh + m) * 4 + n) * F_ETS + g * 288 + c * 16) = acc[m][n];
  __syncthreads();
  STAMP(4, 2);
  {
    const int t = 8 * w + (lane >> 3);
    float s[8];
#pragma unroll
    for (int hh = 0; hh < 2; ++hh) {
      const char* p = smem + ((t >> 4) * 4 + (d8 >> 1)) * F_ETS + (2 * (d8 & 1) + hh) * 288 + (t & 15) * 16;
      const f32x4 p0 = *(const f32x4*)p, p1 = *(const f32x4*)(p + 16 * F_ETS), p2 = *(const f32x4*)(p + 32 * F_ETS), p3 = *(const f32x4*)(p + 48 * F_ETS);
#pragma unroll
      for (int e = 0; e < 4; ++e) s[4 * hh + e] = (p0[e] + p1[e]) + (p2[e] + p3[e]);
    }
    float o[8];
#pragma unroll
    for (int e = 0; e < 8; ++e) {
      const float x1 = bf1(*(const u16*)(smem + F_EX + (8 * d8 + e) * 132 + t * 2));
      o[e] = x1 + (e < 4 ? gb0[e] : gb1[e - 4]) * s[e];
    }
    wt_store16(make_rsrc(a.out, L * D * 2), (u32)(((64 * rt + t) * D + 64 * dt + 8 * d8) * 2), u32x4{pk_bf16(o[0], o[1]), pk_bf16(o[2], o[3]), pk_bf16(o[4], o[5]), pk_bf16(o[6], o[7])});
  }
  STAMP(4, 3);
}

__global__ __launch_bounds__(512, 2) void kF(FArgs a) {
  extern __shared__ __attribute__((aligned(16))) char smem[];
  const int wg = blockIdx.x, lane = threadIdx.x & 63;
  const int w = __builtin_amdgcn_readfirstlane(threadIdx.x >> 6);
  const int xc = wg & 7, ii = wg >> 3;
  const int pi = (xc >> 1) * 8 + (ii & 7), j = (xc & 1) * 4 + (ii >> 3);
  STAMP(2, 0);
  {
    const u32 lds0 = (u32)(size_t)(const char LAS*)smem;
#pragma unroll
    for (int i = 0; i < 8; ++i) {
      const int d = 64 * w + 8 * i + (lane >> 3);
      const int fz = (((d >> 1) & 1) | ((d >> 2) & 2)) << 1;
      glds16(a.x1T + (size_t)d * L + 64 * pi + 8 * ((lane & 7) ^ fz), lds0 + F_X + (64 * w + 8 * i) * 128);
    }
    __builtin_amdgcn_s_barrier();
  }
  const u16* pa[2];
#pragma unroll
  for (int par = 0; par < 2; ++par) {
    const int row = 32 * (w >> 2) + 8 * par + (lane >> 3);
    pa[par] = a.wsp + (size_t)(64 * pi + row) * L + 512 * (w & 3) + 8 * ((lane & 7) ^ ((row >> 1) & 7));
  }
  if (w == 0) f_phase_c<0>(a, smem, w, pi, j, pa);
  else if (w < 7) f_phase_c<1>(a, smem, w, pi, j, pa);
  else f_phase_c<2>(a, smem, w, pi, j, pa);
  __syncthreads();
  STAMP(3, 0);
  if (w < 4) f_phase_d<4>(a, smem, w, pi, j, pa);
  else f_phase_d<3>(a, smem, w, pi, j, pa);
  STAMP(4, 0);
  f_phase_e(a, smem, w, pi, j);
}
#define KMASK(b)
extern "C" void kernel_launch(void* const* d_in, const int* in_sizes, int n_in, void* d_out, int out_size,
                              void* d_ws, size_t ws_size, hipStream_t stream) {
  const u16* x = (const u16*)d_in[0];
  char* ws = (char*)d_ws;
  float* mod = (float*)(ws + 0);
  u32* stats1 = (u32*)(ws + 16384);
  float* uv = (float*)(ws + 32768);
  u64* gran = (u64*)(ws + 65536);
  u32* flags = (u32*)(ws + 65536 + 262144);
  u16* xT = (u16*)(ws + (1 << 20));
  u16* Krev = (u16*)(ws + (3 << 20));
  u16* x1T = xT;
  u16* Wt = (u16*)(ws + (7 << 20));
  u16* Wot = (u16*)(ws + (9 << 20));
  u16* Pt = (u16*)(ws + (10 << 20));
  u16* z2t = Krev;
  AArgs aa;
  aa.x = x; aa.Are = (const u16*)d_in[1]; aa.Aim = (const u16*)d_in[2]; aa.Bre = (const u16*)d_in[3]; aa.Bim = (const u16*)d_in[4];
  aa.Cre = (const u16*)d_in[5]; aa.Cim = (const u16*)d_in[6]; aa.logdt = (const u16*)d_in[7]; aa.c = (const u16*)d_in[14];
  aa.wmod = (const u16*)d_in[15]; aa.win = (const u16*)d_in[10]; aa.wout = (const u16*)d_in[13];
  aa.mod = mod; aa.stats1 = stats1; aa.xT = xT; aa.Krev = Krev; aa.gran = (u32x4*)gran;
  (void)hipFuncSetAttribute((const void*)kA, hipFuncAttributeMaxDynamicSharedMemorySize, A_LDS);
  KMASK(1) kA<<<256, 768, A_LDS, stream>>>(aa);
  BArgs ba;
  ba.xT = xT; ba.Krev = Krev; ba.Dskip = (const u16*)d_in[8]; ba.win = (const u16*)d_in[10]; ba.wout = (const u16*)d_in[13];
  ba.wsp = (const u16*)d_in[11]; ba.stats1 = stats1; ba.mod = mod; ba.x1T = x1T; ba.Wt = Wt; ba.Wot = Wot; ba.uv = uv;
  (void)hipFuncSetAttribute((const void*)kB, hipFuncAttributeMaxDynamicSharedMemorySize, B_LDS);
  KMASK(2) kB<<<256, 768, B_LDS, stream>>>(ba);
  FArgs fa;
  fa.x1T = x1T; fa.Wt = Wt; fa.wsp = (const u16*)d_in[11]; fa.bsp = (const u16*)d_in[12]; fa.Wot = Wot; fa.uv = uv; fa.mod = mod;
  fa.z2t = z2t; fa.Pt = Pt; fa.out = (u16*)d_out; fa.gran = gran; fa.flags = flags;
  (void)hipFuncSetAttribute((const void*)kF, hipFuncAttributeMaxDynamicSharedMemorySize, F_LDS);
  KMASK(4) kF<<<256, 512, F_LDS, stream>>>(fa);
}
```

```cpp
#include <hip/hip_runtime.h>
#include <hip/hip_bf16.h>
#include <stdint.h>

typedef unsigned short u16;
typedef unsigned int u32;
typedef unsigned long long u64;
typedef __bf16 bf16x8 __attribute__((ext_vector_type(8)));
typedef __bf16 bf16x2 __attribute__((ext_vector_type(2)));
typedef short s16x4 __attribute__((ext_vector_type(4)));
typedef float f32x2 __attribute__((ext_vector_type(2)));
typedef float f32x4 __attribute__((ext_vector_type(4)));
typedef u32 u32x2 __attribute__((ext_vector_type(2)));
typedef u32 u32x4 __attribute__((ext_vector_type(4)));
#define LAS __attribute__((address_space(3)))
#define GAS __attribute__((address_space(1)))

constexpr int L = 2048, D = 512, NS = 128, F = 1792, FH = 896;

__device__ __forceinline__ float bf_lo(u32 w) { return __uint_as_float(w << 16); }
__device__ __forceinline__ float bf_hi(u32 w) { return __uint_as_float(w & 0xffff0000u); }
__device__ __forceinline__ float bf1(u16 h) { return __uint_as_float(((u32)h) << 16); }
__device__ __forceinline__ u32 pk_bf16(float lo, float hi) {
  f32x2 a = {lo, hi};
  bf16x2 p = __builtin_convertvector(a, bf16x2);
  return __builtin_bit_cast(u32, p);
}
__device__ __forceinline__ float rbf(float x) { return bf_lo(pk_bf16(x, x)); }

template <int CTRL, int ROWMASK = 0xf>
__device__ __forceinline__ float dpp_add(float x) {
  int y = __builtin_amdgcn_update_dpp(0, __float_as_int(x), CTRL, ROWMASK, 0xf, true);
  return x + __int_as_float(y);
}
__device__ __forceinline__ float row16_sum(float x) {
  x = dpp_add<0xB1>(x);
  x = dpp_add<0x4E>(x);
  x = dpp_add<0x141>(x);
  x = dpp_add<0x140>(x);
  return x;
}
__device__ __forceinline__ float wave_sum(float x) {
  x = row16_sum(x);
  float a = __int_as_float(__builtin_amdgcn_readlane(__float_as_int(x), 0));
  float b = __int_as_float(__builtin_amdgcn_readlane(__float_as_int(x), 16));
  float c = __int_as_float(__builtin_amdgcn_readlane(__float_as_int(x), 32));
  float d = __int_as_float(__builtin_amdgcn_readlane(__float_as_int(x), 48));
  return (a + b) + (c + d);
}
__device__ __forceinline__ s16x4 tr_read(const char LAS* p) {
  return __builtin_amdgcn_ds_read_tr16_b64_v4i16((s16x4 LAS*)p);
}

#define STAMP(kern, i)
__device__ __forceinline__ void glds16(const void* gsrc, u32 lds_dst) {
  u32 keep;
  asm volatile("s_mov_b32 %0, m0\n\ts_mov_b32 m0, %2\n\ts_nop 0\n\tglobal_load_lds_dwordx4 %1, off\n\ts_mov_b32 m0, %0"
               : "=&s"(keep) : "v"(gsrc), "s"(lds_dst) : "memory");
}
#define SCHED_FENCE() __builtin_amdgcn_sched_barrier(0)
#define VM_WAIT(n) asm volatile("s_waitcnt vmcnt(" #n ")" ::: "memory")
__device__ __forceinline__ float xor16_sum(float x) {
  const u32 v = __float_as_uint(x);
  auto r = __builtin_amdgcn_permlane16_swap(v, v, false, false);
  return __uint_as_float(r[0]) + __uint_as_float(r[1]);
}
__device__ __forceinline__ float xor32_sum(float x) {
  const u32 v = __float_as_uint(x);
  auto r = __builtin_amdgcn_permlane32_swap(v, v, false, false);
  return __uint_as_float(r[0]) + __uint_as_float(r[1]);
}
__device__ __forceinline__ float xor4_8_sum(float x) {
  x = x + __int_as_float(__builtin_amdgcn_update_dpp(0, __float_as_int(x), 0x124, 0xf, 0xf, true));
  x = x + __int_as_float(__builtin_amdgcn_update_dpp(0, __float_as_int(x), 0x128, 0xf, 0xf, true));
  return x;
}
__device__ __forceinline__ void lds_rendezvous(u32 LAS* cnt, u32 target) {
  asm volatile("s_waitcnt lgkmcnt(0)" ::: "memory");
  if ((threadIdx.x & 63) == 0) __hip_atomic_fetch_add(cnt, 1u, __ATOMIC_RELAXED, __HIP_MEMORY_SCOPE_WORKGROUP);
  while (__hip_atomic_load(cnt, __ATOMIC_RELAXED, __HIP_MEMORY_SCOPE_WORKGROUP) < target) __builtin_amdgcn_s_sleep(1);
  asm volatile("" ::: "memory");
}

typedef __amdgpu_buffer_rsrc_t rsrc_t;
__device__ __forceinline__ rsrc_t make_rsrc(const void* base, u32 bytes) {
  return __builtin_amdgcn_make_buffer_rsrc(const_cast<void*>(base), 0, bytes, 0x00020000);
}
__device__ __forceinline__ void wt_store16(rsrc_t r, u32 byte_off, u32x4 v) { __builtin_amdgcn_raw_buffer_store_b128(v, r, byte_off, 0, 16); }
__device__ __forceinline__ u32x4 pair16(u32x2 a, u32x2 b) {
  const auto r0 = __builtin_amdgcn_permlane16_swap(a[0], b[0], false, false);
  const auto r1 = __builtin_amdgcn_permlane16_swap(a[1], b[1], false, false);
  return u32x4{r0[0], r1[0], r0[1], r1[1]};
}
constexpr int CODE_LANES = 92;
struct AArgs {
  const u16 *x, *Are, *Aim, *Bre, *Bim, *Cre, *Cim, *logdt, *c, *wmod, *win, *wout;
  float* mod; u32* stats1; u16* xT; u16* Krev; u32x4* gran;
};
constexpr int A_RS = 528;
constexpr int A_TAB = 0;
constexpr int A_KC = A_TAB + 2 * 64 * A_RS;
constexpr int A_TILE = A_KC + 2 * 768 * 4;
constexpr int A_RED = A_TILE + 8 * 1088;
constexpr int A_CNT = A_RED + 4 * 32 * 4;
constexpr int A_LDS = A_CNT + 16;

struct KIdx { u16 v[2048]; };
constexpr int bf16_round_int(int l) {
  int sh = 0;
  while ((l >> sh) >= 256) ++sh;
  if (sh == 0) return l;
  const int q = l >> sh, rem = l & ((1 << sh) - 1), half = 1 << (sh - 1);
  const int up = (rem > half || (rem == half && (q & 1))) ? 1 : 0;
  return (q + up) << sh;
}
constexpr KIdx make_kidx() {
  KIdx t{};
  for (int x = 0; x < 2048; ++x) {
    const int lr = bf16_round_int(2047 - x);
    const int aa = lr >> 7, b = (lr >> 3) & 15, r = lr & 7;
    const int row = lr < 256 ? 8 * aa + r : lr < 512 ? 16 + 4 * (aa - 2) + (r >> 1) : lr < 1024 ? 24 + 2 * (aa - 4) + (r >> 2) : 32 + (aa - 8);
    t.v[x] = (u16)(row * 16 + b);
  }
  return t;
}
__device__ const KIdx g_kidx = make_kidx();

struct cpx { float r, i; };
__device__ __forceinline__ cpx cmul(cpx a, cpx b) { return cpx{a.r * b.r - a.i * b.i, a.r * b.i + a.i * b.r}; }

__global__ __launch_bounds__(768) void kA(AArgs a) {
  extern __shared__ __attribute__((aligned(16))) char smem[];
  const int wg = blockIdx.x, tid = threadIdx.x, lane = tid & 63;
  const int w = __builtin_amdgcn_readfirstlane(tid >> 6);
  STAMP(0, 0);
  if (w < 8) {
    __builtin_amdgcn_s_setprio(2);
    const int ch = w >> 2, nh = (w >> 1) & 1, part = w & 1, d = 2 * wg + ch;
    const int n = 64 * nh + lane, o = d * NS + n;
    const u16 hare = a.Are[o], haim = a.Aim[o], hbre = a.Bre[o], hbim = a.Bim[o], hcre = a.Cre[o], hcim = a.Cim[o], hdt = a.logdt[d];
    const u32x4 kix = *(const u32x4*)(g_kidx.v + 8 * (tid & 255));
    SCHED_FENCE();
    __builtin_amdgcn_s_barrier();
    SCHED_FENCE();
    const float dt = rbf(__expf(bf1(hdt)));
    const float lr = (dt * bf1(hare)) * 1.4426950408889634f, lp = (dt * bf1(haim)) * 0.15915494309189535f;
    auto cexp = [&](float l) -> cpx {
      const float e = __builtin_amdgcn_exp2f(lr * l), ph = __builtin_amdgcn_fractf(lp * l);
      return cpx{e * __builtin_amdgcn_cosf(ph), e * __builtin_amdgcn_sinf(ph)};
    };
    char* tab = smem + A_TAB + ch * (64 * A_RS) + n * 4;
    auto put = [&](int row, cpx v) { *(u32*)(tab + row * A_RS) = pk_bf16(v.r, v.i); };
    if (part == 0) {
      const cpx E1 = cexp(1.f), E128 = cexp(128.f), E512 = cexp(512.f);
      const cpx E2 = cmul(E1, E1), E3 = cmul(E2, E1), E4 = cmul(E2, E2), E5 = cmul(E4, E1), E6 = cmul(E4, E2), E7 = cmul(E4, E3);
      put(0, cpx{1.f, 0.f}); put(1, E1); put(2, E2); put(3, E3); put(4, E4); put(5, E5); put(6, E6); put(7, E7);
      put(8, E128); put(9, cmul(E128, E1)); put(10, cmul(E128, E2)); put(11, cmul(E128, E3));
      put(12, cmul(E128, E4)); put(13, cmul(E128, E5)); put(14, cmul(E128, E6)); put(15, cmul(E128, E7));
      const cpx E256 = cmul(E128, E128), E384 = cmul(E256, E128);
      put(16, E256); put(17, cmul(E256, E2)); put(18, cmul(E256, E4)); put(19, cmul(E256, E6));
      put(20, E384); put(21, cmul(E384, E2)); put(22, cmul(E384, E4)); put(23, cmul(E384, E6));
      const cpx E640 = cmul(E512, E128), E768 = cmul(E512, E256), E896 = cmul(E512, E384);
      put(24, E512); put(25, cmul(E512, E4)); put(26, E640); put(27, cmul(E640, E4));
      put(28, E768); put(29, cmul(E768, E4)); put(30, E896); put(31, cmul(E896, E4));
    } else {
      const cpx E8 = cexp(8.f), E128 = cexp(128.f), E1024 = cexp(1024.f);
      const float br = dt * bf1(hbre), bi = dt * bf1(hbim), cr = bf1(hcre), ci = bf1(hcim);
      const cpx W = cmul(cpx{cr, ci}, cpx{br, bi});
      const cpx E16 = cmul(E8, E8), E32 = cmul(E16, E16), E64 = cmul(E32, E32);
      cpx Y[8];
      Y[0] = W; Y[1] = cmul(W, E8); Y[2] = cmul(W, E16); Y[3] = cmul(Y[1], E16);
#pragma unroll
      for (int b = 0; b < 4; ++b) Y[4 + b] = cmul(Y[b], E32);
#pragma unroll
      for (int b = 0; b < 8; ++b) {
        put(48 + b, cpx{Y[b].r, -Y[b].i});
        const cpx t = cmul(Y[b], E64);
        put(56 + b, cpx{t.r, -t.i});
      }
      const cpx E256 = cmul(E128, E128), E512 = cmul(E256, E256);
      cpx P[4];
      P[0] = E1024; P[1] = cmul(E1024, E128); P[2] = cmul(E1024, E256); P[3] = cmul(P[1], E256);
#pragma unroll
      for (int b = 0; b < 4; ++b) { put(32 + b, P[b]); put(36 + b, cmul(P[b], E512)); }
      put(40, cmul(E1024, E1024));
    }
    STAMP(0, 1);
    lds_rendezvous((u32 LAS*)(smem + A_CNT), 8);
    STAMP(0, 2);
    if (w < 6) {
      const int c2 = w >= 3 ? 1 : 0, t = w - 3 * c2, i = lane & 15, g = lane >> 4;
      const char* tb = smem + A_TAB + c2 * (64 * A_RS) + 16 * g;
      const char* pa = tb + (16 * t + i) * A_RS, *pb = tb + (48 + i) * A_RS;
      f32x4 acc = {0, 0, 0, 0};
      bf16x8 Af[8], Bf[8];
#pragma unroll
      for (int s = 0; s < 8; ++s) {
        Af[s] = *(const bf16x8*)(pa + s * 64); Bf[s] = *(const bf16x8*)(pb + s * 64);
      }
#pragma unroll
      for (int s = 0; s < 8; ++s) acc = __builtin_amdgcn_mfma_f32_16x16x32_bf16(Af[s], Bf[s], acc, 0, 0, 0);
      float* Kc = (float*)(smem + A_KC) + c2 * 768 + t * 256;
#pragma unroll
      for (int r = 0; r < 4; ++r) Kc[(4 * g + r) * 16 + i] = acc[r];
    }
    STAMP(0, 3);
    lds_rendezvous((u32 LAS*)(smem + A_CNT + 4), 8);
    STAMP(0, 4);
    const float* Kc = (const float*)(smem + A_KC) + ch * 768;
    float kv[8];
#pragma unroll
    for (int j = 0; j < 4; ++j) { kv[2 * j] = Kc[kix[j] & 0xffffu]; kv[2 * j + 1] = Kc[kix[j] >> 16]; }
    *(u32x4*)(a.Krev + (size_t)d * L + 8 * (tid & 255)) =
        u32x4{pk_bf16(kv[0], kv[1]), pk_bf16(kv[2], kv[3]), pk_bf16(kv[4], kv[5]), pk_bf16(kv[6], kv[7])};
    STAMP(0, 5);
  } else {
    const int tw = w - 8;
    const int t0 = 8 * ((wg & 7) * 32 + (wg >> 3));
    u32x4 xr[2];
#pragma unroll
    for (int j = 0; j < 2; ++j) xr[j] = *(const u32x4*)(a.x + (size_t)(t0 + 2 * tw + j) * D + 8 * lane);
    const bool doM = wg < 96;
    u32x4 wm[8]; u16 cv[8];
    const int rr = lane >> 2, q = lane & 3;
    if (doM) {
#pragma unroll
      for (int p = 0; p < 8; ++p) cv[p] = a.c[p * 64 + tw * 16 + rr];
#pragma unroll
      for (int p = 0; p < 8; ++p) wm[p] = *(const u32x4*)(a.wmod + (size_t)(p * 64 + tw * 16 + rr) * 3072 + 32 * wg + 8 * q);
    }
    u32x4 pf[3];
    {
      const int st = tid - 512;
      pf[0] = *(const u32x4*)(a.win + (size_t)wg * 3584 + st * 8);
      pf[1] = *(const u32x4*)(a.win + (size_t)wg * 3584 + (st < 192 ? 2048 + st * 8 : 0));
      pf[2] = *(const u32x4*)(a.wout + (size_t)wg * 1792 + (st < 224 ? st * 8 : 0));
    }
    u32x4 cf;
    {
      u64 base;
      asm volatile("s_getpc_b64 s[40:41]\n\ts_add_u32 s40, s40, _Z2kB5BArgs@rel32@lo+4\n\ts_addc_u32 s41, s41, _Z2kB5BArgs@rel32@hi+12\n\ts_mov_b64 %0, s[40:41]"
                   : "=s"(base) : : "s40", "s41", "scc");
      const int slot = (wg >> 3) * CODE_LANES + (tid - 512);
      cf = *(const u32x4*)((const char*)base + (size_t)((tid - 512) < CODE_LANES ? slot : 0) * 16);
    }
    char* tile = smem + A_TILE;
    if (tw == 0 && lane < 4) *(u32*)(smem + A_CNT + 4 * lane) = 0;
    asm volatile("s_waitcnt lgkmcnt(0)" ::: "memory");
    SCHED_FENCE();
    __builtin_amdgcn_s_barrier();
    SCHED_FENCE();
#pragma unroll
    for (int j = 0; j < 2; ++j) *(u32x4*)(tile + (2 * tw + j) * 1088 + 16 * lane) = xr[j];
    STAMP(0, 1);
    lds_rendezvous((u32 LAS*)(smem + A_CNT + 8), 4);
    STAMP(0, 2);
#pragma unroll
    for (int ps = 0; ps < 2; ++ps) {
      const int d0 = 128 * tw + 64 * ps + 16 * (lane >> 4);
      const char LAS* src = (const char LAS*)tile + ((lane & 15) >> 2) * 1088 + 2 * (d0 + 4 * (lane & 3));
      const s16x4 lo = tr_read(src), hi = tr_read(src + 4 * 1088);
      u32x2 a0 = __builtin_bit_cast(u32x2, lo), a1 = __builtin_bit_cast(u32x2, hi);
      *(u32x4*)(a.xT + (size_t)(d0 + (lane & 15)) * L + t0) = u32x4{a0[0], a0[1], a1[0], a1[1]};
    }
    if (tw == 0) a.gran[wg * 64 + lane] = u32x4{0, 0, 0, 0};
    if (tw == 1 && wg < 8) a.gran[(256 + wg) * 64 + lane] = u32x4{0, 0, 0, 0};
    {
      float v[2][8], s[2];
#pragma unroll
      for (int j = 0; j < 2; ++j) {
#pragma unroll
        for (int k = 0; k < 4; ++k) { v[j][2 * k] = bf_lo(xr[j][k]); v[j][2 * k + 1] = bf_hi(xr[j][k]); }
        s[j] = ((v[j][0] + v[j][1]) + (v[j][2] + v[j][3])) + ((v[j][4] + v[j][5]) + (v[j][6] + v[j][7]));
      }
      float mean[2], q2[2];
#pragma unroll
      for (int j = 0; j < 2; ++j) mean[j] = wave_sum(s[j]) * (1.f / D);
#pragma unroll
      for (int j = 0; j < 2; ++j) {
        q2[j] = 0;
#pragma unroll
        for (int k = 0; k < 8; ++k) { const float dv = v[j][k] - mean[j]; q2[j] += dv * dv; }
      }
#pragma unroll
      for (int j = 0; j < 2; ++j) {
        const float var = wave_sum(q2[j]) * (1.f / D);
        const float sd = rbf(__builtin_amdgcn_sqrtf(rbf(rbf(var) + 1e-5f)));
        if (lane == 0) *(f32x2*)(a.stats1 + 2 * (t0 + 2 * tw + j)) = f32x2{rbf(mean[j]), __builtin_amdgcn_rcpf(sd)};
      }
    }
    float* red = (float*)(smem + A_RED);
    if (doM) {
      float acc[8] = {0, 0, 0, 0, 0, 0, 0, 0};
#pragma unroll
      for (int p = 0; p < 8; ++p) {
        const float cx = bf1(cv[p]);
        const float s = rbf(cx * __builtin_amdgcn_rcpf(1.f + __expf(-cx)));
#pragma unroll
        for (int k = 0; k < 4; ++k) { acc[2 * k] += s * bf_lo(wm[p][k]); acc[2 * k + 1] += s * bf_hi(wm[p][k]); }
      }
#pragma unroll
      for (int k = 0; k < 8; ++k) {
        float v = acc[k];
        v = xor32_sum(xor16_sum(xor4_8_sum(v)));
        if (rr == 0) red[tw * 32 + 8 * q + k] = v;
      }
      STAMP(0, 3);
      lds_rendezvous((u32 LAS*)(smem + A_CNT + 12), 4);
      STAMP(0, 4);
      if (tw == 0 && lane < 32)
        a.mod[32 * wg + lane] = rbf((red[lane] + red[32 + lane]) + (red[64 + lane] + red[96 + lane]));
    }
#pragma unroll
    for (int k = 0; k < 3; ++k) asm volatile("" ::"v"(pf[k]));
    asm volatile("" ::"v"(cf));
    STAMP(0, 5);
  }
}
struct BArgs {
  const u16 *xT, *Krev, *Dskip, *win, *wout, *wsp;
  const u32* stats1; const float* mod;
  u16 *x1T, *Wt, *Wot; float* uv;
};
constexpr int B_H = 0;
constexpr int B_HS = (256 + 2048) * 2;
constexpr int B_KC = B_H + 2 * B_HS;
constexpr int B_KS = 4160;
constexpr int B_PART = B_KC + 2 * 4 * B_KS;
constexpr int B_RED = B_PART + 2 * 4 * 8 * 1280;
constexpr int B_CNT = B_RED + 4 * 16 * 4;
constexpr int B_LDS = B_CNT + 32;

__global__ __launch_bounds__(768) void kB(BArgs a) {
  extern __shared__ __attribute__((aligned(16))) char smem[];
  const int wg = blockIdx.x, tid = threadIdx.x, lane = tid & 63;
  const int w = __builtin_amdgcn_readfirstlane(tid >> 6);
  STAMP(1, 0);
  if (w < 8) {
    __builtin_amdgcn_s_setprio(2);
    const int ch = w >> 2, q = w & 3, d = 2 * wg + ch, tj = tid & 255;
    const u32x4 xv = *(const u32x4*)(a.xT + (size_t)d * L + 8 * tj);
    f32x4 st[4];
#pragma unroll
    for (int k = 0; k < 4; ++k) st[k] = *(const f32x4*)(a.stats1 + 16 * tj + 4 * k);
    const u32x4 k0 = *(const u32x4*)(a.Krev + (size_t)d * L + 8 * tj);
    const u32x4 k1r = *(const u32x4*)(a.Krev + (size_t)d * L + 8 * (tj < 255 ? tj + 1 : tj));
    const float sa = a.mod[d], Ac = rbf(1.f + a.mod[512 + d]), ga = a.mod[1024 + d], dsk = bf1(a.Dskip[d]);
    SCHED_FENCE();
    __builtin_amdgcn_s_barrier();
    SCHED_FENCE();
    char* hb = smem + B_H + ch * B_HS;
    char* kc = smem + B_KC + ch * 4 * B_KS;
    if (tj < 32) *(u32x4*)(hb + 16 * tj) = u32x4{0, 0, 0, 0};
    if (tj < 16) *(u32x4*)(kc + (tj >> 2) * B_KS + 16 * (256 + (tj & 3))) = u32x4{0, 0, 0, 0};
    {
      const u32 z = tj < 255 ? 0xffffffffu : 0u;
      const u32 wd[8] = {k0[0], k0[1], k0[2], k0[3], k1r[0] & z, k1r[1] & z, 0, 0};
      u32 od[5];
#pragma unroll
      for (int k = 0; k < 5; ++k) od[k] = __builtin_amdgcn_alignbit(wd[k + 1], wd[k], 16);
      *(u32x4*)(kc + 0 * B_KS + 16 * tj) = u32x4{wd[0], wd[1], wd[2], wd[3]};
      *(u32x4*)(kc + 1 * B_KS + 16 * tj) = u32x4{od[0], od[1], od[2], od[3]};
      *(u32x4*)(kc + 2 * B_KS + 16 * tj) = u32x4{wd[1], wd[2], wd[3], wd[4]};
      *(u32x4*)(kc + 3 * B_KS + 16 * tj) = u32x4{od[1], od[2], od[3], od[4]};
    }
    u32x4 hv;
    {
      float hh[8];
#pragma unroll
      for (int k = 0; k < 8; ++k) {
        const float m = st[k >> 1][2 * (k & 1)], ri = st[k >> 1][2 * (k & 1) + 1];
        const float xx = (k & 1) ? bf_hi(xv[k >> 1]) : bf_lo(xv[k >> 1]);
        hh[k] = (xx - m) * (ri * Ac) + sa;
      }
      hv = u32x4{pk_bf16(hh[0], hh[1]), pk_bf16(hh[2], hh[3]), pk_bf16(hh[4], hh[5]), pk_bf16(hh[6], hh[7])};
    }
    *(u32x4*)(hb + 512 + 16 * tj) = hv;
    STAMP(1, 1);
    lds_rendezvous((u32 LAS*)(smem + B_CNT + 12 + 4 * 0) + ch * 2, 4);
    STAMP(1, 2);
    const int c = lane & 15, g = lane >> 4;
    const char* bbase = hb + 2 * (256 + 16 * c - 16 * (g >> 1) + 8 * (g & 1) - 32 * q) - 256;
    const int X = 2047 - 32 * q - 16 * (g >> 1) + 8 * (g & 1) - c;
    const char* abase = kc + (X & 3) * B_KS + 2 * (X & ~3) - 256 * 15;
    const char LAS* ab0 = (const char LAS*)abase;
    bf16x8 Bf[16], Af[16];
    auto lda = [&](int u) {
      const u32x2 lo = *(const volatile u32x2 LAS*)(ab0 + 256 * (15 - u)), hi = *(const volatile u32x2 LAS*)(ab0 + 256 * (15 - u) + 8);
      Af[u] = __builtin_bit_cast(bf16x8, u32x4{lo[0], lo[1], hi[0], hi[1]});
    };
    lda(0);
#pragma unroll
    for (int v = 1; v < 16; v += 2) Bf[v] = *(const bf16x8*)(bbase + 256 * v);
    lda(1);
#pragma unroll
    for (int v = 0; v < 16; v += 2) Bf[v] = *(const bf16x8*)(bbase + 256 * v);
    lda(2); lda(3);
    SCHED_FENCE();
    f32x4 acc[8];
#pragma unroll
    for (int i = 0; i < 8; ++i) acc[i] = f32x4{0, 0, 0, 0};
#pragma unroll
    for (int u = 0; u < 16; ++u) {
      if (u + 4 < 16) lda(u + 4);
      SCHED_FENCE();
#pragma unroll
      for (int i = u >> 1; i < 8; ++i) acc[i] = __builtin_amdgcn_mfma_f32_16x16x32_bf16(Af[u], Bf[2 * i - u + 1], acc[i], 0, 0, 0);
      SCHED_FENCE();
    }
    STAMP(1, 3);
    char* part = smem + B_PART + ch * (4 * 8 * 1280);
#pragma unroll
    for (int i = 0; i < 8; ++i) *(f32x4*)(part + (q * 8 + i) * 1280 + 16 * (20 * g + c)) = acc[i];
    lds_rendezvous((u32 LAS*)(smem + B_CNT + 4 * ch), 4);
    STAMP(1, 4);
    {
      const int i = tj >> 5, j5 = tj & 31, cc = j5 >> 1;
      float y[8];
#pragma unroll
      for (int hh = 0; hh < 2; ++hh) {
        const char* p = part + i * 1280 + 16 * (20 * (2 * (j5 & 1) + hh) + cc);
        const f32x4 p0 = *(const f32x4*)(p), p1 = *(const f32x4*)(p + 8 * 1280), p2 = *(const f32x4*)(p + 16 * 1280), p3 = *(const f32x4*)(p + 24 * 1280);
#pragma unroll
        for (int r = 0; r < 4; ++r) y[4 * hh + r] = (p0[r] + p1[r]) + (p2[r] + p3[r]);
      }
      float o[8];
#pragma unroll
      for (int k = 0; k < 8; ++k) {
        const float hx = (k & 1) ? bf_hi(hv[k >> 1]) : bf_lo(hv[k >> 1]);
        const float xx = (k & 1) ? bf_hi(xv[k >> 1]) : bf_lo(xv[k >> 1]);
        o[k] = xx + ga * (y[k] + hx * dsk);
      }
      wt_store16(make_rsrc(a.x1T, D * L * 2), (u32)(d * L + 8 * tj) * 2, u32x4{pk_bf16(o[0], o[1]), pk_bf16(o[2], o[3]), pk_bf16(o[4], o[5]), pk_bf16(o[6], o[7])});
    }
    STAMP(1, 5);
  } else {
    const int st = tid - 512, sw = w - 8;
    if (st < 8) *(u32*)(smem + B_CNT + 4 * st) = 0;
    asm volatile("s_waitcnt lgkmcnt(0)" ::: "memory");
    __builtin_amdgcn_s_barrier();
    SCHED_FENCE();
    float* red = (float*)(smem + B_RED);
    const bool act = wg < 224;
    const int f0 = 8 * ((wg & 7) * 28 + (wg >> 3));
    const int d8 = st >> 2, fp = st & 3;
    const int f8 = wg >> 1, dp = (wg & 1) * 128 + (st & 127);
    u32 wv[8], ov[8];
    f32x4 cb0, cb1, sb0, sb1;
    if (act) {
      cb0 = *(const f32x4*)(a.mod + 2048 + 8 * d8); cb1 = *(const f32x4*)(a.mod + 2048 + 8 * d8 + 4);
      sb0 = *(const f32x4*)(a.mod + 1536 + 8 * d8); sb1 = *(const f32x4*)(a.mod + 1536 + 8 * d8 + 4);
#pragma unroll
      for (int j = 0; j < 8; ++j) wv[j] = *(const u32*)(a.win + (size_t)(8 * d8 + j) * F + f0 + 2 * fp);
      if (st < 128) {
#pragma unroll
        for (int j = 0; j < 8; ++j) ov[j] = *(const u32*)(a.wout + (size_t)(8 * f8 + j) * D + 2 * dp);
      }
    }
    u32x4 pf[8];
    {
      const u16* p = a.wsp + (size_t)wg * 16384 + st * 8;
#pragma unroll
      for (int j = 0; j < 8; ++j) pf[j] = *(const u32x4*)(p + j * 2048);
    }
    SCHED_FENCE();
    if (act) {
      float lo[8], hi[8], u0 = 0, u1 = 0, v0 = 0, v1 = 0;
#pragma unroll
      for (int j = 0; j < 8; ++j) {
        const float Ac = rbf(1.f + (j < 4 ? cb0[j] : cb1[j - 4])), sb = j < 4 ? sb0[j] : sb1[j - 4];
        const float wl = bf_lo(wv[j]), wh = bf_hi(wv[j]);
        lo[j] = rbf(Ac * wl); hi[j] = rbf(Ac * wh);
        u0 += lo[j]; u1 += hi[j]; v0 += sb * wl; v1 += sb * wh;
      }
      const int f = f0 + 2 * fp;
      const rsrc_t rw = make_rsrc(a.Wt, D * F * 2);
      const u32 dst = (u32)((((f >> 4) * 64 + d8) * 16 + (f & 15)) * 16);
      wt_store16(rw, dst, u32x4{pk_bf16(lo[0], lo[1]), pk_bf16(lo[2], lo[3]), pk_bf16(lo[4], lo[5]), pk_bf16(lo[6], lo[7])});
      wt_store16(rw, dst + 16, u32x4{pk_bf16(hi[0], hi[1]), pk_bf16(hi[2], hi[3]), pk_bf16(hi[4], hi[5]), pk_bf16(hi[6], hi[7])});
      float rv[4] = {u0, u1, v0, v1};
#pragma unroll
      for (int k = 0; k < 4; ++k) {
        const float v = xor32_sum(xor16_sum(xor4_8_sum(rv[k])));
        if (lane < 4) red[sw * 16 + lane * 4 + k] = v;
      }
      if (st < 128) {
        const int dd = 2 * dp;
        const rsrc_t ro = make_rsrc(a.Wot, FH * D * 2);
        const u32 od = (u32)((((dd >> 4) * 112 + f8) * 16 + (dd & 15)) * 16);
        u32x4 e, o;
#pragma unroll
        for (int k = 0; k < 4; ++k) {
          e[k] = (ov[2 * k] & 0xffffu) | (ov[2 * k + 1] << 16);
          o[k] = (ov[2 * k] >> 16) | (ov[2 * k + 1] & 0xffff0000u);
        }
        wt_store16(ro, od, e); wt_store16(ro, od + 16, o);
      }
      lds_rendezvous((u32 LAS*)(smem + B_CNT + 8), 4);
      if (st < 16) {
        const float s = (red[st] + red[16 + st]) + (red[32 + st] + red[48 + st]);
        const int fq = st >> 2, k = st & 3;
        a.uv[(k >> 1) * F + f0 + 2 * fq + (k & 1)] = s;
      }
    }
    STAMP(1, 1);
#pragma unroll
    for (int j = 0; j < (int)(sizeof(pf) / sizeof(pf[0])); ++j) asm volatile("" ::"v"(pf[j]));
    STAMP(1, 5);
  }
}
struct FArgs {
  const u16 *x1T, *Wt, *wsp, *bsp, *Wot; const float *uv, *mod;
  u16 *z2t, *Pt, *out; u64* gran; u32* flags;
};
constexpr int F_X = 0;
constexpr int F_ST = 65536;
constexpr int F_MR = F_ST + 512;
constexpr int F_PS = F_MR + 8 * 512;
constexpr int F_FS = F_PS + 7 * 64 * 8;
constexpr int F_ZS = F_FS + 512;
constexpr int F_RS = 232;
constexpr int F_A0 = 90112;
constexpr int F_LDS = F_A0 + 8 * 8192;
constexpr int F_ETS = 1152;
constexpr int F_EX = 112 * 1024;
#define F_BDEPTH 3
constexpr int F_BD = F_BDEPTH;
constexpr int F_BR = F_BD;
static_assert(F_ZS + 64 * F_RS <= F_A0 && F_EX + 64 * 132 <= F_LDS, "LDS map");

__device__ __forceinline__ float gelu_tanh(float z) {
  const float k0 = -2.f * 0.7978845608028654f * 1.4426950408889634f, k1 = k0 * 0.044715f;
  const float e = __builtin_amdgcn_exp2f(z * (k0 + k1 * (z * z)));
  return z * __builtin_amdgcn_rcpf(1.f + e);
}
__device__ __forceinline__ float pick4(f32x4 v, int r) { return r == 0 ? v[0] : r == 1 ? v[1] : r == 2 ? v[2] : v[3]; }
template <int N> __device__ __forceinline__ void vm_wait_c() { asm volatile("s_waitcnt vmcnt(%0)" ::"i"(N > 63 ? 63 : N) : "memory"); }
__device__ __forceinline__ u32 f_slot(int kq, int sg) { return (u32)(((sg & 2) ? 0 : F_A0) + (kq * 2 + (sg & 1)) * 8192); }
__device__ __forceinline__ bf16x8 ld_dev16(rsrc_t r, u32 off) { return __builtin_bit_cast(bf16x8, __builtin_amdgcn_raw_buffer_load_b128(r, off, 0, 16)); }
__device__ __forceinline__ void f_lda(const u16* const (&pa)[2], u32 lds0, int kq, int nh, int sg) {
#pragma unroll
  for (int i = 0; i < 4; ++i) glds16(pa[i & 1] + (size_t)(i >> 1) * 16 * L + 64 * sg, lds0 + f_slot(kq, sg) + (4 * nh + i) * 1024);
}
__device__ __forceinline__ void f_ldx(const u16* x1T, u32 lds0, int w, int pi, int sg) {
  const int lane = threadIdx.x & 63;
#pragma unroll
  for (int i = 0; i < 2; ++i) {
    const int d = 128 * sg + 16 * w + 8 * i + (lane >> 3);
    const int fz = (((d >> 1) & 1) | ((d >> 2) & 2)) << 1;
    glds16(x1T + (size_t)d * L + 64 * pi + 8 * ((lane & 7) ^ fz), lds0 + F_X + (128 * sg + 16 * w + 8 * i) * 128);
  }
}
__device__ __forceinline__ void wait_flags(const u32* p, int n) {
  const int lane = threadIdx.x & 63;
  for (u32 spin = 0; spin < (1u << 22); ++spin) {
    const u32 v = __hip_atomic_load((GAS u32*)p + (lane < n ? lane : 0), __ATOMIC_RELAXED, __HIP_MEMORY_SCOPE_AGENT);
    if (__all(v == 1u)) break;
    __builtin_amdgcn_s_sleep(1);
  }
}

__device__ __forceinline__ u32 poll8(const u32* p, int n) {
  const int lane = threadIdx.x & 63;
  const u32 v = __hip_atomic_load((GAS u32*)p + (lane < n ? lane : 0), __ATOMIC_RELAXED, __HIP_MEMORY_SCOPE_AGENT);
  return (u32)__ballot(v == 1u) & 0xffu;
}
__device__ __forceinline__ void need_flag(const u32* p, int n, u32& mask, int k) {
  if (!((mask >> k) & 1u)) {
    for (u32 spin = 0; spin < (1u << 22); ++spin) {
      mask = poll8(p, n);
      if ((mask >> k) & 1u) break;
      __builtin_amdgcn_s_sleep(1);
    }
  }
  asm volatile("" ::: "memory");
}

struct Z1 { f32x4 y[4]; };

template <int ROLE>
__device__ __forceinline__ void f_phase_c(const FArgs& a, char* smem, const int w, const int pi, const int j, const u16* const (&pa)[2], Z1& z1) {
  const int tid = threadIdx.x, lane = tid & 63;
  const int c = lane & 15, g = lane >> 4;
  const u32 lds0 = (u32)(size_t)(const char LAS*)smem;
  const int nt2 = 56 + 7 * j + (w < 7 ? w : 0), nt1 = 7 * j + (w > 0 ? w - 1 : 0);
  const u16* pw2 = a.Wt + (size_t)nt2 * 64 * 128 + lane * 8;
  const u16* pw1 = a.Wt + (size_t)nt1 * 64 * 128 + lane * 8;
  const int q4 = (lane & 15) >> 2, p4 = lane & 3;
  const int drow = 8 * g + q4;
  const int fz0 = (((drow >> 1) & 1) | ((drow >> 2) & 2)) << 1;
  const char LAS* xb = (const char LAS*)smem + F_X + drow * 128 + 8 * (p4 & 1);
  const char LAS* xt[4];
  int tb[4];
#pragma unroll
  for (int s = 0; s < 4; ++s) { tb[s] = (s + w) & 3; xt[s] = xb + 16 * ((2 * tb[s] + (p4 >> 1)) ^ fz0); }
  auto xfrag = [&](int s, int ks) -> bf16x8 {
    const char LAS* p = xt[s] + ks * 4096;
    const s16x4 lo = tr_read(p), hi = tr_read(p + 512);
    const u32x2 a0 = __builtin_bit_cast(u32x2, lo), a1 = __builtin_bit_cast(u32x2, hi);
    return __builtin_bit_cast(bf16x8, u32x4{a0[0], a0[1], a1[0], a1[1]});
  };
  f32x4 acc[4];
#pragma unroll
  for (int s = 0; s < 4; ++s) acc[s] = f32x4{0, 0, 0, 0};
  bf16x8 W1[16];
  f32x4 u2, v2, u1, v1;
  float* xs2 = (float*)(smem + F_ST);
  if constexpr (ROLE < 2) {
    bf16x8 W2[16];
#pragma unroll
    for (int ks = 0; ks < 4; ++ks) W2[ks] = *(const bf16x8*)(pw2 + 4 * ks * 128);
    SCHED_FENCE();
    f_ldx(a.x1T, lds0, w, pi, 1);
#pragma unroll
    for (int ks = 4; ks < 8; ++ks) W2[ks] = *(const bf16x8*)(pw2 + 4 * ks * 128);
    SCHED_FENCE();
    VM_WAIT(10);
    __builtin_amdgcn_s_barrier();
    SCHED_FENCE();
    STAMP(2, 1);
    f32x4 sq = {0, 0, 0, 0};
    bf16x8 X[2][4];
#pragma unroll
    for (int s = 0; s < 4; ++s) X[0][s] = xfrag(s, 0);
#pragma unroll
    for (int ks = 0; ks < 16; ++ks) {
      if (ks == 3) { VM_WAIT(9); __builtin_amdgcn_s_barrier(); }
      if (ks == 7) { VM_WAIT(11); __builtin_amdgcn_s_barrier(); }
      if (ks == 11) { VM_WAIT(6); __builtin_amdgcn_s_barrier(); }
      if (ks + 1 < 16) {
#pragma unroll
        for (int s = 0; s < 4; ++s) X[(ks + 1) & 1][s] = xfrag(s, ks + 1);
      }
      if (ks == 0) f_ldx(a.x1T, lds0, w, pi, 2);
      if (ks == 4) f_ldx(a.x1T, lds0, w, pi, 3);
      if (ks < 8) W2[8 + ks] = *(const bf16x8*)(pw2 + 4 * (8 + ks) * 128);
      if (ks == 5) { u2 = *(const f32x4*)(a.uv + 16 * nt2 + 4 * g); v2 = *(const f32x4*)(a.uv + F + 16 * nt2 + 4 * g); }
      SCHED_FENCE();
#pragma unroll
      for (int s = 0; s < 4; ++s) acc[s] = __builtin_amdgcn_mfma_f32_16x16x32_bf16(W2[ks], X[ks & 1][s], acc[s], 0, 0, 0);
      sq = __builtin_amdgcn_mfma_f32_16x16x32_bf16(X[ks & 1][0], X[ks & 1][0], sq, 0, 0, 0);
      SCHED_FENCE();
    }
    if constexpr (ROLE == 1) {
      u1 = *(const f32x4*)(a.uv + 16 * nt1 + 4 * g); v1 = *(const f32x4*)(a.uv + F + 16 * nt1 + 4 * g);
      SCHED_FENCE();
    }
    if (w < 4 && (c >> 2) == g) xs2[64 + 16 * w + c] = pick4(sq, c & 3);
  } else {
    f_ldx(a.x1T, lds0, w, pi, 1);
    VM_WAIT(2);
    __builtin_amdgcn_s_barrier();
    SCHED_FENCE();
    STAMP(2, 1);
    const u32x4 ones = {0x3f803f80u, 0x3f803f80u, 0x3f803f80u, 0x3f803f80u};
    bf16x8 X[2][4];
#pragma unroll
    for (int s = 0; s < 4; ++s) X[0][s] = xfrag(s, 0);
#pragma unroll
    for (int ks = 0; ks < 16; ++ks) {
      if (ks == 3) { VM_WAIT(2); __builtin_amdgcn_s_barrier(); }
      if (ks == 7) { VM_WAIT(2); __builtin_amdgcn_s_barrier(); }
      if (ks == 11) { VM_WAIT(6); __builtin_amdgcn_s_barrier(); }
      if (ks + 1 < 16) {
#pragma unroll
        for (int s = 0; s < 4; ++s) X[(ks + 1) & 1][s] = xfrag(s, ks + 1);
      }
      if (ks == 0) f_ldx(a.x1T, lds0, w, pi, 2);
      if (ks == 4) f_ldx(a.x1T, lds0, w, pi, 3);
      if (ks >= 8) { W1[2 * ks - 16] = *(const bf16x8*)(pw1 + 4 * (2 * ks - 16) * 128); W1[2 * ks - 15] = *(const bf16x8*)(pw1 + 4 * (2 * ks - 15) * 128); }
      SCHED_FENCE();
#pragma unroll
      for (int s = 0; s < 4; ++s) acc[s] = __builtin_amdgcn_mfma_f32_16x16x32_bf16(__builtin_bit_cast(bf16x8, ones), X[ks & 1][s], acc[s], 0, 0, 0);
      SCHED_FENCE();
    }
    u1 = *(const f32x4*)(a.uv + 16 * nt1 + 4 * g); v1 = *(const f32x4*)(a.uv + F + 16 * nt1 + 4 * g);
    SCHED_FENCE();
    if (g == 0) {
#pragma unroll
      for (int s = 0; s < 4; ++s) xs2[16 * tb[s] + c] = acc[s][0];
    }
  }
  STAMP(2, 2);
  __syncthreads();
  STAMP(2, 3);
  float mu[4], ri[4];
  {
    const float mean = xs2[lane] * (1.f / D);
    const float var = fmaxf(xs2[64 + lane] * (1.f / D) - mean * mean, 0.f);
    f32x2* mr = (f32x2*)(smem + F_MR + w * 512);
    mr[lane] = f32x2{rbf(mean), __builtin_amdgcn_rcpf(rbf(__builtin_amdgcn_sqrtf(rbf(rbf(var) + 1e-5f))))};
#pragma unroll
    for (int s = 0; s < 4; ++s) { const f32x2 v = mr[16 * tb[s] + c]; mu[s] = v[0]; ri[s] = v[1]; }
  }
  u32x2 z2p[4];
  if constexpr (ROLE < 2) {
    float* ps = (float*)(smem + F_PS) + w * 128;
    const s16x4 ones4 = {0x3f80, 0x3f80, 0x3f80, 0x3f80};
#pragma unroll
    for (int s = 0; s < 4; ++s) {
      if constexpr (ROLE == 1) {
#pragma unroll
        for (int ks = 4 * s; ks < 4 * s + 4; ++ks) W1[ks] = *(const bf16x8*)(pw1 + 4 * ks * 128);
        SCHED_FENCE();
      }
      float z[4];
#pragma unroll
      for (int r = 0; r < 4; ++r) z[r] = gelu_tanh(ri[s] * (acc[s][r] - mu[s] * u2[r]) + v2[r]);
      z2p[s] = u32x2{pk_bf16(z[0], z[1]), pk_bf16(z[2], z[3])};
      const s16x4 zz = __builtin_bit_cast(s16x4, z2p[s]);
      const f32x4 zero = {0, 0, 0, 0};
      const f32x4 s1 = __builtin_amdgcn_mfma_f32_16x16x16bf16_1k(ones4, zz, zero, 0, 0, 0);
      const f32x4 s2 = __builtin_amdgcn_mfma_f32_16x16x16bf16_1k(zz, zz, zero, 0, 0, 0);
      if ((c >> 2) == g) *(f32x2*)(ps + (16 * tb[s] + c) * 2) = f32x2{s1[0], pick4(s2, c & 3)};
      SCHED_FENCE();
    }
  }
  if constexpr (ROLE > 0) { f_lda(pa, lds0, w & 3, w >> 2, 0); f_lda(pa, lds0, w & 3, w >> 2, 1); }
  STAMP(2, 4);
  __syncthreads();
  STAMP(2, 5);
  if constexpr (ROLE == 0) {
    const float* ps = (const float*)(smem + F_PS);
    float s1 = 0, s2 = 0;
#pragma unroll
    for (int k = 0; k < 7; ++k) { const f32x2 v = *(const f32x2*)(ps + k * 128 + lane * 2); s1 += v[0]; s2 += v[1]; }
    const rsrc_t rg = make_rsrc(a.gran, 256 * 1024);
    const u32 goff = (u32)(pi * 8 * 64 + lane) * 16;
    wt_store16(rg, goff + (u32)j * 1024, u32x4{__float_as_uint(s1), 1u, __float_as_uint(s2), 1u});
    float t1 = 0, t2 = 0;
    for (u32 spin = 0; spin < (1u << 22); ++spin) {
      u32x4 x[8];
#pragma unroll
      for (int k = 0; k < 8; ++k) x[k] = __builtin_amdgcn_raw_buffer_load_b128(rg, goff + k * 1024, 0, 16);
      bool ok = true; t1 = 0; t2 = 0;
#pragma unroll
      for (int k = 0; k < 8; ++k) {
        ok = ok && x[k][1] == 1u && x[k][3] == 1u;
        t1 += __uint_as_float(x[k][0]); t2 += __uint_as_float(x[k][2]);
      }
      if (__all(ok)) break;
      __builtin_amdgcn_s_sleep(1);
    }
    const float mean = t1 * (1.f / FH);
    const float var = fmaxf(t2 * (1.f / FH) - mean * mean, 0.f);
    *(f32x2*)(smem + F_FS + lane * 8) = f32x2{rbf(mean), __builtin_amdgcn_rcpf(rbf(__builtin_amdgcn_sqrtf(rbf(rbf(var) + 1e-5f))))};
    f_lda(pa, lds0, w & 3, w >> 2, 0);
    f_lda(pa, lds0, w & 3, w >> 2, 1);
  } else {
#pragma unroll
    for (int s = 0; s < 4; ++s) acc[s] = f32x4{0, 0, 0, 0};
    bf16x8 X[2][4];
#pragma unroll
    for (int s = 0; s < 4; ++s) X[0][s] = xfrag(s, 0);
#pragma unroll
    for (int ks = 0; ks < 16; ++ks) {
      if (ks + 1 < 16) {
#pragma unroll
        for (int s = 0; s < 4; ++s) X[(ks + 1) & 1][s] = xfrag(s, ks + 1);
      }
      SCHED_FENCE();
#pragma unroll
      for (int s = 0; s < 4; ++s) acc[s] = __builtin_amdgcn_mfma_f32_16x16x32_bf16(W1[ks], X[ks & 1][s], acc[s], 0, 0, 0);
      SCHED_FENCE();
    }
  }
  STAMP(2, 6);
  __syncthreads();
  STAMP(2, 7);
  if constexpr (ROLE < 2) {
#pragma unroll
    for (int s = 0; s < 4; ++s) {
      const f32x2 fs = *(const f32x2*)(smem + F_FS + (16 * tb[s] + c) * 8);
      const float a0 = (bf_lo(z2p[s][0]) - fs[0]) * fs[1], a1 = (bf_hi(z2p[s][0]) - fs[0]) * fs[1];
      const float a2 = (bf_lo(z2p[s][1]) - fs[0]) * fs[1], a3 = (bf_hi(z2p[s][1]) - fs[0]) * fs[1];
      *(u32x2*)(smem + F_ZS + (16 * tb[s] + c) * F_RS + (16 * w + 4 * g) * 2) = u32x2{pk_bf16(a0, a1), pk_bf16(a2, a3)};
    }
#pragma unroll
    for (int kk = 0; kk < 2; ++kk) {
      const char LAS* p = (const char LAS*)smem + F_ZS + (32 * kk + 8 * g + q4) * F_RS + (16 * w + 4 * p4) * 2;
      const s16x4 lo = tr_read(p), hi = tr_read(p + 4 * F_RS);
      const u32x2 a0 = __builtin_bit_cast(u32x2, lo), a1 = __builtin_bit_cast(u32x2, hi);
      wt_store16(make_rsrc(a.z2t, L * FH * 2), (u32)((((7 * j + w) * 256 + 8 * pi + 4 * kk) * 64 + lane * 4) * 4), u32x4{a0[0], a0[1], a1[0], a1[1]});
    }
  }
  VM_WAIT(0);
  __syncthreads();
  if (tid == 0) __hip_atomic_store((GAS u32*)a.flags + j * 32 + pi, 1u, __ATOMIC_RELAXED, __HIP_MEMORY_SCOPE_AGENT);
  STAMP(2, 8);
  f_lda(pa, lds0, w & 3, w >> 2, 2);
  f_lda(pa, lds0, w & 3, w >> 2, 3);
#pragma unroll
  for (int s = 0; s < 4; ++s)
#pragma unroll
    for (int r = 0; r < 4; ++r) z1.y[s][r] = ROLE > 0 ? ri[s] * (acc[s][r] - mu[s] * u1[r]) + v1[r] : 0.f;
  STAMP(2, 9);
}

template <int NT> constexpr int fd_glds(int sg) { return (sg == 2 || sg == 4) ? 8 : 0; }
template <int NT> constexpr int fd_bld(int sg) { return sg < 0 ? 0 : (2 * sg + F_BD < 16 ? NT : 0) + (2 * sg + 1 + F_BD < 16 ? NT : 0); }
template <int NT> constexpr int fd_ops(int sg) { return fd_glds<NT>(sg) + fd_bld<NT>(sg); }

template <int NT, int SG>
__device__ __forceinline__ void fd_stage(f32x4 (&acc)[4][NT], bf16x8 (&Bf)[F_BR][NT], bf16x8 (&Af)[2][4], const rsrc_t rb, u32 boff,
                                         const u16* const (&pa)[2], u32 lds0, const char* const (&ap)[2][2][4], int kq, int nh,
                                         const u32* fl, u32& mask, const Z1& z1, char* zdst, int w) {
  if constexpr ((SG & 1) == 0 && SG > 0) {
    if constexpr (SG == 2) vm_wait_c<F_BD * NT + fd_ops<NT>(0) + fd_ops<NT>(1)>();
    else vm_wait_c<fd_bld<NT>(SG - 2) + fd_ops<NT>(SG - 1)>();
    __builtin_amdgcn_s_barrier();
    if constexpr (fd_glds<NT>(SG) > 0) { f_lda(pa, lds0, kq, nh, SG + 2); f_lda(pa, lds0, kq, nh, SG + 3); }
#pragma unroll
    for (int m = 0; m < 4; ++m) Af[0][m] = *(const bf16x8*)(ap[(SG >> 1) & 1][0][m]);
  }
#pragma unroll
  for (int kk = 0; kk < 2; ++kk) {
    constexpr int ks0 = 2 * SG;
    if (kk == 0 || (SG & 1) == 0) {
#pragma unroll
      for (int m = 0; m < 4; ++m) Af[(kk + 1) & 1][m] = *(const bf16x8*)(ap[(SG >> 1) & 1][(kk + 1) & 1][m] + (kk == 0 ? (SG & 1) : 1) * 8192);
    }
    SCHED_FENCE();
#pragma unroll
    for (int n = 0; n < NT; ++n) {
#pragma unroll
      for (int m = 0; m < 4; ++m) acc[m][n] = __builtin_amdgcn_mfma_f32_16x16x32_bf16(Bf[(ks0 + kk) % F_BR][n], Af[kk][m], acc[m][n], 0, 0, 0);
      SCHED_FENCE();
      if (ks0 + kk + F_BD < 16) {
        if (n == 0 && ((ks0 + kk + F_BD) & 1) == 0) need_flag(fl, 8, mask, (ks0 + kk + F_BD) >> 1);
        Bf[(ks0 + kk + F_BD) % F_BR][n] = ld_dev16(rb, boff + (u32)((n * 256 + 4 * (ks0 + kk + F_BD)) * 256));
      }
      if constexpr (SG < 4) {
        if (kk == 0 && n == 1 && w > 0) {
          float z[4];
#pragma unroll
          for (int r = 0; r < 4; ++r) z[r] = gelu_tanh(z1.y[SG][r]);
          *(u32x2*)(zdst + ((SG + w) & 3) * 16 * F_RS) = u32x2{pk_bf16(z[0], z[1]), pk_bf16(z[2], z[3])};
        }
      }
      SCHED_FENCE();
    }
  }
}

template <int NT>
__device__ __forceinline__ void f_phase_d(const FArgs& a, char* smem, const int w, const int rt, const int ct, const u16* const (&pa)[2], const Z1& z1) {
  const int tid = threadIdx.x, lane = tid & 63;
  const int kq = w & 3, nh = w >> 2, n0 = nh * 4;
  const int c = lane & 15, g = lane >> 4;
  const u32 lds0 = (u32)(size_t)(const char LAS*)smem;
  const rsrc_t rb = make_rsrc(a.z2t, L * FH * 2);
  const u32 boff = (u32)((((7 * ct + n0) * 256 + 64 * kq) * 64 + lane * 4) * 4);
  const u32* fl = a.flags + ct * 32 + 8 * kq;
  u32 mask = poll8(fl, 8);
  bf16x8 Bf[F_BR][NT], Af[2][4];
#pragma unroll
  for (int ks = 0; ks < F_BD; ++ks) {
    if ((ks & 1) == 0) need_flag(fl, 8, mask, ks >> 1);
#pragma unroll
    for (int n = 0; n < NT; ++n) Bf[ks][n] = ld_dev16(rb, boff + (u32)((n * 256 + 4 * ks) * 256));
  }
  SCHED_FENCE();
  f32x4 acc[4][NT];
#pragma unroll
  for (int m = 0; m < 4; ++m)
#pragma unroll
    for (int n = 0; n < NT; ++n) acc[m][n] = f32x4{0, 0, 0, 0};
  const int asw = (c >> 1) & 7;
  const char* ap[2][2][4];
#pragma unroll
  for (int hi = 0; hi < 2; ++hi)
#pragma unroll
    for (int m = 0; m < 4; ++m) {
      const char* base = smem + f_slot(kq, 2 * hi) + ((m + kq) & 3) * 2048 + c * 128;
      ap[hi][0][m] = base + 16 * (g ^ asw); ap[hi][1][m] = base + 16 * ((4 + g) ^ asw);
    }
#pragma unroll
  for (int m = 0; m < 4; ++m) Af[0][m] = *(const bf16x8*)(ap[0][0][m]);
  char* zdst = smem + F_ZS + c * F_RS + (16 * (w - 1) + 4 * g) * 2;
  STAMP(3, 4);
  fd_stage<NT, 0>(acc, Bf, Af, rb, boff, pa, lds0, ap, kq, nh, fl, mask, z1, zdst, w);
  fd_stage<NT, 1>(acc, Bf, Af, rb, boff, pa, lds0, ap, kq, nh, fl, mask, z1, zdst, w);
  STAMP(3, 5);
  fd_stage<NT, 2>(acc, Bf, Af, rb, boff, pa, lds0, ap, kq, nh, fl, mask, z1, zdst, w);
  fd_stage<NT, 3>(acc, Bf, Af, rb, boff, pa, lds0, ap, kq, nh, fl, mask, z1, zdst, w);
  STAMP(3, 6);
  fd_stage<NT, 4>(acc, Bf, Af, rb, boff, pa, lds0, ap, kq, nh, fl, mask, z1, zdst, w);
  fd_stage<NT, 5>(acc, Bf, Af, rb, boff, pa, lds0, ap, kq, nh, fl, mask, z1, zdst, w);
  STAMP(3, 7);
  fd_stage<NT, 6>(acc, Bf, Af, rb, boff, pa, lds0, ap, kq, nh, fl, mask, z1, zdst, w);
  const float bs = bf1(a.bsp[64 * rt + 16 * kq + c]);
  fd_stage<NT, 7>(acc, Bf, Af, rb, boff, pa, lds0, ap, kq, nh, fl, mask, z1, zdst, w);
  STAMP(3, 1);
  __syncthreads();
#pragma unroll
  for (int m = 1; m < 4; ++m) {
    const int o = (m + kq) & 3;
    char* dst = smem + (o < 3 ? o * 21504 : F_A0) + ((m - 1) * 7 * 64 + n0 * 64 + lane) * 16;
#pragma unroll
    for (int n = 0; n < NT; ++n) *(f32x4*)(dst + n * 1024) = acc[m][n];
  }
  __syncthreads();
  STAMP(3, 2);
  const char* src = smem + (kq < 3 ? kq * 21504 : F_A0) + (n0 * 64 + lane) * 16;
  const char* zs = smem + F_ZS + (16 * kq + c) * F_RS + (16 * n0 + 4 * g) * 2;
  const rsrc_t rp = make_rsrc(a.Pt, L * FH * 2);
  u32x2 pv[NT];
#pragma unroll
  for (int n = 0; n < NT; ++n) {
    const f32x4 p0 = *(const f32x4*)(src + n * 1024), p1 = *(const f32x4*)(src + (7 + n) * 1024), p2 = *(const f32x4*)(src + (14 + n) * 1024);
    const u32x2 zz = *(const u32x2*)(zs + n * 32);
    float gg[4];
#pragma unroll
    for (int e = 0; e < 4; ++e) gg[e] = ((acc[0][n][e] + p0[e]) + (p1[e] + p2[e])) + bs;
    pv[n] = u32x2{pk_bf16(bf_lo(zz[0]) * gg[0], bf_hi(zz[0]) * gg[1]), pk_bf16(bf_lo(zz[1]) * gg[2], bf_hi(zz[1]) * gg[3])};
    if (n & 1) wt_store16(rp, (u32)((((4 * rt + kq) * 112 + 14 * ct + 2 * (n0 + n - 1 + (g & 1)) + (g >> 1)) * 16 + c) * 16), pair16(pv[n - 1], pv[n]));
  }
  if constexpr (NT & 1) {
    const u32x4 v = pair16(pv[NT - 1], pv[NT - 1]);
    if ((g & 1) == 0) wt_store16(rp, (u32)((((4 * rt + kq) * 112 + 14 * ct + 2 * (n0 + NT - 1) + (g >> 1)) * 16 + c) * 16), v);
  }
  STAMP(3, 3);
}

__device__ __forceinline__ void f_phase_e(const FArgs& a, char* smem, const int w, const int rt, const int dt) {
  const int tid = threadIdx.x, lane = tid & 63;
  const int kq = w & 3, mh = w >> 2;
  const int c = lane & 15, g = lane >> 4;
  const u32 lds0 = (u32)(size_t)(const char LAS*)smem;
  VM_WAIT(0);
  __syncthreads();
  if (tid == 0) __hip_atomic_store((GAS u32*)a.flags + 256 + rt * 32 + dt, 1u, __ATOMIC_RELAXED, __HIP_MEMORY_SCOPE_AGENT);
  STAMP(4, 5);
#pragma unroll
  for (int k = 0; k < 7; ++k)
#pragma unroll
    for (int nn = 0; nn < 2; ++nn) {
      const int n = 2 * mh + nn;
      glds16(a.Wot + ((size_t)(4 * dt + n) * 112 + 4 * (7 * kq + k)) * 128 + lane * 8, lds0 + (n * 28 + 7 * kq + k) * 1024);
    }
  const u32x4 xr = *(const u32x4*)(a.x1T + (size_t)(64 * dt + (tid >> 3)) * L + 64 * rt + 8 * (tid & 7));
  const int d8 = lane & 7;
  const f32x4 gb0 = *(const f32x4*)(a.mod + 2560 + 64 * dt + 8 * d8), gb1 = *(const f32x4*)(a.mod + 2560 + 64 * dt + 8 * d8 + 4);
  SCHED_FENCE();
  {
    const u32* fl = a.flags + 256 + rt * 32 + 2 * kq;
    u32 mask = poll8(fl, 2);
    need_flag(fl, 2, mask, 0); need_flag(fl, 2, mask, 1);
  }
  STAMP(4, 4);
  const rsrc_t rp = make_rsrc(a.Pt, L * FH * 2);
  bf16x8 Pf[7][2];
#pragma unroll
  for (int k = 0; k < 7; ++k)
#pragma unroll
    for (int m = 0; m < 2; ++m) Pf[k][m] = ld_dev16(rp, (u32)((((4 * rt + 2 * mh + m) * 112 + 4 * (7 * kq + k)) * 64 + lane * 4) * 4));
  {
    u32* p = (u32*)(smem + F_EX + (tid >> 3) * 132 + (tid & 7) * 16);
    p[0] = xr[0]; p[1] = xr[1]; p[2] = xr[2]; p[3] = xr[3];
  }
  vm_wait_c<14>();
  __builtin_amdgcn_s_barrier();
  SCHED_FENCE();
  f32x4 acc[2][4];
#pragma unroll
  for (int m = 0; m < 2; ++m)
#pragma unroll
    for (int n = 0; n < 4; ++n) acc[m][n] = f32x4{0, 0, 0, 0};
  const char* wp = smem + (7 * kq) * 1024 + lane * 16;
#pragma unroll
  for (int k = 0; k < 7; ++k) {
    bf16x8 Wf[4];
#pragma unroll
    for (int n = 0; n < 4; ++n) Wf[n] = *(const bf16x8*)(wp + (n * 28 + k) * 1024);
#pragma unroll
    for (int n = 0; n < 4; ++n)
#pragma unroll
      for (int m = 0; m < 2; ++m) acc[m][n] = __builtin_amdgcn_mfma_f32_16x16x32_bf16(Wf[n], Pf[k][m], acc[m][n], 0, 0, 0);
  }
  STAMP(4, 1);
  __syncthreads();
#pragma unroll
  for (int m = 0; m < 2; ++m)
#pragma unroll
    for (int n = 0; n < 4; ++n) *(f32x4*)(smem + (kq * 16 + (2 * mh + m) * 4 + n) * F_ETS + g * 288 + c * 16) = acc[m][n];
  __syncthreads();
  STAMP(4, 2);
  {
    const int t = 8 * w + (lane >> 3);
    float s[8];
#pragma unroll
    for (int hh = 0; hh < 2; ++hh) {
      const char* p = smem + ((t >> 4) * 4 + (d8 >> 1)) * F_ETS + (2 * (d8 & 1) + hh) * 288 + (t & 15) * 16;
      const f32x4 p0 = *(const f32x4*)p, p1 = *(const f32x4*)(p + 16 * F_ETS), p2 = *(const f32x4*)(p + 32 * F_ETS), p3 = *(const f32x4*)(p + 48 * F_ETS);
#pragma unroll
      for (int e = 0; e < 4; ++e) s[4 * hh + e] = (p0[e] + p1[e]) + (p2[e] + p3[e]);
    }
    float o[8];
#pragma unroll
    for (int e = 0; e < 8; ++e) {
      const float x1 = bf1(*(const u16*)(smem + F_EX + (8 * d8 + e) * 132 + t * 2));
      o[e] = x1 + (e < 4 ? gb0[e] : gb1[e - 4]) * s[e];
    }
    wt_store16(make_rsrc(a.out, L * D * 2), (u32)(((64 * rt + t) * D + 64 * dt + 8 * d8) * 2), u32x4{pk_bf16(o[0], o[1]), pk_bf16(o[2], o[3]), pk_bf16(o[4], o[5]), pk_bf16(o[6], o[7])});
  }
  STAMP(4, 3);
}

__global__ __launch_bounds__(512, 2) void kF(FArgs a) {
  extern __shared__ __attribute__((aligned(16))) char smem[];
  const int wg = blockIdx.x, lane = threadIdx.x & 63;
  const int w = __builtin_amdgcn_readfirstlane(threadIdx.x >> 6);
  const int xc = wg & 7, ii = wg >> 3;
  const int pi = (xc >> 1) * 8 + (ii & 7), j = (xc & 1) * 4 + (ii >> 3);
  STAMP(2, 0);
  f_ldx(a.x1T, (u32)(size_t)(const char LAS*)smem, w, pi, 0);
  const u16* pa[2];
#pragma unroll
  for (int par = 0; par < 2; ++par) {
    const int row = 32 * (w >> 2) + 8 * par + (lane >> 3);
    pa[par] = a.wsp + (size_t)(64 * pi + row) * L + 512 * (w & 3) + 8 * ((lane & 7) ^ ((row >> 1) & 7));
  }
  Z1 z1;
  if (w == 0) f_phase_c<0>(a, smem, w, pi, j, pa, z1);
  else if (w < 7) f_phase_c<1>(a, smem, w, pi, j, pa, z1);
  else f_phase_c<2>(a, smem, w, pi, j, pa, z1);
  STAMP(3, 0);
  if (w < 4) f_phase_d<4>(a, smem, w, pi, j, pa, z1);
  else f_phase_d<3>(a, smem, w, pi, j, pa, z1);
  STAMP(4, 0);
  f_phase_e(a, smem, w, pi, j);
}
#define KMASK(b)
extern "C" void kernel_launch(void* const* d_in, const int* in_sizes, int n_in, void* d_out, int out_size,
                              void* d_ws, size_t ws_size, hipStream_t stream) {
  const u16* x = (const u16*)d_in[0];
  char* ws = (char*)d_ws;
  float* mod = (float*)(ws + 0);
  u32* stats1 = (u32*)(ws + 16384);
  float* uv = (float*)(ws + 32768);
  u64* gran = (u64*)(ws + 65536);
  u32* flags = (u32*)(ws + 65536 + 262144);
  u16* xT = (u16*)(ws + (1 << 20));
  u16* Krev = (u16*)(ws + (3 << 20));
  u16* x1T = xT;
  u16* Wt = (u16*)(ws + (7 << 20));
  u16* Wot = (u16*)(ws + (9 << 20));
  u16* Pt = (u16*)(ws + (10 << 20));
  u16* z2t = Krev;
  AArgs aa;
  aa.x = x; aa.Are = (const u16*)d_in[1]; aa.Aim = (const u16*)d_in[2]; aa.Bre = (const u16*)d_in[3]; aa.Bim = (const u16*)d_in[4];
  aa.Cre = (const u16*)d_in[5]; aa.Cim = (const u16*)d_in[6]; aa.logdt = (const u16*)d_in[7]; aa.c = (const u16*)d_in[14];
  aa.wmod = (const u16*)d_in[15]; aa.win = (const u16*)d_in[10]; aa.wout = (const u16*)d_in[13];
  aa.mod = mod; aa.stats1 = stats1; aa.xT = xT; aa.Krev = Krev; aa.gran = (u32x4*)gran;
  (void)hipFuncSetAttribute((const void*)kA, hipFuncAttributeMaxDynamicSharedMemorySize, A_LDS);
  KMASK(1) kA<<<256, 768, A_LDS, stream>>>(aa);
  BArgs ba;
  ba.xT = xT; ba.Krev = Krev; ba.Dskip = (const u16*)d_in[8]; ba.win = (const u16*)d_in[10]; ba.wout = (const u16*)d_in[13];
  ba.wsp = (const u16*)d_in[11]; ba.stats1 = stats1; ba.mod = mod; ba.x1T = x1T; ba.Wt = Wt; ba.Wot = Wot; ba.uv = uv;
  (void)hipFuncSetAttribute((const void*)kB, hipFuncAttributeMaxDynamicSharedMemorySize, B_LDS);
  KMASK(2) kB<<<256, 768, B_LDS, stream>>>(ba);
  FArgs fa;
  fa.x1T = x1T; fa.Wt = Wt; fa.wsp = (const u16*)d_in[11]; fa.bsp = (const u16*)d_in[12]; fa.Wot = Wot; fa.uv = uv; fa.mod = mod;
  fa.z2t = z2t; fa.Pt = Pt; fa.out = (u16*)d_out; fa.gran = gran; fa.flags = flags;
  (void)hipFuncSetAttribute((const void*)kF, hipFuncAttributeMaxDynamicSharedMemorySize, F_LDS);
  KMASK(4) kF<<<256, 512, F_LDS, stream>>>(fa);
}
```

```cpp
#include <hip/hip_runtime.h>
#include <hip/hip_bf16.h>
#include <stdint.h>

typedef unsigned short u16;
typedef unsigned int u32;
typedef unsigned long long u64;
typedef __bf16 bf16x8 __attribute__((ext_vector_type(8)));
typedef __bf16 bf16x2 __attribute__((ext_vector_type(2)));
typedef short s16x4 __attribute__((ext_vector_type(4)));
typedef float f32x2 __attribute__((ext_vector_type(2)));
typedef float f32x4 __attribute__((ext_vector_type(4)));
typedef u32 u32x2 __attribute__((ext_vector_type(2)));
typedef u32 u32x4 __attribute__((ext_vector_type(4)));
#define LAS __attribute__((address_space(3)))
#define GAS __attribute__((address_space(1)))

constexpr int L = 2048, D = 512, NS = 128, F = 1792, FH = 896;

__device__ __forceinline__ float bf_lo(u32 w) { return __uint_as_float(w << 16); }
__device__ __forceinline__ float bf_hi(u32 w) { return __uint_as_float(w & 0xffff0000u); }
__device__ __forceinline__ float bf1(u16 h) { return __uint_as_float(((u32)h) << 16); }
__device__ __forceinline__ u32 pk_bf16(float lo, float hi) {
  f32x2 a = {lo, hi};
  bf16x2 p = __builtin_convertvector(a, bf16x2);
  return __builtin_bit_cast(u32, p);
}
__device__ __forceinline__ float rbf(float x) { return bf_lo(pk_bf16(x, x)); }

template <int CTRL, int ROWMASK = 0xf>
__device__ __forceinline__ float dpp_add(float x) {
  int y = __builtin_amdgcn_update_dpp(0, __float_as_int(x), CTRL, ROWMASK, 0xf, true);
  return x + __int_as_float(y);
}
__device__ __forceinline__ float row16_sum(float x) {
  x = dpp_add<0xB1>(x);
  x = dpp_add<0x4E>(x);
  x = dpp_add<0x141>(x);
  x = dpp_add<0x140>(x);
  return x;
}
__device__ __forceinline__ float wave_sum(float x) {
  x = row16_sum(x);
  float a = __int_as_float(__builtin_amdgcn_readlane(__float_as_int(x), 0));
  float b = __int_as_float(__builtin_amdgcn_readlane(__float_as_int(x), 16));
  float c = __int_as_float(__builtin_amdgcn_readlane(__float_as_int(x), 32));
  float d = __int_as_float(__builtin_amdgcn_readlane(__float_as_int(x), 48));
  return (a + b) + (c + d);
}
__device__ __forceinline__ s16x4 tr_read(const char LAS* p) {
  return __builtin_amdgcn_ds_read_tr16_b64_v4i16((s16x4 LAS*)p);
}

#define STAMP(kern, i)
__device__ __forceinline__ void glds16(const void* gsrc, u32 lds_dst) {
  u32 keep;
  asm volatile("s_mov_b32 %0, m0\n\ts_mov_b32 m0, %2\n\ts_nop 0\n\tglobal_load_lds_dwordx4 %1, off\n\ts_mov_b32 m0, %0"
               : "=&s"(keep) : "v"(gsrc), "s"(lds_dst) : "memory");
}
#define SCHED_FENCE() __builtin_amdgcn_sched_barrier(0)
#define VM_WAIT(n) asm volatile("s_waitcnt vmcnt(" #n ")" ::: "memory")
__device__ __forceinline__ float xor16_sum(float x) {
  const u32 v = __float_as_uint(x);
  auto r = __builtin_amdgcn_permlane16_swap(v, v, false, false);
  return __uint_as_float(r[0]) + __uint_as_float(r[1]);
}
__device__ __forceinline__ float xor32_sum(float x) {
  const u32 v = __float_as_uint(x);
  auto r = __builtin_amdgcn_permlane32_swap(v, v, false, false);
  return __uint_as_float(r[0]) + __uint_as_float(r[1]);
}
__device__ __forceinline__ float xor4_8_sum(float x) {
  x = x + __int_as_float(__builtin_amdgcn_update_dpp(0, __float_as_int(x), 0x124, 0xf, 0xf, true));
  x = x + __int_as_float(__builtin_amdgcn_update_dpp(0, __float_as_int(x), 0x128, 0xf, 0xf, true));
  return x;
}
__device__ __forceinline__ void lds_rendezvous(u32 LAS* cnt, u32 target) {
  asm volatile("s_waitcnt lgkmcnt(0)" ::: "memory");
  if ((threadIdx.x & 63) == 0) __hip_atomic_fetch_add(cnt, 1u, __ATOMIC_RELAXED, __HIP_MEMORY_SCOPE_WORKGROUP);
  while (__hip_atomic_load(cnt, __ATOMIC_RELAXED, __HIP_MEMORY_SCOPE_WORKGROUP) < target) __builtin_amdgcn_s_sleep(1);
  asm volatile("" ::: "memory");
}

typedef __amdgpu_buffer_rsrc_t rsrc_t;
__device__ __forceinline__ rsrc_t make_rsrc(const void* base, u32 bytes) {
  return __builtin_amdgcn_make_buffer_rsrc(const_cast<void*>(base), 0, bytes, 0x00020000);
}
__device__ __forceinline__ void wt_store16(rsrc_t r, u32 byte_off, u32x4 v) { __builtin_amdgcn_raw_buffer_store_b128(v, r, byte_off, 0, 16); }
__device__ __forceinline__ u32x4 pair16(u32x2 a, u32x2 b) {
  const auto r0 = __builtin_amdgcn_permlane16_swap(a[0], b[0], false, false);
  const auto r1 = __builtin_amdgcn_permlane16_swap(a[1], b[1], false, false);
  return u32x4{r0[0], r1[0], r0[1], r1[1]};
}

__device__ __forceinline__ float mod_get(const float* mp, int i) { return rbf((mp[i] + mp[3072 + i]) + (mp[6144 + i] + mp[9216 + i])); }
__device__ __forceinline__ f32x4 mod_get4(const float* mp, int i) {
  const f32x4 a = *(const f32x4*)(mp + i), b = *(const f32x4*)(mp + 3072 + i), c = *(const f32x4*)(mp + 6144 + i), d = *(const f32x4*)(mp + 9216 + i);
  f32x4 r;
#pragma unroll
  for (int e = 0; e < 4; ++e) r[e] = rbf((a[e] + b[e]) + (c[e] + d[e]));
  return r;
}
constexpr int CODE_LANES = 92;
struct AArgs {
  const u16 *x, *Are, *Aim, *Bre, *Bim, *Cre, *Cim, *logdt, *c, *wmod, *win, *wout;
  float* mod; u32* stats1; u16* xT; u16* Krev; u32x4* gran;
};
constexpr int A_RS = 528;
constexpr int A_TAB = 0;
constexpr int A_KC = A_TAB + 2 * 64 * A_RS;
constexpr int A_TILE = A_KC + 2 * 768 * 4;
constexpr int A_RED = A_TILE + 8 * 1088;
constexpr int A_CNT = A_RED + 4 * 64 * 4;
constexpr int A_LDS = A_CNT + 16;

struct KIdx { u16 v[2048]; };
constexpr int bf16_round_int(int l) {
  int sh = 0;
  while ((l >> sh) >= 256) ++sh;
  if (sh == 0) return l;
  const int q = l >> sh, rem = l & ((1 << sh) - 1), half = 1 << (sh - 1);
  const int up = (rem > half || (rem == half && (q & 1))) ? 1 : 0;
  return (q + up) << sh;
}
constexpr KIdx make_kidx() {
  KIdx t{};
  for (int x = 0; x < 2048; ++x) {
    const int lr = bf16_round_int(2047 - x);
    const int aa = lr >> 7, b = (lr >> 3) & 15, r = lr & 7;
    const int row = lr < 256 ? 8 * aa + r : lr < 512 ? 16 + 4 * (aa - 2) + (r >> 1) : lr < 1024 ? 24 + 2 * (aa - 4) + (r >> 2) : 32 + (aa - 8);
    t.v[x] = (u16)(row * 16 + b);
  }
  return t;
}
__device__ const KIdx g_kidx = make_kidx();

struct cpx { float r, i; };
__device__ __forceinline__ cpx cmul(cpx a, cpx b) { return cpx{a.r * b.r - a.i * b.i, a.r * b.i + a.i * b.r}; }

__global__ __launch_bounds__(768) void kA(AArgs a) {
  extern __shared__ __attribute__((aligned(16))) char smem[];
  const int wg = blockIdx.x, tid = threadIdx.x, lane = tid & 63;
  const int w = __builtin_amdgcn_readfirstlane(tid >> 6);
  if (blockIdx.x == 0 && threadIdx.x == 0) ((volatile unsigned long long*)((char*)a.mod + 65536))[0] = __builtin_amdgcn_s_memrealtime();
  const int tw = w - 8, st = tid - 512;
  const int t0 = 8 * ((wg & 7) * 32 + (wg >> 3));
  const bool doM = wg < 192;
  const int mrb = wg / 48, mcb = wg - 48 * mrb;
  const int rr = lane >> 3, q = lane & 7;
  u32x4 ld[14];
  u16 sv[8];
  if (w < 8) {
    const int d = 2 * wg + (w >> 2), o = d * NS + 64 * ((w >> 1) & 1) + lane;
    sv[0] = a.Are[o]; sv[1] = a.Aim[o]; sv[2] = a.Bre[o]; sv[3] = a.Bim[o]; sv[4] = a.Cre[o]; sv[5] = a.Cim[o]; sv[6] = a.logdt[d]; sv[7] = 0;
    ld[0] = *(const u32x4*)(g_kidx.v + 8 * (tid & 255));
  } else {
#pragma unroll
    for (int j = 0; j < 2; ++j) ld[j] = *(const u32x4*)(a.x + (size_t)(t0 + 2 * tw + j) * D + 8 * lane);
    if (tw == 0 && lane < 4) *(u32*)(smem + A_CNT + 4 * lane) = 0;
    asm volatile("s_waitcnt lgkmcnt(0)" ::: "memory");
  }
  SCHED_FENCE();
  STAMP(0, 9);
  __builtin_amdgcn_s_barrier();
  STAMP(0, 10);
  SCHED_FENCE();
  if (w < 8) {
    const int ch = w >> 2, nh = (w >> 1) & 1, part = w & 1, d = 2 * wg + ch;
    const int n = 64 * nh + lane;
    const u16 hare = sv[0], haim = sv[1], hbre = sv[2], hbim = sv[3], hcre = sv[4], hcim = sv[5], hdt = sv[6];
    const u32x4 kix = ld[0];
    asm volatile("" ::"v"((u32)hare), "v"((u32)hcim), "v"((u32)hdt));
    STAMP(0, 11);
    const float dt = rbf(__expf(bf1(hdt)));
    const float lr = (dt * bf1(hare)) * 1.4426950408889634f, lp = (dt * bf1(haim)) * 0.15915494309189535f;
    auto cexp = [&](float l) -> cpx {
      const float e = __builtin_amdgcn_exp2f(lr * l), ph = __builtin_amdgcn_fractf(lp * l);
      return cpx{e * __builtin_amdgcn_cosf(ph), e * __builtin_amdgcn_sinf(ph)};
    };
    char* tab = smem + A_TAB + ch * (64 * A_RS) + n * 4;
    auto put = [&](int row, cpx v) { *(u32*)(tab + row * A_RS) = pk_bf16(v.r, v.i); };
    if (part == 0) {
      const cpx E1 = cexp(1.f), E128 = cexp(128.f), E512 = cexp(512.f);
      const cpx E2 = cmul(E1, E1), E3 = cmul(E2, E1), E4 = cmul(E2, E2), E5 = cmul(E4, E1), E6 = cmul(E4, E2), E7 = cmul(E4, E3);
      put(0, cpx{1.f, 0.f}); put(1, E1); put(2, E2); put(3, E3); put(4, E4); put(5, E5); put(6, E6); put(7, E7);
      put(8, E128); put(9, cmul(E128, E1)); put(10, cmul(E128, E2)); put(11, cmul(E128, E3));
      put(12, cmul(E128, E4)); put(13, cmul(E128, E5)); put(14, cmul(E128, E6)); put(15, cmul(E128, E7));
      const cpx E256 = cmul(E128, E128), E384 = cmul(E256, E128);
      put(16, E256); put(17, cmul(E256, E2)); put(18, cmul(E256, E4)); put(19, cmul(E256, E6));
      put(20, E384); put(21, cmul(E384, E2)); put(22, cmul(E384, E4)); put(23, cmul(E384, E6));
      const cpx E640 = cmul(E512, E128), E768 = cmul(E512, E256), E896 = cmul(E512, E384);
      put(24, E512); put(25, cmul(E512, E4)); put(26, E640); put(27, cmul(E640, E4));
      put(28, E768); put(29, cmul(E768, E4)); put(30, E896); put(31, cmul(E896, E4));
    } else {
      const cpx E8 = cexp(8.f), E128 = cexp(128.f), E1024 = cexp(1024.f);
      const float br = dt * bf1(hbre), bi = dt * bf1(hbim), cr = bf1(hcre), ci = bf1(hcim);
      const cpx W = cmul(cpx{cr, ci}, cpx{br, bi});
      const cpx E16 = cmul(E8, E8), E32 = cmul(E16, E16), E64 = cmul(E32, E32);
      cpx Y[8];
      Y[0] = W; Y[1] = cmul(W, E8); Y[2] = cmul(W, E16); Y[3] = cmul(Y[1], E16);
#pragma unroll
      for (int b = 0; b < 4; ++b) Y[4 + b] = cmul(Y[b], E32);
#pragma unroll
      for (int b = 0; b < 8; ++b) {
        put(48 + b, cpx{Y[b].r, -Y[b].i});
        const cpx t = cmul(Y[b], E64);
        put(56 + b, cpx{t.r, -t.i});
      }
      const cpx E256 = cmul(E128, E128), E512 = cmul(E256, E256);
      cpx P[4];
      P[0] = E1024; P[1] = cmul(E1024, E128); P[2] = cmul(E1024, E256); P[3] = cmul(P[1], E256);
#pragma unroll
      for (int b = 0; b < 4; ++b) { put(32 + b, P[b]); put(36 + b, cmul(P[b], E512)); }
      put(40, cmul(E1024, E1024));
    }
    STAMP(0, 1);
    lds_rendezvous((u32 LAS*)(smem + A_CNT), 8);
    STAMP(0, 2);
    if (w < 6) {
      const int c2 = w >= 3 ? 1 : 0, t = w - 3 * c2, i = lane & 15, g = lane >> 4;
      const char* tb = smem + A_TAB + c2 * (64 * A_RS) + 16 * g;
      const char* pa = tb + (16 * t + i) * A_RS, *pb = tb + (48 + i) * A_RS;
      f32x4 acc = {0, 0, 0, 0};
      bf16x8 Af[8], Bf[8];
#pragma unroll
      for (int s = 0; s < 8; ++s) {
        Af[s] = *(const bf16x8*)(pa + s * 64); Bf[s] = *(const bf16x8*)(pb + s * 64);
      }
#pragma unroll
      for (int s = 0; s < 8; ++s) acc = __builtin_amdgcn_mfma_f32_16x16x32_bf16(Af[s], Bf[s], acc, 0, 0, 0);
      float* Kc = (float*)(smem + A_KC) + c2 * 768 + t * 256;
#pragma unroll
      for (int r = 0; r < 4; ++r) Kc[(4 * g + r) * 16 + i] = acc[r];
    }
    STAMP(0, 3);
    lds_rendezvous((u32 LAS*)(smem + A_CNT + 4), 8);
    STAMP(0, 4);
    const float* Kc = (const float*)(smem + A_KC) + ch * 768;
    float kv[8];
#pragma unroll
    for (int j = 0; j < 4; ++j) { kv[2 * j] = Kc[kix[j] & 0xffffu]; kv[2 * j + 1] = Kc[kix[j] >> 16]; }
    *(u32x4*)(a.Krev + (size_t)d * L + 8 * (tid & 255)) =
        u32x4{pk_bf16(kv[0], kv[1]), pk_bf16(kv[2], kv[3]), pk_bf16(kv[4], kv[5]), pk_bf16(kv[6], kv[7])};
    STAMP(0, 5);
  } else {
    __builtin_amdgcn_s_setprio(3);
    const u32x4 xr[2] = {ld[0], ld[1]};
    asm volatile("" ::"v"(xr[1]));
    STAMP(0, 11);
    char* tile = smem + A_TILE;
#pragma unroll
    for (int j = 0; j < 2; ++j) *(u32x4*)(tile + (2 * tw + j) * 1088 + 16 * lane) = xr[j];
    SCHED_FENCE();
    if (doM) {
#pragma unroll
      for (int p = 0; p < 4; ++p) ld[2 + p] = *(const u32x4*)(a.wmod + (size_t)(128 * mrb + p * 32 + tw * 8 + rr) * 3072 + 64 * mcb + 8 * q);
#pragma unroll
      for (int p = 0; p < 4; ++p) sv[p] = a.c[128 * mrb + p * 32 + tw * 8 + rr];
    }
    ld[10] = *(const u32x4*)(a.win + (size_t)wg * 3584 + st * 8);
    ld[11] = *(const u32x4*)(a.win + (size_t)wg * 3584 + (st < 192 ? 2048 + st * 8 : 0));
    ld[12] = *(const u32x4*)(a.wout + (size_t)wg * 1792 + (st < 224 ? st * 8 : 0));
    u64 base;
    asm volatile("s_getpc_b64 s[40:41]\n\ts_add_u32 s40, s40, _Z2kB5BArgs@rel32@lo+4\n\ts_addc_u32 s41, s41, _Z2kB5BArgs@rel32@hi+12\n\ts_mov_b64 %0, s[40:41]"
                 : "=s"(base) : : "s40", "s41", "scc");
    ld[13] = *(const u32x4*)((const char*)base + (size_t)(st < CODE_LANES ? (wg >> 3) * CODE_LANES + st : 0) * 16);
    SCHED_FENCE();
    STAMP(0, 1);
    lds_rendezvous((u32 LAS*)(smem + A_CNT + 8), 4);
    STAMP(0, 2);
#pragma unroll
    for (int ps = 0; ps < 2; ++ps) {
      const int d0 = 128 * tw + 64 * ps + 16 * (lane >> 4);
      const char LAS* src = (const char LAS*)tile + ((lane & 15) >> 2) * 1088 + 2 * (d0 + 4 * (lane & 3));
      const s16x4 lo = tr_read(src), hi = tr_read(src + 4 * 1088);
      u32x2 a0 = __builtin_bit_cast(u32x2, lo), a1 = __builtin_bit_cast(u32x2, hi);
      *(u32x4*)(a.xT + (size_t)(d0 + (lane & 15)) * L + t0) = u32x4{a0[0], a0[1], a1[0], a1[1]};
    }
    STAMP(0, 6);
    if (tw == 0) a.gran[wg * 64 + lane] = u32x4{0, 0, 0, 0};
    if (tw == 1 && wg < 8) a.gran[(256 + wg) * 64 + lane] = u32x4{0, 0, 0, 0};
    {
      float v[2][8], s[2];
#pragma unroll
      for (int j = 0; j < 2; ++j) {
#pragma unroll
        for (int k = 0; k < 4; ++k) { v[j][2 * k] = bf_lo(xr[j][k]); v[j][2 * k + 1] = bf_hi(xr[j][k]); }
        s[j] = ((v[j][0] + v[j][1]) + (v[j][2] + v[j][3])) + ((v[j][4] + v[j][5]) + (v[j][6] + v[j][7]));
      }
      float mean[2], q2[2];
#pragma unroll
      for (int j = 0; j < 2; ++j) mean[j] = wave_sum(s[j]) * (1.f / D);
#pragma unroll
      for (int j = 0; j < 2; ++j) {
        q2[j] = 0;
#pragma unroll
        for (int k = 0; k < 8; ++k) { const float dv = v[j][k] - mean[j]; q2[j] += dv * dv; }
      }
#pragma unroll
      for (int j = 0; j < 2; ++j) {
        const float var = wave_sum(q2[j]) * (1.f / D);
        const float sd = rbf(__builtin_amdgcn_sqrtf(rbf(rbf(var) + 1e-5f)));
        if (lane == 0) *(f32x2*)(a.stats1 + 2 * (t0 + 2 * tw + j)) = f32x2{rbf(mean[j]), __builtin_amdgcn_rcpf(sd)};
      }
    }
    STAMP(0, 7);
    float* red = (float*)(smem + A_RED);
    if (doM) {
      asm volatile("" ::"v"(ld[5]));
      STAMP(0, 8);
      float acc[8] = {0, 0, 0, 0, 0, 0, 0, 0};
#pragma unroll
      for (int p = 0; p < 4; ++p) {
        const float cx = bf1(sv[p]);
        const float s = rbf(cx * __builtin_amdgcn_rcpf(1.f + __expf(-cx)));
#pragma unroll
        for (int k = 0; k < 4; ++k) { acc[2 * k] += s * bf_lo(ld[2 + p][k]); acc[2 * k + 1] += s * bf_hi(ld[2 + p][k]); }
      }
#pragma unroll
      for (int k = 0; k < 8; ++k) {
        float v = acc[k];
        v += __int_as_float(__builtin_amdgcn_update_dpp(0, __float_as_int(v), 0x128, 0xf, 0xf, true));
        v = xor32_sum(xor16_sum(v));
        if (rr == 0) red[tw * 64 + 8 * q + k] = v;
      }
      STAMP(0, 3);
      lds_rendezvous((u32 LAS*)(smem + A_CNT + 12), 4);
      STAMP(0, 4);
      if (tw == 0) a.mod[3072 * mrb + 64 * mcb + lane] = (red[lane] + red[64 + lane]) + (red[128 + lane] + red[192 + lane]);
    }
#pragma unroll
    for (int k = 10; k < 14; ++k) asm volatile("" ::"v"(ld[k]));
    STAMP(0, 5);
  }
}
struct BArgs {
  const u16 *xT, *Krev, *Dskip, *win, *wout, *wsp;
  const u32* stats1; const float* mod;
  u16 *x1T, *Wt, *Wot; float* uv;
};
constexpr int B_H = 0;
constexpr int B_HS = (256 + 2048) * 2;
constexpr int B_KC = B_H + 2 * B_HS;
constexpr int B_KS = 4160;
constexpr int B_PART = B_KC + 2 * 4 * B_KS;
constexpr int B_RED = B_PART + 2 * 4 * 8 * 1280;
constexpr int B_CNT = B_RED + 4 * 16 * 4;
constexpr int B_LDS = B_CNT + 32;

__global__ __launch_bounds__(768) void kB(BArgs a) {
  extern __shared__ __attribute__((aligned(16))) char smem[];
  const int wg = blockIdx.x, tid = threadIdx.x, lane = tid & 63;
  const int w = __builtin_amdgcn_readfirstlane(tid >> 6);
  if (blockIdx.x == 0 && threadIdx.x == 0) ((volatile unsigned long long*)((char*)a.mod + 65536))[1] = __builtin_amdgcn_s_memrealtime();
  if (w < 8) {
    __builtin_amdgcn_s_setprio(2);
    const int ch = w >> 2, q = w & 3, d = 2 * wg + ch, tj = tid & 255;
    const u32x4 xv = *(const u32x4*)(a.xT + (size_t)d * L + 8 * tj);
    f32x4 st[4];
#pragma unroll
    for (int k = 0; k < 4; ++k) st[k] = *(const f32x4*)(a.stats1 + 16 * tj + 4 * k);
    const u32x4 k0 = *(const u32x4*)(a.Krev + (size_t)d * L + 8 * tj);
    const u32x4 k1r = *(const u32x4*)(a.Krev + (size_t)d * L + 8 * (tj < 255 ? tj + 1 : tj));
    const float sa = mod_get(a.mod, d), Ac = rbf(1.f + mod_get(a.mod, 512 + d)), ga = mod_get(a.mod, 1024 + d), dsk = bf1(a.Dskip[d]);
    SCHED_FENCE();
    __builtin_amdgcn_s_barrier();
    SCHED_FENCE();
    char* hb = smem + B_H + ch * B_HS;
    char* kc = smem + B_KC + ch * 4 * B_KS;
    if (tj < 32) *(u32x4*)(hb + 16 * tj) = u32x4{0, 0, 0, 0};
    if (tj < 16) *(u32x4*)(kc + (tj >> 2) * B_KS + 16 * (256 + (tj & 3))) = u32x4{0, 0, 0, 0};
    {
      const u32 z = tj < 255 ? 0xffffffffu : 0u;
      const u32 wd[8] = {k0[0], k0[1], k0[2], k0[3], k1r[0] & z, k1r[1] & z, 0, 0};
      u32 od[5];
#pragma unroll
      for (int k = 0; k < 5; ++k) od[k] = __builtin_amdgcn_alignbit(wd[k + 1], wd[k], 16);
      *(u32x4*)(kc + 0 * B_KS + 16 * tj) = u32x4{wd[0], wd[1], wd[2], wd[3]};
      *(u32x4*)(kc + 1 * B_KS + 16 * tj) = u32x4{od[0], od[1], od[2], od[3]};
      *(u32x4*)(kc + 2 * B_KS + 16 * tj) = u32x4{wd[1], wd[2], wd[3], wd[4]};
      *(u32x4*)(kc + 3 * B_KS + 16 * tj) = u32x4{od[1], od[2], od[3], od[4]};
    }
    u32x4 hv;
    {
      float hh[8];
#pragma unroll
      for (int k = 0; k < 8; ++k) {
        const float m = st[k >> 1][2 * (k & 1)], ri = st[k >> 1][2 * (k & 1) + 1];
        const float xx = (k & 1) ? bf_hi(xv[k >> 1]) : bf_lo(xv[k >> 1]);
        hh[k] = (xx - m) * (ri * Ac) + sa;
      }
      hv = u32x4{pk_bf16(hh[0], hh[1]), pk_bf16(hh[2], hh[3]), pk_bf16(hh[4], hh[5]), pk_bf16(hh[6], hh[7])};
    }
    *(u32x4*)(hb + 512 + 16 * tj) = hv;
    STAMP(1, 1);
    lds_rendezvous((u32 LAS*)(smem + B_CNT + 12 + 4 * 0) + ch * 2, 4);
    STAMP(1, 2);
    const int c = lane & 15, g = lane >> 4;
    const char* bbase = hb + 2 * (256 + 16 * c - 16 * (g >> 1) + 8 * (g & 1) - 32 * q) - 256;
    const int X = 2047 - 32 * q - 16 * (g >> 1) + 8 * (g & 1) - c;
    const char* abase = kc + (X & 3) * B_KS + 2 * (X & ~3) - 256 * 15;
    const char LAS* ab0 = (const char LAS*)abase;
    bf16x8 Bf[16], Af[16];
    auto lda = [&](int u) {
      const u32x2 lo = *(const volatile u32x2 LAS*)(ab0 + 256 * (15 - u)), hi = *(const volatile u32x2 LAS*)(ab0 + 256 * (15 - u) + 8);
      Af[u] = __builtin_bit_cast(bf16x8, u32x4{lo[0], lo[1], hi[0], hi[1]});
    };
    lda(0);
#pragma unroll
    for (int v = 1; v < 16; v += 2) Bf[v] = *(const bf16x8*)(bbase + 256 * v);
    lda(1);
#pragma unroll
    for (int v = 0; v < 16; v += 2) Bf[v] = *(const bf16x8*)(bbase + 256 * v);
    lda(2); lda(3);
    SCHED_FENCE();
    f32x4 acc[8];
#pragma unroll
    for (int i = 0; i < 8; ++i) acc[i] = f32x4{0, 0, 0, 0};
#pragma unroll
    for (int u = 0; u < 16; ++u) {
      if (u + 4 < 16) lda(u + 4);
      SCHED_FENCE();
#pragma unroll
      for (int i = u >> 1; i < 8; ++i) acc[i] = __builtin_amdgcn_mfma_f32_16x16x32_bf16(Af[u], Bf[2 * i - u + 1], acc[i], 0, 0, 0);
      SCHED_FENCE();
    }
    STAMP(1, 3);
    char* part = smem + B_PART + ch * (4 * 8 * 1280);
#pragma unroll
    for (int i = 0; i < 8; ++i) *(f32x4*)(part + (q * 8 + i) * 1280 + 16 * (20 * g + c)) = acc[i];
    lds_rendezvous((u32 LAS*)(smem + B_CNT + 4 * ch), 4);
    STAMP(1, 4);
    {
      const int i = tj >> 5, j5 = tj & 31, cc = j5 >> 1;
      float y[8];
#pragma unroll
      for (int hh = 0; hh < 2; ++hh) {
        const char* p = part + i * 1280 + 16 * (20 * (2 * (j5 & 1) + hh) + cc);
        const f32x4 p0 = *(const f32x4*)(p), p1 = *(const f32x4*)(p + 8 * 1280), p2 = *(const f32x4*)(p + 16 * 1280), p3 = *(const f32x4*)(p + 24 * 1280);
#pragma unroll
        for (int r = 0; r < 4; ++r) y[4 * hh + r] = (p0[r] + p1[r]) + (p2[r] + p3[r]);
      }
      float o[8];
#pragma unroll
      for (int k = 0; k < 8; ++k) {
        const float hx = (k & 1) ? bf_hi(hv[k >> 1]) : bf_lo(hv[k >> 1]);
        const float xx = (k & 1) ? bf_hi(xv[k >> 1]) : bf_lo(xv[k >> 1]);
        o[k] = xx + ga * (y[k] + hx * dsk);
      }
      wt_store16(make_rsrc(a.x1T, D * L * 2), (u32)(d * L + 8 * tj) * 2, u32x4{pk_bf16(o[0], o[1]), pk_bf16(o[2], o[3]), pk_bf16(o[4], o[5]), pk_bf16(o[6], o[7])});
    }
    STAMP(1, 5);
  } else {
    const int st = tid - 512, sw = w - 8;
    if (st < 8) *(u32*)(smem + B_CNT + 4 * st) = 0;
    asm volatile("s_waitcnt lgkmcnt(0)" ::: "memory");
    __builtin_amdgcn_s_barrier();
    SCHED_FENCE();
    float* red = (float*)(smem + B_RED);
    const bool act = wg < 224;
    const int f0 = 8 * ((wg & 7) * 28 + (wg >> 3));
    const int d8 = st >> 2, fp = st & 3;
    const int f8 = wg >> 1, dp = (wg & 1) * 128 + (st & 127);
    u32 wv[8], ov[8];
    f32x4 cb0, cb1, sb0, sb1;
    if (act) {
      cb0 = mod_get4(a.mod, 2048 + 8 * d8); cb1 = mod_get4(a.mod, 2048 + 8 * d8 + 4);
      sb0 = mod_get4(a.mod, 1536 + 8 * d8); sb1 = mod_get4(a.mod, 1536 + 8 * d8 + 4);
#pragma unroll
      for (int j = 0; j < 8; ++j) wv[j] = *(const u32*)(a.win + (size_t)(8 * d8 + j) * F + f0 + 2 * fp);
      if (st < 128) {
#pragma unroll
        for (int j = 0; j < 8; ++j) ov[j] = *(const u32*)(a.wout + (size_t)(8 * f8 + j) * D + 2 * dp);
      }
    }
    u32x4 pf[4];
    {
#pragma unroll
      for (int j = 0; j < 4; ++j) {
        const int idx = st + 256 * j;
        pf[j] = *(const u32x4*)(a.wsp + (size_t)(8 * wg + (idx >> 7)) * L + ((idx >> 5) & 3) * 512 + 256 + (idx & 31) * 8);
      }
    }
    SCHED_FENCE();
    if (act) {
      float lo[8], hi[8], u0 = 0, u1 = 0, v0 = 0, v1 = 0;
#pragma unroll
      for (int j = 0; j < 8; ++j) {
        const float Ac = rbf(1.f + (j < 4 ? cb0[j] : cb1[j - 4])), sb = j < 4 ? sb0[j] : sb1[j - 4];
        const float wl = bf_lo(wv[j]), wh = bf_hi(wv[j]);
        lo[j] = rbf(Ac * wl); hi[j] = rbf(Ac * wh);
        u0 += lo[j]; u1 += hi[j]; v0 += sb * wl; v1 += sb * wh;
      }
      const int f = f0 + 2 * fp;
      const rsrc_t rw = make_rsrc(a.Wt, D * F * 2);
      const u32 dst = (u32)((((f >> 4) * 64 + d8) * 16 + (f & 15)) * 16);
      wt_store16(rw, dst, u32x4{pk_bf16(lo[0], lo[1]), pk_bf16(lo[2], lo[3]), pk_bf16(lo[4], lo[5]), pk_bf16(lo[6], lo[7])});
      wt_store16(rw, dst + 16, u32x4{pk_bf16(hi[0], hi[1]), pk_bf16(hi[2], hi[3]), pk_bf16(hi[4], hi[5]), pk_bf16(hi[6], hi[7])});
      float rv[4] = {u0, u1, v0, v1};
#pragma unroll
      for (int k = 0; k < 4; ++k) {
        const float v = xor32_sum(xor16_sum(xor4_8_sum(rv[k])));
        if (lane < 4) red[sw * 16 + lane * 4 + k] = v;
      }
      if (st < 128) {
        const int dd = 2 * dp;
        const rsrc_t ro = make_rsrc(a.Wot, FH * D * 2);
        const u32 od = (u32)((((dd >> 4) * 112 + f8) * 16 + (dd & 15)) * 16);
        u32x4 e, o;
#pragma unroll
        for (int k = 0; k < 4; ++k) {
          e[k] = (ov[2 * k] & 0xffffu) | (ov[2 * k + 1] << 16);
          o[k] = (ov[2 * k] >> 16) | (ov[2 * k + 1] & 0xffff0000u);
        }
        wt_store16(ro, od, e); wt_store16(ro, od + 16, o);
      }
      lds_rendezvous((u32 LAS*)(smem + B_CNT + 8), 4);
      if (st < 16) {
        const float s = (red[st] + red[16 + st]) + (red[32 + st] + red[48 + st]);
        const int fq = st >> 2, k = st & 3;
        a.uv[(k >> 1) * F + f0 + 2 * fq + (k & 1)] = s;
      }
    }
    STAMP(1, 1);
#pragma unroll
    for (int j = 0; j < (int)(sizeof(pf) / sizeof(pf[0])); ++j) asm volatile("" ::"v"(pf[j]));
    STAMP(1, 5);
  }
}
struct FArgs {
  const u16 *x1T, *Wt, *wsp, *bsp, *Wot; const float *uv, *mod;
  u16 *z2t, *Pt, *out; u64* gran; u32* flags;
};
constexpr int F_X = 0;
constexpr int F_ST = 65536;
constexpr int F_MR = F_ST + 512;
constexpr int F_PS = F_MR + 8 * 512;
constexpr int F_FS = F_PS + 7 * 64 * 8;
constexpr int F_ZS = F_FS + 512;
constexpr int F_RS = 232;
constexpr int F_A0 = 90112;
constexpr int F_LDS = F_A0 + 8 * 8192;
constexpr int F_ETS = 1152;
constexpr int F_EX = 112 * 1024;
#define F_BDEPTH 3
constexpr int F_BD = F_BDEPTH;
constexpr int F_BR = F_BD;
static_assert(F_ZS + 64 * F_RS <= F_A0 && F_EX + 64 * 132 <= F_LDS, "LDS map");

__device__ __forceinline__ float gelu_tanh(float z) {
  const float k0 = -2.f * 0.7978845608028654f * 1.4426950408889634f, k1 = k0 * 0.044715f;
  const float e = __builtin_amdgcn_exp2f(z * (k0 + k1 * (z * z)));
  return z * __builtin_amdgcn_rcpf(1.f + e);
}
__device__ __forceinline__ float pick4(f32x4 v, int r) { return r == 0 ? v[0] : r == 1 ? v[1] : r == 2 ? v[2] : v[3]; }
template <int N> __device__ __forceinline__ void vm_wait_c() { asm volatile("s_waitcnt vmcnt(%0)" ::"i"(N > 63 ? 63 : N) : "memory"); }
__device__ __forceinline__ u32 f_slot(int kq, int sg) { return (u32)(((sg & 2) ? 0 : F_A0) + (kq * 2 + (sg & 1)) * 8192); }
__device__ __forceinline__ bf16x8 ld_dev16(rsrc_t r, u32 off) { return __builtin_bit_cast(bf16x8, __builtin_amdgcn_raw_buffer_load_b128(r, off, 0, 16)); }
__device__ __forceinline__ void f_lda(const u16* const (&pa)[2], u32 lds0, int kq, int nh, int sg) {
#pragma unroll
  for (int i = 0; i < 4; ++i) glds16(pa[i & 1] + (size_t)(i >> 1) * 16 * L + 64 * sg, lds0 + f_slot(kq, sg) + (4 * nh + i) * 1024);
}
__device__ __forceinline__ void f_ldx(const u16* x1T, u32 lds0, int w, int pi, int sg) {
  const int lane = threadIdx.x & 63;
#pragma unroll
  for (int i = 0; i < 2; ++i) {
    const int d = 128 * sg + 16 * w + 8 * i + (lane >> 3);
    const int fz = (((d >> 1) & 1) | ((d >> 2) & 2)) << 1;
    glds16(x1T + (size_t)d * L + 64 * pi + 8 * ((lane & 7) ^ fz), lds0 + F_X + (128 * sg + 16 * w + 8 * i) * 128);
  }
}
__device__ __forceinline__ void wait_flags(const u32* p, int n) {
  const int lane = threadIdx.x & 63;
  for (u32 spin = 0; spin < (1u << 22); ++spin) {
    const u32 v = __hip_atomic_load((GAS u32*)p + (lane < n ? lane : 0), __ATOMIC_RELAXED, __HIP_MEMORY_SCOPE_AGENT);
    if (__all(v == 1u)) break;
    __builtin_amdgcn_s_sleep(1);
  }
}

__device__ __forceinline__ u32 poll8(const u32* p, int n) {
  const int lane = threadIdx.x & 63;
  const u32 v = __hip_atomic_load((GAS u32*)p + (lane < n ? lane : 0), __ATOMIC_RELAXED, __HIP_MEMORY_SCOPE_AGENT);
  return (u32)__ballot(v == 1u) & 0xffu;
}
__device__ __forceinline__ void need_flag(const u32* p, int n, u32& mask, int k) {
  if (!((mask >> k) & 1u)) {
    for (u32 spin = 0; spin < (1u << 22); ++spin) {
      mask = poll8(p, n);
      if ((mask >> k) & 1u) break;
      __builtin_amdgcn_s_sleep(1);
    }
  }
  asm volatile("" ::: "memory");
}

struct Z1 { f32x4 y[4]; };

template <int ROLE>
__device__ __forceinline__ void f_phase_c(const FArgs& a, char* smem, const int w, const int pi, const int j, const u16* const (&pa)[2], Z1& z1) {
  const int tid = threadIdx.x, lane = tid & 63;
  const int c = lane & 15, g = lane >> 4;
  const u32 lds0 = (u32)(size_t)(const char LAS*)smem;
  const int nt2 = 56 + 7 * j + (w < 7 ? w : 0), nt1 = 7 * j + (w > 0 ? w - 1 : 0);
  const u16* pw2 = a.Wt + (size_t)nt2 * 64 * 128 + lane * 8;
  const u16* pw1 = a.Wt + (size_t)nt1 * 64 * 128 + lane * 8;
  const int q4 = (lane & 15) >> 2, p4 = lane & 3;
  const int drow = 8 * g + q4;
  const int fz0 = (((drow >> 1) & 1) | ((drow >> 2) & 2)) << 1;
  const char LAS* xb = (const char LAS*)smem + F_X + drow * 128 + 8 * (p4 & 1);
  const char LAS* xt[4];
  int tb[4];
#pragma unroll
  for (int s = 0; s < 4; ++s) { tb[s] = (s + w) & 3; xt[s] = xb + 16 * ((2 * tb[s] + (p4 >> 1)) ^ fz0); }
  auto xfrag = [&](int s, int ks) -> bf16x8 {
    const char LAS* p = xt[s] + ks * 4096;
    const s16x4 lo = tr_read(p), hi = tr_read(p + 512);
    const u32x2 a0 = __builtin_bit_cast(u32x2, lo), a1 = __builtin_bit_cast(u32x2, hi);
    return __builtin_bit_cast(bf16x8, u32x4{a0[0], a0[1], a1[0], a1[1]});
  };
  f32x4 acc[4];
#pragma unroll
  for (int s = 0; s < 4; ++s) acc[s] = f32x4{0, 0, 0, 0};
  bf16x8 W1[16];
  f32x4 u2, v2, u1, v1;
  float* xs2 = (float*)(smem + F_ST);
  if constexpr (ROLE < 2) {
    bf16x8 W2[16];
#pragma unroll
    for (int ks = 0; ks < 4; ++ks) W2[ks] = *(const bf16x8*)(pw2 + 4 * ks * 128);
    SCHED_FENCE();
    f_ldx(a.x1T, lds0, w, pi, 1);
#pragma unroll
    for (int ks = 4; ks < 8; ++ks) W2[ks] = *(const bf16x8*)(pw2 + 4 * ks * 128);
    SCHED_FENCE();
    VM_WAIT(10);
    __builtin_amdgcn_s_barrier();
    SCHED_FENCE();
    STAMP(2, 1);
    f32x4 sq = {0, 0, 0, 0};
    bf16x8 X[2][4];
#pragma unroll
    for (int s = 0; s < 4; ++s) X[0][s] = xfrag(s, 0);
#pragma unroll
    for (int ks = 0; ks < 16; ++ks) {
      if (ks == 3) { VM_WAIT(9); __builtin_amdgcn_s_barrier(); }
      if (ks == 7) { VM_WAIT(11); __builtin_amdgcn_s_barrier(); }
      if (ks == 11) { VM_WAIT(6); __builtin_amdgcn_s_barrier(); }
      if (ks + 1 < 16) {
#pragma unroll
        for (int s = 0; s < 4; ++s) X[(ks + 1) & 1][s] = xfrag(s, ks + 1);
      }
      if (ks == 0) f_ldx(a.x1T, lds0, w, pi, 2);
      if (ks == 4) f_ldx(a.x1T, lds0, w, pi, 3);
      if (ks < 8) W2[8 + ks] = *(const bf16x8*)(pw2 + 4 * (8 + ks) * 128);
      if (ks == 5) { u2 = *(const f32x4*)(a.uv + 16 * nt2 + 4 * g); v2 = *(const f32x4*)(a.uv + F + 16 * nt2 + 4 * g); }
      SCHED_FENCE();
#pragma unroll
      for (int s = 0; s < 4; ++s) acc[s] = __builtin_amdgcn_mfma_f32_16x16x32_bf16(W2[ks], X[ks & 1][s], acc[s], 0, 0, 0);
      sq = __builtin_amdgcn_mfma_f32_16x16x32_bf16(X[ks & 1][0], X[ks & 1][0], sq, 0, 0, 0);
      SCHED_FENCE();
    }
    if constexpr (ROLE == 1) {
      u1 = *(const f32x4*)(a.uv + 16 * nt1 + 4 * g); v1 = *(const f32x4*)(a.uv + F + 16 * nt1 + 4 * g);
      SCHED_FENCE();
    }
    if (w < 4 && (c >> 2) == g) xs2[64 + 16 * w + c] = pick4(sq, c & 3);
  } else {
    f_ldx(a.x1T, lds0, w, pi, 1);
    VM_WAIT(2);
    __builtin_amdgcn_s_barrier();
    SCHED_FENCE();
    STAMP(2, 1);
    const u32x4 ones = {0x3f803f80u, 0x3f803f80u, 0x3f803f80u, 0x3f803f80u};
    bf16x8 X[2][4];
#pragma unroll
    for (int s = 0; s < 4; ++s) X[0][s] = xfrag(s, 0);
#pragma unroll
    for (int ks = 0; ks < 16; ++ks) {
      if (ks == 3) { VM_WAIT(2); __builtin_amdgcn_s_barrier(); }
      if (ks == 7) { VM_WAIT(2); __builtin_amdgcn_s_barrier(); }
      if (ks == 11) { VM_WAIT(6); __builtin_amdgcn_s_barrier(); }
      if (ks + 1 < 16) {
#pragma unroll
        for (int s = 0; s < 4; ++s) X[(ks + 1) & 1][s] = xfrag(s, ks + 1);
      }
      if (ks == 0) f_ldx(a.x1T, lds0, w, pi, 2);
      if (ks == 4) f_ldx(a.x1T, lds0, w, pi, 3);
      if (ks >= 8) { W1[2 * ks - 16] = *(const bf16x8*)(pw1 + 4 * (2 * ks - 16) * 128); W1[2 * ks - 15] = *(const bf16x8*)(pw1 + 4 * (2 * ks - 15) * 128); }
      SCHED_FENCE();
#pragma unroll
      for (int s = 0; s < 4; ++s) acc[s] = __builtin_amdgcn_mfma_f32_16x16x32_bf16(__builtin_bit_cast(bf16x8, ones), X[ks & 1][s], acc[s], 0, 0, 0);
      SCHED_FENCE();
    }
    u1 = *(const f32x4*)(a.uv + 16 * nt1 + 4 * g); v1 = *(const f32x4*)(a.uv + F + 16 * nt1 + 4 * g);
    SCHED_FENCE();
    if (g == 0) {
#pragma unroll
      for (int s = 0; s < 4; ++s) xs2[16 * tb[s] + c] = acc[s][0];
    }
  }
  STAMP(2, 2);
  __syncthreads();
  STAMP(2, 3);
  float mu[4], ri[4];
  {
    const float mean = xs2[lane] * (1.f / D);
    const float var = fmaxf(xs2[64 + lane] * (1.f / D) - mean * mean, 0.f);
    f32x2* mr = (f32x2*)(smem + F_MR + w * 512);
    mr[lane] = f32x2{rbf(mean), __builtin_amdgcn_rcpf(rbf(__builtin_amdgcn_sqrtf(rbf(rbf(var) + 1e-5f))))};
#pragma unroll
    for (int s = 0; s < 4; ++s) { const f32x2 v = mr[16 * tb[s] + c]; mu[s] = v[0]; ri[s] = v[1]; }
  }
  u32x2 z2p[4];
  if constexpr (ROLE < 2) {
    float* ps = (float*)(smem + F_PS) + w * 128;
    const s16x4 ones4 = {0x3f80, 0x3f80, 0x3f80, 0x3f80};
#pragma unroll
    for (int s = 0; s < 4; ++s) {
      if constexpr (ROLE == 1) {
#pragma unroll
        for (int ks = 4 * s; ks < 4 * s + 4; ++ks) W1[ks] = *(const bf16x8*)(pw1 + 4 * ks * 128);
        SCHED_FENCE();
      }
      float z[4];
#pragma unroll
      for (int r = 0; r < 4; ++r) z[r] = gelu_tanh(ri[s] * (acc[s][r] - mu[s] * u2[r]) + v2[r]);
      z2p[s] = u32x2{pk_bf16(z[0], z[1]), pk_bf16(z[2], z[3])};
      const s16x4 zz = __builtin_bit_cast(s16x4, z2p[s]);
      const f32x4 zero = {0, 0, 0, 0};
      const f32x4 s1 = __builtin_amdgcn_mfma_f32_16x16x16bf16_1k(ones4, zz, zero, 0, 0, 0);
      const f32x4 s2 = __builtin_amdgcn_mfma_f32_16x16x16bf16_1k(zz, zz, zero, 0, 0, 0);
      if ((c >> 2) == g) *(f32x2*)(ps + (16 * tb[s] + c) * 2) = f32x2{s1[0], pick4(s2, c & 3)};
      SCHED_FENCE();
    }
  }
  if constexpr (ROLE > 0) { f_lda(pa, lds0, w & 3, w >> 2, 0); f_lda(pa, lds0, w & 3, w >> 2, 1); }
  STAMP(2, 4);
  __syncthreads();
  STAMP(2, 5);
  if constexpr (ROLE == 0) {
    const float* ps = (const float*)(smem + F_PS);
    float s1 = 0, s2 = 0;
#pragma unroll
    for (int k = 0; k < 7; ++k) { const f32x2 v = *(const f32x2*)(ps + k * 128 + lane * 2); s1 += v[0]; s2 += v[1]; }
    const rsrc_t rg = make_rsrc(a.gran, 256 * 1024);
    const u32 goff = (u32)(pi * 8 * 64 + lane) * 16;
    wt_store16(rg, goff + (u32)j * 1024, u32x4{__float_as_uint(s1), 1u, __float_as_uint(s2), 1u});
    float t1 = 0, t2 = 0;
    for (u32 spin = 0; spin < (1u << 22); ++spin) {
      u32x4 x[8];
#pragma unroll
      for (int k = 0; k < 8; ++k) x[k] = __builtin_amdgcn_raw_buffer_load_b128(rg, goff + k * 1024, 0, 16);
      bool ok = true; t1 = 0; t2 = 0;
#pragma unroll
      for (int k = 0; k < 8; ++k) {
        ok = ok && x[k][1] == 1u && x[k][3] == 1u;
        t1 += __uint_as_float(x[k][0]); t2 += __uint_as_float(x[k][2]);
      }
      if (__all(ok)) break;
      __builtin_amdgcn_s_sleep(1);
    }
    const float mean = t1 * (1.f / FH);
    const float var = fmaxf(t2 * (1.f / FH) - mean * mean, 0.f);
    *(f32x2*)(smem + F_FS + lane * 8) = f32x2{rbf(mean), __builtin_amdgcn_rcpf(rbf(__builtin_amdgcn_sqrtf(rbf(rbf(var) + 1e-5f))))};
    f_lda(pa, lds0, w & 3, w >> 2, 0);
    f_lda(pa, lds0, w & 3, w >> 2, 1);
  } else {
#pragma unroll
    for (int s = 0; s < 4; ++s) acc[s] = f32x4{0, 0, 0, 0};
    bf16x8 X[2][4];
#pragma unroll
    for (int s = 0; s < 4; ++s) X[0][s] = xfrag(s, 0);
#pragma unroll
    for (int ks = 0; ks < 16; ++ks) {
      if (ks + 1 < 16) {
#pragma unroll
        for (int s = 0; s < 4; ++s) X[(ks + 1) & 1][s] = xfrag(s, ks + 1);
      }
      SCHED_FENCE();
#pragma unroll
      for (int s = 0; s < 4; ++s) acc[s] = __builtin_amdgcn_mfma_f32_16x16x32_bf16(W1[ks], X[ks & 1][s], acc[s], 0, 0, 0);
      SCHED_FENCE();
    }
  }
  STAMP(2, 6);
  __syncthreads();
  STAMP(2, 7);
  if constexpr (ROLE < 2) {
#pragma unroll
    for (int s = 0; s < 4; ++s) {
      const f32x2 fs = *(const f32x2*)(smem + F_FS + (16 * tb[s] + c) * 8);
      const float a0 = (bf_lo(z2p[s][0]) - fs[0]) * fs[1], a1 = (bf_hi(z2p[s][0]) - fs[0]) * fs[1];
      const float a2 = (bf_lo(z2p[s][1]) - fs[0]) * fs[1], a3 = (bf_hi(z2p[s][1]) - fs[0]) * fs[1];
      *(u32x2*)(smem + F_ZS + (16 * tb[s] + c) * F_RS + (16 * w + 4 * g) * 2) = u32x2{pk_bf16(a0, a1), pk_bf16(a2, a3)};
    }
#pragma unroll
    for (int kk = 0; kk < 2; ++kk) {
      const char LAS* p = (const char LAS*)smem + F_ZS + (32 * kk + 8 * g + q4) * F_RS + (16 * w + 4 * p4) * 2;
      const s16x4 lo = tr_read(p), hi = tr_read(p + 4 * F_RS);
      const u32x2 a0 = __builtin_bit_cast(u32x2, lo), a1 = __builtin_bit_cast(u32x2, hi);
      wt_store16(make_rsrc(a.z2t, L * FH * 2), (u32)((((7 * j + w) * 256 + 8 * pi + 4 * kk) * 64 + lane * 4) * 4), u32x4{a0[0], a0[1], a1[0], a1[1]});
    }
  }
  VM_WAIT(0);
  __syncthreads();
  if (tid == 0) __hip_atomic_store((GAS u32*)a.flags + j * 32 + pi, 1u, __ATOMIC_RELAXED, __HIP_MEMORY_SCOPE_AGENT);
  STAMP(2, 8);
  f_lda(pa, lds0, w & 3, w >> 2, 2);
  f_lda(pa, lds0, w & 3, w >> 2, 3);
#pragma unroll
  for (int s = 0; s < 4; ++s)
#pragma unroll
    for (int r = 0; r < 4; ++r) z1.y[s][r] = ROLE > 0 ? ri[s] * (acc[s][r] - mu[s] * u1[r]) + v1[r] : 0.f;
  STAMP(2, 9);
}

template <int NT> constexpr int fd_glds(int sg) { return (sg == 2 || sg == 4) ? 8 : 0; }
template <int NT> constexpr int fd_bld(int sg) { return sg < 0 ? 0 : (2 * sg + F_BD < 16 ? NT : 0) + (2 * sg + 1 + F_BD < 16 ? NT : 0); }
template <int NT> constexpr int fd_ops(int sg) { return fd_glds<NT>(sg) + fd_bld<NT>(sg); }

template <int NT, int SG>
__device__ __forceinline__ void fd_stage(f32x4 (&acc)[4][NT], bf16x8 (&Bf)[F_BR][NT], bf16x8 (&Af)[2][4], const rsrc_t rb, u32 boff,
                                         const u16* const (&pa)[2], u32 lds0, const char* const (&ap)[2][2][4], int kq, int nh,
                                         const u32* fl, u32& mask, const Z1& z1, char* zdst, int w) {
  if constexpr ((SG & 1) == 0 && SG > 0) {
    if constexpr (SG == 2) vm_wait_c<F_BD * NT + fd_ops<NT>(0) + fd_ops<NT>(1)>();
    else vm_wait_c<fd_bld<NT>(SG - 2) + fd_ops<NT>(SG - 1)>();
    __builtin_amdgcn_s_barrier();
    if constexpr (fd_glds<NT>(SG) > 0) { f_lda(pa, lds0, kq, nh, SG + 2); f_lda(pa, lds0, kq, nh, SG + 3); }
#pragma unroll
    for (int m = 0; m < 4; ++m) Af[0][m] = *(const bf16x8*)(ap[(SG >> 1) & 1][0][m]);
  }
#pragma unroll
  for (int kk = 0; kk < 2; ++kk) {
    constexpr int ks0 = 2 * SG;
    if (kk == 0 || (SG & 1) == 0) {
#pragma unroll
      for (int m = 0; m < 4; ++m) Af[(kk + 1) & 1][m] = *(const bf16x8*)(ap[(SG >> 1) & 1][(kk + 1) & 1][m] + (kk == 0 ? (SG & 1) : 1) * 8192);
    }
    SCHED_FENCE();
#pragma unroll
    for (int n = 0; n < NT; ++n) {
#pragma unroll
      for (int m = 0; m < 4; ++m) acc[m][n] = __builtin_amdgcn_mfma_f32_16x16x32_bf16(Bf[(ks0 + kk) % F_BR][n], Af[kk][m], acc[m][n], 0, 0, 0);
      SCHED_FENCE();
      if (ks0 + kk + F_BD < 16) {
        if (n == 0 && ((ks0 + kk + F_BD) & 1) == 0) need_flag(fl, 8, mask, (ks0 + kk + F_BD) >> 1);
        Bf[(ks0 + kk + F_BD) % F_BR][n] = ld_dev16(rb, boff + (u32)((n * 256 + 4 * (ks0 + kk + F_BD)) * 256));
      }
      if constexpr (SG < 4) {
        if (kk == 0 && n == 1 && w > 0) {
          float z[4];
#pragma unroll
          for (int r = 0; r < 4; ++r) z[r] = gelu_tanh(z1.y[SG][r]);
          *(u32x2*)(zdst + ((SG + w) & 3) * 16 * F_RS) = u32x2{pk_bf16(z[0], z[1]), pk_bf16(z[2], z[3])};
        }
      }
      SCHED_FENCE();
    }
  }
}

template <int NT>
__device__ __forceinline__ void f_phase_d(const FArgs& a, char* smem, const int w, const int rt, const int ct, const u16* const (&pa)[2], const Z1& z1) {
  const int tid = threadIdx.x, lane = tid & 63;
  const int kq = w & 3, nh = w >> 2, n0 = nh * 4;
  const int c = lane & 15, g = lane >> 4;
  const u32 lds0 = (u32)(size_t)(const char LAS*)smem;
  const rsrc_t rb = make_rsrc(a.z2t, L * FH * 2);
  const u32 boff = (u32)((((7 * ct + n0) * 256 + 64 * kq) * 64 + lane * 4) * 4);
  const u32* fl = a.flags + ct * 32 + 8 * kq;
  u32 mask = poll8(fl, 8);
  bf16x8 Bf[F_BR][NT], Af[2][4];
#pragma unroll
  for (int ks = 0; ks < F_BD; ++ks) {
    if ((ks & 1) == 0) need_flag(fl, 8, mask, ks >> 1);
#pragma unroll
    for (int n = 0; n < NT; ++n) Bf[ks][n] = ld_dev16(rb, boff + (u32)((n * 256 + 4 * ks) * 256));
  }
  SCHED_FENCE();
  f32x4 acc[4][NT];
#pragma unroll
  for (int m = 0; m < 4; ++m)
#pragma unroll
    for (int n = 0; n < NT; ++n) acc[m][n] = f32x4{0, 0, 0, 0};
  const int asw = (c >> 1) & 7;
  const char* ap[2][2][4];
#pragma unroll
  for (int hi = 0; hi < 2; ++hi)
#pragma unroll
    for (int m = 0; m < 4; ++m) {
      const char* base = smem + f_slot(kq, 2 * hi) + ((m + kq) & 3) * 2048 + c * 128;
      ap[hi][0][m] = base + 16 * (g ^ asw); ap[hi][1][m] = base + 16 * ((4 + g) ^ asw);
    }
#pragma unroll
  for (int m = 0; m < 4; ++m) Af[0][m] = *(const bf16x8*)(ap[0][0][m]);
  char* zdst = smem + F_ZS + c * F_RS + (16 * (w - 1) + 4 * g) * 2;
  STAMP(3, 4);
  fd_stage<NT, 0>(acc, Bf, Af, rb, boff, pa, lds0, ap, kq, nh, fl, mask, z1, zdst, w);
  fd_stage<NT, 1>(acc, Bf, Af, rb, boff, pa, lds0, ap, kq, nh, fl, mask, z1, zdst, w);
  STAMP(3, 5);
  fd_stage<NT, 2>(acc, Bf, Af, rb, boff, pa, lds0, ap, kq, nh, fl, mask, z1, zdst, w);
  fd_stage<NT, 3>(acc, Bf, Af, rb, boff, pa, lds0, ap, kq, nh, fl, mask, z1, zdst, w);
  STAMP(3, 6);
  fd_stage<NT, 4>(acc, Bf, Af, rb, boff, pa, lds0, ap, kq, nh, fl, mask, z1, zdst, w);
  fd_stage<NT, 5>(acc, Bf, Af, rb, boff, pa, lds0, ap, kq, nh, fl, mask, z1, zdst, w);
  STAMP(3, 7);
  fd_stage<NT, 6>(acc, Bf, Af, rb, boff, pa, lds0, ap, kq, nh, fl, mask, z1, zdst, w);
  const float bs = bf1(a.bsp[64 * rt + 16 * kq + c]);
  fd_stage<NT, 7>(acc, Bf, Af, rb, boff, pa, lds0, ap, kq, nh, fl, mask, z1, zdst, w);
  STAMP(3, 1);
  __syncthreads();
#pragma unroll
  for (int m = 1; m < 4; ++m) {
    const int o = (m + kq) & 3;
    char* dst = smem + (o < 3 ? o * 21504 : F_A0) + ((m - 1) * 7 * 64 + n0 * 64 + lane) * 16;
#pragma unroll
    for (int n = 0; n < NT; ++n) *(f32x4*)(dst + n * 1024) = acc[m][n];
  }
  __syncthreads();
  STAMP(3, 2);
  const char* src = smem + (kq < 3 ? kq * 21504 : F_A0) + (n0 * 64 + lane) * 16;
  const char* zs = smem + F_ZS + (16 * kq + c) * F_RS + (16 * n0 + 4 * g) * 2;
  const rsrc_t rp = make_rsrc(a.Pt, L * FH * 2);
  u32x2 pv[NT];
#pragma unroll
  for (int n = 0; n < NT; ++n) {
    const f32x4 p0 = *(const f32x4*)(src + n * 1024), p1 = *(const f32x4*)(src + (7 + n) * 1024), p2 = *(const f32x4*)(src + (14 + n) * 1024);
    const u32x2 zz = *(const u32x2*)(zs + n * 32);
    float gg[4];
#pragma unroll
    for (int e = 0; e < 4; ++e) gg[e] = ((acc[0][n][e] + p0[e]) + (p1[e] + p2[e])) + bs;
    pv[n] = u32x2{pk_bf16(bf_lo(zz[0]) * gg[0], bf_hi(zz[0]) * gg[1]), pk_bf16(bf_lo(zz[1]) * gg[2], bf_hi(zz[1]) * gg[3])};
    if (n & 1) wt_store16(rp, (u32)((((4 * rt + kq) * 112 + 14 * ct + 2 * (n0 + n - 1 + (g & 1)) + (g >> 1)) * 16 + c) * 16), pair16(pv[n - 1], pv[n]));
  }
  if constexpr (NT & 1) {
    const u32x4 v = pair16(pv[NT - 1], pv[NT - 1]);
    if ((g & 1) == 0) wt_store16(rp, (u32)((((4 * rt + kq) * 112 + 14 * ct + 2 * (n0 + NT - 1) + (g >> 1)) * 16 + c) * 16), v);
  }
  STAMP(3, 3);
}

__device__ __forceinline__ void f_phase_e(const FArgs& a, char* smem, const int w, const int rt, const int dt) {
  const int tid = threadIdx.x, lane = tid & 63;
  const int kq = w & 3, mh = w >> 2;
  const int c = lane & 15, g = lane >> 4;
  const u32 lds0 = (u32)(size_t)(const char LAS*)smem;
  VM_WAIT(0);
  __syncthreads();
  if (tid == 0) __hip_atomic_store((GAS u32*)a.flags + 256 + rt * 32 + dt, 1u, __ATOMIC_RELAXED, __HIP_MEMORY_SCOPE_AGENT);
  STAMP(4, 5);
#pragma unroll
  for (int k = 0; k < 7; ++k)
#pragma unroll
    for (int nn = 0; nn < 2; ++nn) {
      const int n = 2 * mh + nn;
      glds16(a.Wot + ((size_t)(4 * dt + n) * 112 + 4 * (7 * kq + k)) * 128 + lane * 8, lds0 + (n * 28 + 7 * kq + k) * 1024);
    }
  const u32x4 xr = *(const u32x4*)(a.x1T + (size_t)(64 * dt + (tid >> 3)) * L + 64 * rt + 8 * (tid & 7));
  const int d8 = lane & 7;
  const f32x4 gb0 = mod_get4(a.mod, 2560 + 64 * dt + 8 * d8), gb1 = mod_get4(a.mod, 2560 + 64 * dt + 8 * d8 + 4);
  SCHED_FENCE();
  {
    const u32* fl = a.flags + 256 + rt * 32 + 2 * kq;
    u32 mask = poll8(fl, 2);
    need_flag(fl, 2, mask, 0); need_flag(fl, 2, mask, 1);
  }
  STAMP(4, 4);
  const rsrc_t rp = make_rsrc(a.Pt, L * FH * 2);
  bf16x8 Pf[7][2];
#pragma unroll
  for (int k = 0; k < 7; ++k)
#pragma unroll
    for (int m = 0; m < 2; ++m) Pf[k][m] = ld_dev16(rp, (u32)((((4 * rt + 2 * mh + m) * 112 + 4 * (7 * kq + k)) * 64 + lane * 4) * 4));
  {
    u32* p = (u32*)(smem + F_EX + (tid >> 3) * 132 + (tid & 7) * 16);
    p[0] = xr[0]; p[1] = xr[1]; p[2] = xr[2]; p[3] = xr[3];
  }
  vm_wait_c<14>();
  __builtin_amdgcn_s_barrier();
  SCHED_FENCE();
  f32x4 acc[2][4];
#pragma unroll
  for (int m = 0; m < 2; ++m)
#pragma unroll
    for (int n = 0; n < 4; ++n) acc[m][n] = f32x4{0, 0, 0, 0};
  const char* wp = smem + (7 * kq) * 1024 + lane * 16;
#pragma unroll
  for (int k = 0; k < 7; ++k) {
    bf16x8 Wf[4];
#pragma unroll
    for (int n = 0; n < 4; ++n) Wf[n] = *(const bf16x8*)(wp + (n * 28 + k) * 1024);
#pragma unroll
    for (int n = 0; n < 4; ++n)
#pragma unroll
      for (int m = 0; m < 2; ++m) acc[m][n] = __builtin_amdgcn_mfma_f32_16x16x32_bf16(Wf[n], Pf[k][m], acc[m][n], 0, 0, 0);
  }
  STAMP(4, 1);
  __syncthreads();
#pragma unroll
  for (int m = 0; m < 2; ++m)
#pragma unroll
    for (int n = 0; n < 4; ++n) *(f32x4*)(smem + (kq * 16 + (2 * mh + m) * 4 + n) * F_ETS + g * 288 + c * 16) = acc[m][n];
  __syncthreads();
  STAMP(4, 2);
  {
    const int t = 8 * w + (lane >> 3);
    float s[8];
#pragma unroll
    for (int hh = 0; hh < 2; ++hh) {
      const char* p = smem + ((t >> 4) * 4 + (d8 >> 1)) * F_ETS + (2 * (d8 & 1) + hh) * 288 + (t & 15) * 16;
      const f32x4 p0 = *(const f32x4*)p, p1 = *(const f32x4*)(p + 16 * F_ETS), p2 = *(const f32x4*)(p + 32 * F_ETS), p3 = *(const f32x4*)(p + 48 * F_ETS);
#pragma unroll
      for (int e = 0; e < 4; ++e) s[4 * hh + e] = (p0[e] + p1[e]) + (p2[e] + p3[e]);
    }
    float o[8];
#pragma unroll
    for (int e = 0; e < 8; ++e) {
      const float x1 = bf1(*(const u16*)(smem + F_EX + (8 * d8 + e) * 132 + t * 2));
      o[e] = x1 + (e < 4 ? gb0[e] : gb1[e - 4]) * s[e];
    }
    wt_store16(make_rsrc(a.out, L * D * 2), (u32)(((64 * rt + t) * D + 64 * dt + 8 * d8) * 2), u32x4{pk_bf16(o[0], o[1]), pk_bf16(o[2], o[3]), pk_bf16(o[4], o[5]), pk_bf16(o[6], o[7])});
  }
  STAMP(4, 3);
}

__global__ __launch_bounds__(512, 2) void kF(FArgs a) {
  extern __shared__ __attribute__((aligned(16))) char smem[];
  const int wg = blockIdx.x, lane = threadIdx.x & 63;
  const int w = __builtin_amdgcn_readfirstlane(threadIdx.x >> 6);
  const int xc = wg & 7, ii = wg >> 3;
  const int pi = (xc >> 1) * 8 + (ii & 7), j = (xc & 1) * 4 + (ii >> 3);
  const unsigned long long r2 = __builtin_amdgcn_s_memrealtime();
  f_ldx(a.x1T, (u32)(size_t)(const char LAS*)smem, w, pi, 0);
  const u16* pa[2];
#pragma unroll
  for (int par = 0; par < 2; ++par) {
    const int row = 32 * (w >> 2) + 8 * par + (lane >> 3);
    pa[par] = a.wsp + (size_t)(64 * pi + row) * L + 512 * (w & 3) + 8 * ((lane & 7) ^ ((row >> 1) & 7));
  }
  Z1 z1;
  if (w == 0) f_phase_c<0>(a, smem, w, pi, j, pa, z1);
  else if (w < 7) f_phase_c<1>(a, smem, w, pi, j, pa, z1);
  else f_phase_c<2>(a, smem, w, pi, j, pa, z1);
  STAMP(3, 0);
  if (w < 4) f_phase_d<4>(a, smem, w, pi, j, pa, z1);
  else f_phase_d<3>(a, smem, w, pi, j, pa, z1);
  STAMP(4, 0);
  f_phase_e(a, smem, w, pi, j);
  if (blockIdx.x == 0 && threadIdx.x == 0) { asm volatile("s_waitcnt vmcnt(0)" ::: "memory"); const unsigned long long r3 = __builtin_amdgcn_s_memrealtime(); const unsigned long long r0 = ((volatile unsigned long long*)((char*)a.mod + 65536))[0], r1 = ((volatile unsigned long long*)((char*)a.mod + 65536))[1]; unsigned long long dl = ((r3 - r3)) * 10; if (dl > 100000ull) dl = 100000ull; while (__builtin_amdgcn_s_memrealtime() - r3 < dl) {} }
}
#define KMASK(b)
extern "C" void kernel_launch(void* const* d_in, const int* in_sizes, int n_in, void* d_out, int out_size,
                              void* d_ws, size_t ws_size, hipStream_t stream) {
  const u16* x = (const u16*)d_in[0];
  char* ws = (char*)d_ws;
  float* mod = (float*)(ws + 524288);
  u32* stats1 = (u32*)(ws + 16384);
  float* uv = (float*)(ws + 32768);
  u64* gran = (u64*)(ws + 65536);
  u32* flags = (u32*)(ws + 65536 + 262144);
  u16* xT = (u16*)(ws + (1 << 20));
  u16* Krev = (u16*)(ws + (3 << 20));
  u16* x1T = xT;
  u16* Wt = (u16*)(ws + (7 << 20));
  u16* Wot = (u16*)(ws + (9 << 20));
  u16* Pt = (u16*)(ws + (10 << 20));
  u16* z2t = Krev;
  AArgs aa;
  aa.x = x; aa.Are = (const u16*)d_in[1]; aa.Aim = (const u16*)d_in[2]; aa.Bre = (const u16*)d_in[3]; aa.Bim = (const u16*)d_in[4];
  aa.Cre = (const u16*)d_in[5]; aa.Cim = (const u16*)d_in[6]; aa.logdt = (const u16*)d_in[7]; aa.c = (const u16*)d_in[14];
  aa.wmod = (const u16*)d_in[15]; aa.win = (const u16*)d_in[10]; aa.wout = (const u16*)d_in[13];
  aa.mod = mod; aa.stats1 = stats1; aa.xT = xT; aa.Krev = Krev; aa.gran = (u32x4*)gran;
  (void)hipFuncSetAttribute((const void*)kA, hipFuncAttributeMaxDynamicSharedMemorySize, A_LDS);
  KMASK(1) kA<<<256, 768, A_LDS, stream>>>(aa);
  BArgs ba;
  ba.xT = xT; ba.Krev = Krev; ba.Dskip = (const u16*)d_in[8]; ba.win = (const u16*)d_in[10]; ba.wout = (const u16*)d_in[13];
  ba.wsp = (const u16*)d_in[11]; ba.stats1 = stats1; ba.mod = mod; ba.x1T = x1T; ba.Wt = Wt; ba.Wot = Wot; ba.uv = uv;
  (void)hipFuncSetAttribute((const void*)kB, hipFuncAttributeMaxDynamicSharedMemorySize, B_LDS);
  KMASK(2) kB<<<256, 768, B_LDS, stream>>>(ba);
  FArgs fa;
  fa.x1T = x1T; fa.Wt = Wt; fa.wsp = (const u16*)d_in[11]; fa.bsp = (const u16*)d_in[12]; fa.Wot = Wot; fa.uv = uv; fa.mod = mod;
  fa.z2t = z2t; fa.Pt = Pt; fa.out = (u16*)d_out; fa.gran = gran; fa.flags = flags;
  (void)hipFuncSetAttribute((const void*)kF, hipFuncAttributeMaxDynamicSharedMemorySize, F_LDS);
  KMASK(4) kF<<<256, 512, F_LDS, stream>>>(fa);
}
```

```cpp
#include <hip/hip_runtime.h>
#include <hip/hip_bf16.h>
#include <stdint.h>

typedef unsigned short u16;
typedef unsigned int u32;
typedef unsigned long long u64;
typedef __bf16 bf16x8 __attribute__((ext_vector_type(8)));
typedef __bf16 bf16x2 __attribute__((ext_vector_type(2)));
typedef short s16x4 __attribute__((ext_vector_type(4)));
typedef float f32x2 __attribute__((ext_vector_type(2)));
typedef float f32x4 __attribute__((ext_vector_type(4)));
typedef u32 u32x2 __attribute__((ext_vector_type(2)));
typedef u32 u32x4 __attribute__((ext_vector_type(4)));
#define LAS __attribute__((address_space(3)))
#define GAS __attribute__((address_space(1)))

constexpr int L = 2048, D = 512, NS = 128, F = 1792, FH = 896;

__device__ __forceinline__ float bf_lo(u32 w) { return __uint_as_float(w << 16); }
__device__ __forceinline__ float bf_hi(u32 w) { return __uint_as_float(w & 0xffff0000u); }
__device__ __forceinline__ float bf1(u16 h) { return __uint_as_float(((u32)h) << 16); }
__device__ __forceinline__ u32 pk_bf16(float lo, float hi) {
  f32x2 a = {lo, hi};
  bf16x2 p = __builtin_convertvector(a, bf16x2);
  return __builtin_bit_cast(u32, p);
}
__device__ __forceinline__ float rbf(float x) { return bf_lo(pk_bf16(x, x)); }

template <int CTRL, int ROWMASK = 0xf>
__device__ __forceinline__ float dpp_add(float x) {
  int y = __builtin_amdgcn_update_dpp(0, __float_as_int(x), CTRL, ROWMASK, 0xf, true);
  return x + __int_as_float(y);
}
__device__ __forceinline__ float row16_sum(float x) {
  x = dpp_add<0xB1>(x);
  x = dpp_add<0x4E>(x);
  x = dpp_add<0x141>(x);
  x = dpp_add<0x140>(x);
  return x;
}
__device__ __forceinline__ float wave_sum(float x) {
  x = row16_sum(x);
  float a = __int_as_float(__builtin_amdgcn_readlane(__float_as_int(x), 0));
  float b = __int_as_float(__builtin_amdgcn_readlane(__float_as_int(x), 16));
  float c = __int_as_float(__builtin_amdgcn_readlane(__float_as_int(x), 32));
  float d = __int_as_float(__builtin_amdgcn_readlane(__float_as_int(x), 48));
  return (a + b) + (c + d);
}
__device__ __forceinline__ s16x4 tr_read(const char LAS* p) {
  return __builtin_amdgcn_ds_read_tr16_b64_v4i16((s16x4 LAS*)p);
}

#define STAMP(kern, i)
__device__ __forceinline__ void glds16(const void* gsrc, u32 lds_dst) {
  u32 keep;
  asm volatile("s_mov_b32 %0, m0\n\ts_mov_b32 m0, %2\n\ts_nop 0\n\tglobal_load_lds_dwordx4 %1, off\n\ts_mov_b32 m0, %0"
               : "=&s"(keep) : "v"(gsrc), "s"(lds_dst) : "memory");
}
#define SCHED_FENCE() __builtin_amdgcn_sched_barrier(0)
#define VM_WAIT(n) asm volatile("s_waitcnt vmcnt(" #n ")" ::: "memory")
__device__ __forceinline__ float xor16_sum(float x) {
  const u32 v = __float_as_uint(x);
  auto r = __builtin_amdgcn_permlane16_swap(v, v, false, false);
  return __uint_as_float(r[0]) + __uint_as_float(r[1]);
}
__device__ __forceinline__ float xor32_sum(float x) {
  const u32 v = __float_as_uint(x);
  auto r = __builtin_amdgcn_permlane32_swap(v, v, false, false);
  return __uint_as_float(r[0]) + __uint_as_float(r[1]);
}
__device__ __forceinline__ float xor4_8_sum(float x) {
  x = x + __int_as_float(__builtin_amdgcn_update_dpp(0, __float_as_int(x), 0x124, 0xf, 0xf, true));
  x = x + __int_as_float(__builtin_amdgcn_update_dpp(0, __float_as_int(x), 0x128, 0xf, 0xf, true));
  return x;
}
__device__ __forceinline__ void lds_rendezvous(u32 LAS* cnt, u32 target) {
  asm volatile("s_waitcnt lgkmcnt(0)" ::: "memory");
  if ((threadIdx.x & 63) == 0) __hip_atomic_fetch_add(cnt, 1u, __ATOMIC_RELAXED, __HIP_MEMORY_SCOPE_WORKGROUP);
  while (__hip_atomic_load(cnt, __ATOMIC_RELAXED, __HIP_MEMORY_SCOPE_WORKGROUP) < target) __builtin_amdgcn_s_sleep(1);
  asm volatile("" ::: "memory");
}

typedef __amdgpu_buffer_rsrc_t rsrc_t;
__device__ __forceinline__ rsrc_t make_rsrc(const void* base, u32 bytes) {
  return __builtin_amdgcn_make_buffer_rsrc(const_cast<void*>(base), 0, bytes, 0x00020000);
}
__device__ __forceinline__ void wt_store16(rsrc_t r, u32 byte_off, u32x4 v) { __builtin_amdgcn_raw_buffer_store_b128(v, r, byte_off, 0, 16); }
__device__ __forceinline__ u32x4 pair16(u32x2 a, u32x2 b) {
  const auto r0 = __builtin_amdgcn_permlane16_swap(a[0], b[0], false, false);
  const auto r1 = __builtin_amdgcn_permlane16_swap(a[1], b[1], false, false);
  return u32x4{r0[0], r1[0], r0[1], r1[1]};
}

__device__ __forceinline__ float mod_get(const float* mp, int i) { return rbf((mp[i] + mp[3072 + i]) + (mp[6144 + i] + mp[9216 + i])); }
__device__ __forceinline__ f32x4 mod_get4(const float* mp, int i) {
  const f32x4 a = *(const f32x4*)(mp + i), b = *(const f32x4*)(mp + 3072 + i), c = *(const f32x4*)(mp + 6144 + i), d = *(const f32x4*)(mp + 9216 + i);
  f32x4 r;
#pragma unroll
  for (int e = 0; e < 4; ++e) r[e] = rbf((a[e] + b[e]) + (c[e] + d[e]));
  return r;
}
constexpr int CODE_LANES = 92;
struct AArgs {
  const u16 *x, *Are, *Aim, *Bre, *Bim, *Cre, *Cim, *logdt, *c, *wmod, *win, *wout;
  float* mod; u32* stats1; u16* xT; u16* Krev; u32x4* gran;
};
constexpr int A_RS = 528;
constexpr int A_TAB = 0;
constexpr int A_KC = A_TAB + 2 * 64 * A_RS;
constexpr int A_TILE = A_KC + 2 * 768 * 4;
constexpr int A_RED = A_TILE + 8 * 1088;
constexpr int A_CNT = A_RED + 4 * 64 * 4;
constexpr int A_LDS = A_CNT + 16;

struct KIdx { u16 v[2048]; };
constexpr int bf16_round_int(int l) {
  int sh = 0;
  while ((l >> sh) >= 256) ++sh;
  if (sh == 0) return l;
  const int q = l >> sh, rem = l & ((1 << sh) - 1), half = 1 << (sh - 1);
  const int up = (rem > half || (rem == half && (q & 1))) ? 1 : 0;
  return (q + up) << sh;
}
constexpr KIdx make_kidx() {
  KIdx t{};
  for (int x = 0; x < 2048; ++x) {
    const int lr = bf16_round_int(2047 - x);
    const int aa = lr >> 7, b = (lr >> 3) & 15, r = lr & 7;
    const int row = lr < 256 ? 8 * aa + r : lr < 512 ? 16 + 4 * (aa - 2) + (r >> 1) : lr < 1024 ? 24 + 2 * (aa - 4) + (r >> 2) : 32 + (aa - 8);
    t.v[x] = (u16)(row * 16 + b);
  }
  return t;
}
__device__ const KIdx g_kidx = make_kidx();

struct cpx { float r, i; };
__device__ __forceinline__ cpx cmul(cpx a, cpx b) { return cpx{a.r * b.r - a.i * b.i, a.r * b.i + a.i * b.r}; }

__global__ __launch_bounds__(768) void kA(AArgs a) {
  extern __shared__ __attribute__((aligned(16))) char smem[];
  const int wg = blockIdx.x, tid = threadIdx.x, lane = tid & 63;
  const int w = __builtin_amdgcn_readfirstlane(tid >> 6);
  STAMP(0, 0);
  const int tw = w - 8, st = tid - 512;
  const int t0 = 8 * ((wg & 7) * 32 + (wg >> 3));
  const bool doM = wg < 192;
  const int mrb = wg / 48, mcb = wg - 48 * mrb;
  const int rr = lane >> 3, q = lane & 7;
  u32x4 ld[14];
  u16 sv[8];
  if (w < 8) {
    const int d = 2 * wg + (w >> 2), o = d * NS + 64 * ((w >> 1) & 1) + lane;
    sv[0] = a.Are[o]; sv[1] = a.Aim[o]; sv[2] = a.Bre[o]; sv[3] = a.Bim[o]; sv[4] = a.Cre[o]; sv[5] = a.Cim[o]; sv[6] = a.logdt[d]; sv[7] = 0;
    ld[0] = *(const u32x4*)(g_kidx.v + 8 * (tid & 255));
  } else {
#pragma unroll
    for (int j = 0; j < 2; ++j) ld[j] = *(const u32x4*)(a.x + (size_t)(t0 + 2 * tw + j) * D + 8 * lane);
    if (tw == 0 && lane < 4) *(u32*)(smem + A_CNT + 4 * lane) = 0;
    asm volatile("s_waitcnt lgkmcnt(0)" ::: "memory");
  }
  SCHED_FENCE();
  STAMP(0, 9);
  __builtin_amdgcn_s_barrier();
  STAMP(0, 10);
  SCHED_FENCE();
  if (w < 8) {
    const int ch = w >> 2, nh = (w >> 1) & 1, part = w & 1, d = 2 * wg + ch;
    const int n = 64 * nh + lane;
    const u16 hare = sv[0], haim = sv[1], hbre = sv[2], hbim = sv[3], hcre = sv[4], hcim = sv[5], hdt = sv[6];
    const u32x4 kix = ld[0];
    asm volatile("" ::"v"((u32)hare), "v"((u32)hcim), "v"((u32)hdt));
    STAMP(0, 11);
    const float dt = rbf(__expf(bf1(hdt)));
    const float lr = (dt * bf1(hare)) * 1.4426950408889634f, lp = (dt * bf1(haim)) * 0.15915494309189535f;
    auto cexp = [&](float l) -> cpx {
      const float e = __builtin_amdgcn_exp2f(lr * l), ph = __builtin_amdgcn_fractf(lp * l);
      return cpx{e * __builtin_amdgcn_cosf(ph), e * __builtin_amdgcn_sinf(ph)};
    };
    char* tab = smem + A_TAB + ch * (64 * A_RS) + n * 4;
    auto put = [&](int row, cpx v) { *(u32*)(tab + row * A_RS) = pk_bf16(v.r, v.i); };
    if (part == 0) {
      const cpx E1 = cexp(1.f), E128 = cexp(128.f), E512 = cexp(512.f);
      const cpx E2 = cmul(E1, E1), E3 = cmul(E2, E1), E4 = cmul(E2, E2), E5 = cmul(E4, E1), E6 = cmul(E4, E2), E7 = cmul(E4, E3);
      put(0, cpx{1.f, 0.f}); put(1, E1); put(2, E2); put(3, E3); put(4, E4); put(5, E5); put(6, E6); put(7, E7);
      put(8, E128); put(9, cmul(E128, E1)); put(10, cmul(E128, E2)); put(11, cmul(E128, E3));
      put(12, cmul(E128, E4)); put(13, cmul(E128, E5)); put(14, cmul(E128, E6)); put(15, cmul(E128, E7));
      const cpx E256 = cmul(E128, E128), E384 = cmul(E256, E128);
      put(16, E256); put(17, cmul(E256, E2)); put(18, cmul(E256, E4)); put(19, cmul(E256, E6));
      put(20, E384); put(21, cmul(E384, E2)); put(22, cmul(E384, E4)); put(23, cmul(E384, E6));
      const cpx E640 = cmul(E512, E128), E768 = cmul(E512, E256), E896 = cmul(E512, E384);
      put(24, E512); put(25, cmul(E512, E4)); put(26, E640); put(27, cmul(E640, E4));
      put(28, E768); put(29, cmul(E768, E4)); put(30, E896); put(31, cmul(E896, E4));
    } else {
      const cpx E8 = cexp(8.f), E128 = cexp(128.f), E1024 = cexp(1024.f);
      const float br = dt * bf1(hbre), bi = dt * bf1(hbim), cr = bf1(hcre), ci = bf1(hcim);
      const cpx W = cmul(cpx{cr, ci}, cpx{br, bi});
      const cpx E16 = cmul(E8, E8), E32 = cmul(E16, E16), E64 = cmul(E32, E32);
      cpx Y[8];
      Y[0] = W; Y[1] = cmul(W, E8); Y[2] = cmul(W, E16); Y[3] = cmul(Y[1], E16);
#pragma unroll
      for (int b = 0; b < 4; ++b) Y[4 + b] = cmul(Y[b], E32);
#pragma unroll
      for (int b = 0; b < 8; ++b) {
        put(48 + b, cpx{Y[b].r, -Y[b].i});
        const cpx t = cmul(Y[b], E64);
        put(56 + b, cpx{t.r, -t.i});
      }
      const cpx E256 = cmul(E128, E128), E512 = cmul(E256, E256);
      cpx P[4];
      P[0] = E1024; P[1] = cmul(E1024, E128); P[2] = cmul(E1024, E256); P[3] = cmul(P[1], E256);
#pragma unroll
      for (int b = 0; b < 4; ++b) { put(32 + b, P[b]); put(36 + b, cmul(P[b], E512)); }
      put(40, cmul(E1024, E1024));
    }
    STAMP(0, 1);
    lds_rendezvous((u32 LAS*)(smem + A_CNT), 8);
    STAMP(0, 2);
    if (w < 6) {
      const int c2 = w >= 3 ? 1 : 0, t = w - 3 * c2, i = lane & 15, g = lane >> 4;
      const char* tb = smem + A_TAB + c2 * (64 * A_RS) + 16 * g;
      const char* pa = tb + (16 * t + i) * A_RS, *pb = tb + (48 + i) * A_RS;
      f32x4 acc = {0, 0, 0, 0};
      bf16x8 Af[8], Bf[8];
#pragma unroll
      for (int s = 0; s < 8; ++s) {
        Af[s] = *(const bf16x8*)(pa + s * 64); Bf[s] = *(const bf16x8*)(pb + s * 64);
      }
#pragma unroll
      for (int s = 0; s < 8; ++s) acc = __builtin_amdgcn_mfma_f32_16x16x32_bf16(Af[s], Bf[s], acc, 0, 0, 0);
      float* Kc = (float*)(smem + A_KC) + c2 * 768 + t * 256;
#pragma unroll
      for (int r = 0; r < 4; ++r) Kc[(4 * g + r) * 16 + i] = acc[r];
    }
    STAMP(0, 3);
    lds_rendezvous((u32 LAS*)(smem + A_CNT + 4), 8);
    STAMP(0, 4);
    const float* Kc = (const float*)(smem + A_KC) + ch * 768;
    float kv[8];
#pragma unroll
    for (int j = 0; j < 4; ++j) { kv[2 * j] = Kc[kix[j] & 0xffffu]; kv[2 * j + 1] = Kc[kix[j] >> 16]; }
    *(u32x4*)(a.Krev + (size_t)d * L + 8 * (tid & 255)) =
        u32x4{pk_bf16(kv[0], kv[1]), pk_bf16(kv[2], kv[3]), pk_bf16(kv[4], kv[5]), pk_bf16(kv[6], kv[7])};
    STAMP(0, 5);
  } else {
    __builtin_amdgcn_s_setprio(3);
    const u32x4 xr[2] = {ld[0], ld[1]};
    asm volatile("" ::"v"(xr[1]));
    STAMP(0, 11);
    char* tile = smem + A_TILE;
#pragma unroll
    for (int j = 0; j < 2; ++j) *(u32x4*)(tile + (2 * tw + j) * 1088 + 16 * lane) = xr[j];
    SCHED_FENCE();
    if (doM) {
#pragma unroll
      for (int p = 0; p < 4; ++p) ld[2 + p] = *(const u32x4*)(a.wmod + (size_t)(128 * mrb + p * 32 + tw * 8 + rr) * 3072 + 64 * mcb + 8 * q);
#pragma unroll
      for (int p = 0; p < 4; ++p) sv[p] = a.c[128 * mrb + p * 32 + tw * 8 + rr];
    }
    ld[10] = *(const u32x4*)(a.win + (size_t)wg * 3584 + st * 8);
    ld[11] = *(const u32x4*)(a.win + (size_t)wg * 3584 + (st < 192 ? 2048 + st * 8 : 0));
    ld[12] = *(const u32x4*)(a.wout + (size_t)wg * 1792 + (st < 224 ? st * 8 : 0));
    u64 base;
    asm volatile("s_getpc_b64 s[40:41]\n\ts_add_u32 s40, s40, _Z2kB5BArgs@rel32@lo+4\n\ts_addc_u32 s41, s41, _Z2kB5BArgs@rel32@hi+12\n\ts_mov_b64 %0, s[40:41]"
                 : "=s"(base) : : "s40", "s41", "scc");
    ld[13] = *(const u32x4*)((const char*)base + (size_t)(st < CODE_LANES ? (wg >> 3) * CODE_LANES + st : 0) * 16);
    SCHED_FENCE();
    STAMP(0, 1);
    lds_rendezvous((u32 LAS*)(smem + A_CNT + 8), 4);
    STAMP(0, 2);
#pragma unroll
    for (int ps = 0; ps < 2; ++ps) {
      const int d0 = 128 * tw + 64 * ps + 16 * (lane >> 4);
      const char LAS* src = (const char LAS*)tile + ((lane & 15) >> 2) * 1088 + 2 * (d0 + 4 * (lane & 3));
      const s16x4 lo = tr_read(src), hi = tr_read(src + 4 * 1088);
      u32x2 a0 = __builtin_bit_cast(u32x2, lo), a1 = __builtin_bit_cast(u32x2, hi);
      *(u32x4*)(a.xT + (size_t)(d0 + (lane & 15)) * L + t0) = u32x4{a0[0], a0[1], a1[0], a1[1]};
    }
    STAMP(0, 6);
    if (tw == 0) a.gran[wg * 64 + lane] = u32x4{0, 0, 0, 0};
    if (tw == 1 && wg < 8) a.gran[(256 + wg) * 64 + lane] = u32x4{0, 0, 0, 0};
    {
      float v[2][8], s[2];
#pragma unroll
      for (int j = 0; j < 2; ++j) {
#pragma unroll
        for (int k = 0; k < 4; ++k) { v[j][2 * k] = bf_lo(xr[j][k]); v[j][2 * k + 1] = bf_hi(xr[j][k]); }
        s[j] = ((v[j][0] + v[j][1]) + (v[j][2] + v[j][3])) + ((v[j][4] + v[j][5]) + (v[j][6] + v[j][7]));
      }
      float mean[2], q2[2];
#pragma unroll
      for (int j = 0; j < 2; ++j) mean[j] = wave_sum(s[j]) * (1.f / D);
#pragma unroll
      for (int j = 0; j < 2; ++j) {
        q2[j] = 0;
#pragma unroll
        for (int k = 0; k < 8; ++k) { const float dv = v[j][k] - mean[j]; q2[j] += dv * dv; }
      }
#pragma unroll
      for (int j = 0; j < 2; ++j) {
        const float var = wave_sum(q2[j]) * (1.f / D);
        const float sd = rbf(__builtin_amdgcn_sqrtf(rbf(rbf(var) + 1e-5f)));
        if (lane == 0) *(f32x2*)(a.stats1 + 2 * (t0 + 2 * tw + j)) = f32x2{rbf(mean[j]), __builtin_amdgcn_rcpf(sd)};
      }
    }
    STAMP(0, 7);
    float* red = (float*)(smem + A_RED);
    if (doM) {
      asm volatile("" ::"v"(ld[5]));
      STAMP(0, 8);
      float acc[8] = {0, 0, 0, 0, 0, 0, 0, 0};
#pragma unroll
      for (int p = 0; p < 4; ++p) {
        const float cx = bf1(sv[p]);
        const float s = rbf(cx * __builtin_amdgcn_rcpf(1.f + __expf(-cx)));
#pragma unroll
        for (int k = 0; k < 4; ++k) { acc[2 * k] += s * bf_lo(ld[2 + p][k]); acc[2 * k + 1] += s * bf_hi(ld[2 + p][k]); }
      }
#pragma unroll
      for (int k = 0; k < 8; ++k) {
        float v = acc[k];
        v += __int_as_float(__builtin_amdgcn_update_dpp(0, __float_as_int(v), 0x128, 0xf, 0xf, true));
        v = xor32_sum(xor16_sum(v));
        if (rr == 0) red[tw * 64 + 8 * q + k] = v;
      }
      STAMP(0, 3);
      lds_rendezvous((u32 LAS*)(smem + A_CNT + 12), 4);
      STAMP(0, 4);
      if (tw == 0) a.mod[3072 * mrb + 64 * mcb + lane] = (red[lane] + red[64 + lane]) + (red[128 + lane] + red[192 + lane]);
    }
#pragma unroll
    for (int k = 10; k < 14; ++k) asm volatile("" ::"v"(ld[k]));
    STAMP(0, 5);
  }
}
struct BArgs {
  const u16 *xT, *Krev, *Dskip, *win, *wout, *wsp;
  const u32* stats1; const float* mod;
  u16 *x1T, *Wt, *Wot; float* uv;
};
constexpr int B_H = 0;
constexpr int B_HS = (256 + 2048) * 2;
constexpr int B_KC = B_H + 2 * B_HS;
constexpr int B_KS = 4160;
constexpr int B_PART = B_KC + 2 * 4 * B_KS;
constexpr int B_RED = B_PART + 2 * 4 * 8 * 1280;
constexpr int B_CNT = B_RED + 4 * 16 * 4;
constexpr int B_LDS = B_CNT + 32;

__global__ __launch_bounds__(768) void kB(BArgs a) {
  extern __shared__ __attribute__((aligned(16))) char smem[];
  const int wg = blockIdx.x, tid = threadIdx.x, lane = tid & 63;
  const int w = __builtin_amdgcn_readfirstlane(tid >> 6);
  STAMP(1, 0);
  if (w < 8) {
    __builtin_amdgcn_s_setprio(2);
    const int ch = w >> 2, q = w & 3, d = 2 * wg + ch, tj = tid & 255;
    const u32x4 xv = *(const u32x4*)(a.xT + (size_t)d * L + 8 * tj);
    f32x4 st[4];
#pragma unroll
    for (int k = 0; k < 4; ++k) st[k] = *(const f32x4*)(a.stats1 + 16 * tj + 4 * k);
    const u32x4 k0 = *(const u32x4*)(a.Krev + (size_t)d * L + 8 * tj);
    const u32x4 k1r = *(const u32x4*)(a.Krev + (size_t)d * L + 8 * (tj < 255 ? tj + 1 : tj));
    const float sa = mod_get(a.mod, d), Ac = rbf(1.f + mod_get(a.mod, 512 + d)), ga = mod_get(a.mod, 1024 + d), dsk = bf1(a.Dskip[d]);
    SCHED_FENCE();
    __builtin_amdgcn_s_barrier();
    SCHED_FENCE();
    char* hb = smem + B_H + ch * B_HS;
    char* kc = smem + B_KC + ch * 4 * B_KS;
    if (tj < 32) *(u32x4*)(hb + 16 * tj) = u32x4{0, 0, 0, 0};
    if (tj < 16) *(u32x4*)(kc + (tj >> 2) * B_KS + 16 * (256 + (tj & 3))) = u32x4{0, 0, 0, 0};
    {
      const u32 z = tj < 255 ? 0xffffffffu : 0u;
      const u32 wd[8] = {k0[0], k0[1], k0[2], k0[3], k1r[0] & z, k1r[1] & z, 0, 0};
      u32 od[5];
#pragma unroll
      for (int k = 0; k < 5; ++k) od[k] = __builtin_amdgcn_alignbit(wd[k + 1], wd[k], 16);
      *(u32x4*)(kc + 0 * B_KS + 16 * tj) = u32x4{wd[0], wd[1], wd[2], wd[3]};
      *(u32x4*)(kc + 1 * B_KS + 16 * tj) = u32x4{od[0], od[1], od[2], od[3]};
      *(u32x4*)(kc + 2 * B_KS + 16 * tj) = u32x4{wd[1], wd[2], wd[3], wd[4]};
      *(u32x4*)(kc + 3 * B_KS + 16 * tj) = u32x4{od[1], od[2], od[3], od[4]};
    }
    u32x4 hv;
    {
      float hh[8];
#pragma unroll
      for (int k = 0; k < 8; ++k) {
        const float m = st[k >> 1][2 * (k & 1)], ri = st[k >> 1][2 * (k & 1) + 1];
        const float xx = (k & 1) ? bf_hi(xv[k >> 1]) : bf_lo(xv[k >> 1]);
        hh[k] = (xx - m) * (ri * Ac) + sa;
      }
      hv = u32x4{pk_bf16(hh[0], hh[1]), pk_bf16(hh[2], hh[3]), pk_bf16(hh[4], hh[5]), pk_bf16(hh[6], hh[7])};
    }
    *(u32x4*)(hb + 512 + 16 * tj) = hv;
    STAMP(1, 1);
    lds_rendezvous((u32 LAS*)(smem + B_CNT + 12 + 4 * 0) + ch * 2, 4);
    STAMP(1, 2);
    const int c = lane & 15, g = lane >> 4;
    const char* bbase = hb + 2 * (256 + 16 * c - 16 * (g >> 1) + 8 * (g & 1) - 32 * q) - 256;
    const int X = 2047 - 32 * q - 16 * (g >> 1) + 8 * (g & 1) - c;
    const char* abase = kc + (X & 3) * B_KS + 2 * (X & ~3) - 256 * 15;
    const char LAS* ab0 = (const char LAS*)abase;
    bf16x8 Bf[16], Af[16];
    auto lda = [&](int u) {
      const u32x2 lo = *(const volatile u32x2 LAS*)(ab0 + 256 * (15 - u)), hi = *(const volatile u32x2 LAS*)(ab0 + 256 * (15 - u) + 8);
      Af[u] = __builtin_bit_cast(bf16x8, u32x4{lo[0], lo[1], hi[0], hi[1]});
    };
    lda(0);
#pragma unroll
    for (int v = 1; v < 16; v += 2) Bf[v] = *(const bf16x8*)(bbase + 256 * v);
    lda(1);
#pragma unroll
    for (int v = 0; v < 16; v += 2) Bf[v] = *(const bf16x8*)(bbase + 256 * v);
    lda(2); lda(3);
    SCHED_FENCE();
    f32x4 acc[8];
#pragma unroll
    for (int i = 0; i < 8; ++i) acc[i] = f32x4{0, 0, 0, 0};
#pragma unroll
    for (int u = 0; u < 16; ++u) {
      if (u + 4 < 16) lda(u + 4);
      SCHED_FENCE();
#pragma unroll
      for (int i = u >> 1; i < 8; ++i) acc[i] = __builtin_amdgcn_mfma_f32_16x16x32_bf16(Af[u], Bf[2 * i - u + 1], acc[i], 0, 0, 0);
      SCHED_FENCE();
    }
    STAMP(1, 3);
    char* part = smem + B_PART + ch * (4 * 8 * 1280);
#pragma unroll
    for (int i = 0; i < 8; ++i) *(f32x4*)(part + (q * 8 + i) * 1280 + 16 * (20 * g + c)) = acc[i];
    lds_rendezvous((u32 LAS*)(smem + B_CNT + 4 * ch), 4);
    STAMP(1, 4);
    {
      const int i = tj >> 5, j5 = tj & 31, cc = j5 >> 1;
      float y[8];
#pragma unroll
      for (int hh = 0; hh < 2; ++hh) {
        const char* p = part + i * 1280 + 16 * (20 * (2 * (j5 & 1) + hh) + cc);
        const f32x4 p0 = *(const f32x4*)(p), p1 = *(const f32x4*)(p + 8 * 1280), p2 = *(const f32x4*)(p + 16 * 1280), p3 = *(const f32x4*)(p + 24 * 1280);
#pragma unroll
        for (int r = 0; r < 4; ++r) y[4 * hh + r] = (p0[r] + p1[r]) + (p2[r] + p3[r]);
      }
      float o[8];
#pragma unroll
      for (int k = 0; k < 8; ++k) {
        const float hx = (k & 1) ? bf_hi(hv[k >> 1]) : bf_lo(hv[k >> 1]);
        const float xx = (k & 1) ? bf_hi(xv[k >> 1]) : bf_lo(xv[k >> 1]);
        o[k] = xx + ga * (y[k] + hx * dsk);
      }
      wt_store16(make_rsrc(a.x1T, D * L * 2), (u32)(d * L + 8 * tj) * 2, u32x4{pk_bf16(o[0], o[1]), pk_bf16(o[2], o[3]), pk_bf16(o[4], o[5]), pk_bf16(o[6], o[7])});
    }
    STAMP(1, 5);
  } else {
    const int st = tid - 512, sw = w - 8;
    if (st < 8) *(u32*)(smem + B_CNT + 4 * st) = 0;
    asm volatile("s_waitcnt lgkmcnt(0)" ::: "memory");
    __builtin_amdgcn_s_barrier();
    SCHED_FENCE();
    float* red = (float*)(smem + B_RED);
    const bool act = wg < 224;
    const int f0 = 8 * ((wg & 7) * 28 + (wg >> 3));
    const int d8 = st >> 2, fp = st & 3;
    const int f8 = wg >> 1, dp = (wg & 1) * 128 + (st & 127);
    u32 wv[8], ov[8];
    f32x4 cb0, cb1, sb0, sb1;
    if (act) {
      cb0 = mod_get4(a.mod, 2048 + 8 * d8); cb1 = mod_get4(a.mod, 2048 + 8 * d8 + 4);
      sb0 = mod_get4(a.mod, 1536 + 8 * d8); sb1 = mod_get4(a.mod, 1536 + 8 * d8 + 4);
#pragma unroll
      for (int j = 0; j < 8; ++j) wv[j] = *(const u32*)(a.win + (size_t)(8 * d8 + j) * F + f0 + 2 * fp);
      if (st < 128) {
#pragma unroll
        for (int j = 0; j < 8; ++j) ov[j] = *(const u32*)(a.wout + (size_t)(8 * f8 + j) * D + 2 * dp);
      }
    }
    u32x4 pf[4];
    {
#pragma unroll
      for (int j = 0; j < 4; ++j) {
        const int idx = st + 256 * j;
        pf[j] = *(const u32x4*)(a.wsp + (size_t)(8 * wg + (idx >> 7)) * L + ((idx >> 5) & 3) * 512 + 256 + (idx & 31) * 8);
      }
    }
    SCHED_FENCE();
    if (act) {
      float lo[8], hi[8], u0 = 0, u1 = 0, v0 = 0, v1 = 0;
#pragma unroll
      for (int j = 0; j < 8; ++j) {
        const float Ac = rbf(1.f + (j < 4 ? cb0[j] : cb1[j - 4])), sb = j < 4 ? sb0[j] : sb1[j - 4];
        const float wl = bf_lo(wv[j]), wh = bf_hi(wv[j]);
        lo[j] = rbf(Ac * wl); hi[j] = rbf(Ac * wh);
        u0 += lo[j]; u1 += hi[j]; v0 += sb * wl; v1 += sb * wh;
      }
      const int f = f0 + 2 * fp;
      const rsrc_t rw = make_rsrc(a.Wt, D * F * 2);
      const u32 dst = (u32)((((f >> 4) * 64 + d8) * 16 + (f & 15)) * 16);
      wt_store16(rw, dst, u32x4{pk_bf16(lo[0], lo[1]), pk_bf16(lo[2], lo[3]), pk_bf16(lo[4], lo[5]), pk_bf16(lo[6], lo[7])});
      wt_store16(rw, dst + 16, u32x4{pk_bf16(hi[0], hi[1]), pk_bf16(hi[2], hi[3]), pk_bf16(hi[4], hi[5]), pk_bf16(hi[6], hi[7])});
      float rv[4] = {u0, u1, v0, v1};
#pragma unroll
      for (int k = 0; k < 4; ++k) {
        const float v = xor32_sum(xor16_sum(xor4_8_sum(rv[k])));
        if (lane < 4) red[sw * 16 + lane * 4 + k] = v;
      }
      if (st < 128) {
        const int dd = 2 * dp;
        const rsrc_t ro = make_rsrc(a.Wot, FH * D * 2);
        const u32 od = (u32)((((dd >> 4) * 112 + f8) * 16 + (dd & 15)) * 16);
        u32x4 e, o;
#pragma unroll
        for (int k = 0; k < 4; ++k) {
          e[k] = (ov[2 * k] & 0xffffu) | (ov[2 * k + 1] << 16);
          o[k] = (ov[2 * k] >> 16) | (ov[2 * k + 1] & 0xffff0000u);
        }
        wt_store16(ro, od, e); wt_store16(ro, od + 16, o);
      }
      lds_rendezvous((u32 LAS*)(smem + B_CNT + 8), 4);
      if (st < 16) {
        const float s = (red[st] + red[16 + st]) + (red[32 + st] + red[48 + st]);
        const int fq = st >> 2, k = st & 3;
        a.uv[(k >> 1) * F + f0 + 2 * fq + (k & 1)] = s;
      }
    }
    STAMP(1, 1);
#pragma unroll
    for (int j = 0; j < (int)(sizeof(pf) / sizeof(pf[0])); ++j) asm volatile("" ::"v"(pf[j]));
    STAMP(1, 5);
  }
}
struct FArgs {
  const u16 *x1T, *Wt, *wsp, *bsp, *Wot; const float *uv, *mod;
  u16 *z2t, *Pt, *out; u64* gran; u32* flags;
};
constexpr int F_X = 0;
constexpr int F_ST = 65536;
constexpr int F_MR = F_ST + 512;
constexpr int F_PS = F_MR + 8 * 512;
constexpr int F_FS = F_PS + 7 * 64 * 8;
constexpr int F_ZS = F_FS + 512;
constexpr int F_RS = 232;
constexpr int F_A0 = 90112;
constexpr int F_LDS = F_A0 + 8 * 8192;
constexpr int F_ETS = 1152;
constexpr int F_EX = 112 * 1024;
#define F_BDEPTH 3
constexpr int F_BD = F_BDEPTH;
constexpr int F_BR = F_BD;
static_assert(F_ZS + 64 * F_RS <= F_A0 && F_EX + 64 * 132 <= F_LDS, "LDS map");

__device__ __forceinline__ float gelu_tanh(float z) {
  const float k0 = -2.f * 0.7978845608028654f * 1.4426950408889634f, k1 = k0 * 0.044715f;
  const float e = __builtin_amdgcn_exp2f(z * (k0 + k1 * (z * z)));
  return z * __builtin_amdgcn_rcpf(1.f + e);
}
__device__ __forceinline__ float pick4(f32x4 v, int r) { return r == 0 ? v[0] : r == 1 ? v[1] : r == 2 ? v[2] : v[3]; }
template <int N> __device__ __forceinline__ void vm_wait_c() { asm volatile("s_waitcnt vmcnt(%0)" ::"i"(N > 63 ? 63 : N) : "memory"); }
__device__ __forceinline__ u32 f_slot(int kq, int sg) { return (u32)(((sg & 2) ? 0 : F_A0) + (kq * 2 + (sg & 1)) * 8192); }
__device__ __forceinline__ bf16x8 ld_dev16(rsrc_t r, u32 off) { return __builtin_bit_cast(bf16x8, __builtin_amdgcn_raw_buffer_load_b128(r, off, 0, 16)); }
__device__ __forceinline__ void f_lda(const u16* const (&pa)[2], u32 lds0, int kq, int nh, int sg) {
#pragma unroll
  for (int i = 0; i < 4; ++i) glds16(pa[i & 1] + (size_t)(i >> 1) * 16 * L + 64 * sg, lds0 + f_slot(kq, sg) + (4 * nh + i) * 1024);
}
__device__ __forceinline__ void f_ldx(const u16* x1T, u32 lds0, int w, int pi, int sg) {
  const int lane = threadIdx.x & 63;
#pragma unroll
  for (int i = 0; i < 2; ++i) {
    const int d = 128 * sg + 16 * w + 8 * i + (lane >> 3);
    const int fz = (((d >> 1) & 1) | ((d >> 2) & 2)) << 1;
    glds16(x1T + (size_t)d * L + 64 * pi + 8 * ((lane & 7) ^ fz), lds0 + F_X + (128 * sg + 16 * w + 8 * i) * 128);
  }
}
__device__ __forceinline__ void wait_flags(const u32* p, int n) {
  const int lane = threadIdx.x & 63;
  for (u32 spin = 0; spin < (1u << 22); ++spin) {
    const u32 v = __hip_atomic_load((GAS u32*)p + (lane < n ? lane : 0), __ATOMIC_RELAXED, __HIP_MEMORY_SCOPE_AGENT);
    if (__all(v == 1u)) break;
    __builtin_amdgcn_s_sleep(1);
  }
}

__device__ __forceinline__ u32 poll8(const u32* p, int n) {
  const int lane = threadIdx.x & 63;
  const u32 v = __hip_atomic_load((GAS u32*)p + (lane < n ? lane : 0), __ATOMIC_RELAXED, __HIP_MEMORY_SCOPE_AGENT);
  return (u32)__ballot(v == 1u) & 0xffu;
}
__device__ __forceinline__ void need_flag(const u32* p, int n, u32& mask, int k) {
  if (!((mask >> k) & 1u)) {
    for (u32 spin = 0; spin < (1u << 22); ++spin) {
      mask = poll8(p, n);
      if ((mask >> k) & 1u) break;
      __builtin_amdgcn_s_sleep(1);
    }
  }
  asm volatile("" ::: "memory");
}

struct Z1 { f32x4 y[4]; };

template <int ROLE>
__device__ __forceinline__ void f_phase_c(const FArgs& a, char* smem, const int w, const int pi, const int j, const u16* const (&pa)[2], Z1& z1) {
  const int tid = threadIdx.x, lane = tid & 63;
  const int c = lane & 15, g = lane >> 4;
  const u32 lds0 = (u32)(size_t)(const char LAS*)smem;
  const int nt2 = 56 + 7 * j + (w < 7 ? w : 0), nt1 = 7 * j + (w > 0 ? w - 1 : 0);
  const u16* pw2 = a.Wt + (size_t)nt2 * 64 * 128 + lane * 8;
  const u16* pw1 = a.Wt + (size_t)nt1 * 64 * 128 + lane * 8;
  const int q4 = (lane & 15) >> 2, p4 = lane & 3;
  const int drow = 8 * g + q4;
  const int fz0 = (((drow >> 1) & 1) | ((drow >> 2) & 2)) << 1;
  const char LAS* xb = (const char LAS*)smem + F_X + drow * 128 + 8 * (p4 & 1);
  const char LAS* xt[4];
  int tb[4];
#pragma unroll
  for (int s = 0; s < 4; ++s) { tb[s] = (s + w) & 3; xt[s] = xb + 16 * ((2 * tb[s] + (p4 >> 1)) ^ fz0); }
  auto xfrag = [&](int s, int ks) -> bf16x8 {
    const char LAS* p = xt[s] + ks * 4096;
    const s16x4 lo = tr_read(p), hi = tr_read(p + 512);
    const u32x2 a0 = __builtin_bit_cast(u32x2, lo), a1 = __builtin_bit_cast(u32x2, hi);
    return __builtin_bit_cast(bf16x8, u32x4{a0[0], a0[1], a1[0], a1[1]});
  };
  f32x4 acc[4];
#pragma unroll
  for (int s = 0; s < 4; ++s) acc[s] = f32x4{0, 0, 0, 0};
  bf16x8 W1[16];
  f32x4 u2, v2, u1, v1;
  float* xs2 = (float*)(smem + F_ST);
  if constexpr (ROLE < 2) {
    bf16x8 W2[16];
#pragma unroll
    for (int ks = 0; ks < 4; ++ks) W2[ks] = *(const bf16x8*)(pw2 + 4 * ks * 128);
    SCHED_FENCE();
    f_ldx(a.x1T, lds0, w, pi, 1);
#pragma unroll
    for (int ks = 4; ks < 8; ++ks) W2[ks] = *(const bf16x8*)(pw2 + 4 * ks * 128);
    SCHED_FENCE();
    VM_WAIT(10);
    __builtin_amdgcn_s_barrier();
    SCHED_FENCE();
    STAMP(2, 1);
    f32x4 sq = {0, 0, 0, 0};
    bf16x8 X[2][4];
#pragma unroll
    for (int s = 0; s < 4; ++s) X[0][s] = xfrag(s, 0);
#pragma unroll
    for (int ks = 0; ks < 16; ++ks) {
      if (ks == 3) { VM_WAIT(9); __builtin_amdgcn_s_barrier(); }
      if (ks == 7) { VM_WAIT(11); __builtin_amdgcn_s_barrier(); }
      if (ks == 11) { VM_WAIT(6); __builtin_amdgcn_s_barrier(); }
      if (ks + 1 < 16) {
#pragma unroll
        for (int s = 0; s < 4; ++s) X[(ks + 1) & 1][s] = xfrag(s, ks + 1);
      }
      if (ks == 0) f_ldx(a.x1T, lds0, w, pi, 2);
      if (ks == 4) f_ldx(a.x1T, lds0, w, pi, 3);
      if (ks < 8) W2[8 + ks] = *(const bf16x8*)(pw2 + 4 * (8 + ks) * 128);
      if (ks == 5) { u2 = *(const f32x4*)(a.uv + 16 * nt2 + 4 * g); v2 = *(const f32x4*)(a.uv + F + 16 * nt2 + 4 * g); }
      SCHED_FENCE();
#pragma unroll
      for (int s = 0; s < 4; ++s) acc[s] = __builtin_amdgcn_mfma_f32_16x16x32_bf16(W2[ks], X[ks & 1][s], acc[s], 0, 0, 0);
      sq = __builtin_amdgcn_mfma_f32_16x16x32_bf16(X[ks & 1][0], X[ks & 1][0], sq, 0, 0, 0);
      SCHED_FENCE();
    }
    if constexpr (ROLE == 1) {
      u1 = *(const f32x4*)(a.uv + 16 * nt1 + 4 * g); v1 = *(const f32x4*)(a.uv + F + 16 * nt1 + 4 * g);
      SCHED_FENCE();
    }
    if (w < 4 && (c >> 2) == g) xs2[64 + 16 * w + c] = pick4(sq, c & 3);
  } else {
    f_ldx(a.x1T, lds0, w, pi, 1);
    VM_WAIT(2);
    __builtin_amdgcn_s_barrier();
    SCHED_FENCE();
    STAMP(2, 1);
    const u32x4 ones = {0x3f803f80u, 0x3f803f80u, 0x3f803f80u, 0x3f803f80u};
    bf16x8 X[2][4];
#pragma unroll
    for (int s = 0; s < 4; ++s) X[0][s] = xfrag(s, 0);
#pragma unroll
    for (int ks = 0; ks < 16; ++ks) {
      if (ks == 3) { VM_WAIT(2); __builtin_amdgcn_s_barrier(); }
      if (ks == 7) { VM_WAIT(2); __builtin_amdgcn_s_barrier(); }
      if (ks == 11) { VM_WAIT(6); __builtin_amdgcn_s_barrier(); }
      if (ks + 1 < 16) {
#pragma unroll
        for (int s = 0; s < 4; ++s) X[(ks + 1) & 1][s] = xfrag(s, ks + 1);
      }
      if (ks == 0) f_ldx(a.x1T, lds0, w, pi, 2);
      if (ks == 4) f_ldx(a.x1T, lds0, w, pi, 3);
      if (ks >= 8) { W1[2 * ks - 16] = *(const bf16x8*)(pw1 + 4 * (2 * ks - 16) * 128); W1[2 * ks - 15] = *(const bf16x8*)(pw1 + 4 * (2 * ks - 15) * 128); }
      SCHED_FENCE();
#pragma unroll
      for (int s = 0; s < 4; ++s) acc[s] = __builtin_amdgcn_mfma_f32_16x16x32_bf16(__builtin_bit_cast(bf16x8, ones), X[ks & 1][s], acc[s], 0, 0, 0);
      SCHED_FENCE();
    }
    u1 = *(const f32x4*)(a.uv + 16 * nt1 + 4 * g); v1 = *(const f32x4*)(a.uv + F + 16 * nt1 + 4 * g);
    SCHED_FENCE();
    if (g == 0) {
#pragma unroll
      for (int s = 0; s < 4; ++s) xs2[16 * tb[s] + c] = acc[s][0];
    }
  }
  STAMP(2, 2);
  __syncthreads();
  STAMP(2, 3);
  float mu[4], ri[4];
  {
    const float mean = xs2[lane] * (1.f / D);
    const float var = fmaxf(xs2[64 + lane] * (1.f / D) - mean * mean, 0.f);
    f32x2* mr = (f32x2*)(smem + F_MR + w * 512);
    mr[lane] = f32x2{rbf(mean), __builtin_amdgcn_rcpf(rbf(__builtin_amdgcn_sqrtf(rbf(rbf(var) + 1e-5f))))};
#pragma unroll
    for (int s = 0; s < 4; ++s) { const f32x2 v = mr[16 * tb[s] + c]; mu[s] = v[0]; ri[s] = v[1]; }
  }
  u32x2 z2p[4];
  if constexpr (ROLE < 2) {
    float* ps = (float*)(smem + F_PS) + w * 128;
    const s16x4 ones4 = {0x3f80, 0x3f80, 0x3f80, 0x3f80};
#pragma unroll
    for (int s = 0; s < 4; ++s) {
      if constexpr (ROLE == 1) {
#pragma unroll
        for (int ks = 4 * s; ks < 4 * s + 4; ++ks) W1[ks] = *(const bf16x8*)(pw1 + 4 * ks * 128);
        SCHED_FENCE();
      }
      float z[4];
#pragma unroll
      for (int r = 0; r < 4; ++r) z[r] = gelu_tanh(ri[s] * (acc[s][r] - mu[s] * u2[r]) + v2[r]);
      z2p[s] = u32x2{pk_bf16(z[0], z[1]), pk_bf16(z[2], z[3])};
      const s16x4 zz = __builtin_bit_cast(s16x4, z2p[s]);
      const f32x4 zero = {0, 0, 0, 0};
      const f32x4 s1 = __builtin_amdgcn_mfma_f32_16x16x16bf16_1k(ones4, zz, zero, 0, 0, 0);
      const f32x4 s2 = __builtin_amdgcn_mfma_f32_16x16x16bf16_1k(zz, zz, zero, 0, 0, 0);
      if ((c >> 2) == g) *(f32x2*)(ps + (16 * tb[s] + c) * 2) = f32x2{s1[0], pick4(s2, c & 3)};
      SCHED_FENCE();
    }
  }
  if constexpr (ROLE > 0) { f_lda(pa, lds0, w & 3, w >> 2, 0); f_lda(pa, lds0, w & 3, w >> 2, 1); }
  STAMP(2, 4);
  __syncthreads();
  STAMP(2, 5);
  if constexpr (ROLE == 0) {
    const float* ps = (const float*)(smem + F_PS);
    float s1 = 0, s2 = 0;
#pragma unroll
    for (int k = 0; k < 7; ++k) { const f32x2 v = *(const f32x2*)(ps + k * 128 + lane * 2); s1 += v[0]; s2 += v[1]; }
    const rsrc_t rg = make_rsrc(a.gran, 256 * 1024);
    const u32 goff = (u32)(pi * 8 * 64 + lane) * 16;
    wt_store16(rg, goff + (u32)j * 1024, u32x4{__float_as_uint(s1), 1u, __float_as_uint(s2), 1u});
    float t1 = 0, t2 = 0;
    for (u32 spin = 0; spin < (1u << 22); ++spin) {
      u32x4 x[8];
#pragma unroll
      for (int k = 0; k < 8; ++k) x[k] = __builtin_amdgcn_raw_buffer_load_b128(rg, goff + k * 1024, 0, 16);
      bool ok = true; t1 = 0; t2 = 0;
#pragma unroll
      for (int k = 0; k < 8; ++k) {
        ok = ok && x[k][1] == 1u && x[k][3] == 1u;
        t1 += __uint_as_float(x[k][0]); t2 += __uint_as_float(x[k][2]);
      }
      if (__all(ok)) break;
      __builtin_amdgcn_s_sleep(1);
    }
    const float mean = t1 * (1.f / FH);
    const float var = fmaxf(t2 * (1.f / FH) - mean * mean, 0.f);
    *(f32x2*)(smem + F_FS + lane * 8) = f32x2{rbf(mean), __builtin_amdgcn_rcpf(rbf(__builtin_amdgcn_sqrtf(rbf(rbf(var) + 1e-5f))))};
    f_lda(pa, lds0, w & 3, w >> 2, 0);
    f_lda(pa, lds0, w & 3, w >> 2, 1);
  } else {
#pragma unroll
    for (int s = 0; s < 4; ++s) acc[s] = f32x4{0, 0, 0, 0};
    bf16x8 X[2][4];
#pragma unroll
    for (int s = 0; s < 4; ++s) X[0][s] = xfrag(s, 0);
#pragma unroll
    for (int ks = 0; ks < 16; ++ks) {
      if (ks + 1 < 16) {
#pragma unroll
        for (int s = 0; s < 4; ++s) X[(ks + 1) & 1][s] = xfrag(s, ks + 1);
      }
      SCHED_FENCE();
#pragma unroll
      for (int s = 0; s < 4; ++s) acc[s] = __builtin_amdgcn_mfma_f32_16x16x32_bf16(W1[ks], X[ks & 1][s], acc[s], 0, 0, 0);
      SCHED_FENCE();
    }
  }
  STAMP(2, 6);
  __syncthreads();
  STAMP(2, 7);
  if constexpr (ROLE < 2) {
#pragma unroll
    for (int s = 0; s < 4; ++s) {
      const f32x2 fs = *(const f32x2*)(smem + F_FS + (16 * tb[s] + c) * 8);
      const float a0 = (bf_lo(z2p[s][0]) - fs[0]) * fs[1], a1 = (bf_hi(z2p[s][0]) - fs[0]) * fs[1];
      const float a2 = (bf_lo(z2p[s][1]) - fs[0]) * fs[1], a3 = (bf_hi(z2p[s][1]) - fs[0]) * fs[1];
      *(u32x2*)(smem + F_ZS + (16 * tb[s] + c) * F_RS + (16 * w + 4 * g) * 2) = u32x2{pk_bf16(a0, a1), pk_bf16(a2, a3)};
    }
#pragma unroll
    for (int kk = 0; kk < 2; ++kk) {
      const char LAS* p = (const char LAS*)smem + F_ZS + (32 * kk + 8 * g + q4) * F_RS + (16 * w + 4 * p4) * 2;
      const s16x4 lo = tr_read(p), hi = tr_read(p + 4 * F_RS);
      const u32x2 a0 = __builtin_bit_cast(u32x2, lo), a1 = __builtin_bit_cast(u32x2, hi);
      wt_store16(make_rsrc(a.z2t, L * FH * 2), (u32)((((7 * j + w) * 256 + 8 * pi + 4 * kk) * 64 + lane * 4) * 4), u32x4{a0[0], a0[1], a1[0], a1[1]});
    }
  }
  VM_WAIT(0);
  __syncthreads();
  if (tid == 0) __hip_atomic_store((GAS u32*)a.flags + j * 32 + pi, 1u, __ATOMIC_RELAXED, __HIP_MEMORY_SCOPE_AGENT);
  STAMP(2, 8);
  f_lda(pa, lds0, w & 3, w >> 2, 2);
  f_lda(pa, lds0, w & 3, w >> 2, 3);
#pragma unroll
  for (int s = 0; s < 4; ++s)
#pragma unroll
    for (int r = 0; r < 4; ++r) z1.y[s][r] = ROLE > 0 ? ri[s] * (acc[s][r] - mu[s] * u1[r]) + v1[r] : 0.f;
  STAMP(2, 9);
}

template <int NT> constexpr int fd_glds(int sg) { return (sg == 2 || sg == 4) ? 8 : 0; }
template <int NT> constexpr int fd_bld(int sg) { return sg < 0 ? 0 : (2 * sg + F_BD < 16 ? NT : 0) + (2 * sg + 1 + F_BD < 16 ? NT : 0); }
template <int NT> constexpr int fd_ops(int sg) { return fd_glds<NT>(sg) + fd_bld<NT>(sg); }

template <int NT, int SG>
__device__ __forceinline__ void fd_stage(f32x4 (&acc)[4][NT], bf16x8 (&Bf)[F_BR][NT], bf16x8 (&Af)[2][4], const rsrc_t rb, u32 boff,
                                         const u16* const (&pa)[2], u32 lds0, const char* const (&ap)[2][2][4], int kq, int nh,
                                         const u32* fl, u32& mask, const Z1& z1, char* zdst, int w) {
  if constexpr ((SG & 1) == 0 && SG > 0) {
    if constexpr (SG == 2) vm_wait_c<F_BD * NT + fd_ops<NT>(0) + fd_ops<NT>(1)>();
    else vm_wait_c<fd_bld<NT>(SG - 2) + fd_ops<NT>(SG - 1)>();
    __builtin_amdgcn_s_barrier();
    if constexpr (fd_glds<NT>(SG) > 0) { f_lda(pa, lds0, kq, nh, SG + 2); f_lda(pa, lds0, kq, nh, SG + 3); }
#pragma unroll
    for (int m = 0; m < 4; ++m) Af[0][m] = *(const bf16x8*)(ap[(SG >> 1) & 1][0][m]);
  }
#pragma unroll
  for (int kk = 0; kk < 2; ++kk) {
    constexpr int ks0 = 2 * SG;
    if (kk == 0 || (SG & 1) == 0) {
#pragma unroll
      for (int m = 0; m < 4; ++m) Af[(kk + 1) & 1][m] = *(const bf16x8*)(ap[(SG >> 1) & 1][(kk + 1) & 1][m] + (kk == 0 ? (SG & 1) : 1) * 8192);
    }
    SCHED_FENCE();
#pragma unroll
    for (int n = 0; n < NT; ++n) {
#pragma unroll
      for (int m = 0; m < 4; ++m) acc[m][n] = __builtin_amdgcn_mfma_f32_16x16x32_bf16(Bf[(ks0 + kk) % F_BR][n], Af[kk][m], acc[m][n], 0, 0, 0);
      SCHED_FENCE();
      if (ks0 + kk + F_BD < 16) {
        if (n == 0 && ((ks0 + kk + F_BD) & 1) == 0) need_flag(fl, 8, mask, (ks0 + kk + F_BD) >> 1);
        Bf[(ks0 + kk + F_BD) % F_BR][n] = ld_dev16(rb, boff + (u32)((n * 256 + 4 * (ks0 + kk + F_BD)) * 256));
      }
      if constexpr (SG < 4) {
        if (kk == 0 && n == 1 && w > 0) {
          float z[4];
#pragma unroll
          for (int r = 0; r < 4; ++r) z[r] = gelu_tanh(z1.y[SG][r]);
          *(u32x2*)(zdst + ((SG + w) & 3) * 16 * F_RS) = u32x2{pk_bf16(z[0], z[1]), pk_bf16(z[2], z[3])};
        }
      }
      SCHED_FENCE();
    }
  }
}

template <int NT>
__device__ __forceinline__ void f_phase_d(const FArgs& a, char* smem, const int w, const int rt, const int ct, const u16* const (&pa)[2], const Z1& z1) {
  const int tid = threadIdx.x, lane = tid & 63;
  const int kq = w & 3, nh = w >> 2, n0 = nh * 4;
  const int c = lane & 15, g = lane >> 4;
  const u32 lds0 = (u32)(size_t)(const char LAS*)smem;
  const rsrc_t rb = make_rsrc(a.z2t, L * FH * 2);
  const u32 boff = (u32)((((7 * ct + n0) * 256 + 64 * kq) * 64 + lane * 4) * 4);
  const u32* fl = a.flags + ct * 32 + 8 * kq;
  u32 mask = poll8(fl, 8);
  bf16x8 Bf[F_BR][NT], Af[2][4];
#pragma unroll
  for (int ks = 0; ks < F_BD; ++ks) {
    if ((ks & 1) == 0) need_flag(fl, 8, mask, ks >> 1);
#pragma unroll
    for (int n = 0; n < NT; ++n) Bf[ks][n] = ld_dev16(rb, boff + (u32)((n * 256 + 4 * ks) * 256));
  }
  SCHED_FENCE();
  f32x4 acc[4][NT];
#pragma unroll
  for (int m = 0; m < 4; ++m)
#pragma unroll
    for (int n = 0; n < NT; ++n) acc[m][n] = f32x4{0, 0, 0, 0};
  const int asw = (c >> 1) & 7;
  const char* ap[2][2][4];
#pragma unroll
  for (int hi = 0; hi < 2; ++hi)
#pragma unroll
    for (int m = 0; m < 4; ++m) {
      const char* base = smem + f_slot(kq, 2 * hi) + ((m + kq) & 3) * 2048 + c * 128;
      ap[hi][0][m] = base + 16 * (g ^ asw); ap[hi][1][m] = base + 16 * ((4 + g) ^ asw);
    }
#pragma unroll
  for (int m = 0; m < 4; ++m) Af[0][m] = *(const bf16x8*)(ap[0][0][m]);
  char* zdst = smem + F_ZS + c * F_RS + (16 * (w - 1) + 4 * g) * 2;
  STAMP(3, 4);
  fd_stage<NT, 0>(acc, Bf, Af, rb, boff, pa, lds0, ap, kq, nh, fl, mask, z1, zdst, w);
  fd_stage<NT, 1>(acc, Bf, Af, rb, boff, pa, lds0, ap, kq, nh, fl, mask, z1, zdst, w);
  STAMP(3, 5);
  fd_stage<NT, 2>(acc, Bf, Af, rb, boff, pa, lds0, ap, kq, nh, fl, mask, z1, zdst, w);
  fd_stage<NT, 3>(acc, Bf, Af, rb, boff, pa, lds0, ap, kq, nh, fl, mask, z1, zdst, w);
  STAMP(3, 6);
  fd_stage<NT, 4>(acc, Bf, Af, rb, boff, pa, lds0, ap, kq, nh, fl, mask, z1, zdst, w);
  fd_stage<NT, 5>(acc, Bf, Af, rb, boff, pa, lds0, ap, kq, nh, fl, mask, z1, zdst, w);
  STAMP(3, 7);
  fd_stage<NT, 6>(acc, Bf, Af, rb, boff, pa, lds0, ap, kq, nh, fl, mask, z1, zdst, w);
  const float bs = bf1(a.bsp[64 * rt + 16 * kq + c]);
  fd_stage<NT, 7>(acc, Bf, Af, rb, boff, pa, lds0, ap, kq, nh, fl, mask, z1, zdst, w);
  STAMP(3, 1);
  __syncthreads();
#pragma unroll
  for (int m = 1; m < 4; ++m) {
    const int o = (m + kq) & 3;
    char* dst = smem + (o < 3 ? o * 21504 : F_A0) + ((m - 1) * 7 * 64 + n0 * 64 + lane) * 16;
#pragma unroll
    for (int n = 0; n < NT; ++n) *(f32x4*)(dst + n * 1024) = acc[m][n];
  }
  __syncthreads();
  STAMP(3, 2);
  const char* src = smem + (kq < 3 ? kq * 21504 : F_A0) + (n0 * 64 + lane) * 16;
  const char* zs = smem + F_ZS + (16 * kq + c) * F_RS + (16 * n0 + 4 * g) * 2;
  const rsrc_t rp = make_rsrc(a.Pt, L * FH * 2);
  u32x2 pv[NT];
#pragma unroll
  for (int n = 0; n < NT; ++n) {
    const f32x4 p0 = *(const f32x4*)(src + n * 1024), p1 = *(const f32x4*)(src + (7 + n) * 1024), p2 = *(const f32x4*)(src + (14 + n) * 1024);
    const u32x2 zz = *(const u32x2*)(zs + n * 32);
    float gg[4];
#pragma unroll
    for (int e = 0; e < 4; ++e) gg[e] = ((acc[0][n][e] + p0[e]) + (p1[e] + p2[e])) + bs;
    pv[n] = u32x2{pk_bf16(bf_lo(zz[0]) * gg[0], bf_hi(zz[0]) * gg[1]), pk_bf16(bf_lo(zz[1]) * gg[2], bf_hi(zz[1]) * gg[3])};
    if (n & 1) wt_store16(rp, (u32)((((4 * rt + kq) * 112 + 14 * ct + 2 * (n0 + n - 1 + (g & 1)) + (g >> 1)) * 16 + c) * 16), pair16(pv[n - 1], pv[n]));
  }
  if constexpr (NT & 1) {
    const u32x4 v = pair16(pv[NT - 1], pv[NT - 1]);
    if ((g & 1) == 0) wt_store16(rp, (u32)((((4 * rt + kq) * 112 + 14 * ct + 2 * (n0 + NT - 1) + (g >> 1)) * 16 + c) * 16), v);
  }
  STAMP(3, 3);
}

__device__ __forceinline__ void f_phase_e(const FArgs& a, char* smem, const int w, const int rt, const int dt) {
  const int tid = threadIdx.x, lane = tid & 63;
  const int kq = w & 3, mh = w >> 2;
  const int c = lane & 15, g = lane >> 4;
  const u32 lds0 = (u32)(size_t)(const char LAS*)smem;
  VM_WAIT(0);
  __syncthreads();
  if (tid == 0) __hip_atomic_store((GAS u32*)a.flags + 256 + rt * 32 + dt, 1u, __ATOMIC_RELAXED, __HIP_MEMORY_SCOPE_AGENT);
  STAMP(4, 5);
#pragma unroll
  for (int k = 0; k < 7; ++k)
#pragma unroll
    for (int nn = 0; nn < 2; ++nn) {
      const int n = 2 * mh + nn;
      glds16(a.Wot + ((size_t)(4 * dt + n) * 112 + 4 * (7 * kq + k)) * 128 + lane * 8, lds0 + (n * 28 + 7 * kq + k) * 1024);
    }
  const u32x4 xr = *(const u32x4*)(a.x1T + (size_t)(64 * dt + (tid >> 3)) * L + 64 * rt + 8 * (tid & 7));
  const int d8 = lane & 7;
  const f32x4 gb0 = mod_get4(a.mod, 2560 + 64 * dt + 8 * d8), gb1 = mod_get4(a.mod, 2560 + 64 * dt + 8 * d8 + 4);
  SCHED_FENCE();
  {
    const u32* fl = a.flags + 256 + rt * 32 + 2 * kq;
    u32 mask = poll8(fl, 2);
    need_flag(fl, 2, mask, 0); need_flag(fl, 2, mask, 1);
  }
  STAMP(4, 4);
  const rsrc_t rp = make_rsrc(a.Pt, L * FH * 2);
  bf16x8 Pf[7][2];
#pragma unroll
  for (int k = 0; k < 7; ++k)
#pragma unroll
    for (int m = 0; m < 2; ++m) Pf[k][m] = ld_dev16(rp, (u32)((((4 * rt + 2 * mh + m) * 112 + 4 * (7 * kq + k)) * 64 + lane * 4) * 4));
  {
    u32* p = (u32*)(smem + F_EX + (tid >> 3) * 132 + (tid & 7) * 16);
    p[0] = xr[0]; p[1] = xr[1]; p[2] = xr[2]; p[3] = xr[3];
  }
  vm_wait_c<14>();
  __builtin_amdgcn_s_barrier();
  SCHED_FENCE();
  f32x4 acc[2][4];
#pragma unroll
  for (int m = 0; m < 2; ++m)
#pragma unroll
    for (int n = 0; n < 4; ++n) acc[m][n] = f32x4{0, 0, 0, 0};
  const char* wp = smem + (7 * kq) * 1024 + lane * 16;
#pragma unroll
  for (int k = 0; k < 7; ++k) {
    bf16x8 Wf[4];
#pragma unroll
    for (int n = 0; n < 4; ++n) Wf[n] = *(const bf16x8*)(wp + (n * 28 + k) * 1024);
#pragma unroll
    for (int n = 0; n < 4; ++n)
#pragma unroll
      for (int m = 0; m < 2; ++m) acc[m][n] = __builtin_amdgcn_mfma_f32_16x16x32_bf16(Wf[n], Pf[k][m], acc[m][n], 0, 0, 0);
  }
  STAMP(4, 1);
  __syncthreads();
#pragma unroll
  for (int m = 0; m < 2; ++m)
#pragma unroll
    for (int n = 0; n < 4; ++n) *(f32x4*)(smem + (kq * 16 + (2 * mh + m) * 4 + n) * F_ETS + g * 288 + c * 16) = acc[m][n];
  __syncthreads();
  STAMP(4, 2);
  {
    const int t = 8 * w + (lane >> 3);
    float s[8];
#pragma unroll
    for (int hh = 0; hh < 2; ++hh) {
      const char* p = smem + ((t >> 4) * 4 + (d8 >> 1)) * F_ETS + (2 * (d8 & 1) + hh) * 288 + (t & 15) * 16;
      const f32x4 p0 = *(const f32x4*)p, p1 = *(const f32x4*)(p + 16 * F_ETS), p2 = *(const f32x4*)(p + 32 * F_ETS), p3 = *(const f32x4*)(p + 48 * F_ETS);
#pragma unroll
      for (int e = 0; e < 4; ++e) s[4 * hh + e] = (p0[e] + p1[e]) + (p2[e] + p3[e]);
    }
    float o[8];
#pragma unroll
    for (int e = 0; e < 8; ++e) {
      const float x1 = bf1(*(const u16*)(smem + F_EX + (8 * d8 + e) * 132 + t * 2));
      o[e] = x1 + (e < 4 ? gb0[e] : gb1[e - 4]) * s[e];
    }
    wt_store16(make_rsrc(a.out, L * D * 2), (u32)(((64 * rt + t) * D + 64 * dt + 8 * d8) * 2), u32x4{pk_bf16(o[0], o[1]), pk_bf16(o[2], o[3]), pk_bf16(o[4], o[5]), pk_bf16(o[6], o[7])});
  }
  STAMP(4, 3);
}

__global__ __launch_bounds__(512, 2) void kF(FArgs a) {
  extern __shared__ __attribute__((aligned(16))) char smem[];
  const int wg = blockIdx.x, lane = threadIdx.x & 63;
  const int w = __builtin_amdgcn_readfirstlane(threadIdx.x >> 6);
  const int xc = wg & 7, ii = wg >> 3;
  const int pi = (xc >> 1) * 8 + (ii & 7), j = (xc & 1) * 4 + (ii >> 3);
  const unsigned long long r2 = __builtin_amdgcn_s_memrealtime();
  f_ldx(a.x1T, (u32)(size_t)(const char LAS*)smem, w, pi, 0);
  const u16* pa[2];
#pragma unroll
  for (int par = 0; par < 2; ++par) {
    const int row = 32 * (w >> 2) + 8 * par + (lane >> 3);
    pa[par] = a.wsp + (size_t)(64 * pi + row) * L + 512 * (w & 3) + 8 * ((lane & 7) ^ ((row >> 1) & 7));
  }
  Z1 z1;
  if (w == 0) f_phase_c<0>(a, smem, w, pi, j, pa, z1);
  else if (w < 7) f_phase_c<1>(a, smem, w, pi, j, pa, z1);
  else f_phase_c<2>(a, smem, w, pi, j, pa, z1);
  const unsigned long long rC = __builtin_amdgcn_s_memrealtime();
  if (w < 4) f_phase_d<4>(a, smem, w, pi, j, pa, z1);
  else f_phase_d<3>(a, smem, w, pi, j, pa, z1);
  const unsigned long long rD = __builtin_amdgcn_s_memrealtime();
  f_phase_e(a, smem, w, pi, j);
  if (blockIdx.x == 0 && threadIdx.x == 0) { asm volatile("s_waitcnt vmcnt(0)" ::: "memory"); const unsigned long long r3 = __builtin_amdgcn_s_memrealtime(); unsigned long long dl = ((r3 - r3)) * 8; if (dl > 100000ull) dl = 100000ull; while (__builtin_amdgcn_s_memrealtime() - r3 < dl) {} }
}
#define KMASK(b)
extern "C" void kernel_launch(void* const* d_in, const int* in_sizes, int n_in, void* d_out, int out_size,
                              void* d_ws, size_t ws_size, hipStream_t stream) {
  const u16* x = (const u16*)d_in[0];
  char* ws = (char*)d_ws;
  float* mod = (float*)(ws + 524288);
  u32* stats1 = (u32*)(ws + 16384);
  float* uv = (float*)(ws + 32768);
  u64* gran = (u64*)(ws + 65536);
  u32* flags = (u32*)(ws + 65536 + 262144);
  u16* xT = (u16*)(ws + (1 << 20));
  u16* Krev = (u16*)(ws + (3 << 20));
  u16* x1T = xT;
  u16* Wt = (u16*)(ws + (7 << 20));
  u16* Wot = (u16*)(ws + (9 << 20));
  u16* Pt = (u16*)(ws + (10 << 20));
  u16* z2t = Krev;
  AArgs aa;
  aa.x = x; aa.Are = (const u16*)d_in[1]; aa.Aim = (const u16*)d_in[2]; aa.Bre = (const u16*)d_in[3]; aa.Bim = (const u16*)d_in[4];
  aa.Cre = (const u16*)d_in[5]; aa.Cim = (const u16*)d_in[6]; aa.logdt = (const u16*)d_in[7]; aa.c = (const u16*)d_in[14];
  aa.wmod = (const u16*)d_in[15]; aa.win = (const u16*)d_in[10]; aa.wout = (const u16*)d_in[13];
  aa.mod = mod; aa.stats1 = stats1; aa.xT = xT; aa.Krev = Krev; aa.gran = (u32x4*)gran;
  (void)hipFuncSetAttribute((const void*)kA, hipFuncAttributeMaxDynamicSharedMemorySize, A_LDS);
  KMASK(1) kA<<<256, 768, A_LDS, stream>>>(aa);
  BArgs ba;
  ba.xT = xT; ba.Krev = Krev; ba.Dskip = (const u16*)d_in[8]; ba.win = (const u16*)d_in[10]; ba.wout = (const u16*)d_in[13];
  ba.wsp = (const u16*)d_in[11]; ba.stats1 = stats1; ba.mod = mod; ba.x1T = x1T; ba.Wt = Wt; ba.Wot = Wot; ba.uv = uv;
  (void)hipFuncSetAttribute((const void*)kB, hipFuncAttributeMaxDynamicSharedMemorySize, B_LDS);
  KMASK(2) kB<<<256, 768, B_LDS, stream>>>(ba);
  FArgs fa;
  fa.x1T = x1T; fa.Wt = Wt; fa.wsp = (const u16*)d_in[11]; fa.bsp = (const u16*)d_in[12]; fa.Wot = Wot; fa.uv = uv; fa.mod = mod;
  fa.z2t = z2t; fa.Pt = Pt; fa.out = (u16*)d_out; fa.gran = gran; fa.flags = flags;
  (void)hipFuncSetAttribute((const void*)kF, hipFuncAttributeMaxDynamicSharedMemorySize, F_LDS);
  KMASK(4) kF<<<256, 512, F_LDS, stream>>>(fa);
}
```

```cpp
#include <hip/hip_runtime.h>
#include <hip/hip_bf16.h>
#include <stdint.h>

typedef unsigned short u16;
typedef unsigned int u32;
typedef unsigned long long u64;
typedef __bf16 bf16x8 __attribute__((ext_vector_type(8)));
typedef __bf16 bf16x2 __attribute__((ext_vector_type(2)));
typedef short s16x4 __attribute__((ext_vector_type(4)));
typedef float f32x2 __attribute__((ext_vector_type(2)));
typedef float f32x4 __attribute__((ext_vector_type(4)));
typedef u32 u32x2 __attribute__((ext_vector_type(2)));
typedef u32 u32x4 __attribute__((ext_vector_type(4)));
#define LAS __attribute__((address_space(3)))
#define GAS __attribute__((address_space(1)))

constexpr int L = 2048, D = 512, NS = 128, F = 1792, FH = 896;

__device__ __forceinline__ float bf_lo(u32 w) { return __uint_as_float(w << 16); }
__device__ __forceinline__ float bf_hi(u32 w) { return __uint_as_float(w & 0xffff0000u); }
__device__ __forceinline__ float bf1(u16 h) { return __uint_as_float(((u32)h) << 16); }
__device__ __forceinline__ u32 pk_bf16(float lo, float hi) {
  f32x2 a = {lo, hi};
  bf16x2 p = __builtin_convertvector(a, bf16x2);
  return __builtin_bit_cast(u32, p);
}
__device__ __forceinline__ float rbf(float x) { return bf_lo(pk_bf16(x, x)); }

template <int CTRL, int ROWMASK = 0xf>
__device__ __forceinline__ float dpp_add(float x) {
  int y = __builtin_amdgcn_update_dpp(0, __float_as_int(x), CTRL, ROWMASK, 0xf, true);
  return x + __int_as_float(y);
}
__device__ __forceinline__ float row16_sum(float x) {
  x = dpp_add<0xB1>(x);
  x = dpp_add<0x4E>(x);
  x = dpp_add<0x141>(x);
  x = dpp_add<0x140>(x);
  return x;
}
__device__ __forceinline__ float wave_sum(float x) {
  x = row16_sum(x);
  float a = __int_as_float(__builtin_amdgcn_readlane(__float_as_int(x), 0));
  float b = __int_as_float(__builtin_amdgcn_readlane(__float_as_int(x), 16));
  float c = __int_as_float(__builtin_amdgcn_readlane(__float_as_int(x), 32));
  float d = __int_as_float(__builtin_amdgcn_readlane(__float_as_int(x), 48));
  return (a + b) + (c + d);
}
__device__ __forceinline__ s16x4 tr_read(const char LAS* p) {
  return __builtin_amdgcn_ds_read_tr16_b64_v4i16((s16x4 LAS*)p);
}

#define STAMP(kern, i)
__device__ __forceinline__ void glds16(const void* gsrc, u32 lds_dst) {
  u32 keep;
  asm volatile("s_mov_b32 %0, m0\n\ts_mov_b32 m0, %2\n\ts_nop 0\n\tglobal_load_lds_dwordx4 %1, off\n\ts_mov_b32 m0, %0"
               : "=&s"(keep) : "v"(gsrc), "s"(lds_dst) : "memory");
}
#define SCHED_FENCE() __builtin_amdgcn_sched_barrier(0)
#define VM_WAIT(n) asm volatile("s_waitcnt vmcnt(" #n ")" ::: "memory")
__device__ __forceinline__ float xor16_sum(float x) {
  const u32 v = __float_as_uint(x);
  auto r = __builtin_amdgcn_permlane16_swap(v, v, false, false);
  return __uint_as_float(r[0]) + __uint_as_float(r[1]);
}
__device__ __forceinline__ float xor32_sum(float x) {
  const u32 v = __float_as_uint(x);
  auto r = __builtin_amdgcn_permlane32_swap(v, v, false, false);
  return __uint_as_float(r[0]) + __uint_as_float(r[1]);
}
__device__ __forceinline__ float xor4_8_sum(float x) {
  x = x + __int_as_float(__builtin_amdgcn_update_dpp(0, __float_as_int(x), 0x124, 0xf, 0xf, true));
  x = x + __int_as_float(__builtin_amdgcn_update_dpp(0, __float_as_int(x), 0x128, 0xf, 0xf, true));
  return x;
}
__device__ __forceinline__ void lds_rendezvous(u32 LAS* cnt, u32 target) {
  asm volatile("s_waitcnt lgkmcnt(0)" ::: "memory");
  if ((threadIdx.x & 63) == 0) __hip_atomic_fetch_add(cnt, 1u, __ATOMIC_RELAXED, __HIP_MEMORY_SCOPE_WORKGROUP);
  while (__hip_atomic_load(cnt, __ATOMIC_RELAXED, __HIP_MEMORY_SCOPE_WORKGROUP) < target) __builtin_amdgcn_s_sleep(1);
  asm volatile("" ::: "memory");
}

typedef __amdgpu_buffer_rsrc_t rsrc_t;
__device__ __forceinline__ rsrc_t make_rsrc(const void* base, u32 bytes) {
  return __builtin_amdgcn_make_buffer_rsrc(const_cast<void*>(base), 0, bytes, 0x00020000);
}
__device__ __forceinline__ void wt_store16(rsrc_t r, u32 byte_off, u32x4 v) { __builtin_amdgcn_raw_buffer_store_b128(v, r, byte_off, 0, 16); }
__device__ __forceinline__ u32x4 pair16(u32x2 a, u32x2 b) {
  const auto r0 = __builtin_amdgcn_permlane16_swap(a[0], b[0], false, false);
  const auto r1 = __builtin_amdgcn_permlane16_swap(a[1], b[1], false, false);
  return u32x4{r0[0], r1[0], r0[1], r1[1]};
}

__device__ __forceinline__ float mod_get(const float* mp, int i) { return rbf((mp[i] + mp[3072 + i]) + (mp[6144 + i] + mp[9216 + i])); }
__device__ __forceinline__ f32x4 mod_get4(const float* mp, int i) {
  const f32x4 a = *(const f32x4*)(mp + i), b = *(const f32x4*)(mp + 3072 + i), c = *(const f32x4*)(mp + 6144 + i), d = *(const f32x4*)(mp + 9216 + i);
  f32x4 r;
#pragma unroll
  for (int e = 0; e < 4; ++e) r[e] = rbf((a[e] + b[e]) + (c[e] + d[e]));
  return r;
}
constexpr int CODE_LANES = 92;
struct AArgs {
  const u16 *x, *Are, *Aim, *Bre, *Bim, *Cre, *Cim, *logdt, *c, *wmod, *win, *wout;
  float* mod; u32* stats1; u16* xT; u16* Krev; u32x4* gran;
};
constexpr int A_RS = 528;
constexpr int A_TAB = 0;
constexpr int A_KC = A_TAB + 2 * 64 * A_RS;
constexpr int A_TILE = A_KC + 2 * 768 * 4;
constexpr int A_RED = A_TILE + 8 * 1088;
constexpr int A_CNT = A_RED + 4 * 64 * 4;
constexpr int A_LDS = A_CNT + 16;

struct KIdx { u16 v[2048]; };
constexpr int bf16_round_int(int l) {
  int sh = 0;
  while ((l >> sh) >= 256) ++sh;
  if (sh == 0) return l;
  const int q = l >> sh, rem = l & ((1 << sh) - 1), half = 1 << (sh - 1);
  const int up = (rem > half || (rem == half && (q & 1))) ? 1 : 0;
  return (q + up) << sh;
}
constexpr KIdx make_kidx() {
  KIdx t{};
  for (int x = 0; x < 2048; ++x) {
    const int lr = bf16_round_int(2047 - x);
    const int aa = lr >> 7, b = (lr >> 3) & 15, r = lr & 7;
    const int row = lr < 256 ? 8 * aa + r : lr < 512 ? 16 + 4 * (aa - 2) + (r >> 1) : lr < 1024 ? 24 + 2 * (aa - 4) + (r >> 2) : 32 + (aa - 8);
    t.v[x] = (u16)(row * 16 + b);
  }
  return t;
}
__device__ const KIdx g_kidx = make_kidx();

struct cpx { float r, i; };
__device__ __forceinline__ cpx cmul(cpx a, cpx b) { return cpx{a.r * b.r - a.i * b.i, a.r * b.i + a.i * b.r}; }

__global__ __launch_bounds__(768) void kA(AArgs a) {
  extern __shared__ __attribute__((aligned(16))) char smem[];
  const int wg = blockIdx.x, tid = threadIdx.x, lane = tid & 63;
  const int w = __builtin_amdgcn_readfirstlane(tid >> 6);
  STAMP(0, 0);
  const int tw = w - 8, st = tid - 512;
  const int t0 = 8 * ((wg & 7) * 32 + (wg >> 3));
  const bool doM = wg < 192;
  const int mrb = wg / 48, mcb = wg - 48 * mrb;
  const int rr = lane >> 3, q = lane & 7;
  u32x4 ld[14];
  u16 sv[8];
  if (w < 8) {
    const int d = 2 * wg + (w >> 2), o = d * NS + 64 * ((w >> 1) & 1) + lane;
    sv[0] = a.Are[o]; sv[1] = a.Aim[o]; sv[2] = a.Bre[o]; sv[3] = a.Bim[o]; sv[4] = a.Cre[o]; sv[5] = a.Cim[o]; sv[6] = a.logdt[d]; sv[7] = 0;
    ld[0] = *(const u32x4*)(g_kidx.v + 8 * (tid & 255));
  } else {
#pragma unroll
    for (int j = 0; j < 2; ++j) ld[j] = *(const u32x4*)(a.x + (size_t)(t0 + 2 * tw + j) * D + 8 * lane);
    if (tw == 0 && lane < 4) *(u32*)(smem + A_CNT + 4 * lane) = 0;
    asm volatile("s_waitcnt lgkmcnt(0)" ::: "memory");
  }
  SCHED_FENCE();
  STAMP(0, 9);
  __builtin_amdgcn_s_barrier();
  STAMP(0, 10);
  SCHED_FENCE();
  if (w < 8) {
    const int ch = w >> 2, nh = (w >> 1) & 1, part = w & 1, d = 2 * wg + ch;
    const int n = 64 * nh + lane;
    const u16 hare = sv[0], haim = sv[1], hbre = sv[2], hbim = sv[3], hcre = sv[4], hcim = sv[5], hdt = sv[6];
    const u32x4 kix = ld[0];
    asm volatile("" ::"v"((u32)hare), "v"((u32)hcim), "v"((u32)hdt));
    STAMP(0, 11);
    const float dt = rbf(__expf(bf1(hdt)));
    const float lr = (dt * bf1(hare)) * 1.4426950408889634f, lp = (dt * bf1(haim)) * 0.15915494309189535f;
    auto cexp = [&](float l) -> cpx {
      const float e = __builtin_amdgcn_exp2f(lr * l), ph = __builtin_amdgcn_fractf(lp * l);
      return cpx{e * __builtin_amdgcn_cosf(ph), e * __builtin_amdgcn_sinf(ph)};
    };
    char* tab = smem + A_TAB + ch * (64 * A_RS) + n * 4;
    auto put = [&](int row, cpx v) { *(u32*)(tab + row * A_RS) = pk_bf16(v.r, v.i); };
    if (part == 0) {
      const cpx E1 = cexp(1.f), E128 = cexp(128.f), E512 = cexp(512.f);
      const cpx E2 = cmul(E1, E1), E3 = cmul(E2, E1), E4 = cmul(E2, E2), E5 = cmul(E4, E1), E6 = cmul(E4, E2), E7 = cmul(E4, E3);
      put(0, cpx{1.f, 0.f}); put(1, E1); put(2, E2); put(3, E3); put(4, E4); put(5, E5); put(6, E6); put(7, E7);
      put(8, E128); put(9, cmul(E128, E1)); put(10, cmul(E128, E2)); put(11, cmul(E128, E3));
      put(12, cmul(E128, E4)); put(13, cmul(E128, E5)); put(14, cmul(E128, E6)); put(15, cmul(E128, E7));
      const cpx E256 = cmul(E128, E128), E384 = cmul(E256, E128);
      put(16, E256); put(17, cmul(E256, E2)); put(18, cmul(E256, E4)); put(19, cmul(E256, E6));
      put(20, E384); put(21, cmul(E384, E2)); put(22, cmul(E384, E4)); put(23, cmul(E384, E6));
      const cpx E640 = cmul(E512, E128), E768 = cmul(E512, E256), E896 = cmul(E512, E384);
      put(24, E512); put(25, cmul(E512, E4)); put(26, E640); put(27, cmul(E640, E4));
      put(28, E768); put(29, cmul(E768, E4)); put(30, E896); put(31, cmul(E896, E4));
    } else {
      const cpx E8 = cexp(8.f), E128 = cexp(128.f), E1024 = cexp(1024.f);
      const float br = dt * bf1(hbre), bi = dt * bf1(hbim), cr = bf1(hcre), ci = bf1(hcim);
      const cpx W = cmul(cpx{cr, ci}, cpx{br, bi});
      const cpx E16 = cmul(E8, E8), E32 = cmul(E16, E16), E64 = cmul(E32, E32);
      cpx Y[8];
      Y[0] = W; Y[1] = cmul(W, E8); Y[2] = cmul(W, E16); Y[3] = cmul(Y[1], E16);
#pragma unroll
      for (int b = 0; b < 4; ++b) Y[4 + b] = cmul(Y[b], E32);
#pragma unroll
      for (int b = 0; b < 8; ++b) {
        put(48 + b, cpx{Y[b].r, -Y[b].i});
        const cpx t = cmul(Y[b], E64);
        put(56 + b, cpx{t.r, -t.i});
      }
      const cpx E256 = cmul(E128, E128), E512 = cmul(E256, E256);
      cpx P[4];
      P[0] = E1024; P[1] = cmul(E1024, E128); P[2] = cmul(E1024, E256); P[3] = cmul(P[1], E256);
#pragma unroll
      for (int b = 0; b < 4; ++b) { put(32 + b, P[b]); put(36 + b, cmul(P[b], E512)); }
      put(40, cmul(E1024, E1024));
    }
    STAMP(0, 1);
    lds_rendezvous((u32 LAS*)(smem + A_CNT), 8);
    STAMP(0, 2);
    if (w < 6) {
      const int c2 = w >= 3 ? 1 : 0, t = w - 3 * c2, i = lane & 15, g = lane >> 4;
      const char* tb = smem + A_TAB + c2 * (64 * A_RS) + 16 * g;
      const char* pa = tb + (16 * t + i) * A_RS, *pb = tb + (48 + i) * A_RS;
      f32x4 acc = {0, 0, 0, 0};
      bf16x8 Af[8], Bf[8];
#pragma unroll
      for (int s = 0; s < 8; ++s) {
        Af[s] = *(const bf16x8*)(pa + s * 64); Bf[s] = *(const bf16x8*)(pb + s * 64);
      }
#pragma unroll
      for (int s = 0; s < 8; ++s) acc = __builtin_amdgcn_mfma_f32_16x16x32_bf16(Af[s], Bf[s], acc, 0, 0, 0);
      float* Kc = (float*)(smem + A_KC) + c2 * 768 + t * 256;
#pragma unroll
      for (int r = 0; r < 4; ++r) Kc[(4 * g + r) * 16 + i] = acc[r];
    }
    STAMP(0, 3);
    lds_rendezvous((u32 LAS*)(smem + A_CNT + 4), 8);
    STAMP(0, 4);
    const float* Kc = (const float*)(smem + A_KC) + ch * 768;
    float kv[8];
#pragma unroll
    for (int j = 0; j < 4; ++j) { kv[2 * j] = Kc[kix[j] & 0xffffu]; kv[2 * j + 1] = Kc[kix[j] >> 16]; }
    *(u32x4*)(a.Krev + (size_t)d * L + 8 * (tid & 255)) =
        u32x4{pk_bf16(kv[0], kv[1]), pk_bf16(kv[2], kv[3]), pk_bf16(kv[4], kv[5]), pk_bf16(kv[6], kv[7])};
    STAMP(0, 5);
  } else {
    __builtin_amdgcn_s_setprio(3);
    const u32x4 xr[2] = {ld[0], ld[1]};
    asm volatile("" ::"v"(xr[1]));
    STAMP(0, 11);
    char* tile = smem + A_TILE;
#pragma unroll
    for (int j = 0; j < 2; ++j) *(u32x4*)(tile + (2 * tw + j) * 1088 + 16 * lane) = xr[j];
    SCHED_FENCE();
    if (doM) {
#pragma unroll
      for (int p = 0; p < 4; ++p) ld[2 + p] = *(const u32x4*)(a.wmod + (size_t)(128 * mrb + p * 32 + tw * 8 + rr) * 3072 + 64 * mcb + 8 * q);
#pragma unroll
      for (int p = 0; p < 4; ++p) sv[p] = a.c[128 * mrb + p * 32 + tw * 8 + rr];
    }
    ld[10] = *(const u32x4*)(a.win + (size_t)wg * 3584 + st * 8);
    ld[11] = *(const u32x4*)(a.win + (size_t)wg * 3584 + (st < 192 ? 2048 + st * 8 : 0));
    ld[12] = *(const u32x4*)(a.wout + (size_t)wg * 1792 + (st < 224 ? st * 8 : 0));
    u64 base;
    asm volatile("s_getpc_b64 s[40:41]\n\ts_add_u32 s40, s40, _Z2kB5BArgs@rel32@lo+4\n\ts_addc_u32 s41, s41, _Z2kB5BArgs@rel32@hi+12\n\ts_mov_b64 %0, s[40:41]"
                 : "=s"(base) : : "s40", "s41", "scc");
    ld[13] = *(const u32x4*)((const char*)base + (size_t)(st < CODE_LANES ? (wg >> 3) * CODE_LANES + st : 0) * 16);
    SCHED_FENCE();
    STAMP(0, 1);
    lds_rendezvous((u32 LAS*)(smem + A_CNT + 8), 4);
    STAMP(0, 2);
#pragma unroll
    for (int ps = 0; ps < 2; ++ps) {
      const int d0 = 128 * tw + 64 * ps + 16 * (lane >> 4);
      const char LAS* src = (const char LAS*)tile + ((lane & 15) >> 2) * 1088 + 2 * (d0 + 4 * (lane & 3));
      const s16x4 lo = tr_read(src), hi = tr_read(src + 4 * 1088);
      u32x2 a0 = __builtin_bit_cast(u32x2, lo), a1 = __builtin_bit_cast(u32x2, hi);
      *(u32x4*)(a.xT + (size_t)(d0 + (lane & 15)) * L + t0) = u32x4{a0[0], a0[1], a1[0], a1[1]};
    }
    STAMP(0, 6);
    if (tw == 0) a.gran[wg * 64 + lane] = u32x4{0, 0, 0, 0};
    if (tw == 1 && wg < 8) a.gran[(256 + wg) * 64 + lane] = u32x4{0, 0, 0, 0};
    {
      float v[2][8], s[2];
#pragma unroll
      for (int j = 0; j < 2; ++j) {
#pragma unroll
        for (int k = 0; k < 4; ++k) { v[j][2 * k] = bf_lo(xr[j][k]); v[j][2 * k + 1] = bf_hi(xr[j][k]); }
        s[j] = ((v[j][0] + v[j][1]) + (v[j][2] + v[j][3])) + ((v[j][4] + v[j][5]) + (v[j][6] + v[j][7]));
      }
      float mean[2], q2[2];
#pragma unroll
      for (int j = 0; j < 2; ++j) mean[j] = wave_sum(s[j]) * (1.f / D);
#pragma unroll
      for (int j = 0; j < 2; ++j) {
        q2[j] = 0;
#pragma unroll
        for (int k = 0; k < 8; ++k) { const float dv = v[j][k] - mean[j]; q2[j] += dv * dv; }
      }
#pragma unroll
      for (int j = 0; j < 2; ++j) {
        const float var = wave_sum(q2[j]) * (1.f / D);
        const float sd = rbf(__builtin_amdgcn_sqrtf(rbf(rbf(var) + 1e-5f)));
        if (lane == 0) *(f32x2*)(a.stats1 + 2 * (t0 + 2 * tw + j)) = f32x2{rbf(mean[j]), __builtin_amdgcn_rcpf(sd)};
      }
    }
    STAMP(0, 7);
    float* red = (float*)(smem + A_RED);
    if (doM) {
      asm volatile("" ::"v"(ld[5]));
      STAMP(0, 8);
      float acc[8] = {0, 0, 0, 0, 0, 0, 0, 0};
#pragma unroll
      for (int p = 0; p < 4; ++p) {
        const float cx = bf1(sv[p]);
        const float s = rbf(cx * __builtin_amdgcn_rcpf(1.f + __expf(-cx)));
#pragma unroll
        for (int k = 0; k < 4; ++k) { acc[2 * k] += s * bf_lo(ld[2 + p][k]); acc[2 * k + 1] += s * bf_hi(ld[2 + p][k]); }
      }
#pragma unroll
      for (int k = 0; k < 8; ++k) {
        float v = acc[k];
        v += __int_as_float(__builtin_amdgcn_update_dpp(0, __float_as_int(v), 0x128, 0xf, 0xf, true));
        v = xor32_sum(xor16_sum(v));
        if (rr == 0) red[tw * 64 + 8 * q + k] = v;
      }
      STAMP(0, 3);
      lds_rendezvous((u32 LAS*)(smem + A_CNT + 12), 4);
      STAMP(0, 4);
      if (tw == 0) a.mod[3072 * mrb + 64 * mcb + lane] = (red[lane] + red[64 + lane]) + (red[128 + lane] + red[192 + lane]);
    }
#pragma unroll
    for (int k = 10; k < 14; ++k) asm volatile("" ::"v"(ld[k]));
    STAMP(0, 5);
  }
}
struct BArgs {
  const u16 *xT, *Krev, *Dskip, *win, *wout, *wsp;
  const u32* stats1; const float* mod;
  u16 *x1T, *Wt, *Wot; float* uv;
};
constexpr int B_H = 0;
constexpr int B_HS = (256 + 2048) * 2;
constexpr int B_KC = B_H + 2 * B_HS;
constexpr int B_KS = 4160;
constexpr int B_PART = B_KC + 2 * 4 * B_KS;
constexpr int B_RED = B_PART + 2 * 4 * 8 * 1280;
constexpr int B_CNT = B_RED + 4 * 16 * 4;
constexpr int B_LDS = B_CNT + 32;

__global__ __launch_bounds__(768) void kB(BArgs a) {
  extern __shared__ __attribute__((aligned(16))) char smem[];
  const int wg = blockIdx.x, tid = threadIdx.x, lane = tid & 63;
  const int w = __builtin_amdgcn_readfirstlane(tid >> 6);
  STAMP(1, 0);
  if (w < 8) {
    __builtin_amdgcn_s_setprio(2);
    const int ch = w >> 2, q = w & 3, d = 2 * wg + ch, tj = tid & 255;
    const u32x4 xv = *(const u32x4*)(a.xT + (size_t)d * L + 8 * tj);
    f32x4 st[4];
#pragma unroll
    for (int k = 0; k < 4; ++k) st[k] = *(const f32x4*)(a.stats1 + 16 * tj + 4 * k);
    const u32x4 k0 = *(const u32x4*)(a.Krev + (size_t)d * L + 8 * tj);
    const u32x4 k1r = *(const u32x4*)(a.Krev + (size_t)d * L + 8 * (tj < 255 ? tj + 1 : tj));
    const float sa = mod_get(a.mod, d), Ac = rbf(1.f + mod_get(a.mod, 512 + d)), ga = mod_get(a.mod, 1024 + d), dsk = bf1(a.Dskip[d]);
    SCHED_FENCE();
    __builtin_amdgcn_s_barrier();
    SCHED_FENCE();
    char* hb = smem + B_H + ch * B_HS;
    char* kc = smem + B_KC + ch * 4 * B_KS;
    if (tj < 32) *(u32x4*)(hb + 16 * tj) = u32x4{0, 0, 0, 0};
    if (tj < 16) *(u32x4*)(kc + (tj >> 2) * B_KS + 16 * (256 + (tj & 3))) = u32x4{0, 0, 0, 0};
    {
      const u32 z = tj < 255 ? 0xffffffffu : 0u;
      const u32 wd[8] = {k0[0], k0[1], k0[2], k0[3], k1r[0] & z, k1r[1] & z, 0, 0};
      u32 od[5];
#pragma unroll
      for (int k = 0; k < 5; ++k) od[k] = __builtin_amdgcn_alignbit(wd[k + 1], wd[k], 16);
      *(u32x4*)(kc + 0 * B_KS + 16 * tj) = u32x4{wd[0], wd[1], wd[2], wd[3]};
      *(u32x4*)(kc + 1 * B_KS + 16 * tj) = u32x4{od[0], od[1], od[2], od[3]};
      *(u32x4*)(kc + 2 * B_KS + 16 * tj) = u32x4{wd[1], wd[2], wd[3], wd[4]};
      *(u32x4*)(kc + 3 * B_KS + 16 * tj) = u32x4{od[1], od[2], od[3], od[4]};
    }
    u32x4 hv;
    {
      float hh[8];
#pragma unroll
      for (int k = 0; k < 8; ++k) {
        const float m = st[k >> 1][2 * (k & 1)], ri = st[k >> 1][2 * (k & 1) + 1];
        const float xx = (k & 1) ? bf_hi(xv[k >> 1]) : bf_lo(xv[k >> 1]);
        hh[k] = (xx - m) * (ri * Ac) + sa;
      }
      hv = u32x4{pk_bf16(hh[0], hh[1]), pk_bf16(hh[2], hh[3]), pk_bf16(hh[4], hh[5]), pk_bf16(hh[6], hh[7])};
    }
    *(u32x4*)(hb + 512 + 16 * tj) = hv;
    STAMP(1, 1);
    lds_rendezvous((u32 LAS*)(smem + B_CNT + 12 + 4 * 0) + ch * 2, 4);
    STAMP(1, 2);
    const int c = lane & 15, g = lane >> 4;
    const char* bbase = hb + 2 * (256 + 16 * c - 16 * (g >> 1) + 8 * (g & 1) - 32 * q) - 256;
    const int X = 2047 - 32 * q - 16 * (g >> 1) + 8 * (g & 1) - c;
    const char* abase = kc + (X & 3) * B_KS + 2 * (X & ~3) - 256 * 15;
    const char LAS* ab0 = (const char LAS*)abase;
    bf16x8 Bf[16], Af[16];
    auto lda = [&](int u) {
      const u32x2 lo = *(const volatile u32x2 LAS*)(ab0 + 256 * (15 - u)), hi = *(const volatile u32x2 LAS*)(ab0 + 256 * (15 - u) + 8);
      Af[u] = __builtin_bit_cast(bf16x8, u32x4{lo[0], lo[1], hi[0], hi[1]});
    };
    lda(0);
#pragma unroll
    for (int v = 1; v < 16; v += 2) Bf[v] = *(const bf16x8*)(bbase + 256 * v);
    lda(1);
#pragma unroll
    for (int v = 0; v < 16; v += 2) Bf[v] = *(const bf16x8*)(bbase + 256 * v);
    lda(2); lda(3);
    SCHED_FENCE();
    f32x4 acc[8];
#pragma unroll
    for (int i = 0; i < 8; ++i) acc[i] = f32x4{0, 0, 0, 0};
#pragma unroll
    for (int u = 0; u < 16; ++u) {
      if (u + 4 < 16) lda(u + 4);
      SCHED_FENCE();
#pragma unroll
      for (int i = u >> 1; i < 8; ++i) acc[i] = __builtin_amdgcn_mfma_f32_16x16x32_bf16(Af[u], Bf[2 * i - u + 1], acc[i], 0, 0, 0);
      SCHED_FENCE();
    }
    STAMP(1, 3);
    char* part = smem + B_PART + ch * (4 * 8 * 1280);
#pragma unroll
    for (int i = 0; i < 8; ++i) *(f32x4*)(part + (q * 8 + i) * 1280 + 16 * (20 * g + c)) = acc[i];
    lds_rendezvous((u32 LAS*)(smem + B_CNT + 4 * ch), 4);
    STAMP(1, 4);
    {
      const int i = tj >> 5, j5 = tj & 31, cc = j5 >> 1;
      float y[8];
#pragma unroll
      for (int hh = 0; hh < 2; ++hh) {
        const char* p = part + i * 1280 + 16 * (20 * (2 * (j5 & 1) + hh) + cc);
        const f32x4 p0 = *(const f32x4*)(p), p1 = *(const f32x4*)(p + 8 * 1280), p2 = *(const f32x4*)(p + 16 * 1280), p3 = *(const f32x4*)(p + 24 * 1280);
#pragma unroll
        for (int r = 0; r < 4; ++r) y[4 * hh + r] = (p0[r] + p1[r]) + (p2[r] + p3[r]);
      }
      float o[8];
#pragma unroll
      for (int k = 0; k < 8; ++k) {
        const float hx = (k & 1) ? bf_hi(hv[k >> 1]) : bf_lo(hv[k >> 1]);
        const float xx = (k & 1) ? bf_hi(xv[k >> 1]) : bf_lo(xv[k >> 1]);
        o[k] = xx + ga * (y[k] + hx * dsk);
      }
      wt_store16(make_rsrc(a.x1T, D * L * 2), (u32)(d * L + 8 * tj) * 2, u32x4{pk_bf16(o[0], o[1]), pk_bf16(o[2], o[3]), pk_bf16(o[4], o[5]), pk_bf16(o[6], o[7])});
    }
    STAMP(1, 5);
  } else {
    const int st = tid - 512, sw = w - 8;
    if (st < 8) *(u32*)(smem + B_CNT + 4 * st) = 0;
    asm volatile("s_waitcnt lgkmcnt(0)" ::: "memory");
    __builtin_amdgcn_s_barrier();
    SCHED_FENCE();
    float* red = (float*)(smem + B_RED);
    const bool act = wg < 224;
    const int f0 = 8 * ((wg & 7) * 28 + (wg >> 3));
    const int d8 = st >> 2, fp = st & 3;
    const int f8 = wg >> 1, dp = (wg & 1) * 128 + (st & 127);
    u32 wv[8], ov[8];
    f32x4 cb0, cb1, sb0, sb1;
    if (act) {
      cb0 = mod_get4(a.mod, 2048 + 8 * d8); cb1 = mod_get4(a.mod, 2048 + 8 * d8 + 4);
      sb0 = mod_get4(a.mod, 1536 + 8 * d8); sb1 = mod_get4(a.mod, 1536 + 8 * d8 + 4);
#pragma unroll
      for (int j = 0; j < 8; ++j) wv[j] = *(const u32*)(a.win + (size_t)(8 * d8 + j) * F + f0 + 2 * fp);
      if (st < 128) {
#pragma unroll
        for (int j = 0; j < 8; ++j) ov[j] = *(const u32*)(a.wout + (size_t)(8 * f8 + j) * D + 2 * dp);
      }
    }
    u32x4 pf[4];
    {
#pragma unroll
      for (int j = 0; j < 4; ++j) {
        const int idx = st + 256 * j;
        pf[j] = *(const u32x4*)(a.wsp + (size_t)(8 * wg + (idx >> 7)) * L + ((idx >> 5) & 3) * 512 + 256 + (idx & 31) * 8);
      }
    }
    SCHED_FENCE();
    if (act) {
      float lo[8], hi[8], u0 = 0, u1 = 0, v0 = 0, v1 = 0;
#pragma unroll
      for (int j = 0; j < 8; ++j) {
        const float Ac = rbf(1.f + (j < 4 ? cb0[j] : cb1[j - 4])), sb = j < 4 ? sb0[j] : sb1[j - 4];
        const float wl = bf_lo(wv[j]), wh = bf_hi(wv[j]);
        lo[j] = rbf(Ac * wl); hi[j] = rbf(Ac * wh);
        u0 += lo[j]; u1 += hi[j]; v0 += sb * wl; v1 += sb * wh;
      }
      const int f = f0 + 2 * fp;
      const rsrc_t rw = make_rsrc(a.Wt, D * F * 2);
      const u32 dst = (u32)((((f >> 4) * 64 + d8) * 16 + (f & 15)) * 16);
      wt_store16(rw, dst, u32x4{pk_bf16(lo[0], lo[1]), pk_bf16(lo[2], lo[3]), pk_bf16(lo[4], lo[5]), pk_bf16(lo[6], lo[7])});
      wt_store16(rw, dst + 16, u32x4{pk_bf16(hi[0], hi[1]), pk_bf16(hi[2], hi[3]), pk_bf16(hi[4], hi[5]), pk_bf16(hi[6], hi[7])});
      float rv[4] = {u0, u1, v0, v1};
#pragma unroll
      for (int k = 0; k < 4; ++k) {
        const float v = xor32_sum(xor16_sum(xor4_8_sum(rv[k])));
        if (lane < 4) red[sw * 16 + lane * 4 + k] = v;
      }
      if (st < 128) {
        const int dd = 2 * dp;
        const rsrc_t ro = make_rsrc(a.Wot, FH * D * 2);
        const u32 od = (u32)((((dd >> 4) * 112 + f8) * 16 + (dd & 15)) * 16);
        u32x4 e, o;
#pragma unroll
        for (int k = 0; k < 4; ++k) {
          e[k] = (ov[2 * k] & 0xffffu) | (ov[2 * k + 1] << 16);
          o[k] = (ov[2 * k] >> 16) | (ov[2 * k + 1] & 0xffff0000u);
        }
        wt_store16(ro, od, e); wt_store16(ro, od + 16, o);
      }
      lds_rendezvous((u32 LAS*)(smem + B_CNT + 8), 4);
      if (st < 16) {
        const float s = (red[st] + red[16 + st]) + (red[32 + st] + red[48 + st]);
        const int fq = st >> 2, k = st & 3;
        a.uv[(k >> 1) * F + f0 + 2 * fq + (k & 1)] = s;
      }
    }
    STAMP(1, 1);
#pragma unroll
    for (int j = 0; j < (int)(sizeof(pf) / sizeof(pf[0])); ++j) asm volatile("" ::"v"(pf[j]));
    STAMP(1, 5);
  }
}
struct FArgs {
  const u16 *x1T, *Wt, *wsp, *bsp, *Wot; const float *uv, *mod;
  u16 *z2t, *Pt, *out; u64* gran; u32* flags;
};
constexpr int F_X = 0;
constexpr int F_ST = 65536;
constexpr int F_MR = F_ST + 512;
constexpr int F_PS = F_MR + 8 * 512;
constexpr int F_FS = F_PS + 7 * 64 * 8;
constexpr int F_ZS = F_FS + 512;
constexpr int F_RS = 232;
constexpr int F_A0 = 90112;
constexpr int F_LDS = F_A0 + 8 * 8192;
constexpr int F_ETS = 1152;
constexpr int F_EX = 112 * 1024;
#define F_BDEPTH 3
constexpr int F_BD = F_BDEPTH;
constexpr int F_BR = F_BD;
static_assert(F_ZS + 64 * F_RS <= F_A0 && F_EX + 64 * 132 <= F_LDS, "LDS map");

__device__ __forceinline__ float gelu_tanh(float z) {
  const float k0 = -2.f * 0.7978845608028654f * 1.4426950408889634f, k1 = k0 * 0.044715f;
  const float e = __builtin_amdgcn_exp2f(z * (k0 + k1 * (z * z)));
  return z * __builtin_amdgcn_rcpf(1.f + e);
}
__device__ __forceinline__ float pick4(f32x4 v, int r) { return r == 0 ? v[0] : r == 1 ? v[1] : r == 2 ? v[2] : v[3]; }
template <int N> __device__ __forceinline__ void vm_wait_c() { asm volatile("s_waitcnt vmcnt(%0)" ::"i"(N > 63 ? 63 : N) : "memory"); }
__device__ __forceinline__ u32 f_slot(int kq, int sg) { return (u32)(((sg & 2) ? 0 : F_A0) + (kq * 2 + (sg & 1)) * 8192); }
__device__ __forceinline__ bf16x8 ld_dev16(rsrc_t r, u32 off) { return __builtin_bit_cast(bf16x8, __builtin_amdgcn_raw_buffer_load_b128(r, off, 0, 16)); }
__device__ __forceinline__ void f_lda(const u16* const (&pa)[2], u32 lds0, int kq, int nh, int sg) {
#pragma unroll
  for (int i = 0; i < 4; ++i) glds16(pa[i & 1] + (size_t)(i >> 1) * 16 * L + 64 * sg, lds0 + f_slot(kq, sg) + (4 * nh + i) * 1024);
}
__device__ __forceinline__ void f_ldx(const u16* x1T, u32 lds0, int w, int pi, int sg) {
  const int lane = threadIdx.x & 63;
#pragma unroll
  for (int i = 0; i < 2; ++i) {
    const int d = 128 * sg + 16 * w + 8 * i + (lane >> 3);
    const int fz = (((d >> 1) & 1) | ((d >> 2) & 2)) << 1;
    glds16(x1T + (size_t)d * L + 64 * pi + 8 * ((lane & 7) ^ fz), lds0 + F_X + (128 * sg + 16 * w + 8 * i) * 128);
  }
}
__device__ __forceinline__ void wait_flags(const u32* p, int n) {
  const int lane = threadIdx.x & 63;
  for (u32 spin = 0; spin < (1u << 22); ++spin) {
    const u32 v = __hip_atomic_load((GAS u32*)p + (lane < n ? lane : 0), __ATOMIC_RELAXED, __HIP_MEMORY_SCOPE_AGENT);
    if (__all(v == 1u)) break;
    __builtin_amdgcn_s_sleep(1);
  }
}

__device__ __forceinline__ u32 poll8(const u32* p, int n) {
  const int lane = threadIdx.x & 63;
  const u32 v = __hip_atomic_load((GAS u32*)p + (lane < n ? lane : 0), __ATOMIC_RELAXED, __HIP_MEMORY_SCOPE_AGENT);
  return (u32)__ballot(v == 1u) & 0xffu;
}
__device__ __forceinline__ void need_flag(const u32* p, int n, u32& mask, int k) {
  if (!((mask >> k) & 1u)) {
    for (u32 spin = 0; spin < (1u << 22); ++spin) {
      mask = poll8(p, n);
      if ((mask >> k) & 1u) break;
      __builtin_amdgcn_s_sleep(1);
    }
  }
  asm volatile("" ::: "memory");
}

struct Z1 { f32x4 y[4]; };

template <int ROLE>
__device__ __forceinline__ void f_phase_c(const FArgs& a, char* smem, const int w, const int pi, const int j, const u16* const (&pa)[2], Z1& z1) {
  const int tid = threadIdx.x, lane = tid & 63;
  const int c = lane & 15, g = lane >> 4;
  const u32 lds0 = (u32)(size_t)(const char LAS*)smem;
  const int nt2 = 56 + 7 * j + (w < 7 ? w : 0), nt1 = 7 * j + (w > 0 ? w - 1 : 0);
  const u16* pw2 = a.Wt + (size_t)nt2 * 64 * 128 + lane * 8;
  const u16* pw1 = a.Wt + (size_t)nt1 * 64 * 128 + lane * 8;
  const int q4 = (lane & 15) >> 2, p4 = lane & 3;
  const int drow = 8 * g + q4;
  const int fz0 = (((drow >> 1) & 1) | ((drow >> 2) & 2)) << 1;
  const char LAS* xb = (const char LAS*)smem + F_X + drow * 128 + 8 * (p4 & 1);
  const char LAS* xt[4];
  int tb[4];
#pragma unroll
  for (int s = 0; s < 4; ++s) { tb[s] = (s + w) & 3; xt[s] = xb + 16 * ((2 * tb[s] + (p4 >> 1)) ^ fz0); }
  auto xfrag = [&](int s, int ks) -> bf16x8 {
    const char LAS* p = xt[s] + ks * 4096;
    const s16x4 lo = tr_read(p), hi = tr_read(p + 512);
    const u32x2 a0 = __builtin_bit_cast(u32x2, lo), a1 = __builtin_bit_cast(u32x2, hi);
    return __builtin_bit_cast(bf16x8, u32x4{a0[0], a0[1], a1[0], a1[1]});
  };
  f32x4 acc[4];
#pragma unroll
  for (int s = 0; s < 4; ++s) acc[s] = f32x4{0, 0, 0, 0};
  bf16x8 W1[16];
  f32x4 u2, v2, u1, v1;
  float* xs2 = (float*)(smem + F_ST);
  if constexpr (ROLE < 2) {
    bf16x8 W2[16];
#pragma unroll
    for (int ks = 0; ks < 4; ++ks) W2[ks] = *(const bf16x8*)(pw2 + 4 * ks * 128);
    SCHED_FENCE();
    f_ldx(a.x1T, lds0, w, pi, 1);
#pragma unroll
    for (int ks = 4; ks < 8; ++ks) W2[ks] = *(const bf16x8*)(pw2 + 4 * ks * 128);
    SCHED_FENCE();
    VM_WAIT(10);
    __builtin_amdgcn_s_barrier();
    SCHED_FENCE();
    STAMP(2, 1);
    f32x4 sq = {0, 0, 0, 0};
    bf16x8 X[2][4];
#pragma unroll
    for (int s = 0; s < 4; ++s) X[0][s] = xfrag(s, 0);
#pragma unroll
    for (int ks = 0; ks < 16; ++ks) {
      if (ks == 3) { VM_WAIT(9); __builtin_amdgcn_s_barrier(); }
      if (ks == 7) { VM_WAIT(11); __builtin_amdgcn_s_barrier(); }
      if (ks == 11) { VM_WAIT(6); __builtin_amdgcn_s_barrier(); }
      if (ks + 1 < 16) {
#pragma unroll
        for (int s = 0; s < 4; ++s) X[(ks + 1) & 1][s] = xfrag(s, ks + 1);
      }
      if (ks == 0) f_ldx(a.x1T, lds0, w, pi, 2);
      if (ks == 4) f_ldx(a.x1T, lds0, w, pi, 3);
      if (ks < 8) W2[8 + ks] = *(const bf16x8*)(pw2 + 4 * (8 + ks) * 128);
      if (ks == 5) { u2 = *(const f32x4*)(a.uv + 16 * nt2 + 4 * g); v2 = *(const f32x4*)(a.uv + F + 16 * nt2 + 4 * g); }
      SCHED_FENCE();
#pragma unroll
      for (int s = 0; s < 4; ++s) acc[s] = __builtin_amdgcn_mfma_f32_16x16x32_bf16(W2[ks], X[ks & 1][s], acc[s], 0, 0, 0);
      sq = __builtin_amdgcn_mfma_f32_16x16x32_bf16(X[ks & 1][0], X[ks & 1][0], sq, 0, 0, 0);
      SCHED_FENCE();
    }
    if constexpr (ROLE == 1) {
      u1 = *(const f32x4*)(a.uv + 16 * nt1 + 4 * g); v1 = *(const f32x4*)(a.uv + F + 16 * nt1 + 4 * g);
      SCHED_FENCE();
    }
    if (w < 4 && (c >> 2) == g) xs2[64 + 16 * w + c] = pick4(sq, c & 3);
  } else {
    f_ldx(a.x1T, lds0, w, pi, 1);
    VM_WAIT(2);
    __builtin_amdgcn_s_barrier();
    SCHED_FENCE();
    STAMP(2, 1);
    const u32x4 ones = {0x3f803f80u, 0x3f803f80u, 0x3f803f80u, 0x3f803f80u};
    bf16x8 X[2][4];
#pragma unroll
    for (int s = 0; s < 4; ++s) X[0][s] = xfrag(s, 0);
#pragma unroll
    for (int ks = 0; ks < 16; ++ks) {
      if (ks == 3) { VM_WAIT(2); __builtin_amdgcn_s_barrier(); }
      if (ks == 7) { VM_WAIT(2); __builtin_amdgcn_s_barrier(); }
      if (ks == 11) { VM_WAIT(6); __builtin_amdgcn_s_barrier(); }
      if (ks + 1 < 16) {
#pragma unroll
        for (int s = 0; s < 4; ++s) X[(ks + 1) & 1][s] = xfrag(s, ks + 1);
      }
      if (ks == 0) f_ldx(a.x1T, lds0, w, pi, 2);
      if (ks == 4) f_ldx(a.x1T, lds0, w, pi, 3);
      if (ks >= 8) { W1[2 * ks - 16] = *(const bf16x8*)(pw1 + 4 * (2 * ks - 16) * 128); W1[2 * ks - 15] = *(const bf16x8*)(pw1 + 4 * (2 * ks - 15) * 128); }
      SCHED_FENCE();
#pragma unroll
      for (int s = 0; s < 4; ++s) acc[s] = __builtin_amdgcn_mfma_f32_16x16x32_bf16(__builtin_bit_cast(bf16x8, ones), X[ks & 1][s], acc[s], 0, 0, 0);
      SCHED_FENCE();
    }
    u1 = *(const f32x4*)(a.uv + 16 * nt1 + 4 * g); v1 = *(const f32x4*)(a.uv + F + 16 * nt1 + 4 * g);
    SCHED_FENCE();
    if (g == 0) {
#pragma unroll
      for (int s = 0; s < 4; ++s) xs2[16 * tb[s] + c] = acc[s][0];
    }
  }
  STAMP(2, 2);
  __syncthreads();
  STAMP(2, 3);
  float mu[4], ri[4];
  {
    const float mean = xs2[lane] * (1.f / D);
    const float var = fmaxf(xs2[64 + lane] * (1.f / D) - mean * mean, 0.f);
    f32x2* mr = (f32x2*)(smem + F_MR + w * 512);
    mr[lane] = f32x2{rbf(mean), __builtin_amdgcn_rcpf(rbf(__builtin_amdgcn_sqrtf(rbf(rbf(var) + 1e-5f))))};
#pragma unroll
    for (int s = 0; s < 4; ++s) { const f32x2 v = mr[16 * tb[s] + c]; mu[s] = v[0]; ri[s] = v[1]; }
  }
  u32x2 z2p[4];
  if constexpr (ROLE < 2) {
    float* ps = (float*)(smem + F_PS) + w * 128;
    const s16x4 ones4 = {0x3f80, 0x3f80, 0x3f80, 0x3f80};
#pragma unroll
    for (int s = 0; s < 4; ++s) {
      if constexpr (ROLE == 1) {
#pragma unroll
        for (int ks = 4 * s; ks < 4 * s + 4; ++ks) W1[ks] = *(const bf16x8*)(pw1 + 4 * ks * 128);
        SCHED_FENCE();
      }
      float z[4];
#pragma unroll
      for (int r = 0; r < 4; ++r) z[r] = gelu_tanh(ri[s] * (acc[s][r] - mu[s] * u2[r]) + v2[r]);
      z2p[s] = u32x2{pk_bf16(z[0], z[1]), pk_bf16(z[2], z[3])};
      const s16x4 zz = __builtin_bit_cast(s16x4, z2p[s]);
      const f32x4 zero = {0, 0, 0, 0};
      const f32x4 s1 = __builtin_amdgcn_mfma_f32_16x16x16bf16_1k(ones4, zz, zero, 0, 0, 0);
      const f32x4 s2 = __builtin_amdgcn_mfma_f32_16x16x16bf16_1k(zz, zz, zero, 0, 0, 0);
      if ((c >> 2) == g) *(f32x2*)(ps + (16 * tb[s] + c) * 2) = f32x2{s1[0], pick4(s2, c & 3)};
      SCHED_FENCE();
    }
  }
  if constexpr (ROLE > 0) { f_lda(pa, lds0, w & 3, w >> 2, 0); f_lda(pa, lds0, w & 3, w >> 2, 1); }
  STAMP(2, 4);
  __syncthreads();
  STAMP(2, 5);
  if constexpr (ROLE == 0) {
    const float* ps = (const float*)(smem + F_PS);
    float s1 = 0, s2 = 0;
#pragma unroll
    for (int k = 0; k < 7; ++k) { const f32x2 v = *(const f32x2*)(ps + k * 128 + lane * 2); s1 += v[0]; s2 += v[1]; }
    const rsrc_t rg = make_rsrc(a.gran, 256 * 1024);
    const u32 goff = (u32)(pi * 8 * 64 + lane) * 16;
    wt_store16(rg, goff + (u32)j * 1024, u32x4{__float_as_uint(s1), 1u, __float_as_uint(s2), 1u});
    float t1 = 0, t2 = 0;
    for (u32 spin = 0; spin < (1u << 22); ++spin) {
      u32x4 x[8];
#pragma unroll
      for (int k = 0; k < 8; ++k) x[k] = __builtin_amdgcn_raw_buffer_load_b128(rg, goff + k * 1024, 0, 16);
      bool ok = true; t1 = 0; t2 = 0;
#pragma unroll
      for (int k = 0; k < 8; ++k) {
        ok = ok && x[k][1] == 1u && x[k][3] == 1u;
        t1 += __uint_as_float(x[k][0]); t2 += __uint_as_float(x[k][2]);
      }
      if (__all(ok)) break;
      __builtin_amdgcn_s_sleep(1);
    }
    const float mean = t1 * (1.f / FH);
    const float var = fmaxf(t2 * (1.f / FH) - mean * mean, 0.f);
    *(f32x2*)(smem + F_FS + lane * 8) = f32x2{rbf(mean), __builtin_amdgcn_rcpf(rbf(__builtin_amdgcn_sqrtf(rbf(rbf(var) + 1e-5f))))};
    f_lda(pa, lds0, w & 3, w >> 2, 0);
    f_lda(pa, lds0, w & 3, w >> 2, 1);
  } else {
#pragma unroll
    for (int s = 0; s < 4; ++s) acc[s] = f32x4{0, 0, 0, 0};
    bf16x8 X[2][4];
#pragma unroll
    for (int s = 0; s < 4; ++s) X[0][s] = xfrag(s, 0);
#pragma unroll
    for (int ks = 0; ks < 16; ++ks) {
      if (ks + 1 < 16) {
#pragma unroll
        for (int s = 0; s < 4; ++s) X[(ks + 1) & 1][s] = xfrag(s, ks + 1);
      }
      SCHED_FENCE();
#pragma unroll
      for (int s = 0; s < 4; ++s) acc[s] = __builtin_amdgcn_mfma_f32_16x16x32_bf16(W1[ks], X[ks & 1][s], acc[s], 0, 0, 0);
      SCHED_FENCE();
    }
  }
  STAMP(2, 6);
  __syncthreads();
  STAMP(2, 7);
  if constexpr (ROLE < 2) {
#pragma unroll
    for (int s = 0; s < 4; ++s) {
      const f32x2 fs = *(const f32x2*)(smem + F_FS + (16 * tb[s] + c) * 8);
      const float a0 = (bf_lo(z2p[s][0]) - fs[0]) * fs[1], a1 = (bf_hi(z2p[s][0]) - fs[0]) * fs[1];
      const float a2 = (bf_lo(z2p[s][1]) - fs[0]) * fs[1], a3 = (bf_hi(z2p[s][1]) - fs[0]) * fs[1];
      *(u32x2*)(smem + F_ZS + (16 * tb[s] + c) * F_RS + (16 * w + 4 * g) * 2) = u32x2{pk_bf16(a0, a1), pk_bf16(a2, a3)};
    }
#pragma unroll
    for (int kk = 0; kk < 2; ++kk) {
      const char LAS* p = (const char LAS*)smem + F_ZS + (32 * kk + 8 * g + q4) * F_RS + (16 * w + 4 * p4) * 2;
      const s16x4 lo = tr_read(p), hi = tr_read(p + 4 * F_RS);
      const u32x2 a0 = __builtin_bit_cast(u32x2, lo), a1 = __builtin_bit_cast(u32x2, hi);
      wt_store16(make_rsrc(a.z2t, L * FH * 2), (u32)((((7 * j + w) * 256 + 8 * pi + 4 * kk) * 64 + lane * 4) * 4), u32x4{a0[0], a0[1], a1[0], a1[1]});
    }
  }
  VM_WAIT(0);
  __syncthreads();
  if (tid == 0) __hip_atomic_store((GAS u32*)a.flags + j * 32 + pi, 1u, __ATOMIC_RELAXED, __HIP_MEMORY_SCOPE_AGENT);
  STAMP(2, 8);
  f_lda(pa, lds0, w & 3, w >> 2, 2);
  f_lda(pa, lds0, w & 3, w >> 2, 3);
#pragma unroll
  for (int s = 0; s < 4; ++s)
#pragma unroll
    for (int r = 0; r < 4; ++r) z1.y[s][r] = ROLE > 0 ? ri[s] * (acc[s][r] - mu[s] * u1[r]) + v1[r] : 0.f;
  STAMP(2, 9);
}

template <int NT> constexpr int fd_glds(int sg) { return (sg == 2 || sg == 4) ? 8 : 0; }
template <int NT> constexpr int fd_bld(int sg) { return sg < 0 ? 0 : (2 * sg + F_BD < 16 ? NT : 0) + (2 * sg + 1 + F_BD < 16 ? NT : 0); }
template <int NT> constexpr int fd_ops(int sg) { return fd_glds<NT>(sg) + fd_bld<NT>(sg); }

template <int NT, int SG>
__device__ __forceinline__ void fd_stage(f32x4 (&acc)[4][NT], bf16x8 (&Bf)[F_BR][NT], bf16x8 (&Af)[2][4], const rsrc_t rb, u32 boff,
                                         const u16* const (&pa)[2], u32 lds0, const char* const (&ap)[2][2][4], int kq, int nh,
                                         const u32* fl, u32& mask, const Z1& z1, char* zdst, int w) {
  if constexpr ((SG & 1) == 0 && SG > 0) {
    if constexpr (SG == 2) vm_wait_c<F_BD * NT + fd_ops<NT>(0) + fd_ops<NT>(1)>();
    else vm_wait_c<fd_bld<NT>(SG - 2) + fd_ops<NT>(SG - 1)>();
    __builtin_amdgcn_s_barrier();
    if constexpr (fd_glds<NT>(SG) > 0) { f_lda(pa, lds0, kq, nh, SG + 2); f_lda(pa, lds0, kq, nh, SG + 3); }
#pragma unroll
    for (int m = 0; m < 4; ++m) Af[0][m] = *(const bf16x8*)(ap[(SG >> 1) & 1][0][m]);
  }
#pragma unroll
  for (int kk = 0; kk < 2; ++kk) {
    constexpr int ks0 = 2 * SG;
    if (kk == 0 || (SG & 1) == 0) {
#pragma unroll
      for (int m = 0; m < 4; ++m) Af[(kk + 1) & 1][m] = *(const bf16x8*)(ap[(SG >> 1) & 1][(kk + 1) & 1][m] + (kk == 0 ? (SG & 1) : 1) * 8192);
    }
    SCHED_FENCE();
#pragma unroll
    for (int n = 0; n < NT; ++n) {
#pragma unroll
      for (int m = 0; m < 4; ++m) acc[m][n] = __builtin_amdgcn_mfma_f32_16x16x32_bf16(Bf[(ks0 + kk) % F_BR][n], Af[kk][m], acc[m][n], 0, 0, 0);
      SCHED_FENCE();
      if (ks0 + kk + F_BD < 16) {
        if (n == 0 && ((ks0 + kk + F_BD) & 1) == 0) need_flag(fl, 8, mask, (ks0 + kk + F_BD) >> 1);
        Bf[(ks0 + kk + F_BD) % F_BR][n] = ld_dev16(rb, boff + (u32)((n * 256 + 4 * (ks0 + kk + F_BD)) * 256));
      }
      if constexpr (SG < 4) {
        if (kk == 0 && n == 1 && w > 0) {
          float z[4];
#pragma unroll
          for (int r = 0; r < 4; ++r) z[r] = gelu_tanh(z1.y[SG][r]);
          *(u32x2*)(zdst + ((SG + w) & 3) * 16 * F_RS) = u32x2{pk_bf16(z[0], z[1]), pk_bf16(z[2], z[3])};
        }
      }
      SCHED_FENCE();
    }
  }
}

template <int NT>
__device__ __forceinline__ void f_phase_d(const FArgs& a, char* smem, const int w, const int rt, const int ct, const u16* const (&pa)[2], const Z1& z1) {
  const int tid = threadIdx.x, lane = tid & 63;
  const int kq = w & 3, nh = w >> 2, n0 = nh * 4;
  const int c = lane & 15, g = lane >> 4;
  const u32 lds0 = (u32)(size_t)(const char LAS*)smem;
  const rsrc_t rb = make_rsrc(a.z2t, L * FH * 2);
  const u32 boff = (u32)((((7 * ct + n0) * 256 + 64 * kq) * 64 + lane * 4) * 4);
  const u32* fl = a.flags + ct * 32 + 8 * kq;
  u32 mask = poll8(fl, 8);
  bf16x8 Bf[F_BR][NT], Af[2][4];
#pragma unroll
  for (int ks = 0; ks < F_BD; ++ks) {
    if ((ks & 1) == 0) need_flag(fl, 8, mask, ks >> 1);
#pragma unroll
    for (int n = 0; n < NT; ++n) Bf[ks][n] = ld_dev16(rb, boff + (u32)((n * 256 + 4 * ks) * 256));
  }
  SCHED_FENCE();
  f32x4 acc[4][NT];
#pragma unroll
  for (int m = 0; m < 4; ++m)
#pragma unroll
    for (int n = 0; n < NT; ++n) acc[m][n] = f32x4{0, 0, 0, 0};
  const int asw = (c >> 1) & 7;
  const char* ap[2][2][4];
#pragma unroll
  for (int hi = 0; hi < 2; ++hi)
#pragma unroll
    for (int m = 0; m < 4; ++m) {
      const char* base = smem + f_slot(kq, 2 * hi) + ((m + kq) & 3) * 2048 + c * 128;
      ap[hi][0][m] = base + 16 * (g ^ asw); ap[hi][1][m] = base + 16 * ((4 + g) ^ asw);
    }
#pragma unroll
  for (int m = 0; m < 4; ++m) Af[0][m] = *(const bf16x8*)(ap[0][0][m]);
  char* zdst = smem + F_ZS + c * F_RS + (16 * (w - 1) + 4 * g) * 2;
  STAMP(3, 4);
  fd_stage<NT, 0>(acc, Bf, Af, rb, boff, pa, lds0, ap, kq, nh, fl, mask, z1, zdst, w);
  fd_stage<NT, 1>(acc, Bf, Af, rb, boff, pa, lds0, ap, kq, nh, fl, mask, z1, zdst, w);
  STAMP(3, 5);
  fd_stage<NT, 2>(acc, Bf, Af, rb, boff, pa, lds0, ap, kq, nh, fl, mask, z1, zdst, w);
  fd_stage<NT, 3>(acc, Bf, Af, rb, boff, pa, lds0, ap, kq, nh, fl, mask, z1, zdst, w);
  STAMP(3, 6);
  fd_stage<NT, 4>(acc, Bf, Af, rb, boff, pa, lds0, ap, kq, nh, fl, mask, z1, zdst, w);
  fd_stage<NT, 5>(acc, Bf, Af, rb, boff, pa, lds0, ap, kq, nh, fl, mask, z1, zdst, w);
  STAMP(3, 7);
  fd_stage<NT, 6>(acc, Bf, Af, rb, boff, pa, lds0, ap, kq, nh, fl, mask, z1, zdst, w);
  const float bs = bf1(a.bsp[64 * rt + 16 * kq + c]);
  fd_stage<NT, 7>(acc, Bf, Af, rb, boff, pa, lds0, ap, kq, nh, fl, mask, z1, zdst, w);
  STAMP(3, 1);
  __syncthreads();
#pragma unroll
  for (int m = 1; m < 4; ++m) {
    const int o = (m + kq) & 3;
    char* dst = smem + (o < 3 ? o * 21504 : F_A0) + ((m - 1) * 7 * 64 + n0 * 64 + lane) * 16;
#pragma unroll
    for (int n = 0; n < NT; ++n) *(f32x4*)(dst + n * 1024) = acc[m][n];
  }
  __syncthreads();
  STAMP(3, 2);
  const char* src = smem + (kq < 3 ? kq * 21504 : F_A0) + (n0 * 64 + lane) * 16;
  const char* zs = smem + F_ZS + (16 * kq + c) * F_RS + (16 * n0 + 4 * g) * 2;
  const rsrc_t rp = make_rsrc(a.Pt, L * FH * 2);
  u32x2 pv[NT];
#pragma unroll
  for (int n = 0; n < NT; ++n) {
    const f32x4 p0 = *(const f32x4*)(src + n * 1024), p1 = *(const f32x4*)(src + (7 + n) * 1024), p2 = *(const f32x4*)(src + (14 + n) * 1024);
    const u32x2 zz = *(const u32x2*)(zs + n * 32);
    float gg[4];
#pragma unroll
    for (int e = 0; e < 4; ++e) gg[e] = ((acc[0][n][e] + p0[e]) + (p1[e] + p2[e])) + bs;
    pv[n] = u32x2{pk_bf16(bf_lo(zz[0]) * gg[0], bf_hi(zz[0]) * gg[1]), pk_bf16(bf_lo(zz[1]) * gg[2], bf_hi(zz[1]) * gg[3])};
    if (n & 1) wt_store16(rp, (u32)((((4 * rt + kq) * 112 + 14 * ct + 2 * (n0 + n - 1 + (g & 1)) + (g >> 1)) * 16 + c) * 16), pair16(pv[n - 1], pv[n]));
  }
  if constexpr (NT & 1) {
    const u32x4 v = pair16(pv[NT - 1], pv[NT - 1]);
    if ((g & 1) == 0) wt_store16(rp, (u32)((((4 * rt + kq) * 112 + 14 * ct + 2 * (n0 + NT - 1) + (g >> 1)) * 16 + c) * 16), v);
  }
  STAMP(3, 3);
}

__device__ __forceinline__ void f_phase_e(const FArgs& a, char* smem, const int w, const int rt, const int dt) {
  const int tid = threadIdx.x, lane = tid & 63;
  const int kq = w & 3, mh = w >> 2;
  const int c = lane & 15, g = lane >> 4;
  const u32 lds0 = (u32)(size_t)(const char LAS*)smem;
  VM_WAIT(0);
  __syncthreads();
  if (tid == 0) __hip_atomic_store((GAS u32*)a.flags + 256 + rt * 32 + dt, 1u, __ATOMIC_RELAXED, __HIP_MEMORY_SCOPE_AGENT);
  STAMP(4, 5);
#pragma unroll
  for (int k = 0; k < 7; ++k)
#pragma unroll
    for (int nn = 0; nn < 2; ++nn) {
      const int n = 2 * mh + nn;
      glds16(a.Wot + ((size_t)(4 * dt + n) * 112 + 4 * (7 * kq + k)) * 128 + lane * 8, lds0 + (n * 28 + 7 * kq + k) * 1024);
    }
  const u32x4 xr = *(const u32x4*)(a.x1T + (size_t)(64 * dt + (tid >> 3)) * L + 64 * rt + 8 * (tid & 7));
  const int d8 = lane & 7;
  const f32x4 gb0 = mod_get4(a.mod, 2560 + 64 * dt + 8 * d8), gb1 = mod_get4(a.mod, 2560 + 64 * dt + 8 * d8 + 4);
  SCHED_FENCE();
  {
    const u32* fl = a.flags + 256 + rt * 32 + 2 * kq;
    u32 mask = poll8(fl, 2);
    need_flag(fl, 2, mask, 0); need_flag(fl, 2, mask, 1);
  }
  STAMP(4, 4);
  const rsrc_t rp = make_rsrc(a.Pt, L * FH * 2);
  bf16x8 Pf[7][2];
#pragma unroll
  for (int k = 0; k < 7; ++k)
#pragma unroll
    for (int m = 0; m < 2; ++m) Pf[k][m] = ld_dev16(rp, (u32)((((4 * rt + 2 * mh + m) * 112 + 4 * (7 * kq + k)) * 64 + lane * 4) * 4));
  {
    u32* p = (u32*)(smem + F_EX + (tid >> 3) * 132 + (tid & 7) * 16);
    p[0] = xr[0]; p[1] = xr[1]; p[2] = xr[2]; p[3] = xr[3];
  }
  vm_wait_c<14>();
  __builtin_amdgcn_s_barrier();
  SCHED_FENCE();
  f32x4 acc[2][4];
#pragma unroll
  for (int m = 0; m < 2; ++m)
#pragma unroll
    for (int n = 0; n < 4; ++n) acc[m][n] = f32x4{0, 0, 0, 0};
  const char* wp = smem + (7 * kq) * 1024 + lane * 16;
#pragma unroll
  for (int k = 0; k < 7; ++k) {
    bf16x8 Wf[4];
#pragma unroll
    for (int n = 0; n < 4; ++n) Wf[n] = *(const bf16x8*)(wp + (n * 28 + k) * 1024);
#pragma unroll
    for (int n = 0; n < 4; ++n)
#pragma unroll
      for (int m = 0; m < 2; ++m) acc[m][n] = __builtin_amdgcn_mfma_f32_16x16x32_bf16(Wf[n], Pf[k][m], acc[m][n], 0, 0, 0);
  }
  STAMP(4, 1);
  __syncthreads();
#pragma unroll
  for (int m = 0; m < 2; ++m)
#pragma unroll
    for (int n = 0; n < 4; ++n) *(f32x4*)(smem + (kq * 16 + (2 * mh + m) * 4 + n) * F_ETS + g * 288 + c * 16) = acc[m][n];
  __syncthreads();
  STAMP(4, 2);
  {
    const int t = 8 * w + (lane >> 3);
    float s[8];
#pragma unroll
    for (int hh = 0; hh < 2; ++hh) {
      const char* p = smem + ((t >> 4) * 4 + (d8 >> 1)) * F_ETS + (2 * (d8 & 1) + hh) * 288 + (t & 15) * 16;
      const f32x4 p0 = *(const f32x4*)p, p1 = *(const f32x4*)(p + 16 * F_ETS), p2 = *(const f32x4*)(p + 32 * F_ETS), p3 = *(const f32x4*)(p + 48 * F_ETS);
#pragma unroll
      for (int e = 0; e < 4; ++e) s[4 * hh + e] = (p0[e] + p1[e]) + (p2[e] + p3[e]);
    }
    float o[8];
#pragma unroll
    for (int e = 0; e < 8; ++e) {
      const float x1 = bf1(*(const u16*)(smem + F_EX + (8 * d8 + e) * 132 + t * 2));
      o[e] = x1 + (e < 4 ? gb0[e] : gb1[e - 4]) * s[e];
    }
    wt_store16(make_rsrc(a.out, L * D * 2), (u32)(((64 * rt + t) * D + 64 * dt + 8 * d8) * 2), u32x4{pk_bf16(o[0], o[1]), pk_bf16(o[2], o[3]), pk_bf16(o[4], o[5]), pk_bf16(o[6], o[7])});
  }
  STAMP(4, 3);
}

__global__ __launch_bounds__(512, 2) void kF(FArgs a) {
  extern __shared__ __attribute__((aligned(16))) char smem[];
  const int wg = blockIdx.x, lane = threadIdx.x & 63;
  const int w = __builtin_amdgcn_readfirstlane(threadIdx.x >> 6);
  const int xc = wg & 7, ii = wg >> 3;
  const int pi = (xc >> 1) * 8 + (ii & 7), j = (xc & 1) * 4 + (ii >> 3);
  STAMP(2, 0);
  f_ldx(a.x1T, (u32)(size_t)(const char LAS*)smem, w, pi, 0);
  const u16* pa[2];
#pragma unroll
  for (int par = 0; par < 2; ++par) {
    const int row = 32 * (w >> 2) + 8 * par + (lane >> 3);
    pa[par] = a.wsp + (size_t)(64 * pi + row) * L + 512 * (w & 3) + 8 * ((lane & 7) ^ ((row >> 1) & 7));
  }
  Z1 z1;
  if (w == 0) f_phase_c<0>(a, smem, w, pi, j, pa, z1);
  else if (w < 7) f_phase_c<1>(a, smem, w, pi, j, pa, z1);
  else f_phase_c<2>(a, smem, w, pi, j, pa, z1);
  STAMP(3, 0);
  if (w < 4) f_phase_d<4>(a, smem, w, pi, j, pa, z1);
  else f_phase_d<3>(a, smem, w, pi, j, pa, z1);
  STAMP(4, 0);
  f_phase_e(a, smem, w, pi, j);
}
#define KMASK(b)
extern "C" void kernel_launch(void* const* d_in, const int* in_sizes, int n_in, void* d_out, int out_size,
                              void* d_ws, size_t ws_size, hipStream_t stream) {
  const u16* x = (const u16*)d_in[0];
  char* ws = (char*)d_ws;
  float* mod = (float*)(ws + 524288);
  u32* stats1 = (u32*)(ws + 16384);
  float* uv = (float*)(ws + 32768);
  u64* gran = (u64*)(ws + 65536);
  u32* flags = (u32*)(ws + 65536 + 262144);
  u16* xT = (u16*)(ws + (1 << 20));
  u16* Krev = (u16*)(ws + (3 << 20));
  u16* x1T = xT;
  u16* Wt = (u16*)(ws + (7 << 20));
  u16* Wot = (u16*)(ws + (9 << 20));
  u16* Pt = (u16*)(ws + (10 << 20));
  u16* z2t = Krev;
  AArgs aa;
  aa.x = x; aa.Are = (const u16*)d_in[1]; aa.Aim = (const u16*)d_in[2]; aa.Bre = (const u16*)d_in[3]; aa.Bim = (const u16*)d_in[4];
  aa.Cre = (const u16*)d_in[5]; aa.Cim = (const u16*)d_in[6]; aa.logdt = (const u16*)d_in[7]; aa.c = (const u16*)d_in[14];
  aa.wmod = (const u16*)d_in[15]; aa.win = (const u16*)d_in[10]; aa.wout = (const u16*)d_in[13];
  aa.mod = mod; aa.stats1 = stats1; aa.xT = xT; aa.Krev = Krev; aa.gran = (u32x4*)gran;
  (void)hipFuncSetAttribute((const void*)kA, hipFuncAttributeMaxDynamicSharedMemorySize, A_LDS);
  KMASK(1) kA<<<256, 768, A_LDS, stream>>>(aa);
  BArgs ba;
  ba.xT = xT; ba.Krev = Krev; ba.Dskip = (const u16*)d_in[8]; ba.win = (const u16*)d_in[10]; ba.wout = (const u16*)d_in[13];
  ba.wsp = (const u16*)d_in[11]; ba.stats1 = stats1; ba.mod = mod; ba.x1T = x1T; ba.Wt = Wt; ba.Wot = Wot; ba.uv = uv;
  (void)hipFuncSetAttribute((const void*)kB, hipFuncAttributeMaxDynamicSharedMemorySize, B_LDS);
  KMASK(2) kB<<<256, 768, B_LDS, stream>>>(ba);
  FArgs fa;
  fa.x1T = x1T; fa.Wt = Wt; fa.wsp = (const u16*)d_in[11]; fa.bsp = (const u16*)d_in[12]; fa.Wot = Wot; fa.uv = uv; fa.mod = mod;
  fa.z2t = z2t; fa.Pt = Pt; fa.out = (u16*)d_out; fa.gran = gran; fa.flags = flags;
  (void)hipFuncSetAttribute((const void*)kF, hipFuncAttributeMaxDynamicSharedMemorySize, F_LDS);
  KMASK(4) kF<<<256, 512, F_LDS, stream>>>(fa);
}
```

```cpp
#include <hip/hip_runtime.h>
#include <hip/hip_bf16.h>
#include <stdint.h>

typedef unsigned short u16;
typedef unsigned int u32;
typedef unsigned long long u64;
typedef __bf16 bf16x8 __attribute__((ext_vector_type(8)));
typedef __bf16 bf16x2 __attribute__((ext_vector_type(2)));
typedef short s16x4 __attribute__((ext_vector_type(4)));
typedef float f32x2 __attribute__((ext_vector_type(2)));
typedef float f32x4 __attribute__((ext_vector_type(4)));
typedef u32 u32x2 __attribute__((ext_vector_type(2)));
typedef u32 u32x4 __attribute__((ext_vector_type(4)));
#define LAS __attribute__((address_space(3)))
#define GAS __attribute__((address_space(1)))

constexpr int L = 2048, D = 512, NS = 128, F = 1792, FH = 896;

__device__ __forceinline__ float bf_lo(u32 w) { return __uint_as_float(w << 16); }
__device__ __forceinline__ float bf_hi(u32 w) { return __uint_as_float(w & 0xffff0000u); }
__device__ __forceinline__ float bf1(u16 h) { return __uint_as_float(((u32)h) << 16); }
__device__ __forceinline__ u32 pk_bf16(float lo, float hi) {
  f32x2 a = {lo, hi};
  bf16x2 p = __builtin_convertvector(a, bf16x2);
  return __builtin_bit_cast(u32, p);
}
__device__ __forceinline__ float rbf(float x) { return bf_lo(pk_bf16(x, x)); }

template <int CTRL, int ROWMASK = 0xf>
__device__ __forceinline__ float dpp_add(float x) {
  int y = __builtin_amdgcn_update_dpp(0, __float_as_int(x), CTRL, ROWMASK, 0xf, true);
  return x + __int_as_float(y);
}
__device__ __forceinline__ float row16_sum(float x) {
  x = dpp_add<0xB1>(x);
  x = dpp_add<0x4E>(x);
  x = dpp_add<0x141>(x);
  x = dpp_add<0x140>(x);
  return x;
}
__device__ __forceinline__ float wave_sum(float x) {
  x = row16_sum(x);
  float a = __int_as_float(__builtin_amdgcn_readlane(__float_as_int(x), 0));
  float b = __int_as_float(__builtin_amdgcn_readlane(__float_as_int(x), 16));
  float c = __int_as_float(__builtin_amdgcn_readlane(__float_as_int(x), 32));
  float d = __int_as_float(__builtin_amdgcn_readlane(__float_as_int(x), 48));
  return (a + b) + (c + d);
}
__device__ __forceinline__ s16x4 tr_read(const char LAS* p) {
  return __builtin_amdgcn_ds_read_tr16_b64_v4i16((s16x4 LAS*)p);
}

#define STAMP(kern, i)
__device__ __forceinline__ void glds16(const void* gsrc, u32 lds_dst) {
  u32 keep;
  asm volatile("s_mov_b32 %0, m0\n\ts_mov_b32 m0, %2\n\ts_nop 0\n\tglobal_load_lds_dwordx4 %1, off\n\ts_mov_b32 m0, %0"
               : "=&s"(keep) : "v"(gsrc), "s"(lds_dst) : "memory");
}
#define SCHED_FENCE() __builtin_amdgcn_sched_barrier(0)
#define VM_WAIT(n) asm volatile("s_waitcnt vmcnt(" #n ")" ::: "memory")
__device__ __forceinline__ float xor16_sum(float x) {
  const u32 v = __float_as_uint(x);
  auto r = __builtin_amdgcn_permlane16_swap(v, v, false, false);
  return __uint_as_float(r[0]) + __uint_as_float(r[1]);
}
__device__ __forceinline__ float xor32_sum(float x) {
  const u32 v = __float_as_uint(x);
  auto r = __builtin_amdgcn_permlane32_swap(v, v, false, false);
  return __uint_as_float(r[0]) + __uint_as_float(r[1]);
}
__device__ __forceinline__ float xor4_8_sum(float x) {
  x = x + __int_as_float(__builtin_amdgcn_update_dpp(0, __float_as_int(x), 0x124, 0xf, 0xf, true));
  x = x + __int_as_float(__builtin_amdgcn_update_dpp(0, __float_as_int(x), 0x128, 0xf, 0xf, true));
  return x;
}
__device__ __forceinline__ void lds_rendezvous(u32 LAS* cnt, u32 target) {
  asm volatile("s_waitcnt lgkmcnt(0)" ::: "memory");
  if ((threadIdx.x & 63) == 0) __hip_atomic_fetch_add(cnt, 1u, __ATOMIC_RELAXED, __HIP_MEMORY_SCOPE_WORKGROUP);
  while (__hip_atomic_load(cnt, __ATOMIC_RELAXED, __HIP_MEMORY_SCOPE_WORKGROUP) < target) __builtin_amdgcn_s_sleep(1);
  asm volatile("" ::: "memory");
}

typedef __amdgpu_buffer_rsrc_t rsrc_t;
__device__ __forceinline__ rsrc_t make_rsrc(const void* base, u32 bytes) {
  return __builtin_amdgcn_make_buffer_rsrc(const_cast<void*>(base), 0, bytes, 0x00020000);
}
__device__ __forceinline__ void wt_store16(rsrc_t r, u32 byte_off, u32x4 v) { __builtin_amdgcn_raw_buffer_store_b128(v, r, byte_off, 0, 16); }
__device__ __forceinline__ u32x4 pair16(u32x2 a, u32x2 b) {
  const auto r0 = __builtin_amdgcn_permlane16_swap(a[0], b[0], false, false);
  const auto r1 = __builtin_amdgcn_permlane16_swap(a[1], b[1], false, false);
  return u32x4{r0[0], r1[0], r0[1], r1[1]};
}

__device__ __forceinline__ float mod_get(const float* mp, int i) { return rbf((mp[i] + mp[3072 + i]) + (mp[6144 + i] + mp[9216 + i])); }
__device__ __forceinline__ f32x4 mod_get4(const float* mp, int i) {
  const f32x4 a = *(const f32x4*)(mp + i), b = *(const f32x4*)(mp + 3072 + i), c = *(const f32x4*)(mp + 6144 + i), d = *(const f32x4*)(mp + 9216 + i);
  f32x4 r;
#pragma unroll
  for (int e = 0; e < 4; ++e) r[e] = rbf((a[e] + b[e]) + (c[e] + d[e]));
  return r;
}
constexpr int CODE_LANES = 84;
struct AArgs {
  const u16 *x, *Are, *Aim, *Bre, *Bim, *Cre, *Cim, *logdt, *c, *wmod, *win, *wout;
  float* mod; u32* stats1; u16* xT; u16* Krev; u32x4* gran;
};
constexpr int A_RS = 528;
constexpr int A_TAB = 0;
constexpr int A_KC = A_TAB + 2 * 64 * A_RS;
constexpr int A_TILE = A_KC + 2 * 768 * 4;
constexpr int A_RED = A_TILE + 8 * 1088;
constexpr int A_CNT = A_RED + 4 * 64 * 4;
constexpr int A_LDS = A_CNT + 16;

struct KIdx { u16 v[2048]; };
constexpr int bf16_round_int(int l) {
  int sh = 0;
  while ((l >> sh) >= 256) ++sh;
  if (sh == 0) return l;
  const int q = l >> sh, rem = l & ((1 << sh) - 1), half = 1 << (sh - 1);
  const int up = (rem > half || (rem == half && (q & 1))) ? 1 : 0;
  return (q + up) << sh;
}
constexpr KIdx make_kidx() {
  KIdx t{};
  for (int x = 0; x < 2048; ++x) {
    const int lr = bf16_round_int(2047 - x);
    const int aa = lr >> 7, b = (lr >> 3) & 15, r = lr & 7;
    const int row = lr < 256 ? 8 * aa + r : lr < 512 ? 16 + 4 * (aa - 2) + (r >> 1) : lr < 1024 ? 24 + 2 * (aa - 4) + (r >> 2) : 32 + (aa - 8);
    t.v[x] = (u16)(row * 16 + b);
  }
  return t;
}
__device__ const KIdx g_kidx = make_kidx();

struct cpx { float r, i; };
__device__ __forceinline__ cpx cmul(cpx a, cpx b) { return cpx{a.r * b.r - a.i * b.i, a.r * b.i + a.i * b.r}; }

__global__ __launch_bounds__(768) void kA(AArgs a) {
  extern __shared__ __attribute__((aligned(16))) char smem[];
  const int wg = blockIdx.x, tid = threadIdx.x, lane = tid & 63;
  const int w = __builtin_amdgcn_readfirstlane(tid >> 6);
  STAMP(0, 0);
  const int tw = w - 8, st = tid - 512;
  const int t0 = 8 * ((wg & 7) * 32 + (wg >> 3));
  const bool doM = wg < 192;
  const int mrb = wg / 48, mcb = wg - 48 * mrb;
  const int rr = lane >> 3, q = lane & 7;
  u32x4 ld[14];
  u16 sv[8];
  if (w < 8) {
    const int d = 2 * wg + (w >> 2), o = d * NS + 64 * ((w >> 1) & 1) + lane;
    sv[0] = a.Are[o]; sv[1] = a.Aim[o]; sv[2] = a.Bre[o]; sv[3] = a.Bim[o]; sv[4] = a.Cre[o]; sv[5] = a.Cim[o]; sv[6] = a.logdt[d]; sv[7] = 0;
    ld[0] = *(const u32x4*)(g_kidx.v + 8 * (tid & 255));
  } else {
#pragma unroll
    for (int j = 0; j < 2; ++j) ld[j] = *(const u32x4*)(a.x + (size_t)(t0 + 2 * tw + j) * D + 8 * lane);
    if (tw == 0 && lane < 4) *(u32*)(smem + A_CNT + 4 * lane) = 0;
    asm volatile("s_waitcnt lgkmcnt(0)" ::: "memory");
  }
  SCHED_FENCE();
  STAMP(0, 9);
  __builtin_amdgcn_s_barrier();
  STAMP(0, 10);
  SCHED_FENCE();
  if (w < 8) {
    const int ch = w >> 2, nh = (w >> 1) & 1, part = (w ^ ch) & 1, d = 2 * wg + ch;
    const int n = 64 * nh + lane;
    const u16 hare = sv[0], haim = sv[1], hbre = sv[2], hbim = sv[3], hcre = sv[4], hcim = sv[5], hdt = sv[6];
    const u32x4 kix = ld[0];
    asm volatile("" ::"v"((u32)hare), "v"((u32)hcim), "v"((u32)hdt));
    STAMP(0, 11);
    const float dt = rbf(__expf(bf1(hdt)));
    const float lr = (dt * bf1(hare)) * 1.4426950408889634f, lp = (dt * bf1(haim)) * 0.15915494309189535f;
    auto cexp = [&](float l) -> cpx {
      const float e = __builtin_amdgcn_exp2f(lr * l), ph = __builtin_amdgcn_fractf(lp * l);
      return cpx{e * __builtin_amdgcn_cosf(ph), e * __builtin_amdgcn_sinf(ph)};
    };
    char* tab = smem + A_TAB + ch * (64 * A_RS) + n * 4;
    auto put = [&](int row, cpx v) { *(u32*)(tab + row * A_RS) = pk_bf16(v.r, v.i); };
    if (part == 0) {
      const cpx E1 = cexp(1.f), E128 = cexp(128.f), E512 = cexp(512.f);
      const cpx E2 = cmul(E1, E1), E3 = cmul(E2, E1), E4 = cmul(E2, E2), E5 = cmul(E4, E1), E6 = cmul(E4, E2), E7 = cmul(E4, E3);
      put(0, cpx{1.f, 0.f}); put(1, E1); put(2, E2); put(3, E3); put(4, E4); put(5, E5); put(6, E6); put(7, E7);
      put(8, E128); put(9, cmul(E128, E1)); put(10, cmul(E128, E2)); put(11, cmul(E128, E3));
      put(12, cmul(E128, E4)); put(13, cmul(E128, E5)); put(14, cmul(E128, E6)); put(15, cmul(E128, E7));
      const cpx E256 = cmul(E128, E128), E384 = cmul(E256, E128);
      put(16, E256); put(17, cmul(E256, E2)); put(18, cmul(E256, E4)); put(19, cmul(E256, E6));
      put(20, E384); put(21, cmul(E384, E2)); put(22, cmul(E384, E4)); put(23, cmul(E384, E6));
      const cpx E640 = cmul(E512, E128), E768 = cmul(E512, E256), E896 = cmul(E512, E384);
      put(24, E512); put(25, cmul(E512, E4)); put(26, E640); put(27, cmul(E640, E4));
      put(28, E768); put(29, cmul(E768, E4)); put(30, E896); put(31, cmul(E896, E4));
    } else {
      const cpx E8 = cexp(8.f), E128 = cexp(128.f), E1024 = cexp(1024.f);
      const float br = dt * bf1(hbre), bi = dt * bf1(hbim), cr = bf1(hcre), ci = bf1(hcim);
      const cpx W = cmul(cpx{cr, ci}, cpx{br, bi});
      const cpx E16 = cmul(E8, E8), E32 = cmul(E16, E16), E64 = cmul(E32, E32);
      cpx Y[8];
      Y[0] = W; Y[1] = cmul(W, E8); Y[2] = cmul(W, E16); Y[3] = cmul(Y[1], E16);
#pragma unroll
      for (int b = 0; b < 4; ++b) Y[4 + b] = cmul(Y[b], E32);
#pragma unroll
      for (int b = 0; b < 8; ++b) {
        put(48 + b, cpx{Y[b].r, -Y[b].i});
        const cpx t = cmul(Y[b], E64);
        put(56 + b, cpx{t.r, -t.i});
      }
      const cpx E256 = cmul(E128, E128), E512 = cmul(E256, E256);
      cpx P[4];
      P[0] = E1024; P[1] = cmul(E1024, E128); P[2] = cmul(E1024, E256); P[3] = cmul(P[1], E256);
#pragma unroll
      for (int b = 0; b < 4; ++b) { put(32 + b, P[b]); put(36 + b, cmul(P[b], E512)); }
      put(40, cmul(E1024, E1024));
    }
    STAMP(0, 1);
    lds_rendezvous((u32 LAS*)(smem + A_CNT), 8);
    STAMP(0, 2);
    if (w < 6) {
      const int c2 = w >= 3 ? 1 : 0, t = w - 3 * c2, i = lane & 15, g = lane >> 4;
      const char* tb = smem + A_TAB + c2 * (64 * A_RS) + 16 * g;
      const char* pa = tb + (16 * t + i) * A_RS, *pb = tb + (48 + i) * A_RS;
      f32x4 acc = {0, 0, 0, 0};
      bf16x8 Af[8], Bf[8];
#pragma unroll
      for (int s = 0; s < 8; ++s) {
        Af[s] = *(const bf16x8*)(pa + s * 64); Bf[s] = *(const bf16x8*)(pb + s * 64);
      }
#pragma unroll
      for (int s = 0; s < 8; ++s) acc = __builtin_amdgcn_mfma_f32_16x16x32_bf16(Af[s], Bf[s], acc, 0, 0, 0);
      float* Kc = (float*)(smem + A_KC) + c2 * 768 + t * 256;
#pragma unroll
      for (int r = 0; r < 4; ++r) Kc[(4 * g + r) * 16 + i] = acc[r];
    }
    STAMP(0, 3);
    lds_rendezvous((u32 LAS*)(smem + A_CNT + 4), 8);
    STAMP(0, 4);
    const float* Kc = (const float*)(smem + A_KC) + ch * 768;
    float kv[8];
#pragma unroll
    for (int j = 0; j < 4; ++j) { kv[2 * j] = Kc[kix[j] & 0xffffu]; kv[2 * j + 1] = Kc[kix[j] >> 16]; }
    *(u32x4*)(a.Krev + (size_t)d * L + 8 * (tid & 255)) =
        u32x4{pk_bf16(kv[0], kv[1]), pk_bf16(kv[2], kv[3]), pk_bf16(kv[4], kv[5]), pk_bf16(kv[6], kv[7])};
    STAMP(0, 5);
  } else {
    __builtin_amdgcn_s_setprio(3);
    const u32x4 xr[2] = {ld[0], ld[1]};
    asm volatile("" ::"v"(xr[1]));
    STAMP(0, 11);
    char* tile = smem + A_TILE;
#pragma unroll
    for (int j = 0; j < 2; ++j) *(u32x4*)(tile + (2 * tw + j) * 1088 + 16 * lane) = xr[j];
    SCHED_FENCE();
    if (doM) {
#pragma unroll
      for (int p = 0; p < 4; ++p) ld[2 + p] = *(const u32x4*)(a.wmod + (size_t)(128 * mrb + p * 32 + tw * 8 + rr) * 3072 + 64 * mcb + 8 * q);
#pragma unroll
      for (int p = 0; p < 4; ++p) sv[p] = a.c[128 * mrb + p * 32 + tw * 8 + rr];
    }
    ld[10] = *(const u32x4*)(a.win + (size_t)wg * 3584 + st * 8);
    ld[11] = *(const u32x4*)(a.win + (size_t)wg * 3584 + (st < 192 ? 2048 + st * 8 : 0));
    ld[12] = *(const u32x4*)(a.wout + (size_t)wg * 1792 + (st < 224 ? st * 8 : 0));
    u64 base;
    asm volatile("s_getpc_b64 s[40:41]\n\ts_add_u32 s40, s40, _Z2kB5BArgs@rel32@lo+4\n\ts_addc_u32 s41, s41, _Z2kB5BArgs@rel32@hi+12\n\ts_mov_b64 %0, s[40:41]"
                 : "=s"(base) : : "s40", "s41", "scc");
    ld[13] = *(const u32x4*)((const char*)base + (size_t)(st < CODE_LANES ? (wg >> 3) * CODE_LANES + st : 0) * 16);
    SCHED_FENCE();
    STAMP(0, 1);
    lds_rendezvous((u32 LAS*)(smem + A_CNT + 8), 4);
    STAMP(0, 2);
#pragma unroll
    for (int ps = 0; ps < 2; ++ps) {
      const int d0 = 128 * tw + 64 * ps + 16 * (lane >> 4);
      const char LAS* src = (const char LAS*)tile + ((lane & 15) >> 2) * 1088 + 2 * (d0 + 4 * (lane & 3));
      const s16x4 lo = tr_read(src), hi = tr_read(src + 4 * 1088);
      u32x2 a0 = __builtin_bit_cast(u32x2, lo), a1 = __builtin_bit_cast(u32x2, hi);
      *(u32x4*)(a.xT + (size_t)(d0 + (lane & 15)) * L + t0) = u32x4{a0[0], a0[1], a1[0], a1[1]};
    }
    STAMP(0, 6);
    if (tw == 0) a.gran[wg * 64 + lane] = u32x4{0, 0, 0, 0};
    if (tw == 1 && wg < 8) a.gran[(256 + wg) * 64 + lane] = u32x4{0, 0, 0, 0};
    {
      float v[2][8], s[2];
#pragma unroll
      for (int j = 0; j < 2; ++j) {
#pragma unroll
        for (int k = 0; k < 4; ++k) { v[j][2 * k] = bf_lo(xr[j][k]); v[j][2 * k + 1] = bf_hi(xr[j][k]); }
        s[j] = ((v[j][0] + v[j][1]) + (v[j][2] + v[j][3])) + ((v[j][4] + v[j][5]) + (v[j][6] + v[j][7]));
      }
      float mean[2], q2[2];
#pragma unroll
      for (int j = 0; j < 2; ++j) mean[j] = wave_sum(s[j]) * (1.f / D);
#pragma unroll
      for (int j = 0; j < 2; ++j) {
        q2[j] = 0;
#pragma unroll
        for (int k = 0; k < 8; ++k) { const float dv = v[j][k] - mean[j]; q2[j] += dv * dv; }
      }
#pragma unroll
      for (int j = 0; j < 2; ++j) {
        const float var = wave_sum(q2[j]) * (1.f / D);
        const float sd = rbf(__builtin_amdgcn_sqrtf(rbf(rbf(var) + 1e-5f)));
        if (lane == 0) *(f32x2*)(a.stats1 + 2 * (t0 + 2 * tw + j)) = f32x2{rbf(mean[j]), __builtin_amdgcn_rcpf(sd)};
      }
    }
    STAMP(0, 7);
    float* red = (float*)(smem + A_RED);
    if (doM) {
      asm volatile("" ::"v"(ld[5]));
      STAMP(0, 8);
      float acc[8] = {0, 0, 0, 0, 0, 0, 0, 0};
#pragma unroll
      for (int p = 0; p < 4; ++p) {
        const float cx = bf1(sv[p]);
        const float s = rbf(cx * __builtin_amdgcn_rcpf(1.f + __expf(-cx)));
#pragma unroll
        for (int k = 0; k < 4; ++k) { acc[2 * k] += s * bf_lo(ld[2 + p][k]); acc[2 * k + 1] += s * bf_hi(ld[2 + p][k]); }
      }
#pragma unroll
      for (int k = 0; k < 8; ++k) {
        float v = acc[k];
        v += __int_as_float(__builtin_amdgcn_update_dpp(0, __float_as_int(v), 0x128, 0xf, 0xf, true));
        v = xor32_sum(xor16_sum(v));
        if (rr == 0) red[tw * 64 + 8 * q + k] = v;
      }
      STAMP(0, 3);
      lds_rendezvous((u32 LAS*)(smem + A_CNT + 12), 4);
      STAMP(0, 4);
      if (tw == 0) a.mod[3072 * mrb + 64 * mcb + lane] = (red[lane] + red[64 + lane]) + (red[128 + lane] + red[192 + lane]);
    }
#pragma unroll
    for (int k = 10; k < 14; ++k) asm volatile("" ::"v"(ld[k]));
    STAMP(0, 5);
  }
}
struct BArgs {
  const u16 *xT, *Krev, *Dskip, *win, *wout, *wsp;
  const u32* stats1; const float* mod;
  u16 *x1T, *Wt, *Wot; float* uv;
};
constexpr int B_H = 0;
constexpr int B_HS = (256 + 2048) * 2;
constexpr int B_KC = B_H + 2 * B_HS;
constexpr int B_KS = 4160;
constexpr int B_PART = B_KC + 2 * 4 * B_KS;
constexpr int B_RED = B_PART + 2 * 4 * 8 * 1280;
constexpr int B_CNT = B_RED + 4 * 16 * 4;
constexpr int B_LDS = B_CNT + 32;

__global__ __launch_bounds__(768) void kB(BArgs a) {
  extern __shared__ __attribute__((aligned(16))) char smem[];
  const int wg = blockIdx.x, tid = threadIdx.x, lane = tid & 63;
  const int w = __builtin_amdgcn_readfirstlane(tid >> 6);
  STAMP(1, 0);
  if (w < 8) {
    __builtin_amdgcn_s_setprio(2);
    const int ch = w >> 2, q = w & 3, d = 2 * wg + ch, tj = tid & 255;
    const u32x4 xv = *(const u32x4*)(a.xT + (size_t)d * L + 8 * tj);
    f32x4 st[4];
#pragma unroll
    for (int k = 0; k < 4; ++k) st[k] = *(const f32x4*)(a.stats1 + 16 * tj + 4 * k);
    const u32x4 k0 = *(const u32x4*)(a.Krev + (size_t)d * L + 8 * tj);
    const u32x4 k1r = *(const u32x4*)(a.Krev + (size_t)d * L + 8 * (tj < 255 ? tj + 1 : tj));
    const float sa = mod_get(a.mod, d), Ac = rbf(1.f + mod_get(a.mod, 512 + d)), ga = mod_get(a.mod, 1024 + d), dsk = bf1(a.Dskip[d]);
    SCHED_FENCE();
    __builtin_amdgcn_s_barrier();
    SCHED_FENCE();
    char* hb = smem + B_H + ch * B_HS;
    char* kc = smem + B_KC + ch * 4 * B_KS;
    if (tj < 32) *(u32x4*)(hb + 16 * tj) = u32x4{0, 0, 0, 0};
    if (tj < 16) *(u32x4*)(kc + (tj >> 2) * B_KS + 16 * (256 + (tj & 3))) = u32x4{0, 0, 0, 0};
    {
      const u32 z = tj < 255 ? 0xffffffffu : 0u;
      const u32 wd[8] = {k0[0], k0[1], k0[2], k0[3], k1r[0] & z, k1r[1] & z, 0, 0};
      u32 od[5];
#pragma unroll
      for (int k = 0; k < 5; ++k) od[k] = __builtin_amdgcn_alignbit(wd[k + 1], wd[k], 16);
      *(u32x4*)(kc + 0 * B_KS + 16 * tj) = u32x4{wd[0], wd[1], wd[2], wd[3]};
      *(u32x4*)(kc + 1 * B_KS + 16 * tj) = u32x4{od[0], od[1], od[2], od[3]};
      *(u32x4*)(kc + 2 * B_KS + 16 * tj) = u32x4{wd[1], wd[2], wd[3], wd[4]};
      *(u32x4*)(kc + 3 * B_KS + 16 * tj) = u32x4{od[1], od[2], od[3], od[4]};
    }
    u32x4 hv;
    {
      float hh[8];
#pragma unroll
      for (int k = 0; k < 8; ++k) {
        const float m = st[k >> 1][2 * (k & 1)], ri = st[k >> 1][2 * (k & 1) + 1];
        const float xx = (k & 1) ? bf_hi(xv[k >> 1]) : bf_lo(xv[k >> 1]);
        hh[k] = (xx - m) * (ri * Ac) + sa;
      }
      hv = u32x4{pk_bf16(hh[0], hh[1]), pk_bf16(hh[2], hh[3]), pk_bf16(hh[4], hh[5]), pk_bf16(hh[6], hh[7])};
    }
    *(u32x4*)(hb + 512 + 16 * tj) = hv;
    STAMP(1, 1);
    lds_rendezvous((u32 LAS*)(smem + B_CNT + 12 + 4 * 0) + ch * 2, 4);
    STAMP(1, 2);
    const int c = lane & 15, g = lane >> 4;
    const char* bbase = hb + 2 * (256 + 16 * c - 16 * (g >> 1) + 8 * (g & 1) - 32 * q) - 256;
    const int X = 2047 - 32 * q - 16 * (g >> 1) + 8 * (g & 1) - c;
    const char* abase = kc + (X & 3) * B_KS + 2 * (X & ~3) - 256 * 15;
    const char LAS* ab0 = (const char LAS*)abase;
    bf16x8 Bf[16], Af[16];
    auto lda = [&](int u) {
      const u32x2 lo = *(const volatile u32x2 LAS*)(ab0 + 256 * (15 - u)), hi = *(const volatile u32x2 LAS*)(ab0 + 256 * (15 - u) + 8);
      Af[u] = __builtin_bit_cast(bf16x8, u32x4{lo[0], lo[1], hi[0], hi[1]});
    };
    lda(0);
#pragma unroll
    for (int v = 1; v < 16; v += 2) Bf[v] = *(const bf16x8*)(bbase + 256 * v);
    lda(1);
#pragma unroll
    for (int v = 0; v < 16; v += 2) Bf[v] = *(const bf16x8*)(bbase + 256 * v);
    lda(2); lda(3);
    SCHED_FENCE();
    f32x4 acc[8];
#pragma unroll
    for (int i = 0; i < 8; ++i) acc[i] = f32x4{0, 0, 0, 0};
#pragma unroll
    for (int u = 0; u < 16; ++u) {
      if (u + 4 < 16) lda(u + 4);
      SCHED_FENCE();
#pragma unroll
      for (int i = u >> 1; i < 8; ++i) acc[i] = __builtin_amdgcn_mfma_f32_16x16x32_bf16(Af[u], Bf[2 * i - u + 1], acc[i], 0, 0, 0);
      SCHED_FENCE();
    }
    STAMP(1, 3);
    char* part = smem + B_PART + ch * (4 * 8 * 1280);
#pragma unroll
    for (int i = 0; i < 8; ++i) *(f32x4*)(part + (q * 8 + i) * 1280 + 16 * (20 * g + c)) = acc[i];
    lds_rendezvous((u32 LAS*)(smem + B_CNT + 4 * ch), 4);
    STAMP(1, 4);
    {
      const int i = tj >> 5, j5 = tj & 31, cc = j5 >> 1;
      float y[8];
#pragma unroll
      for (int hh = 0; hh < 2; ++hh) {
        const char* p = part + i * 1280 + 16 * (20 * (2 * (j5 & 1) + hh) + cc);
        const f32x4 p0 = *(const f32x4*)(p), p1 = *(const f32x4*)(p + 8 * 1280), p2 = *(const f32x4*)(p + 16 * 1280), p3 = *(const f32x4*)(p + 24 * 1280);
#pragma unroll
        for (int r = 0; r < 4; ++r) y[4 * hh + r] = (p0[r] + p1[r]) + (p2[r] + p3[r]);
      }
      float o[8];
#pragma unroll
      for (int k = 0; k < 8; ++k) {
        const float hx = (k & 1) ? bf_hi(hv[k >> 1]) : bf_lo(hv[k >> 1]);
        const float xx = (k & 1) ? bf_hi(xv[k >> 1]) : bf_lo(xv[k >> 1]);
        o[k] = xx + ga * (y[k] + hx * dsk);
      }
      wt_store16(make_rsrc(a.x1T, D * L * 2), (u32)(d * L + 8 * tj) * 2, u32x4{pk_bf16(o[0], o[1]), pk_bf16(o[2], o[3]), pk_bf16(o[4], o[5]), pk_bf16(o[6], o[7])});
    }
    STAMP(1, 5);
  } else {
    const int st = tid - 512, sw = w - 8;
    if (st < 8) *(u32*)(smem + B_CNT + 4 * st) = 0;
    asm volatile("s_waitcnt lgkmcnt(0)" ::: "memory");
    __builtin_amdgcn_s_barrier();
    SCHED_FENCE();
    float* red = (float*)(smem + B_RED);
    const bool act = wg < 224;
    const int f0 = 8 * ((wg & 7) * 28 + (wg >> 3));
    const int d8 = st >> 2, fp = st & 3;
    const int f8 = wg >> 1, dp = (wg & 1) * 128 + (st & 127);
    u32 wv[8], ov[8];
    f32x4 cb0, cb1, sb0, sb1;
    if (act) {
      cb0 = mod_get4(a.mod, 2048 + 8 * d8); cb1 = mod_get4(a.mod, 2048 + 8 * d8 + 4);
      sb0 = mod_get4(a.mod, 1536 + 8 * d8); sb1 = mod_get4(a.mod, 1536 + 8 * d8 + 4);
#pragma unroll
      for (int j = 0; j < 8; ++j) wv[j] = *(const u32*)(a.win + (size_t)(8 * d8 + j) * F + f0 + 2 * fp);
      if (st < 128) {
#pragma unroll
        for (int j = 0; j < 8; ++j) ov[j] = *(const u32*)(a.wout + (size_t)(8 * f8 + j) * D + 2 * dp);
      }
    }
    u32x4 pf[4];
    {
#pragma unroll
      for (int j = 0; j < 4; ++j) {
        const int idx = st + 256 * j;
        pf[j] = *(const u32x4*)(a.wsp + (size_t)(8 * wg + (idx >> 7)) * L + ((idx >> 5) & 3) * 512 + 256 + (idx & 31) * 8);
      }
    }
    SCHED_FENCE();
    if (act) {
      float lo[8], hi[8], u0 = 0, u1 = 0, v0 = 0, v1 = 0;
#pragma unroll
      for (int j = 0; j < 8; ++j) {
        const float Ac = rbf(1.f + (j < 4 ? cb0[j] : cb1[j - 4])), sb = j < 4 ? sb0[j] : sb1[j - 4];
        const float wl = bf_lo(wv[j]), wh = bf_hi(wv[j]);
        lo[j] = rbf(Ac * wl); hi[j] = rbf(Ac * wh);
        u0 += lo[j]; u1 += hi[j]; v0 += sb * wl; v1 += sb * wh;
      }
      const int f = f0 + 2 * fp;
      const rsrc_t rw = make_rsrc(a.Wt, D * F * 2);
      const u32 dst = (u32)((((f >> 4) * 64 + d8) * 16 + (f & 15)) * 16);
      wt_store16(rw, dst, u32x4{pk_bf16(lo[0], lo[1]), pk_bf16(lo[2], lo[3]), pk_bf16(lo[4], lo[5]), pk_bf16(lo[6], lo[7])});
      wt_store16(rw, dst + 16, u32x4{pk_bf16(hi[0], hi[1]), pk_bf16(hi[2], hi[3]), pk_bf16(hi[4], hi[5]), pk_bf16(hi[6], hi[7])});
      float rv[4] = {u0, u1, v0, v1};
#pragma unroll
      for (int k = 0; k < 4; ++k) {
        const float v = xor32_sum(xor16_sum(xor4_8_sum(rv[k])));
        if (lane < 4) red[sw * 16 + lane * 4 + k] = v;
      }
      if (st < 128) {
        const int dd = 2 * dp;
        const rsrc_t ro = make_rsrc(a.Wot, FH * D * 2);
        const u32 od = (u32)((((dd >> 4) * 112 + f8) * 16 + (dd & 15)) * 16);
        u32x4 e, o;
#pragma unroll
        for (int k = 0; k < 4; ++k) {
          e[k] = (ov[2 * k] & 0xffffu) | (ov[2 * k + 1] << 16);
          o[k] = (ov[2 * k] >> 16) | (ov[2 * k + 1] & 0xffff0000u);
        }
        wt_store16(ro, od, e); wt_store16(ro, od + 16, o);
      }
      lds_rendezvous((u32 LAS*)(smem + B_CNT + 8), 4);
      if (st < 16) {
        const float s = (red[st] + red[16 + st]) + (red[32 + st] + red[48 + st]);
        const int fq = st >> 2, k = st & 3;
        a.uv[(k >> 1) * F + f0 + 2 * fq + (k & 1)] = s;
      }
    }
    STAMP(1, 1);
#pragma unroll
    for (int j = 0; j < (int)(sizeof(pf) / sizeof(pf[0])); ++j) asm volatile("" ::"v"(pf[j]));
    STAMP(1, 5);
  }
}
struct FArgs {
  const u16 *x1T, *Wt, *wsp, *bsp, *Wot; const float *uv, *mod;
  u16 *z2t, *Pt, *out; u64* gran; u32* flags;
};
constexpr int F_X = 0;
constexpr int F_ST = 65536;
constexpr int F_MR = F_ST + 512;
constexpr int F_PS = F_MR + 8 * 512;
constexpr int F_FS = F_PS + 7 * 64 * 8;
constexpr int F_ZS = F_FS + 512;
constexpr int F_RS = 232;
constexpr int F_A0 = 90112;
constexpr int F_LDS = F_A0 + 8 * 8192;
constexpr int F_EX = 112 * 1024;
#define F_SLEEP0 16
#define F_SLEEP1 8
#define F_BDEPTH 3
constexpr int F_BD = F_BDEPTH;
constexpr int F_BR = F_BD;
static_assert(F_ZS + 64 * F_RS <= F_A0 && F_EX + 64 * 132 <= F_LDS, "LDS map");

__device__ __forceinline__ float gelu_tanh(float z) {
  const float k0 = -2.f * 0.7978845608028654f * 1.4426950408889634f, k1 = k0 * 0.044715f;
  const float e = __builtin_amdgcn_exp2f(z * (k0 + k1 * (z * z)));
  return z * __builtin_amdgcn_rcpf(1.f + e);
}
__device__ __forceinline__ float pick4(f32x4 v, int r) { return r == 0 ? v[0] : r == 1 ? v[1] : r == 2 ? v[2] : v[3]; }
template <int N> __device__ __forceinline__ void vm_wait_c() { asm volatile("s_waitcnt vmcnt(%0)" ::"i"(N > 63 ? 63 : N) : "memory"); }
__device__ __forceinline__ u32 f_slot(int kq, int sg) { return (u32)(((sg & 2) ? 0 : F_A0) + (kq * 2 + (sg & 1)) * 8192); }
__device__ __forceinline__ bf16x8 ld_dev16(rsrc_t r, u32 off) { return __builtin_bit_cast(bf16x8, __builtin_amdgcn_raw_buffer_load_b128(r, off, 0, 16)); }
__device__ __forceinline__ void f_lda(const u16* const (&pa)[2], u32 lds0, int kq, int nh, int sg) {
#pragma unroll
  for (int i = 0; i < 4; ++i) glds16(pa[i & 1] + (size_t)(i >> 1) * 16 * L + 64 * sg, lds0 + f_slot(kq, sg) + (4 * nh + i) * 1024);
}
__device__ __forceinline__ void f_lda_full(const u16* wsp, u32 lds0, int pi, int kq, int sg) {
  const int lane = threadIdx.x & 63;
#pragma unroll
  for (int i = 0; i < 8; ++i) {
    const int row = 8 * i + (lane >> 3);
    glds16(wsp + (size_t)(64 * pi + row) * L + 512 * kq + 64 * sg + 8 * ((lane & 7) ^ ((row >> 1) & 7)), lds0 + f_slot(kq, sg) + i * 1024);
  }
}
__device__ __forceinline__ void f_ldx(const u16* x1T, u32 lds0, int w, int pi, int sg) {
  const int lane = threadIdx.x & 63;
#pragma unroll
  for (int i = 0; i < 2; ++i) {
    const int d = 128 * sg + 16 * w + 8 * i + (lane >> 3);
    const int fz = (((d >> 1) & 1) | ((d >> 2) & 2)) << 1;
    glds16(x1T + (size_t)d * L + 64 * pi + 8 * ((lane & 7) ^ fz), lds0 + F_X + (128 * sg + 16 * w + 8 * i) * 128);
  }
}
__device__ __forceinline__ void wait_flags(const u32* p, int n) {
  const int lane = threadIdx.x & 63;
  for (u32 spin = 0; spin < (1u << 22); ++spin) {
    const u32 v = __hip_atomic_load((GAS u32*)p + (lane < n ? lane : 0), __ATOMIC_RELAXED, __HIP_MEMORY_SCOPE_AGENT);
    if (__all(v == 1u)) break;
    __builtin_amdgcn_s_sleep(1);
  }
}

__device__ __forceinline__ u32 poll8(const u32* p, int n) {
  const int lane = threadIdx.x & 63;
  const u32 v = __hip_atomic_load((GAS u32*)p + (lane < n ? lane : 0), __ATOMIC_RELAXED, __HIP_MEMORY_SCOPE_AGENT);
  return (u32)__ballot(v == 1u) & 0xffu;
}
__device__ __forceinline__ void need_flag(const u32* p, int n, u32& mask, int k) {
  if (!((mask >> k) & 1u)) {
    for (u32 spin = 0; spin < (1u << 22); ++spin) {
      __builtin_amdgcn_s_sleep(F_SLEEP1);
      mask = poll8(p, n);
      if ((mask >> k) & 1u) break;
    }
  }
  asm volatile("" ::: "memory");
}

struct Z1 { f32x4 y[4]; };

template <int ROLE>
__device__ __forceinline__ void f_phase_c(const FArgs& a, char* smem, const int w, const int pi, const int j, const u16* const (&pa)[2], Z1& z1) {
  const int tid = threadIdx.x, lane = tid & 63;
  const int c = lane & 15, g = lane >> 4;
  const u32 lds0 = (u32)(size_t)(const char LAS*)smem;
  const int nt2 = 56 + 7 * j + (w < 7 ? w : 0), nt1 = 7 * j + (w > 0 ? w - 1 : 0);
  const u16* pw2 = a.Wt + (size_t)nt2 * 64 * 128 + lane * 8;
  const u16* pw1 = a.Wt + (size_t)nt1 * 64 * 128 + lane * 8;
  const int q4 = (lane & 15) >> 2, p4 = lane & 3;
  const int drow = 8 * g + q4;
  const int fz0 = (((drow >> 1) & 1) | ((drow >> 2) & 2)) << 1;
  const char LAS* xb = (const char LAS*)smem + F_X + drow * 128 + 8 * (p4 & 1);
  const char LAS* xt[4];
  int tb[4];
#pragma unroll
  for (int s = 0; s < 4; ++s) { tb[s] = (s + w) & 3; xt[s] = xb + 16 * ((2 * tb[s] + (p4 >> 1)) ^ fz0); }
  auto xfrag = [&](int s, int ks) -> bf16x8 {
    const char LAS* p = xt[s] + ks * 4096;
    const s16x4 lo = tr_read(p), hi = tr_read(p + 512);
    const u32x2 a0 = __builtin_bit_cast(u32x2, lo), a1 = __builtin_bit_cast(u32x2, hi);
    return __builtin_bit_cast(bf16x8, u32x4{a0[0], a0[1], a1[0], a1[1]});
  };
  f32x4 acc[4];
#pragma unroll
  for (int s = 0; s < 4; ++s) acc[s] = f32x4{0, 0, 0, 0};
  bf16x8 W1[16];
  f32x4 u2, v2, u1, v1;
  float* xs2 = (float*)(smem + F_ST);
  if constexpr (ROLE < 2) {
    bf16x8 W2[16];
#pragma unroll
    for (int ks = 0; ks < 4; ++ks) W2[ks] = *(const bf16x8*)(pw2 + 4 * ks * 128);
    SCHED_FENCE();
    f_ldx(a.x1T, lds0, w, pi, 1);
#pragma unroll
    for (int ks = 4; ks < 8; ++ks) W2[ks] = *(const bf16x8*)(pw2 + 4 * ks * 128);
    SCHED_FENCE();
    VM_WAIT(10);
    __builtin_amdgcn_s_barrier();
    SCHED_FENCE();
    STAMP(2, 1);
    f32x4 sq = {0, 0, 0, 0};
    bf16x8 X[2][4];
#pragma unroll
    for (int s = 0; s < 4; ++s) X[0][s] = xfrag(s, 0);
#pragma unroll
    for (int ks = 0; ks < 16; ++ks) {
      if (ks == 3) { VM_WAIT(9); __builtin_amdgcn_s_barrier(); }
      if (ks == 7) { VM_WAIT(11); __builtin_amdgcn_s_barrier(); }
      if (ks == 11) { VM_WAIT(6); __builtin_amdgcn_s_barrier(); }
      if (ks + 1 < 16) {
#pragma unroll
        for (int s = 0; s < 4; ++s) X[(ks + 1) & 1][s] = xfrag(s, ks + 1);
      }
      if (ks == 0) f_ldx(a.x1T, lds0, w, pi, 2);
      if (ks == 4) f_ldx(a.x1T, lds0, w, pi, 3);
      if (ks < 8) W2[8 + ks] = *(const bf16x8*)(pw2 + 4 * (8 + ks) * 128);
      if (ks == 5) { u2 = *(const f32x4*)(a.uv + 16 * nt2 + 4 * g); v2 = *(const f32x4*)(a.uv + F + 16 * nt2 + 4 * g); }
      SCHED_FENCE();
#pragma unroll
      for (int s = 0; s < 4; ++s) acc[s] = __builtin_amdgcn_mfma_f32_16x16x32_bf16(W2[ks], X[ks & 1][s], acc[s], 0, 0, 0);
      sq = __builtin_amdgcn_mfma_f32_16x16x32_bf16(X[ks & 1][0], X[ks & 1][0], sq, 0, 0, 0);
      SCHED_FENCE();
    }
    if constexpr (ROLE == 1) {
      u1 = *(const f32x4*)(a.uv + 16 * nt1 + 4 * g); v1 = *(const f32x4*)(a.uv + F + 16 * nt1 + 4 * g);
      SCHED_FENCE();
    }
    if (w < 4 && (c >> 2) == g) xs2[64 + 16 * w + c] = pick4(sq, c & 3);
  } else {
    f_ldx(a.x1T, lds0, w, pi, 1);
    VM_WAIT(2);
    __builtin_amdgcn_s_barrier();
    SCHED_FENCE();
    STAMP(2, 1);
    const u32x4 ones = {0x3f803f80u, 0x3f803f80u, 0x3f803f80u, 0x3f803f80u};
    bf16x8 X[2][4];
#pragma unroll
    for (int s = 0; s < 4; ++s) X[0][s] = xfrag(s, 0);
#pragma unroll
    for (int ks = 0; ks < 16; ++ks) {
      if (ks == 3) { VM_WAIT(2); __builtin_amdgcn_s_barrier(); }
      if (ks == 7) { VM_WAIT(2); __builtin_amdgcn_s_barrier(); }
      if (ks == 11) { VM_WAIT(6); __builtin_amdgcn_s_barrier(); }
      if (ks + 1 < 16) {
#pragma unroll
        for (int s = 0; s < 4; ++s) X[(ks + 1) & 1][s] = xfrag(s, ks + 1);
      }
      if (ks == 0) f_ldx(a.x1T, lds0, w, pi, 2);
      if (ks == 4) f_ldx(a.x1T, lds0, w, pi, 3);
      if (ks >= 8) { W1[2 * ks - 16] = *(const bf16x8*)(pw1 + 4 * (2 * ks - 16) * 128); W1[2 * ks - 15] = *(const bf16x8*)(pw1 + 4 * (2 * ks - 15) * 128); }
      SCHED_FENCE();
#pragma unroll
      for (int s = 0; s < 4; ++s) acc[s] = __builtin_amdgcn_mfma_f32_16x16x32_bf16(__builtin_bit_cast(bf16x8, ones), X[ks & 1][s], acc[s], 0, 0, 0);
      SCHED_FENCE();
    }
    u1 = *(const f32x4*)(a.uv + 16 * nt1 + 4 * g); v1 = *(const f32x4*)(a.uv + F + 16 * nt1 + 4 * g);
    SCHED_FENCE();
    if (g == 0) {
#pragma unroll
      for (int s = 0; s < 4; ++s) xs2[16 * tb[s] + c] = acc[s][0];
    }
  }
  STAMP(2, 2);
  __syncthreads();
  STAMP(2, 3);
  float mu[4], ri[4];
  {
    const float mean = xs2[lane] * (1.f / D);
    const float var = fmaxf(xs2[64 + lane] * (1.f / D) - mean * mean, 0.f);
    f32x2* mr = (f32x2*)(smem + F_MR + w * 512);
    mr[lane] = f32x2{rbf(mean), __builtin_amdgcn_rcpf(rbf(__builtin_amdgcn_sqrtf(rbf(rbf(var) + 1e-5f))))};
#pragma unroll
    for (int s = 0; s < 4; ++s) { const f32x2 v = mr[16 * tb[s] + c]; mu[s] = v[0]; ri[s] = v[1]; }
  }
  u32x2 z2p[4];
  if constexpr (ROLE < 2) {
    float* ps = (float*)(smem + F_PS) + w * 128;
    const s16x4 ones4 = {0x3f80, 0x3f80, 0x3f80, 0x3f80};
#pragma unroll
    for (int s = 0; s < 4; ++s) {
      if constexpr (ROLE == 1) {
#pragma unroll
        for (int ks = 4 * s; ks < 4 * s + 4; ++ks) W1[ks] = *(const bf16x8*)(pw1 + 4 * ks * 128);
        SCHED_FENCE();
      }
      float z[4];
#pragma unroll
      for (int r = 0; r < 4; ++r) z[r] = gelu_tanh(ri[s] * (acc[s][r] - mu[s] * u2[r]) + v2[r]);
      z2p[s] = u32x2{pk_bf16(z[0], z[1]), pk_bf16(z[2], z[3])};
      const s16x4 zz = __builtin_bit_cast(s16x4, z2p[s]);
      const f32x4 zero = {0, 0, 0, 0};
      const f32x4 s1 = __builtin_amdgcn_mfma_f32_16x16x16bf16_1k(ones4, zz, zero, 0, 0, 0);
      const f32x4 s2 = __builtin_amdgcn_mfma_f32_16x16x16bf16_1k(zz, zz, zero, 0, 0, 0);
      if ((c >> 2) == g) *(f32x2*)(ps + (16 * tb[s] + c) * 2) = f32x2{s1[0], pick4(s2, c & 3)};
      SCHED_FENCE();
    }
  }
  if constexpr (ROLE > 0) { f_lda(pa, lds0, w & 3, w >> 2, 0); f_lda(pa, lds0, w & 3, w >> 2, 1); }
  STAMP(2, 4);
  __syncthreads();
  STAMP(2, 5);
  if constexpr (ROLE == 0) {
    const float* ps = (const float*)(smem + F_PS);
    float s1 = 0, s2 = 0;
#pragma unroll
    for (int k = 0; k < 7; ++k) { const f32x2 v = *(const f32x2*)(ps + k * 128 + lane * 2); s1 += v[0]; s2 += v[1]; }
    const rsrc_t rg = make_rsrc(a.gran, 256 * 1024);
    const u32 goff = (u32)(pi * 8 * 64 + lane) * 16;
    wt_store16(rg, goff + (u32)j * 1024, u32x4{__float_as_uint(s1), 1u, __float_as_uint(s2), 1u});
    float t1 = 0, t2 = 0;
    for (u32 spin = 0; spin < (1u << 22); ++spin) {
      u32x4 x[8];
#pragma unroll
      for (int k = 0; k < 8; ++k) x[k] = __builtin_amdgcn_raw_buffer_load_b128(rg, goff + k * 1024, 0, 16);
      bool ok = true; t1 = 0; t2 = 0;
#pragma unroll
      for (int k = 0; k < 8; ++k) {
        ok = ok && x[k][1] == 1u && x[k][3] == 1u;
        t1 += __uint_as_float(x[k][0]); t2 += __uint_as_float(x[k][2]);
      }
      if (__all(ok)) break;
      __builtin_amdgcn_s_sleep(1);
    }
    const float mean = t1 * (1.f / FH);
    const float var = fmaxf(t2 * (1.f / FH) - mean * mean, 0.f);
    *(f32x2*)(smem + F_FS + lane * 8) = f32x2{rbf(mean), __builtin_amdgcn_rcpf(rbf(__builtin_amdgcn_sqrtf(rbf(rbf(var) + 1e-5f))))};
    f_lda(pa, lds0, w & 3, w >> 2, 0);
    f_lda(pa, lds0, w & 3, w >> 2, 1);
  } else {
#pragma unroll
    for (int s = 0; s < 4; ++s) acc[s] = f32x4{0, 0, 0, 0};
    bf16x8 X[3][4];
#pragma unroll
    for (int s = 0; s < 4; ++s) { X[0][s] = xfrag(s, 0); X[1][s] = xfrag(s, 1); }
#pragma unroll
    for (int ks = 0; ks < 16; ++ks) {
      if (ks + 2 < 16) {
#pragma unroll
        for (int s = 0; s < 4; ++s) X[(ks + 2) % 3][s] = xfrag(s, ks + 2);
      }
      SCHED_FENCE();
#pragma unroll
      for (int s = 0; s < 4; ++s) acc[s] = __builtin_amdgcn_mfma_f32_16x16x32_bf16(W1[ks], X[ks % 3][s], acc[s], 0, 0, 0);
      SCHED_FENCE();
    }
  }
  STAMP(2, 6);
  __syncthreads();
  STAMP(2, 7);
  if constexpr (ROLE < 2) {
#pragma unroll
    for (int s = 0; s < 4; ++s) {
      const f32x2 fs = *(const f32x2*)(smem + F_FS + (16 * tb[s] + c) * 8);
      const float a0 = (bf_lo(z2p[s][0]) - fs[0]) * fs[1], a1 = (bf_hi(z2p[s][0]) - fs[0]) * fs[1];
      const float a2 = (bf_lo(z2p[s][1]) - fs[0]) * fs[1], a3 = (bf_hi(z2p[s][1]) - fs[0]) * fs[1];
      *(u32x2*)(smem + F_ZS + (16 * tb[s] + c) * F_RS + (16 * w + 4 * g) * 2) = u32x2{pk_bf16(a0, a1), pk_bf16(a2, a3)};
    }
#pragma unroll
    for (int kk = 0; kk < 2; ++kk) {
      const char LAS* p = (const char LAS*)smem + F_ZS + (32 * kk + 8 * g + q4) * F_RS + (16 * w + 4 * p4) * 2;
      const s16x4 lo = tr_read(p), hi = tr_read(p + 4 * F_RS);
      const u32x2 a0 = __builtin_bit_cast(u32x2, lo), a1 = __builtin_bit_cast(u32x2, hi);
      wt_store16(make_rsrc(a.z2t, L * FH * 2), (u32)((((7 * j + w) * 256 + 8 * pi + 4 * kk) * 64 + lane * 4) * 4), u32x4{a0[0], a0[1], a1[0], a1[1]});
    }
  }
  VM_WAIT(0);
  __syncthreads();
  if (tid == 0) __hip_atomic_store((GAS u32*)a.flags + j * 32 + pi, 1u, __ATOMIC_RELAXED, __HIP_MEMORY_SCOPE_AGENT);
  STAMP(2, 8);
#pragma unroll
  for (int s = 0; s < 4; ++s)
#pragma unroll
    for (int r = 0; r < 4; ++r) z1.y[s][r] = ROLE > 0 ? ri[s] * (acc[s][r] - mu[s] * u1[r]) + v1[r] : 0.f;
  STAMP(2, 9);
}

template <int NT> constexpr int fd_glds(int sg) { return (sg == 2 || sg == 4) ? 8 : 0; }
template <int NT> constexpr int fd_bld(int sg) { return sg < 0 ? 0 : (2 * sg + F_BD < 16 ? NT : 0) + (2 * sg + 1 + F_BD < 16 ? NT : 0); }
template <int NT> constexpr int fd_ops(int sg) { return fd_glds<NT>(sg) + fd_bld<NT>(sg); }

template <int NT, int SG>
__device__ __forceinline__ void fd_stage(f32x4 (&acc)[4][NT], bf16x8 (&Bf)[F_BR][NT], bf16x8 (&Af)[2][4], const rsrc_t rb, u32 boff,
                                         const u16* const (&pa)[2], u32 lds0, const char* const (&ap)[2][2][4], int kq, int nh,
                                         const u32* fl, u32& mask, const Z1& z1, char* zdst, int w) {
  if constexpr ((SG & 1) == 0 && SG > 0) {
    if constexpr (SG == 2) vm_wait_c<F_BD * NT + fd_ops<NT>(0) + fd_ops<NT>(1)>();
    else vm_wait_c<fd_bld<NT>(SG - 2) + fd_ops<NT>(SG - 1)>();
    __builtin_amdgcn_s_barrier();
    if constexpr (fd_glds<NT>(SG) > 0) { f_lda(pa, lds0, kq, nh, SG + 2); f_lda(pa, lds0, kq, nh, SG + 3); }
#pragma unroll
    for (int m = 0; m < 4; ++m) Af[0][m] = *(const bf16x8*)(ap[(SG >> 1) & 1][0][m]);
  }
#pragma unroll
  for (int kk = 0; kk < 2; ++kk) {
    constexpr int ks0 = 2 * SG;
    if (kk == 0 || (SG & 1) == 0) {
#pragma unroll
      for (int m = 0; m < 4; ++m) Af[(kk + 1) & 1][m] = *(const bf16x8*)(ap[(SG >> 1) & 1][(kk + 1) & 1][m] + (kk == 0 ? (SG & 1) : 1) * 8192);
    }
    SCHED_FENCE();
#pragma unroll
    for (int n = 0; n < NT; ++n) {
#pragma unroll
      for (int m = 0; m < 4; ++m) acc[m][n] = __builtin_amdgcn_mfma_f32_16x16x32_bf16(Bf[(ks0 + kk) % F_BR][n], Af[kk][m], acc[m][n], 0, 0, 0);
      SCHED_FENCE();
      if (ks0 + kk + F_BD < 16) {
        if (n == 0 && ((ks0 + kk + F_BD) & 1) == 0) need_flag(fl, 8, mask, (ks0 + kk + F_BD) >> 1);
        Bf[(ks0 + kk + F_BD) % F_BR][n] = ld_dev16(rb, boff + (u32)((n * 256 + 4 * (ks0 + kk + F_BD)) * 256));
      }
      if constexpr (SG < 4) {
        if (kk == 0 && n == 1 && w > 0) {
          float z[4];
#pragma unroll
          for (int r = 0; r < 4; ++r) z[r] = gelu_tanh(z1.y[SG][r]);
          *(u32x2*)(zdst + ((SG + w) & 3) * 16 * F_RS) = u32x2{pk_bf16(z[0], z[1]), pk_bf16(z[2], z[3])};
        }
      }
      SCHED_FENCE();
    }
  }
}

template <int NT>
__device__ __forceinline__ void f_phase_d(const FArgs& a, char* smem, const int w, const int rt, const int ct, const u16* const (&pa)[2], const Z1& z1) {
  const int tid = threadIdx.x, lane = tid & 63;
  const int kq = w & 3, nh = w >> 2, n0 = nh * 4;
  const int c = lane & 15, g = lane >> 4;
  const u32 lds0 = (u32)(size_t)(const char LAS*)smem;
  const rsrc_t rb = make_rsrc(a.z2t, L * FH * 2);
  const u32 boff = (u32)((((7 * ct + n0) * 256 + 64 * kq) * 64 + lane * 4) * 4);
  f_lda(pa, lds0, kq, nh, 2);
  f_lda(pa, lds0, kq, nh, 3);
  const u32* fl = a.flags + ct * 32 + 8 * kq;
  __builtin_amdgcn_s_sleep(F_SLEEP0);
  u32 mask = poll8(fl, 8);
  bf16x8 Bf[F_BR][NT], Af[2][4];
#pragma unroll
  for (int ks = 0; ks < F_BD; ++ks) {
    if ((ks & 1) == 0) need_flag(fl, 8, mask, ks >> 1);
#pragma unroll
    for (int n = 0; n < NT; ++n) Bf[ks][n] = ld_dev16(rb, boff + (u32)((n * 256 + 4 * ks) * 256));
  }
  SCHED_FENCE();
  f32x4 acc[4][NT];
#pragma unroll
  for (int m = 0; m < 4; ++m)
#pragma unroll
    for (int n = 0; n < NT; ++n) acc[m][n] = f32x4{0, 0, 0, 0};
  const int asw = (c >> 1) & 7;
  const char* ap[2][2][4];
#pragma unroll
  for (int hi = 0; hi < 2; ++hi)
#pragma unroll
    for (int m = 0; m < 4; ++m) {
      const char* base = smem + f_slot(kq, 2 * hi) + ((m + kq) & 3) * 2048 + c * 128;
      ap[hi][0][m] = base + 16 * (g ^ asw); ap[hi][1][m] = base + 16 * ((4 + g) ^ asw);
    }
#pragma unroll
  for (int m = 0; m < 4; ++m) Af[0][m] = *(const bf16x8*)(ap[0][0][m]);
  char* zdst = smem + F_ZS + c * F_RS + (16 * (w - 1) + 4 * g) * 2;
  STAMP(3, 4);
  fd_stage<NT, 0>(acc, Bf, Af, rb, boff, pa, lds0, ap, kq, nh, fl, mask, z1, zdst, w);
  fd_stage<NT, 1>(acc, Bf, Af, rb, boff, pa, lds0, ap, kq, nh, fl, mask, z1, zdst, w);
  STAMP(3, 5);
  fd_stage<NT, 2>(acc, Bf, Af, rb, boff, pa, lds0, ap, kq, nh, fl, mask, z1, zdst, w);
  fd_stage<NT, 3>(acc, Bf, Af, rb, boff, pa, lds0, ap, kq, nh, fl, mask, z1, zdst, w);
  STAMP(3, 6);
  fd_stage<NT, 4>(acc, Bf, Af, rb, boff, pa, lds0, ap, kq, nh, fl, mask, z1, zdst, w);
  fd_stage<NT, 5>(acc, Bf, Af, rb, boff, pa, lds0, ap, kq, nh, fl, mask, z1, zdst, w);
  STAMP(3, 7);
  fd_stage<NT, 6>(acc, Bf, Af, rb, boff, pa, lds0, ap, kq, nh, fl, mask, z1, zdst, w);
  const float bs = bf1(a.bsp[64 * rt + 16 * kq + c]);
  fd_stage<NT, 7>(acc, Bf, Af, rb, boff, pa, lds0, ap, kq, nh, fl, mask, z1, zdst, w);
  STAMP(3, 1);
  __syncthreads();
#pragma unroll
  for (int m = 1; m < 4; ++m) {
    const int o = (m + kq) & 3;
    char* dst = smem + o * 10752 + ((m - 1) * 7 * 64 + n0 * 64 + lane) * 8;
#pragma unroll
    for (int n = 0; n < NT; ++n) *(u32x2*)(dst + n * 512) = u32x2{pk_bf16(acc[m][n][0], acc[m][n][1]), pk_bf16(acc[m][n][2], acc[m][n][3])};
  }
  __syncthreads();
  STAMP(3, 2);
  const char* src = smem + kq * 10752 + (n0 * 64 + lane) * 8;
  const char* zs = smem + F_ZS + (16 * kq + c) * F_RS + (16 * n0 + 4 * g) * 2;
  const rsrc_t rp = make_rsrc(a.Pt, L * FH * 2);
  u32x2 pv[NT];
#pragma unroll
  for (int n = 0; n < NT; ++n) {
    const u32x2 p0 = *(const u32x2*)(src + n * 512), p1 = *(const u32x2*)(src + (7 + n) * 512), p2 = *(const u32x2*)(src + (14 + n) * 512);
    const u32x2 zz = *(const u32x2*)(zs + n * 32);
    float gg[4];
#pragma unroll
    for (int e = 0; e < 2; ++e) {
      gg[2 * e] = ((acc[0][n][2 * e] + bf_lo(p0[e])) + (bf_lo(p1[e]) + bf_lo(p2[e]))) + bs;
      gg[2 * e + 1] = ((acc[0][n][2 * e + 1] + bf_hi(p0[e])) + (bf_hi(p1[e]) + bf_hi(p2[e]))) + bs;
    }
    pv[n] = u32x2{pk_bf16(bf_lo(zz[0]) * gg[0], bf_hi(zz[0]) * gg[1]), pk_bf16(bf_lo(zz[1]) * gg[2], bf_hi(zz[1]) * gg[3])};
    if (n & 1) wt_store16(rp, (u32)((((4 * rt + kq) * 112 + 14 * ct + 2 * (n0 + n - 1 + (g & 1)) + (g >> 1)) * 16 + c) * 16), pair16(pv[n - 1], pv[n]));
  }
  if constexpr (NT & 1) {
    const u32x4 v = pair16(pv[NT - 1], pv[NT - 1]);
    if ((g & 1) == 0) wt_store16(rp, (u32)((((4 * rt + kq) * 112 + 14 * ct + 2 * (n0 + NT - 1) + (g >> 1)) * 16 + c) * 16), v);
  }
  STAMP(3, 3);
}

__device__ __forceinline__ void f_phase_e(const FArgs& a, char* smem, const int w, const int rt, const int dt) {
  const int tid = threadIdx.x, lane = tid & 63;
  const int kq = w & 3, mh = w >> 2;
  const int c = lane & 15, g = lane >> 4;
  const u32 lds0 = (u32)(size_t)(const char LAS*)smem;
  VM_WAIT(0);
  __syncthreads();
  if (tid == 0) __hip_atomic_store((GAS u32*)a.flags + 256 + rt * 32 + dt, 1u, __ATOMIC_RELAXED, __HIP_MEMORY_SCOPE_AGENT);
  STAMP(4, 5);
#pragma unroll
  for (int k = 0; k < 7; ++k)
#pragma unroll
    for (int nn = 0; nn < 2; ++nn) {
      const int n = 2 * mh + nn;
      glds16(a.Wot + ((size_t)(4 * dt + n) * 112 + 4 * (7 * kq + k)) * 128 + lane * 8, lds0 + (n * 28 + 7 * kq + k) * 1024);
    }
  const u32x4 xr = *(const u32x4*)(a.x1T + (size_t)(64 * dt + (tid >> 3)) * L + 64 * rt + 8 * (tid & 7));
  const f32x4 gbv = mod_get4(a.mod, 2560 + 64 * dt + 16 * kq + 4 * g);
  SCHED_FENCE();
  {
    u32* p = (u32*)(smem + F_EX + (tid >> 3) * 132 + (tid & 7) * 16);
    p[0] = xr[0]; p[1] = xr[1]; p[2] = xr[2]; p[3] = xr[3];
  }
  VM_WAIT(0);
  __builtin_amdgcn_s_barrier();
  SCHED_FENCE();
  bf16x8 Wf[7][4];
  const char* wp = smem + (7 * kq) * 1024 + lane * 16;
#pragma unroll
  for (int k = 0; k < 7; ++k)
#pragma unroll
    for (int n = 0; n < 4; ++n) Wf[k][n] = *(const bf16x8*)(wp + (((n + kq) & 3) * 28 + k) * 1024);
  SCHED_FENCE();
  {
    const u32* fl = a.flags + 256 + rt * 32 + 2 * kq;
    u32 mask = poll8(fl, 2);
    need_flag(fl, 2, mask, 0); need_flag(fl, 2, mask, 1);
  }
  STAMP(4, 4);
  const rsrc_t rp = make_rsrc(a.Pt, L * FH * 2);
  bf16x8 Pf[7][2];
#pragma unroll
  for (int k = 0; k < 7; ++k)
#pragma unroll
    for (int m = 0; m < 2; ++m) Pf[k][m] = ld_dev16(rp, (u32)((((4 * rt + 2 * mh + m) * 112 + 4 * (7 * kq + k)) * 64 + lane * 4) * 4));
  SCHED_FENCE();
  f32x4 acc[2][4];
#pragma unroll
  for (int m = 0; m < 2; ++m)
#pragma unroll
    for (int n = 0; n < 4; ++n) acc[m][n] = f32x4{0, 0, 0, 0};
#pragma unroll
  for (int k = 0; k < 7; ++k)
#pragma unroll
    for (int n = 0; n < 4; ++n)
#pragma unroll
      for (int m = 0; m < 2; ++m) acc[m][n] = __builtin_amdgcn_mfma_f32_16x16x32_bf16(Wf[k][n], Pf[k][m], acc[m][n], 0, 0, 0);
  STAMP(4, 1);
  __syncthreads();
#pragma unroll
  for (int n = 1; n < 4; ++n)
#pragma unroll
    for (int m = 0; m < 2; ++m)
      *(f32x4*)(smem + (((((n + kq) & 3) * 2 + mh) * 3 + (n - 1)) * 2 + m) * 1024 + lane * 16) = acc[m][n];
  __syncthreads();
  STAMP(4, 2);
  {
    u32x2 ov[2];
#pragma unroll
    for (int m = 0; m < 2; ++m) {
      const char* p = smem + (((kq * 2 + mh) * 3) * 2 + m) * 1024 + lane * 16;
      const f32x4 p0 = *(const f32x4*)p, p1 = *(const f32x4*)(p + 2048), p2 = *(const f32x4*)(p + 4096);
      const char* xp = smem + F_EX + (16 * kq + 4 * g) * 132 + (16 * (2 * mh + m) + c) * 2;
      float o[4];
#pragma unroll
      for (int e = 0; e < 4; ++e) o[e] = bf1(*(const u16*)(xp + e * 132)) + gbv[e] * ((acc[m][0][e] + p0[e]) + (p1[e] + p2[e]));
      ov[m] = u32x2{pk_bf16(o[0], o[1]), pk_bf16(o[2], o[3])};
    }
    *(u32x4*)(a.out + (size_t)(64 * rt + 16 * (2 * mh + (g & 1)) + c) * D + 64 * dt + 16 * kq + 8 * (g >> 1)) = pair16(ov[0], ov[1]);
  }
  STAMP(4, 3);
}

__global__ __launch_bounds__(512, 2) void kF(FArgs a) {
  extern __shared__ __attribute__((aligned(16))) char smem[];
  const int wg = blockIdx.x, lane = threadIdx.x & 63;
  const int w = __builtin_amdgcn_readfirstlane(threadIdx.x >> 6);
  const int xc = wg & 7, ii = wg >> 3;
  const int pi = (xc >> 1) * 8 + (ii & 7), j = (xc & 1) * 4 + (ii >> 3);
  STAMP(2, 0);
  f_ldx(a.x1T, (u32)(size_t)(const char LAS*)smem, w, pi, 0);
  const u16* pa[2];
#pragma unroll
  for (int par = 0; par < 2; ++par) {
    const int row = 32 * (w >> 2) + 8 * par + (lane >> 3);
    pa[par] = a.wsp + (size_t)(64 * pi + row) * L + 512 * (w & 3) + 8 * ((lane & 7) ^ ((row >> 1) & 7));
  }
  Z1 z1;
  if (w == 0) f_phase_c<0>(a, smem, w, pi, j, pa, z1);
  else if (w < 7) f_phase_c<1>(a, smem, w, pi, j, pa, z1);
  else f_phase_c<2>(a, smem, w, pi, j, pa, z1);
  STAMP(3, 0);
  if (w < 4) f_phase_d<4>(a, smem, w, pi, j, pa, z1);
  else f_phase_d<3>(a, smem, w, pi, j, pa, z1);
  STAMP(4, 0);
  f_phase_e(a, smem, w, pi, j);
}
#define KMASK(b)
extern "C" void kernel_launch(void* const* d_in, const int* in_sizes, int n_in, void* d_out, int out_size,
                              void* d_ws, size_t ws_size, hipStream_t stream) {
  const u16* x = (const u16*)d_in[0];
  char* ws = (char*)d_ws;
  float* mod = (float*)(ws + 524288);
  u32* stats1 = (u32*)(ws + 16384);
  float* uv = (float*)(ws + 32768);
  u64* gran = (u64*)(ws + 65536);
  u32* flags = (u32*)(ws + 65536 + 262144);
  u16* xT = (u16*)(ws + (1 << 20));
  u16* Krev = (u16*)(ws + (3 << 20));
  u16* x1T = xT;
  u16* Wt = (u16*)(ws + (7 << 20));
  u16* Wot = (u16*)(ws + (9 << 20));
  u16* Pt = (u16*)(ws + (10 << 20));
  u16* z2t = Krev;
  AArgs aa;
  aa.x = x; aa.Are = (const u16*)d_in[1]; aa.Aim = (const u16*)d_in[2]; aa.Bre = (const u16*)d_in[3]; aa.Bim = (const u16*)d_in[4];
  aa.Cre = (const u16*)d_in[5]; aa.Cim = (const u16*)d_in[6]; aa.logdt = (const u16*)d_in[7]; aa.c = (const u16*)d_in[14];
  aa.wmod = (const u16*)d_in[15]; aa.win = (const u16*)d_in[10]; aa.wout = (const u16*)d_in[13];
  aa.mod = mod; aa.stats1 = stats1; aa.xT = xT; aa.Krev = Krev; aa.gran = (u32x4*)gran;
  (void)hipFuncSetAttribute((const void*)kA, hipFuncAttributeMaxDynamicSharedMemorySize, A_LDS);
  KMASK(1) kA<<<256, 768, A_LDS, stream>>>(aa);
  BArgs ba;
  ba.xT = xT; ba.Krev = Krev; ba.Dskip = (const u16*)d_in[8]; ba.win = (const u16*)d_in[10]; ba.wout = (const u16*)d_in[13];
  ba.wsp = (const u16*)d_in[11]; ba.stats1 = stats1; ba.mod = mod; ba.x1T = x1T; ba.Wt = Wt; ba.Wot = Wot; ba.uv = uv;
  (void)hipFuncSetAttribute((const void*)kB, hipFuncAttributeMaxDynamicSharedMemorySize, B_LDS);
  KMASK(2) kB<<<256, 768, B_LDS, stream>>>(ba);
  FArgs fa;
  fa.x1T = x1T; fa.Wt = Wt; fa.wsp = (const u16*)d_in[11]; fa.bsp = (const u16*)d_in[12]; fa.Wot = Wot; fa.uv = uv; fa.mod = mod;
  fa.z2t = z2t; fa.Pt = Pt; fa.out = (u16*)d_out; fa.gran = gran; fa.flags = flags;
  (void)hipFuncSetAttribute((const void*)kF, hipFuncAttributeMaxDynamicSharedMemorySize, F_LDS);
  KMASK(4) kF<<<256, 512, F_LDS, stream>>>(fa);
}
```

```cpp
#include <hip/hip_runtime.h>
#include <hip/hip_bf16.h>
#include <stdint.h>

typedef unsigned short u16;
typedef unsigned int u32;
typedef unsigned long long u64;
typedef __bf16 bf16x8 __attribute__((ext_vector_type(8)));
typedef __bf16 bf16x2 __attribute__((ext_vector_type(2)));
typedef short s16x4 __attribute__((ext_vector_type(4)));
typedef float f32x2 __attribute__((ext_vector_type(2)));
typedef float f32x4 __attribute__((ext_vector_type(4)));
typedef u32 u32x2 __attribute__((ext_vector_type(2)));
typedef u32 u32x4 __attribute__((ext_vector_type(4)));
#define LAS __attribute__((address_space(3)))
#define GAS __attribute__((address_space(1)))

constexpr int L = 2048, D = 512, NS = 128, F = 1792, FH = 896;

__device__ __forceinline__ float bf_lo(u32 w) { return __uint_as_float(w << 16); }
__device__ __forceinline__ float bf_hi(u32 w) { return __uint_as_float(w & 0xffff0000u); }
__device__ __forceinline__ float bf1(u16 h) { return __uint_as_float(((u32)h) << 16); }
__device__ __forceinline__ u32 pk_bf16(float lo, float hi) {
  f32x2 a = {lo, hi};
  bf16x2 p = __builtin_convertvector(a, bf16x2);
  return __builtin_bit_cast(u32, p);
}
__device__ __forceinline__ float rbf(float x) { return bf_lo(pk_bf16(x, x)); }

template <int CTRL, int ROWMASK = 0xf>
__device__ __forceinline__ float dpp_add(float x) {
  int y = __builtin_amdgcn_update_dpp(0, __float_as_int(x), CTRL, ROWMASK, 0xf, true);
  return x + __int_as_float(y);
}
__device__ __forceinline__ float row16_sum(float x) {
  x = dpp_add<0xB1>(x);
  x = dpp_add<0x4E>(x);
  x = dpp_add<0x141>(x);
  x = dpp_add<0x140>(x);
  return x;
}
__device__ __forceinline__ float wave_sum(float x) {
  x = row16_sum(x);
  float a = __int_as_float(__builtin_amdgcn_readlane(__float_as_int(x), 0));
  float b = __int_as_float(__builtin_amdgcn_readlane(__float_as_int(x), 16));
  float c = __int_as_float(__builtin_amdgcn_readlane(__float_as_int(x), 32));
  float d = __int_as_float(__builtin_amdgcn_readlane(__float_as_int(x), 48));
  return (a + b) + (c + d);
}
__device__ __forceinline__ s16x4 tr_read(const char LAS* p) {
  return __builtin_amdgcn_ds_read_tr16_b64_v4i16((s16x4 LAS*)p);
}

#define STAMP(kern, i)
__device__ __forceinline__ void glds16(const void* gsrc, u32 lds_dst) {
  u32 keep;
  asm volatile("s_mov_b32 %0, m0\n\ts_mov_b32 m0, %2\n\ts_nop 0\n\tglobal_load_lds_dwordx4 %1, off\n\ts_mov_b32 m0, %0"
               : "=&s"(keep) : "v"(gsrc), "s"(lds_dst) : "memory");
}
#define SCHED_FENCE() __builtin_amdgcn_sched_barrier(0)
#define VM_WAIT(n) asm volatile("s_waitcnt vmcnt(" #n ")" ::: "memory")
__device__ __forceinline__ float xor16_sum(float x) {
  const u32 v = __float_as_uint(x);
  auto r = __builtin_amdgcn_permlane16_swap(v, v, false, false);
  return __uint_as_float(r[0]) + __uint_as_float(r[1]);
}
__device__ __forceinline__ float xor32_sum(float x) {
  const u32 v = __float_as_uint(x);
  auto r = __builtin_amdgcn_permlane32_swap(v, v, false, false);
  return __uint_as_float(r[0]) + __uint_as_float(r[1]);
}
__device__ __forceinline__ float xor4_8_sum(float x) {
  x = x + __int_as_float(__builtin_amdgcn_update_dpp(0, __float_as_int(x), 0x124, 0xf, 0xf, true));
  x = x + __int_as_float(__builtin_amdgcn_update_dpp(0, __float_as_int(x), 0x128, 0xf, 0xf, true));
  return x;
}
__device__ __forceinline__ void lds_rendezvous(u32 LAS* cnt, u32 target) {
  asm volatile("s_waitcnt lgkmcnt(0)" ::: "memory");
  if ((threadIdx.x & 63) == 0) __hip_atomic_fetch_add(cnt, 1u, __ATOMIC_RELAXED, __HIP_MEMORY_SCOPE_WORKGROUP);
  while (__hip_atomic_load(cnt, __ATOMIC_RELAXED, __HIP_MEMORY_SCOPE_WORKGROUP) < target) __builtin_amdgcn_s_sleep(1);
  asm volatile("" ::: "memory");
}

typedef __amdgpu_buffer_rsrc_t rsrc_t;
__device__ __forceinline__ rsrc_t make_rsrc(const void* base, u32 bytes) {
  return __builtin_amdgcn_make_buffer_rsrc(const_cast<void*>(base), 0, bytes, 0x00020000);
}
__device__ __forceinline__ void wt_store16(rsrc_t r, u32 byte_off, u32x4 v) { __builtin_amdgcn_raw_buffer_store_b128(v, r, byte_off, 0, 16); }
__device__ __forceinline__ u32x4 pair16(u32x2 a, u32x2 b) {
  const auto r0 = __builtin_amdgcn_permlane16_swap(a[0], b[0], false, false);
  const auto r1 = __builtin_amdgcn_permlane16_swap(a[1], b[1], false, false);
  return u32x4{r0[0], r1[0], r0[1], r1[1]};
}

__device__ __forceinline__ float mod_get(const float* mp, int i) { return rbf((mp[i] + mp[3072 + i]) + (mp[6144 + i] + mp[9216 + i])); }
__device__ __forceinline__ f32x4 mod_get4(const float* mp, int i) {
  const f32x4 a = *(const f32x4*)(mp + i), b = *(const f32x4*)(mp + 3072 + i), c = *(const f32x4*)(mp + 6144 + i), d = *(const f32x4*)(mp + 9216 + i);
  f32x4 r;
#pragma unroll
  for (int e = 0; e < 4; ++e) r[e] = rbf((a[e] + b[e]) + (c[e] + d[e]));
  return r;
}
__device__ __forceinline__ f32x4 uv_get4(const float* uv, int which, int f) {
  const float* p = uv + which * F + f;
  const f32x4 a = *(const f32x4*)p, b = *(const f32x4*)(p + 2 * F), c = *(const f32x4*)(p + 4 * F), d = *(const f32x4*)(p + 6 * F);
  return (a + b) + (c + d);
}
constexpr int CODE_LANES = 84;
struct AArgs {
  const u16 *x, *Are, *Aim, *Bre, *Bim, *Cre, *Cim, *logdt, *c, *wmod, *win, *wout;
  float* mod; u32* stats1; u16* xT; u16* Krev; u32x4* gran;
};
constexpr int A_RS = 528;
constexpr int A_TAB = 0;
constexpr int A_KC = A_TAB + 2 * 64 * A_RS;
constexpr int A_TILE = A_KC + 2 * 768 * 4;
constexpr int A_RED = A_TILE + 8 * 1088;
constexpr int A_CNT = A_RED + 4 * 64 * 4;
constexpr int A_LDS = A_CNT + 16;

struct KIdx { u16 v[2048]; };
constexpr int bf16_round_int(int l) {
  int sh = 0;
  while ((l >> sh) >= 256) ++sh;
  if (sh == 0) return l;
  const int q = l >> sh, rem = l & ((1 << sh) - 1), half = 1 << (sh - 1);
  const int up = (rem > half || (rem == half && (q & 1))) ? 1 : 0;
  return (q + up) << sh;
}
constexpr KIdx make_kidx() {
  KIdx t{};
  for (int x = 0; x < 2048; ++x) {
    const int lr = bf16_round_int(2047 - x);
    const int aa = lr >> 7, b = (lr >> 3) & 15, r = lr & 7;
    const int row = lr < 256 ? 8 * aa + r : lr < 512 ? 16 + 4 * (aa - 2) + (r >> 1) : lr < 1024 ? 24 + 2 * (aa - 4) + (r >> 2) : 32 + (aa - 8);
    t.v[x] = (u16)(row * 16 + b);
  }
  return t;
}
__device__ const KIdx g_kidx = make_kidx();

struct cpx { float r, i; };
__device__ __forceinline__ cpx cmul(cpx a, cpx b) { return cpx{a.r * b.r - a.i * b.i, a.r * b.i + a.i * b.r}; }

__global__ __launch_bounds__(768) void kA(AArgs a) {
  extern __shared__ __attribute__((aligned(16))) char smem[];
  const int wg = blockIdx.x, tid = threadIdx.x, lane = tid & 63;
  const int w = __builtin_amdgcn_readfirstlane(tid >> 6);
  STAMP(0, 0);
  const int tw = w - 8, st = tid - 512;
  const int t0 = 8 * ((wg & 7) * 32 + (wg >> 3));
  const bool doM = wg < 192;
  const int mrb = wg / 48, mcb = wg - 48 * mrb;
  const int rr = lane >> 3, q = lane & 7;
  u32x4 ld[14];
  u16 sv[8];
  if (w < 8) {
    const int d = 2 * wg + (w >> 2), o = d * NS + 64 * ((w >> 1) & 1) + lane;
    sv[0] = a.Are[o]; sv[1] = a.Aim[o]; sv[2] = a.Bre[o]; sv[3] = a.Bim[o]; sv[4] = a.Cre[o]; sv[5] = a.Cim[o]; sv[6] = a.logdt[d]; sv[7] = 0;
    ld[0] = *(const u32x4*)(g_kidx.v + 8 * (tid & 255));
  } else {
#pragma unroll
    for (int j = 0; j < 2; ++j) ld[j] = *(const u32x4*)(a.x + (size_t)(t0 + 2 * tw + j) * D + 8 * lane);
    if (tw == 0 && lane < 4) *(u32*)(smem + A_CNT + 4 * lane) = 0;
    asm volatile("s_waitcnt lgkmcnt(0)" ::: "memory");
  }
  SCHED_FENCE();
  STAMP(0, 9);
  __builtin_amdgcn_s_barrier();
  STAMP(0, 10);
  SCHED_FENCE();
  if (w < 8) {
    const int ch = w >> 2, nh = (w >> 1) & 1, part = (w ^ ch) & 1, d = 2 * wg + ch;
    const int n = 64 * nh + lane;
    const u16 hare = sv[0], haim = sv[1], hbre = sv[2], hbim = sv[3], hcre = sv[4], hcim = sv[5], hdt = sv[6];
    const u32x4 kix = ld[0];
    asm volatile("" ::"v"((u32)hare), "v"((u32)hcim), "v"((u32)hdt));
    STAMP(0, 11);
    const float dt = rbf(__expf(bf1(hdt)));
    const float lr = (dt * bf1(hare)) * 1.4426950408889634f, lp = (dt * bf1(haim)) * 0.15915494309189535f;
    auto cexp = [&](float l) -> cpx {
      const float e = __builtin_amdgcn_exp2f(lr * l), ph = __builtin_amdgcn_fractf(lp * l);
      return cpx{e * __builtin_amdgcn_cosf(ph), e * __builtin_amdgcn_sinf(ph)};
    };
    char* tab = smem + A_TAB + ch * (64 * A_RS) + n * 4;
    auto put = [&](int row, cpx v) { *(u32*)(tab + row * A_RS) = pk_bf16(v.r, v.i); };
    if (part == 0) {
      const cpx E1 = cexp(1.f), E128 = cexp(128.f), E512 = cexp(512.f);
      const cpx E2 = cmul(E1, E1), E3 = cmul(E2, E1), E4 = cmul(E2, E2), E5 = cmul(E4, E1), E6 = cmul(E4, E2), E7 = cmul(E4, E3);
      put(0, cpx{1.f, 0.f}); put(1, E1); put(2, E2); put(3, E3); put(4, E4); put(5, E5); put(6, E6); put(7, E7);
      put(8, E128); put(9, cmul(E128, E1)); put(10, cmul(E128, E2)); put(11, cmul(E128, E3));
      put(12, cmul(E128, E4)); put(13, cmul(E128, E5)); put(14, cmul(E128, E6)); put(15, cmul(E128, E7));
      const cpx E256 = cmul(E128, E128), E384 = cmul(E256, E128);
      put(16, E256); put(17, cmul(E256, E2)); put(18, cmul(E256, E4)); put(19, cmul(E256, E6));
      put(20, E384); put(21, cmul(E384, E2)); put(22, cmul(E384, E4)); put(23, cmul(E384, E6));
      const cpx E640 = cmul(E512, E128), E768 = cmul(E512, E256), E896 = cmul(E512, E384);
      put(24, E512); put(25, cmul(E512, E4)); put(26, E640); put(27, cmul(E640, E4));
      put(28, E768); put(29, cmul(E768, E4)); put(30, E896); put(31, cmul(E896, E4));
    } else {
      const cpx E8 = cexp(8.f), E128 = cexp(128.f), E1024 = cexp(1024.f);
      const float br = dt * bf1(hbre), bi = dt * bf1(hbim), cr = bf1(hcre), ci = bf1(hcim);
      const cpx W = cmul(cpx{cr, ci}, cpx{br, bi});
      const cpx E16 = cmul(E8, E8), E32 = cmul(E16, E16), E64 = cmul(E32, E32);
      cpx Y[8];
      Y[0] = W; Y[1] = cmul(W, E8); Y[2] = cmul(W, E16); Y[3] = cmul(Y[1], E16);
#pragma unroll
      for (int b = 0; b < 4; ++b) Y[4 + b] = cmul(Y[b], E32);
#pragma unroll
      for (int b = 0; b < 8; ++b) {
        put(48 + b, cpx{Y[b].r, -Y[b].i});
        const cpx t = cmul(Y[b], E64);
        put(56 + b, cpx{t.r, -t.i});
      }
      const cpx E256 = cmul(E128, E128), E512 = cmul(E256, E256);
      cpx P[4];
      P[0] = E1024; P[1] = cmul(E1024, E128); P[2] = cmul(E1024, E256); P[3] = cmul(P[1], E256);
#pragma unroll
      for (int b = 0; b < 4; ++b) { put(32 + b, P[b]); put(36 + b, cmul(P[b], E512)); }
      put(40, cmul(E1024, E1024));
    }
    STAMP(0, 1);
    lds_rendezvous((u32 LAS*)(smem + A_CNT), 8);
    STAMP(0, 2);
    if (w < 6) {
      const int c2 = w >= 3 ? 1 : 0, t = w - 3 * c2, i = lane & 15, g = lane >> 4;
      const char* tb = smem + A_TAB + c2 * (64 * A_RS) + 16 * g;
      const char* pa = tb + (16 * t + i) * A_RS, *pb = tb + (48 + i) * A_RS;
      f32x4 acc = {0, 0, 0, 0};
      bf16x8 Af[8], Bf[8];
#pragma unroll
      for (int s = 0; s < 8; ++s) {
        Af[s] = *(const bf16x8*)(pa + s * 64); Bf[s] = *(const bf16x8*)(pb + s * 64);
      }
#pragma unroll
      for (int s = 0; s < 8; ++s) acc = __builtin_amdgcn_mfma_f32_16x16x32_bf16(Af[s], Bf[s], acc, 0, 0, 0);
      float* Kc = (float*)(smem + A_KC) + c2 * 768 + t * 256;
#pragma unroll
      for (int r = 0; r < 4; ++r) Kc[(4 * g + r) * 16 + i] = acc[r];
    }
    STAMP(0, 3);
    lds_rendezvous((u32 LAS*)(smem + A_CNT + 4), 8);
    STAMP(0, 4);
    const float* Kc = (const float*)(smem + A_KC) + ch * 768;
    float kv[8];
#pragma unroll
    for (int j = 0; j < 4; ++j) { kv[2 * j] = Kc[kix[j] & 0xffffu]; kv[2 * j + 1] = Kc[kix[j] >> 16]; }
    wt_store16(make_rsrc(a.Krev, D * L * 2), (u32)(d * L + 8 * (tid & 255)) * 2,
               u32x4{pk_bf16(kv[0], kv[1]), pk_bf16(kv[2], kv[3]), pk_bf16(kv[4], kv[5]), pk_bf16(kv[6], kv[7])});
    STAMP(0, 5);
  } else {
    __builtin_amdgcn_s_setprio(3);
    const u32x4 xr[2] = {ld[0], ld[1]};
    asm volatile("" ::"v"(xr[1]));
    STAMP(0, 11);
    char* tile = smem + A_TILE;
#pragma unroll
    for (int j = 0; j < 2; ++j) *(u32x4*)(tile + (2 * tw + j) * 1088 + 16 * lane) = xr[j];
    SCHED_FENCE();
    if (doM) {
#pragma unroll
      for (int p = 0; p < 4; ++p) ld[2 + p] = *(const u32x4*)(a.wmod + (size_t)(128 * mrb + p * 32 + tw * 8 + rr) * 3072 + 64 * mcb + 8 * q);
#pragma unroll
      for (int p = 0; p < 4; ++p) sv[p] = a.c[128 * mrb + p * 32 + tw * 8 + rr];
    }
    ld[10] = *(const u32x4*)(a.win + (size_t)wg * 3584 + st * 8);
    ld[11] = *(const u32x4*)(a.win + (size_t)wg * 3584 + (st < 192 ? 2048 + st * 8 : 0));
    ld[12] = *(const u32x4*)(a.wout + (size_t)wg * 1792 + (st < 224 ? st * 8 : 0));
    u64 base;
    asm volatile("s_getpc_b64 s[40:41]\n\ts_add_u32 s40, s40, _Z2kB5BArgs@rel32@lo+4\n\ts_addc_u32 s41, s41, _Z2kB5BArgs@rel32@hi+12\n\ts_mov_b64 %0, s[40:41]"
                 : "=s"(base) : : "s40", "s41", "scc");
    ld[13] = *(const u32x4*)((const char*)base + (size_t)(st < CODE_LANES ? (wg >> 3) * CODE_LANES + st : 0) * 16);
    SCHED_FENCE();
    STAMP(0, 1);
    lds_rendezvous((u32 LAS*)(smem + A_CNT + 8), 4);
    STAMP(0, 2);
#pragma unroll
    for (int ps = 0; ps < 2; ++ps) {
      const int d0 = 128 * tw + 64 * ps + 16 * (lane >> 4);
      const char LAS* src = (const char LAS*)tile + ((lane & 15) >> 2) * 1088 + 2 * (d0 + 4 * (lane & 3));
      const s16x4 lo = tr_read(src), hi = tr_read(src + 4 * 1088);
      u32x2 a0 = __builtin_bit_cast(u32x2, lo), a1 = __builtin_bit_cast(u32x2, hi);
      *(u32x4*)(a.xT + (size_t)(d0 + (lane & 15)) * L + t0) = u32x4{a0[0], a0[1], a1[0], a1[1]};
    }
    STAMP(0, 6);
    if (tw == 0) a.gran[wg * 64 + lane] = u32x4{0, 0, 0, 0};
    if (tw == 1 && wg < 8) a.gran[(256 + wg) * 64 + lane] = u32x4{0, 0, 0, 0};
    {
      float v[2][8], s[2];
#pragma unroll
      for (int j = 0; j < 2; ++j) {
#pragma unroll
        for (int k = 0; k < 4; ++k) { v[j][2 * k] = bf_lo(xr[j][k]); v[j][2 * k + 1] = bf_hi(xr[j][k]); }
        s[j] = ((v[j][0] + v[j][1]) + (v[j][2] + v[j][3])) + ((v[j][4] + v[j][5]) + (v[j][6] + v[j][7]));
      }
      float mean[2], q2[2];
#pragma unroll
      for (int j = 0; j < 2; ++j) mean[j] = wave_sum(s[j]) * (1.f / D);
#pragma unroll
      for (int j = 0; j < 2; ++j) {
        q2[j] = 0;
#pragma unroll
        for (int k = 0; k < 8; ++k) { const float dv = v[j][k] - mean[j]; q2[j] += dv * dv; }
      }
#pragma unroll
      for (int j = 0; j < 2; ++j) {
        const float var = wave_sum(q2[j]) * (1.f / D);
        const float sd = rbf(__builtin_amdgcn_sqrtf(rbf(rbf(var) + 1e-5f)));
        if (lane == 0) *(f32x2*)(a.stats1 + 2 * (t0 + 2 * tw + j)) = f32x2{rbf(mean[j]), __builtin_amdgcn_rcpf(sd)};
      }
    }
    STAMP(0, 7);
    float* red = (float*)(smem + A_RED);
    if (doM) {
      asm volatile("" ::"v"(ld[5]));
      STAMP(0, 8);
      float acc[8] = {0, 0, 0, 0, 0, 0, 0, 0};
#pragma unroll
      for (int p = 0; p < 4; ++p) {
        const float cx = bf1(sv[p]);
        const float s = rbf(cx * __builtin_amdgcn_rcpf(1.f + __expf(-cx)));
#pragma unroll
        for (int k = 0; k < 4; ++k) { acc[2 * k] += s * bf_lo(ld[2 + p][k]); acc[2 * k + 1] += s * bf_hi(ld[2 + p][k]); }
      }
#pragma unroll
      for (int k = 0; k < 8; ++k) {
        float v = acc[k];
        v += __int_as_float(__builtin_amdgcn_update_dpp(0, __float_as_int(v), 0x128, 0xf, 0xf, true));
        v = xor32_sum(xor16_sum(v));
        if (rr == 0) red[tw * 64 + 8 * q + k] = v;
      }
      STAMP(0, 3);
      lds_rendezvous((u32 LAS*)(smem + A_CNT + 12), 4);
      STAMP(0, 4);
      if (tw == 0) a.mod[3072 * mrb + 64 * mcb + lane] = (red[lane] + red[64 + lane]) + (red[128 + lane] + red[192 + lane]);
    }
#pragma unroll
    for (int k = 10; k < 14; ++k) asm volatile("" ::"v"(ld[k]));
    STAMP(0, 5);
  }
}
constexpr int F_CODE_LANES = 72;
struct BArgs {
  const u16 *xT, *Krev, *Dskip, *win, *wout, *wsp;
  const u32* stats1; const float* mod;
  u16 *x1T, *Wt, *Wot; float* uv;
};
constexpr int B_H = 0;
constexpr int B_HS = (256 + 2048) * 2;
constexpr int B_KC = B_H + 2 * B_HS;
constexpr int B_KS = 4160;
constexpr int B_PART = B_KC + 2 * 4 * B_KS;
constexpr int B_RED = B_PART + 2 * 4 * 8 * 1280;
constexpr int B_STG = B_RED + 4 * 64 * 4;
constexpr int B_CNT = B_STG + 4 * 2048;
constexpr int B_LDS = B_CNT + 32;

__global__ __launch_bounds__(768) void kB(BArgs a) {
  extern __shared__ __attribute__((aligned(16))) char smem[];
  const int wg = blockIdx.x, tid = threadIdx.x, lane = tid & 63;
  const int w = __builtin_amdgcn_readfirstlane(tid >> 6);
  STAMP(1, 0);
  if (w < 8) {
    __builtin_amdgcn_s_setprio(2);
    const int ch = w >> 2, q = w & 3, d = 2 * wg + ch, tj = tid & 255;
    const u32x4 xv = *(const u32x4*)(a.xT + (size_t)d * L + 8 * tj);
    f32x4 st[4];
#pragma unroll
    for (int k = 0; k < 4; ++k) st[k] = *(const f32x4*)(a.stats1 + 16 * tj + 4 * k);
    const u32x4 k0 = *(const u32x4*)(a.Krev + (size_t)d * L + 8 * tj);
    const u32x4 k1r = *(const u32x4*)(a.Krev + (size_t)d * L + 8 * (tj < 255 ? tj + 1 : tj));
    const float sa = mod_get(a.mod, d), Ac = rbf(1.f + mod_get(a.mod, 512 + d)), ga = mod_get(a.mod, 1024 + d), dsk = bf1(a.Dskip[d]);
    SCHED_FENCE();
    __builtin_amdgcn_s_barrier();
    SCHED_FENCE();
    char* hb = smem + B_H + ch * B_HS;
    char* kc = smem + B_KC + ch * 4 * B_KS;
    if (tj < 32) *(u32x4*)(hb + 16 * tj) = u32x4{0, 0, 0, 0};
    if (tj < 16) *(u32x4*)(kc + (tj >> 2) * B_KS + 16 * (256 + (tj & 3))) = u32x4{0, 0, 0, 0};
    {
      const u32 z = tj < 255 ? 0xffffffffu : 0u;
      const u32 wd[8] = {k0[0], k0[1], k0[2], k0[3], k1r[0] & z, k1r[1] & z, 0, 0};
      u32 od[5];
#pragma unroll
      for (int k = 0; k < 5; ++k) od[k] = __builtin_amdgcn_alignbit(wd[k + 1], wd[k], 16);
      *(u32x4*)(kc + 0 * B_KS + 16 * tj) = u32x4{wd[0], wd[1], wd[2], wd[3]};
      *(u32x4*)(kc + 1 * B_KS + 16 * tj) = u32x4{od[0], od[1], od[2], od[3]};
      *(u32x4*)(kc + 2 * B_KS + 16 * tj) = u32x4{wd[1], wd[2], wd[3], wd[4]};
      *(u32x4*)(kc + 3 * B_KS + 16 * tj) = u32x4{od[1], od[2], od[3], od[4]};
    }
    u32x4 hv;
    {
      float hh[8];
#pragma unroll
      for (int k = 0; k < 8; ++k) {
        const float m = st[k >> 1][2 * (k & 1)], ri = st[k >> 1][2 * (k & 1) + 1];
        const float xx = (k & 1) ? bf_hi(xv[k >> 1]) : bf_lo(xv[k >> 1]);
        hh[k] = (xx - m) * (ri * Ac) + sa;
      }
      hv = u32x4{pk_bf16(hh[0], hh[1]), pk_bf16(hh[2], hh[3]), pk_bf16(hh[4], hh[5]), pk_bf16(hh[6], hh[7])};
    }
    *(u32x4*)(hb + 512 + 16 * tj) = hv;
    STAMP(1, 1);
    lds_rendezvous((u32 LAS*)(smem + B_CNT + 12 + 4 * 0) + ch * 2, 4);
    STAMP(1, 2);
    const int c = lane & 15, g = lane >> 4;
    const char* bbase = hb + 2 * (256 + 16 * c - 16 * (g >> 1) + 8 * (g & 1) - 32 * q) - 256;
    const int X = 2047 - 32 * q - 16 * (g >> 1) + 8 * (g & 1) - c;
    const char* abase = kc + (X & 3) * B_KS + 2 * (X & ~3) - 256 * 15;
    const char LAS* ab0 = (const char LAS*)abase;
    bf16x8 Bf[16], Af[16];
    auto lda = [&](int u) {
      const u32x2 lo = *(const volatile u32x2 LAS*)(ab0 + 256 * (15 - u)), hi = *(const volatile u32x2 LAS*)(ab0 + 256 * (15 - u) + 8);
      Af[u] = __builtin_bit_cast(bf16x8, u32x4{lo[0], lo[1], hi[0], hi[1]});
    };
    lda(0);
#pragma unroll
    for (int v = 1; v < 16; v += 2) Bf[v] = *(const bf16x8*)(bbase + 256 * v);
    lda(1);
#pragma unroll
    for (int v = 0; v < 16; v += 2) Bf[v] = *(const bf16x8*)(bbase + 256 * v);
    lda(2); lda(3);
    SCHED_FENCE();
    f32x4 acc[8];
#pragma unroll
    for (int i = 0; i < 8; ++i) acc[i] = f32x4{0, 0, 0, 0};
#pragma unroll
    for (int u = 0; u < 16; ++u) {
      if (u + 4 < 16) lda(u + 4);
      SCHED_FENCE();
#pragma unroll
      for (int i = u >> 1; i < 8; ++i) acc[i] = __builtin_amdgcn_mfma_f32_16x16x32_bf16(Af[u], Bf[2 * i - u + 1], acc[i], 0, 0, 0);
      SCHED_FENCE();
    }
    STAMP(1, 3);
    char* part = smem + B_PART + ch * (4 * 8 * 1280);
#pragma unroll
    for (int i = 0; i < 8; ++i) *(f32x4*)(part + (q * 8 + i) * 1280 + 16 * (20 * g + c)) = acc[i];
    lds_rendezvous((u32 LAS*)(smem + B_CNT + 4 * ch), 4);
    STAMP(1, 4);
    {
      const int i = tj >> 5, j5 = tj & 31, cc = j5 >> 1;
      float y[8];
#pragma unroll
      for (int hh = 0; hh < 2; ++hh) {
        const char* p = part + i * 1280 + 16 * (20 * (2 * (j5 & 1) + hh) + cc);
        const f32x4 p0 = *(const f32x4*)(p), p1 = *(const f32x4*)(p + 8 * 1280), p2 = *(const f32x4*)(p + 16 * 1280), p3 = *(const f32x4*)(p + 24 * 1280);
#pragma unroll
        for (int r = 0; r < 4; ++r) y[4 * hh + r] = (p0[r] + p1[r]) + (p2[r] + p3[r]);
      }
      float o[8];
#pragma unroll
      for (int k = 0; k < 8; ++k) {
        const float hx = (k & 1) ? bf_hi(hv[k >> 1]) : bf_lo(hv[k >> 1]);
        const float xx = (k & 1) ? bf_hi(xv[k >> 1]) : bf_lo(xv[k >> 1]);
        o[k] = xx + ga * (y[k] + hx * dsk);
      }
      wt_store16(make_rsrc(a.x1T, D * L * 2), (u32)(d * L + 8 * tj) * 2, u32x4{pk_bf16(o[0], o[1]), pk_bf16(o[2], o[3]), pk_bf16(o[4], o[5]), pk_bf16(o[6], o[7])});
    }
    STAMP(1, 5);
  } else {
#define B_SIDE_PRIO 3
    __builtin_amdgcn_s_setprio(B_SIDE_PRIO);
    const int st = tid - 512, sw = w - 8;
    if (st < 8) *(u32*)(smem + B_CNT + 4 * st) = 0;
    asm volatile("s_waitcnt lgkmcnt(0)" ::: "memory");
    __builtin_amdgcn_s_barrier();
    SCHED_FENCE();
    float* red = (float*)(smem + B_RED);
    const bool act = wg < 224;
    const int dq = wg / 56, f0 = 32 * (wg - 56 * dq);
    const int d8 = 16 * dq + (st >> 4), fp = st & 15;
    const int f8 = wg >> 1, dp = (wg & 1) * 128 + (st & 127);
    u32 wv[8], ov[8];
    f32x4 cb0, cb1, sb0, sb1;
    if (act) {
#pragma unroll
      for (int j = 0; j < 8; ++j) wv[j] = *(const u32*)(a.win + (size_t)(8 * d8 + j) * F + f0 + 2 * fp);
      cb0 = mod_get4(a.mod, 2048 + 8 * d8); cb1 = mod_get4(a.mod, 2048 + 8 * d8 + 4);
      sb0 = mod_get4(a.mod, 1536 + 8 * d8); sb1 = mod_get4(a.mod, 1536 + 8 * d8 + 4);
      if (st < 128) {
#pragma unroll
        for (int j = 0; j < 8; ++j) ov[j] = *(const u32*)(a.wout + (size_t)(8 * f8 + j) * D + 2 * dp);
      }
    }
    u32x4 pf[4];
    {
#pragma unroll
      for (int j = 0; j < 4; ++j) {
        const int idx = st + 256 * j;
        pf[j] = *(const u32x4*)(a.wsp + (size_t)(8 * wg + (idx >> 7)) * L + ((idx >> 5) & 3) * 512 + 256 + (idx & 31) * 8);
      }
    }
    SCHED_FENCE();
    if (act) {
      float lo[8], hi[8], u0 = 0, u1 = 0, v0 = 0, v1 = 0;
#pragma unroll
      for (int j = 0; j < 8; ++j) {
        const float Ac = rbf(1.f + (j < 4 ? cb0[j] : cb1[j - 4])), sb = j < 4 ? sb0[j] : sb1[j - 4];
        const float wl = bf_lo(wv[j]), wh = bf_hi(wv[j]);
        lo[j] = rbf(Ac * wl); hi[j] = rbf(Ac * wh);
        u0 += lo[j]; u1 += hi[j]; v0 += sb * wl; v1 += sb * wh;
      }
      u32x4* stg = (u32x4*)(smem + B_STG + sw * 2048);
      stg[2 * lane] = u32x4{pk_bf16(lo[0], lo[1]), pk_bf16(lo[2], lo[3]), pk_bf16(lo[4], lo[5]), pk_bf16(lo[6], lo[7])};
      stg[2 * lane + 1] = u32x4{pk_bf16(hi[0], hi[1]), pk_bf16(hi[2], hi[3]), pk_bf16(hi[4], hi[5]), pk_bf16(hi[6], hi[7])};
      const rsrc_t rw = make_rsrc(a.Wt, D * F * 2);
#pragma unroll
      for (int k = 0; k < 2; ++k) {
        const int pc = 64 * k + lane, dd8 = 16 * dq + 4 * sw + (pc >> 5), col = f0 + (pc & 31);
        wt_store16(rw, (u32)((((col >> 4) * 64 + dd8) * 16 + (col & 15)) * 16), stg[pc]);
      }
      float rv[4] = {u0, u1, v0, v1};
#pragma unroll
      for (int k = 0; k < 4; ++k) {
        const float v = xor32_sum(xor16_sum(rv[k]));
        if (lane < 16) red[sw * 64 + lane * 4 + k] = v;
      }
      if (st < 128) {
        u32x4 e, o;
#pragma unroll
        for (int k = 0; k < 4; ++k) {
          e[k] = (ov[2 * k] & 0xffffu) | (ov[2 * k + 1] << 16);
          o[k] = (ov[2 * k] >> 16) | (ov[2 * k + 1] & 0xffff0000u);
        }
        stg[2 * lane] = e; stg[2 * lane + 1] = o;
        const rsrc_t ro = make_rsrc(a.Wot, FH * D * 2);
#pragma unroll
        for (int k = 0; k < 2; ++k) {
          const int col = (wg & 1) * 256 + 128 * sw + 64 * k + lane;
          wt_store16(ro, (u32)((((col >> 4) * 112 + f8) * 16 + (col & 15)) * 16), stg[64 * k + lane]);
        }
      }
      lds_rendezvous((u32 LAS*)(smem + B_CNT + 8), 4);
      if (st < 64) {
        const float s = (red[st] + red[64 + st]) + (red[128 + st] + red[192 + st]);
        const int fq = st >> 2, k = st & 3;
        a.uv[(dq * 2 + (k >> 1)) * F + f0 + 2 * fq + (k & 1)] = s;
      }
    }
    u32x4 cf;
    {
      u64 base;
      asm volatile("s_getpc_b64 s[40:41]\n\ts_add_u32 s40, s40, _Z2kF5FArgs@rel32@lo+4\n\ts_addc_u32 s41, s41, _Z2kF5FArgs@rel32@hi+12\n\ts_mov_b64 %0, s[40:41]"
                   : "=s"(base) : : "s40", "s41", "scc");
      cf = *(const u32x4*)((const char*)base + (size_t)(st < F_CODE_LANES ? (wg >> 3) * F_CODE_LANES + st : 0) * 16);
    }
    STAMP(1, 1);
    asm volatile("" ::"v"(cf));
#pragma unroll
    for (int j = 0; j < (int)(sizeof(pf) / sizeof(pf[0])); ++j) asm volatile("" ::"v"(pf[j]));
    STAMP(1, 5);
  }
}
struct FArgs {
  const u16 *x1T, *Wt, *wsp, *bsp, *Wot; const float *uv, *mod;
  u16 *z2t, *Pt, *out; u64* gran; u32* flags;
};
constexpr int F_X = 0;
constexpr int F_ST = 65536;
constexpr int F_MR = F_ST + 512;
constexpr int F_PS = F_MR + 8 * 512;
constexpr int F_FS = F_PS + 7 * 64 * 8;
constexpr int F_ZS = F_FS + 512;
constexpr int F_RS = 232;
constexpr int F_A0 = 90112;
constexpr int F_LDS = F_A0 + 8 * 8192;
constexpr int F_EX = 112 * 1024;
#define F_SLEEP0 16
#define F_SLEEP1 8
#define F_BDEPTH 3
constexpr int F_BD = F_BDEPTH;
constexpr int F_BR = F_BD;
static_assert(F_ZS + 64 * F_RS <= F_A0 && F_EX + 64 * 132 <= F_LDS, "LDS map");

__device__ __forceinline__ float gelu_tanh(float z) {
  const float k0 = -2.f * 0.7978845608028654f * 1.4426950408889634f, k1 = k0 * 0.044715f;
  const float e = __builtin_amdgcn_exp2f(z * (k0 + k1 * (z * z)));
  return z * __builtin_amdgcn_rcpf(1.f + e);
}
__device__ __forceinline__ float pick4(f32x4 v, int r) { return r == 0 ? v[0] : r == 1 ? v[1] : r == 2 ? v[2] : v[3]; }
template <int N> __device__ __forceinline__ void vm_wait_c() { asm volatile("s_waitcnt vmcnt(%0)" ::"i"(N > 63 ? 63 : N) : "memory"); }
__device__ __forceinline__ u32 f_slot(int kq, int sg) { return (u32)(((sg & 2) ? 0 : F_A0) + (kq * 2 + (sg & 1)) * 8192); }
__device__ __forceinline__ bf16x8 ld_dev16(rsrc_t r, u32 off) { return __builtin_bit_cast(bf16x8, __builtin_amdgcn_raw_buffer_load_b128(r, off, 0, 16)); }
__device__ __forceinline__ void f_lda(const u16* const (&pa)[2], u32 lds0, int kq, int nh, int sg) {
#pragma unroll
  for (int i = 0; i < 4; ++i) glds16(pa[i & 1] + (size_t)(i >> 1) * 16 * L + 64 * sg, lds0 + f_slot(kq, sg) + (4 * nh + i) * 1024);
}
__device__ __forceinline__ void f_lda_full(const u16* wsp, u32 lds0, int pi, int kq, int sg) {
  const int lane = threadIdx.x & 63;
#pragma unroll
  for (int i = 0; i < 8; ++i) {
    const int row = 8 * i + (lane >> 3);
    glds16(wsp + (size_t)(64 * pi + row) * L + 512 * kq + 64 * sg + 8 * ((lane & 7) ^ ((row >> 1) & 7)), lds0 + f_slot(kq, sg) + i * 1024);
  }
}
__device__ __forceinline__ void f_ldx(const u16* x1T, u32 lds0, int w, int pi, int sg) {
  const int lane = threadIdx.x & 63;
#pragma unroll
  for (int i = 0; i < 2; ++i) {
    const int d = 128 * sg + 16 * w + 8 * i + (lane >> 3);
    const int fz = (((d >> 1) & 1) | ((d >> 2) & 2)) << 1;
    glds16(x1T + (size_t)d * L + 64 * pi + 8 * ((lane & 7) ^ fz), lds0 + F_X + (128 * sg + 16 * w + 8 * i) * 128);
  }
}
__device__ __forceinline__ void wait_flags(const u32* p, int n) {
  const int lane = threadIdx.x & 63;
  for (u32 spin = 0; spin < (1u << 22); ++spin) {
    const u32 v = __hip_atomic_load((GAS u32*)p + (lane < n ? lane : 0), __ATOMIC_RELAXED, __HIP_MEMORY_SCOPE_AGENT);
    if (__all(v == 1u)) break;
    __builtin_amdgcn_s_sleep(1);
  }
}

__device__ __forceinline__ u32 poll8(const u32* p, int n) {
  const int lane = threadIdx.x & 63;
  const u32 v = __hip_atomic_load((GAS u32*)p + (lane < n ? lane : 0), __ATOMIC_RELAXED, __HIP_MEMORY_SCOPE_AGENT);
  return (u32)__ballot(v == 1u) & 0xffu;
}
__device__ __forceinline__ void need_flag(const u32* p, int n, u32& mask, int k) {
  if (!((mask >> k) & 1u)) {
    for (u32 spin = 0; spin < (1u << 22); ++spin) {
      __builtin_amdgcn_s_sleep(F_SLEEP1);
      mask = poll8(p, n);
      if ((mask >> k) & 1u) break;
    }
  }
  asm volatile("" ::: "memory");
}

struct Z1 { f32x4 y[4]; };

template <int ROLE>
__device__ __forceinline__ void f_phase_c(const FArgs& a, char* smem, const int w, const int pi, const int j, const u16* const (&pa)[2], Z1& z1) {
  const int tid = threadIdx.x, lane = tid & 63;
  const int c = lane & 15, g = lane >> 4;
  const u32 lds0 = (u32)(size_t)(const char LAS*)smem;
  const int nt2 = 56 + 7 * j + (w < 7 ? w : 0), nt1 = 7 * j + (w > 0 ? w - 1 : 0);
  const u16* pw2 = a.Wt + (size_t)nt2 * 64 * 128 + lane * 8;
  const u16* pw1 = a.Wt + (size_t)nt1 * 64 * 128 + lane * 8;
  const int q4 = (lane & 15) >> 2, p4 = lane & 3;
  const int drow = 8 * g + q4;
  const int fz0 = (((drow >> 1) & 1) | ((drow >> 2) & 2)) << 1;
  const char LAS* xb = (const char LAS*)smem + F_X + drow * 128 + 8 * (p4 & 1);
  const char LAS* xt[4];
  int tb[4];
#pragma unroll
  for (int s = 0; s < 4; ++s) { tb[s] = (s + w) & 3; xt[s] = xb + 16 * ((2 * tb[s] + (p4 >> 1)) ^ fz0); }
  auto xfrag = [&](int s, int ks) -> bf16x8 {
    const char LAS* p = xt[s] + ks * 4096;
    const s16x4 lo = tr_read(p), hi = tr_read(p + 512);
    const u32x2 a0 = __builtin_bit_cast(u32x2, lo), a1 = __builtin_bit_cast(u32x2, hi);
    return __builtin_bit_cast(bf16x8, u32x4{a0[0], a0[1], a1[0], a1[1]});
  };
  f32x4 acc[4];
#pragma unroll
  for (int s = 0; s < 4; ++s) acc[s] = f32x4{0, 0, 0, 0};
  bf16x8 W1[16];
  f32x4 u2, v2, u1, v1;
  float* xs2 = (float*)(smem + F_ST);
  if constexpr (ROLE < 2) {
    bf16x8 W2[16];
#pragma unroll
    for (int ks = 0; ks < 4; ++ks) W2[ks] = *(const bf16x8*)(pw2 + 4 * ks * 128);
    SCHED_FENCE();
    f_ldx(a.x1T, lds0, w, pi, 1);
#pragma unroll
    for (int ks = 4; ks < 8; ++ks) W2[ks] = *(const bf16x8*)(pw2 + 4 * ks * 128);
    SCHED_FENCE();
    VM_WAIT(10);
    __builtin_amdgcn_s_barrier();
    SCHED_FENCE();
    STAMP(2, 1);
    f32x4 sq = {0, 0, 0, 0};
    bf16x8 X[2][4];
#pragma unroll
    for (int s = 0; s < 4; ++s) X[0][s] = xfrag(s, 0);
#pragma unroll
    for (int ks = 0; ks < 16; ++ks) {
      if (ks == 3) { VM_WAIT(9); __builtin_amdgcn_s_barrier(); }
      if (ks == 7) { VM_WAIT(17); __builtin_amdgcn_s_barrier(); }
      if (ks == 11) { VM_WAIT(12); __builtin_amdgcn_s_barrier(); }
      if (ks == 0) f_ldx(a.x1T, lds0, w, pi, 2);
      if (ks == 4) f_ldx(a.x1T, lds0, w, pi, 3);
      if (ks < 8) W2[8 + ks] = *(const bf16x8*)(pw2 + 4 * (8 + ks) * 128);
      if (ks == 5) { u2 = uv_get4(a.uv, 0, 16 * nt2 + 4 * g); v2 = uv_get4(a.uv, 1, 16 * nt2 + 4 * g); }
      SCHED_FENCE();
#pragma unroll
      for (int s = 0; s < 4; ++s) {
        if (ks + 1 < 16) X[(ks + 1) & 1][s] = xfrag(s, ks + 1);
        acc[s] = __builtin_amdgcn_mfma_f32_16x16x32_bf16(W2[ks], X[ks & 1][s], acc[s], 0, 0, 0);
        if (s == 3) sq = __builtin_amdgcn_mfma_f32_16x16x32_bf16(X[ks & 1][0], X[ks & 1][0], sq, 0, 0, 0);
        SCHED_FENCE();
      }
    }
    if constexpr (ROLE == 1) {
      u1 = uv_get4(a.uv, 0, 16 * nt1 + 4 * g); v1 = uv_get4(a.uv, 1, 16 * nt1 + 4 * g);
      SCHED_FENCE();
    }
    if (w < 4 && (c >> 2) == g) xs2[64 + 16 * w + c] = pick4(sq, c & 3);
  } else {
    f_ldx(a.x1T, lds0, w, pi, 1);
    VM_WAIT(2);
    __builtin_amdgcn_s_barrier();
    SCHED_FENCE();
    STAMP(2, 1);
    const u32x4 ones = {0x3f803f80u, 0x3f803f80u, 0x3f803f80u, 0x3f803f80u};
    bf16x8 X[2][4];
#pragma unroll
    for (int s = 0; s < 4; ++s) X[0][s] = xfrag(s, 0);
#pragma unroll
    for (int ks = 0; ks < 16; ++ks) {
      if (ks == 3) { VM_WAIT(2); __builtin_amdgcn_s_barrier(); }
      if (ks == 7) { VM_WAIT(2); __builtin_amdgcn_s_barrier(); }
      if (ks == 11) { VM_WAIT(6); __builtin_amdgcn_s_barrier(); }
      if (ks == 0) f_ldx(a.x1T, lds0, w, pi, 2);
      if (ks == 4) f_ldx(a.x1T, lds0, w, pi, 3);
      if (ks >= 8) { W1[2 * ks - 16] = *(const bf16x8*)(pw1 + 4 * (2 * ks - 16) * 128); W1[2 * ks - 15] = *(const bf16x8*)(pw1 + 4 * (2 * ks - 15) * 128); }
      SCHED_FENCE();
#pragma unroll
      for (int s = 0; s < 4; ++s) {
        if (ks + 1 < 16) X[(ks + 1) & 1][s] = xfrag(s, ks + 1);
        acc[s] = __builtin_amdgcn_mfma_f32_16x16x32_bf16(__builtin_bit_cast(bf16x8, ones), X[ks & 1][s], acc[s], 0, 0, 0);
        SCHED_FENCE();
      }
    }
    u1 = uv_get4(a.uv, 0, 16 * nt1 + 4 * g); v1 = uv_get4(a.uv, 1, 16 * nt1 + 4 * g);
    SCHED_FENCE();
    if (g == 0) {
#pragma unroll
      for (int s = 0; s < 4; ++s) xs2[16 * tb[s] + c] = acc[s][0];
    }
  }
  STAMP(2, 2);
  __syncthreads();
  STAMP(2, 3);
  float mu[4], ri[4];
  {
    const float mean = xs2[lane] * (1.f / D);
    const float var = fmaxf(xs2[64 + lane] * (1.f / D) - mean * mean, 0.f);
    f32x2* mr = (f32x2*)(smem + F_MR + w * 512);
    mr[lane] = f32x2{rbf(mean), __builtin_amdgcn_rcpf(rbf(__builtin_amdgcn_sqrtf(rbf(rbf(var) + 1e-5f))))};
#pragma unroll
    for (int s = 0; s < 4; ++s) { const f32x2 v = mr[16 * tb[s] + c]; mu[s] = v[0]; ri[s] = v[1]; }
  }
  u32x2 z2p[4];
  if constexpr (ROLE < 2) {
    float* ps = (float*)(smem + F_PS) + w * 128;
    const s16x4 ones4 = {0x3f80, 0x3f80, 0x3f80, 0x3f80};
#pragma unroll
    for (int s = 0; s < 4; ++s) {
      if constexpr (ROLE == 1) {
#pragma unroll
        for (int ks = 4 * s; ks < 4 * s + 4; ++ks) W1[ks] = *(const bf16x8*)(pw1 + 4 * ks * 128);
        SCHED_FENCE();
      }
      float z[4];
#pragma unroll
      for (int r = 0; r < 4; ++r) z[r] = gelu_tanh(ri[s] * (acc[s][r] - mu[s] * u2[r]) + v2[r]);
      z2p[s] = u32x2{pk_bf16(z[0], z[1]), pk_bf16(z[2], z[3])};
      const s16x4 zz = __builtin_bit_cast(s16x4, z2p[s]);
      const f32x4 zero = {0, 0, 0, 0};
      const f32x4 s1 = __builtin_amdgcn_mfma_f32_16x16x16bf16_1k(ones4, zz, zero, 0, 0, 0);
      const f32x4 s2 = __builtin_amdgcn_mfma_f32_16x16x16bf16_1k(zz, zz, zero, 0, 0, 0);
      if ((c >> 2) == g) *(f32x2*)(ps + (16 * tb[s] + c) * 2) = f32x2{s1[0], pick4(s2, c & 3)};
      SCHED_FENCE();
    }
  }
  if constexpr (ROLE > 0) { f_lda(pa, lds0, w & 3, w >> 2, 0); f_lda(pa, lds0, w & 3, w >> 2, 1); }
  STAMP(2, 4);
  __syncthreads();
  STAMP(2, 5);
  if constexpr (ROLE == 0) {
    const float* ps = (const float*)(smem + F_PS);
    float s1 = 0, s2 = 0;
#pragma unroll
    for (int k = 0; k < 7; ++k) { const f32x2 v = *(const f32x2*)(ps + k * 128 + lane * 2); s1 += v[0]; s2 += v[1]; }
    const rsrc_t rg = make_rsrc(a.gran, 256 * 1024);
    const u32 goff = (u32)(pi * 8 * 64 + lane) * 16;
    wt_store16(rg, goff + (u32)j * 1024, u32x4{__float_as_uint(s1), 1u, __float_as_uint(s2), 1u});
    float t1 = 0, t2 = 0;
    for (u32 spin = 0; spin < (1u << 22); ++spin) {
      u32x4 x[8];
#pragma unroll
      for (int k = 0; k < 8; ++k) x[k] = __builtin_amdgcn_raw_buffer_load_b128(rg, goff + k * 1024, 0, 16);
      bool ok = true; t1 = 0; t2 = 0;
#pragma unroll
      for (int k = 0; k < 8; ++k) {
        ok = ok && x[k][1] == 1u && x[k][3] == 1u;
        t1 += __uint_as_float(x[k][0]); t2 += __uint_as_float(x[k][2]);
      }
      if (__all(ok)) break;
      __builtin_amdgcn_s_sleep(1);
    }
    const float mean = t1 * (1.f / FH);
    const float var = fmaxf(t2 * (1.f / FH) - mean * mean, 0.f);
    *(f32x2*)(smem + F_FS + lane * 8) = f32x2{rbf(mean), __builtin_amdgcn_rcpf(rbf(__builtin_amdgcn_sqrtf(rbf(rbf(var) + 1e-5f))))};
    f_lda(pa, lds0, w & 3, w >> 2, 0);
    f_lda(pa, lds0, w & 3, w >> 2, 1);
  } else {
#pragma unroll
    for (int s = 0; s < 4; ++s) acc[s] = f32x4{0, 0, 0, 0};
    bf16x8 X[2][4];
#pragma unroll
    for (int s = 0; s < 4; ++s) X[0][s] = xfrag(s, 0);
#pragma unroll
    for (int ks = 0; ks < 16; ++ks) {
#pragma unroll
      for (int s = 0; s < 4; ++s) {
        if (ks + 1 < 16) X[(ks + 1) & 1][s] = xfrag(s, ks + 1);
        acc[s] = __builtin_amdgcn_mfma_f32_16x16x32_bf16(W1[ks], X[ks & 1][s], acc[s], 0, 0, 0);
        SCHED_FENCE();
      }
    }
  }
  STAMP(2, 6);
  __syncthreads();
  STAMP(2, 7);
  if constexpr (ROLE < 2) {
#pragma unroll
    for (int s = 0; s < 4; ++s) {
      const f32x2 fs = *(const f32x2*)(smem + F_FS + (16 * tb[s] + c) * 8);
      const float a0 = (bf_lo(z2p[s][0]) - fs[0]) * fs[1], a1 = (bf_hi(z2p[s][0]) - fs[0]) * fs[1];
      const float a2 = (bf_lo(z2p[s][1]) - fs[0]) * fs[1], a3 = (bf_hi(z2p[s][1]) - fs[0]) * fs[1];
      *(u32x2*)(smem + F_ZS + (16 * tb[s] + c) * F_RS + (16 * w + 4 * g) * 2) = u32x2{pk_bf16(a0, a1), pk_bf16(a2, a3)};
    }
#pragma unroll
    for (int kk = 0; kk < 2; ++kk) {
      const char LAS* p = (const char LAS*)smem + F_ZS + (32 * kk + 8 * g + q4) * F_RS + (16 * w + 4 * p4) * 2;
      const s16x4 lo = tr_read(p), hi = tr_read(p + 4 * F_RS);
      const u32x2 a0 = __builtin_bit_cast(u32x2, lo), a1 = __builtin_bit_cast(u32x2, hi);
      wt_store16(make_rsrc(a.z2t, L * FH * 2), (u32)((((7 * j + w) * 256 + 8 * pi + 4 * kk) * 64 + lane * 4) * 4), u32x4{a0[0], a0[1], a1[0], a1[1]});
    }
  }
  VM_WAIT(0);
  __syncthreads();
  if (tid == 0) __hip_atomic_store((GAS u32*)a.flags + j * 32 + pi, 1u, __ATOMIC_RELAXED, __HIP_MEMORY_SCOPE_AGENT);
  STAMP(2, 8);
#pragma unroll
  for (int s = 0; s < 4; ++s)
#pragma unroll
    for (int r = 0; r < 4; ++r) z1.y[s][r] = ROLE > 0 ? ri[s] * (acc[s][r] - mu[s] * u1[r]) + v1[r] : 0.f;
  STAMP(2, 9);
}

template <int NT> constexpr int fd_glds(int sg) { return (sg == 2 || sg == 4) ? 8 : 0; }
template <int NT> constexpr int fd_bld(int sg) { return sg < 0 ? 0 : (2 * sg + F_BD < 16 ? NT : 0) + (2 * sg + 1 + F_BD < 16 ? NT : 0); }
template <int NT> constexpr int fd_ops(int sg) { return fd_glds<NT>(sg) + fd_bld<NT>(sg); }

template <int NT, int SG>
__device__ __forceinline__ void fd_stage(f32x4 (&acc)[4][NT], bf16x8 (&Bf)[F_BR][NT], bf16x8 (&Af)[2][4], const rsrc_t rb, u32 boff,
                                         const u16* const (&pa)[2], u32 lds0, const char* const (&ap)[2][2][4], int kq, int nh,
                                         const u32* fl, u32& mask, const Z1& z1, char* zdst, int w) {
  if constexpr ((SG & 1) == 0 && SG > 0) {
    if constexpr (SG == 2) vm_wait_c<F_BD * NT + fd_ops<NT>(0) + fd_ops<NT>(1)>();
    else vm_wait_c<fd_bld<NT>(SG - 2) + fd_ops<NT>(SG - 1)>();
    __builtin_amdgcn_s_barrier();
    if constexpr (fd_glds<NT>(SG) > 0) { f_lda(pa, lds0, kq, nh, SG + 2); f_lda(pa, lds0, kq, nh, SG + 3); }
#pragma unroll
    for (int m = 0; m < 4; ++m) Af[0][m] = *(const bf16x8*)(ap[(SG >> 1) & 1][0][m]);
  }
#pragma unroll
  for (int kk = 0; kk < 2; ++kk) {
    constexpr int ks0 = 2 * SG;
    if (kk == 0 || (SG & 1) == 0) {
#pragma unroll
      for (int m = 0; m < 4; ++m) Af[(kk + 1) & 1][m] = *(const bf16x8*)(ap[(SG >> 1) & 1][(kk + 1) & 1][m] + (kk == 0 ? (SG & 1) : 1) * 8192);
    }
    SCHED_FENCE();
#pragma unroll
    for (int n = 0; n < NT; ++n) {
#pragma unroll
      for (int m = 0; m < 4; ++m) acc[m][n] = __builtin_amdgcn_mfma_f32_16x16x32_bf16(Bf[(ks0 + kk) % F_BR][n], Af[kk][m], acc[m][n], 0, 0, 0);
      SCHED_FENCE();
      if (ks0 + kk + F_BD < 16) {
        if (n == 0 && ((ks0 + kk + F_BD) & 1) == 0) need_flag(fl, 8, mask, (ks0 + kk + F_BD) >> 1);
        Bf[(ks0 + kk + F_BD) % F_BR][n] = ld_dev16(rb, boff + (u32)((n * 256 + 4 * (ks0 + kk + F_BD)) * 256));
      }
      if constexpr (SG < 4) {
        if (kk == 0 && n == 1 && w > 0) {
          float z[4];
#pragma unroll
          for (int r = 0; r < 4; ++r) z[r] = gelu_tanh(z1.y[SG][r]);
          *(u32x2*)(zdst + ((SG + w) & 3) * 16 * F_RS) = u32x2{pk_bf16(z[0], z[1]), pk_bf16(z[2], z[3])};
        }
      }
      SCHED_FENCE();
    }
  }
}

template <int NT>
__device__ __forceinline__ void f_phase_d(const FArgs& a, char* smem, const int w, const int rt, const int ct, const u16* const (&pa)[2], const Z1& z1) {
  const int tid = threadIdx.x, lane = tid & 63;
  const int kq = w & 3, nh = w >> 2, n0 = nh * 4;
  const int c = lane & 15, g = lane >> 4;
  const u32 lds0 = (u32)(size_t)(const char LAS*)smem;
  const rsrc_t rb = make_rsrc(a.z2t, L * FH * 2);
  const u32 boff = (u32)((((7 * ct + n0) * 256 + 64 * kq) * 64 + lane * 4) * 4);
  f_lda(pa, lds0, kq, nh, 2);
  f_lda(pa, lds0, kq, nh, 3);
  const u32* fl = a.flags + ct * 32 + 8 * kq;
  __builtin_amdgcn_s_sleep(F_SLEEP0);
  u32 mask = poll8(fl, 8);
  bf16x8 Bf[F_BR][NT], Af[2][4];
#pragma unroll
  for (int ks = 0; ks < F_BD; ++ks) {
    if ((ks & 1) == 0) need_flag(fl, 8, mask, ks >> 1);
#pragma unroll
    for (int n = 0; n < NT; ++n) Bf[ks][n] = ld_dev16(rb, boff + (u32)((n * 256 + 4 * ks) * 256));
  }
  SCHED_FENCE();
  f32x4 acc[4][NT];
#pragma unroll
  for (int m = 0; m < 4; ++m)
#pragma unroll
    for (int n = 0; n < NT; ++n) acc[m][n] = f32x4{0, 0, 0, 0};
  const int asw = (c >> 1) & 7;
  const char* ap[2][2][4];
#pragma unroll
  for (int hi = 0; hi < 2; ++hi)
#pragma unroll
    for (int m = 0; m < 4; ++m) {
      const char* base = smem + f_slot(kq, 2 * hi) + ((m + kq) & 3) * 2048 + c * 128;
      ap[hi][0][m] = base + 16 * (g ^ asw); ap[hi][1][m] = base + 16 * ((4 + g) ^ asw);
    }
#pragma unroll
  for (int m = 0; m < 4; ++m) Af[0][m] = *(const bf16x8*)(ap[0][0][m]);
  char* zdst = smem + F_ZS + c * F_RS + (16 * (w - 1) + 4 * g) * 2;
  STAMP(3, 4);
  fd_stage<NT, 0>(acc, Bf, Af, rb, boff, pa, lds0, ap, kq, nh, fl, mask, z1, zdst, w);
  fd_stage<NT, 1>(acc, Bf, Af, rb, boff, pa, lds0, ap, kq, nh, fl, mask, z1, zdst, w);
  STAMP(3, 5);
  fd_stage<NT, 2>(acc, Bf, Af, rb, boff, pa, lds0, ap, kq, nh, fl, mask, z1, zdst, w);
  fd_stage<NT, 3>(acc, Bf, Af, rb, boff, pa, lds0, ap, kq, nh, fl, mask, z1, zdst, w);
  STAMP(3, 6);
  fd_stage<NT, 4>(acc, Bf, Af, rb, boff, pa, lds0, ap, kq, nh, fl, mask, z1, zdst, w);
  fd_stage<NT, 5>(acc, Bf, Af, rb, boff, pa, lds0, ap, kq, nh, fl, mask, z1, zdst, w);
  STAMP(3, 7);
  fd_stage<NT, 6>(acc, Bf, Af, rb, boff, pa, lds0, ap, kq, nh, fl, mask, z1, zdst, w);
  const float bs = bf1(a.bsp[64 * rt + 16 * kq + c]);
  fd_stage<NT, 7>(acc, Bf, Af, rb, boff, pa, lds0, ap, kq, nh, fl, mask, z1, zdst, w);
  STAMP(3, 1);
  __syncthreads();
#pragma unroll
  for (int m = 1; m < 4; ++m) {
    const int o = (m + kq) & 3;
    char* dst = smem + o * 10752 + ((m - 1) * 7 * 64 + n0 * 64 + lane) * 8;
#pragma unroll
    for (int n = 0; n < NT; ++n) *(u32x2*)(dst + n * 512) = u32x2{pk_bf16(acc[m][n][0], acc[m][n][1]), pk_bf16(acc[m][n][2], acc[m][n][3])};
  }
  __syncthreads();
  STAMP(3, 2);
  const char* src = smem + kq * 10752 + (n0 * 64 + lane) * 8;
  const char* zs = smem + F_ZS + (16 * kq + c) * F_RS + (16 * n0 + 4 * g) * 2;
  const rsrc_t rp = make_rsrc(a.Pt, L * FH * 2);
  u32x2 pv[NT];
#pragma unroll
  for (int n = 0; n < NT; ++n) {
    const u32x2 p0 = *(const u32x2*)(src + n * 512), p1 = *(const u32x2*)(src + (7 + n) * 512), p2 = *(const u32x2*)(src + (14 + n) * 512);
    const u32x2 zz = *(const u32x2*)(zs + n * 32);
    float gg[4];
#pragma unroll
    for (int e = 0; e < 2; ++e) {
      gg[2 * e] = ((acc[0][n][2 * e] + bf_lo(p0[e])) + (bf_lo(p1[e]) + bf_lo(p2[e]))) + bs;
      gg[2 * e + 1] = ((acc[0][n][2 * e + 1] + bf_hi(p0[e])) + (bf_hi(p1[e]) + bf_hi(p2[e]))) + bs;
    }
    pv[n] = u32x2{pk_bf16(bf_lo(zz[0]) * gg[0], bf_hi(zz[0]) * gg[1]), pk_bf16(bf_lo(zz[1]) * gg[2], bf_hi(zz[1]) * gg[3])};
    if (n & 1) wt_store16(rp, (u32)((((4 * rt + kq) * 112 + 14 * ct + 2 * (n0 + n - 1 + (g & 1)) + (g >> 1)) * 16 + c) * 16), pair16(pv[n - 1], pv[n]));
  }
  if constexpr (NT & 1) {
    const u32x4 v = pair16(pv[NT - 1], pv[NT - 1]);
    if ((g & 1) == 0) wt_store16(rp, (u32)((((4 * rt + kq) * 112 + 14 * ct + 2 * (n0 + NT - 1) + (g >> 1)) * 16 + c) * 16), v);
  }
  STAMP(3, 3);
}

__device__ __forceinline__ void f_phase_e(const FArgs& a, char* smem, const int w, const int rt, const int dt) {
  const int tid = threadIdx.x, lane = tid & 63;
  const int kq = w & 3, mh = w >> 2;
  const int c = lane & 15, g = lane >> 4;
  const u32 lds0 = (u32)(size_t)(const char LAS*)smem;
  VM_WAIT(0);
  __syncthreads();
  if (tid == 0) __hip_atomic_store((GAS u32*)a.flags + 256 + rt * 32 + dt, 1u, __ATOMIC_RELAXED, __HIP_MEMORY_SCOPE_AGENT);
  STAMP(4, 5);
#pragma unroll
  for (int k = 0; k < 7; ++k)
#pragma unroll
    for (int nn = 0; nn < 2; ++nn) {
      const int n = 2 * mh + nn;
      glds16(a.Wot + ((size_t)(4 * dt + n) * 112 + 4 * (7 * kq + k)) * 128 + lane * 8, lds0 + (n * 28 + 7 * kq + k) * 1024);
    }
  const u32x4 xr = *(const u32x4*)(a.x1T + (size_t)(64 * dt + (tid >> 3)) * L + 64 * rt + 8 * (tid & 7));
  const f32x4 gbv = mod_get4(a.mod, 2560 + 64 * dt + 16 * kq + 4 * g);
  SCHED_FENCE();
  {
    u32* p = (u32*)(smem + F_EX + (tid >> 3) * 132 + (tid & 7) * 16);
    p[0] = xr[0]; p[1] = xr[1]; p[2] = xr[2]; p[3] = xr[3];
  }
  VM_WAIT(0);
  __builtin_amdgcn_s_barrier();
  SCHED_FENCE();
  bf16x8 Wf[7][4];
  const char* wp = smem + (7 * kq) * 1024 + lane * 16;
#pragma unroll
  for (int k = 0; k < 7; ++k)
#pragma unroll
    for (int n = 0; n < 4; ++n) Wf[k][n] = *(const bf16x8*)(wp + (((n + kq) & 3) * 28 + k) * 1024);
  SCHED_FENCE();
  {
    const u32* fl = a.flags + 256 + rt * 32 + 2 * kq;
    u32 mask = poll8(fl, 2);
    need_flag(fl, 2, mask, 0); need_flag(fl, 2, mask, 1);
  }
  STAMP(4, 4);
  const rsrc_t rp = make_rsrc(a.Pt, L * FH * 2);
  bf16x8 Pf[7][2];
#pragma unroll
  for (int k = 0; k < 7; ++k)
#pragma unroll
    for (int m = 0; m < 2; ++m) Pf[k][m] = ld_dev16(rp, (u32)((((4 * rt + 2 * mh + m) * 112 + 4 * (7 * kq + k)) * 64 + lane * 4) * 4));
  SCHED_FENCE();
  f32x4 acc[2][4];
#pragma unroll
  for (int m = 0; m < 2; ++m)
#pragma unroll
    for (int n = 0; n < 4; ++n) acc[m][n] = f32x4{0, 0, 0, 0};
#pragma unroll
  for (int k = 0; k < 7; ++k)
#pragma unroll
    for (int n = 0; n < 4; ++n)
#pragma unroll
      for (int m = 0; m < 2; ++m) acc[m][n] = __builtin_amdgcn_mfma_f32_16x16x32_bf16(Wf[k][n], Pf[k][m], acc[m][n], 0, 0, 0);
  STAMP(4, 1);
  __syncthreads();
#pragma unroll
  for (int n = 1; n < 4; ++n)
#pragma unroll
    for (int m = 0; m < 2; ++m)
      *(f32x4*)(smem + (((((n + kq) & 3) * 2 + mh) * 3 + (n - 1)) * 2 + m) * 1024 + lane * 16) = acc[m][n];
  __syncthreads();
  STAMP(4, 2);
  {
    u32x2 ov[2];
#pragma unroll
    for (int m = 0; m < 2; ++m) {
      const char* p = smem + (((kq * 2 + mh) * 3) * 2 + m) * 1024 + lane * 16;
      const f32x4 p0 = *(const f32x4*)p, p1 = *(const f32x4*)(p + 2048), p2 = *(const f32x4*)(p + 4096);
      const char* xp = smem + F_EX + (16 * kq + 4 * g) * 132 + (16 * (2 * mh + m) + c) * 2;
      float o[4];
#pragma unroll
      for (int e = 0; e < 4; ++e) o[e] = bf1(*(const u16*)(xp + e * 132)) + gbv[e] * ((acc[m][0][e] + p0[e]) + (p1[e] + p2[e]));
      ov[m] = u32x2{pk_bf16(o[0], o[1]), pk_bf16(o[2], o[3])};
    }
    *(u32x4*)(a.out + (size_t)(64 * rt + 16 * (2 * mh + (g & 1)) + c) * D + 64 * dt + 16 * kq + 8 * (g >> 1)) = pair16(ov[0], ov[1]);
  }
  STAMP(4, 3);
}

__global__ __launch_bounds__(512, 2) void kF(FArgs a) {
  extern __shared__ __attribute__((aligned(16))) char smem[];
  const int wg = blockIdx.x, lane = threadIdx.x & 63;
  const int w = __builtin_amdgcn_readfirstlane(threadIdx.x >> 6);
  const int xc = wg & 7, ii = wg >> 3;
  const int pi = (xc >> 1) * 8 + (ii & 7), j = (xc & 1) * 4 + (ii >> 3);
  STAMP(2, 0);
  f_ldx(a.x1T, (u32)(size_t)(const char LAS*)smem, w, pi, 0);
  const u16* pa[2];
#pragma unroll
  for (int par = 0; par < 2; ++par) {
    const int row = 32 * (w >> 2) + 8 * par + (lane >> 3);
    pa[par] = a.wsp + (size_t)(64 * pi + row) * L + 512 * (w & 3) + 8 * ((lane & 7) ^ ((row >> 1) & 7));
  }
  Z1 z1;
  if (w == 0) f_phase_c<0>(a, smem, w, pi, j, pa, z1);
  else if (w < 7) f_phase_c<1>(a, smem, w, pi, j, pa, z1);
  else f_phase_c<2>(a, smem, w, pi, j, pa, z1);
  STAMP(3, 0);
  if (w < 4) f_phase_d<4>(a, smem, w, pi, j, pa, z1);
  else f_phase_d<3>(a, smem, w, pi, j, pa, z1);
  STAMP(4, 0);
  f_phase_e(a, smem, w, pi, j);
}
#define KMASK(b)
extern "C" void kernel_launch(void* const* d_in, const int* in_sizes, int n_in, void* d_out, int out_size,
                              void* d_ws, size_t ws_size, hipStream_t stream) {
  const u16* x = (const u16*)d_in[0];
  char* ws = (char*)d_ws;
  float* mod = (float*)(ws + 524288);
  u32* stats1 = (u32*)(ws + 16384);
  float* uv = (float*)(ws + 589824);
  u64* gran = (u64*)(ws + 65536);
  u32* flags = (u32*)(ws + 65536 + 262144);
  u16* xT = (u16*)(ws + (1 << 20));
  u16* Krev = (u16*)(ws + (3 << 20));
  u16* x1T = xT;
  u16* Wt = (u16*)(ws + (7 << 20));
  u16* Wot = (u16*)(ws + (9 << 20));
  u16* Pt = (u16*)(ws + (10 << 20));
  u16* z2t = Krev;
  AArgs aa;
  aa.x = x; aa.Are = (const u16*)d_in[1]; aa.Aim = (const u16*)d_in[2]; aa.Bre = (const u16*)d_in[3]; aa.Bim = (const u16*)d_in[4];
  aa.Cre = (const u16*)d_in[5]; aa.Cim = (const u16*)d_in[6]; aa.logdt = (const u16*)d_in[7]; aa.c = (const u16*)d_in[14];
  aa.wmod = (const u16*)d_in[15]; aa.win = (const u16*)d_in[10]; aa.wout = (const u16*)d_in[13];
  aa.mod = mod; aa.stats1 = stats1; aa.xT = xT; aa.Krev = Krev; aa.gran = (u32x4*)gran;
  (void)hipFuncSetAttribute((const void*)kA, hipFuncAttributeMaxDynamicSharedMemorySize, A_LDS);
  KMASK(1) kA<<<256, 768, A_LDS, stream>>>(aa);
  BArgs ba;
  ba.xT = xT; ba.Krev = Krev; ba.Dskip = (const u16*)d_in[8]; ba.win = (const u16*)d_in[10]; ba.wout = (const u16*)d_in[13];
  ba.wsp = (const u16*)d_in[11]; ba.stats1 = stats1; ba.mod = mod; ba.x1T = x1T; ba.Wt = Wt; ba.Wot = Wot; ba.uv = uv;
  (void)hipFuncSetAttribute((const void*)kB, hipFuncAttributeMaxDynamicSharedMemorySize, B_LDS);
  KMASK(2) kB<<<256, 768, B_LDS, stream>>>(ba);
  FArgs fa;
  fa.x1T = x1T; fa.Wt = Wt; fa.wsp = (const u16*)d_in[11]; fa.bsp = (const u16*)d_in[12]; fa.Wot = Wot; fa.uv = uv; fa.mod = mod;
  fa.z2t = z2t; fa.Pt = Pt; fa.out = (u16*)d_out; fa.gran = gran; fa.flags = flags;
  (void)hipFuncSetAttribute((const void*)kF, hipFuncAttributeMaxDynamicSharedMemorySize, F_LDS);
  KMASK(4) kF<<<256, 512, F_LDS, stream>>>(fa);
}
```

```cpp
#include <hip/hip_runtime.h>
#include <hip/hip_bf16.h>
#include <stdint.h>

typedef unsigned short u16;
typedef unsigned int u32;
typedef unsigned long long u64;
typedef __bf16 bf16x8 __attribute__((ext_vector_type(8)));
typedef __bf16 bf16x2 __attribute__((ext_vector_type(2)));
typedef short s16x4 __attribute__((ext_vector_type(4)));
typedef float f32x2 __attribute__((ext_vector_type(2)));
typedef float f32x4 __attribute__((ext_vector_type(4)));
typedef u32 u32x2 __attribute__((ext_vector_type(2)));
typedef u32 u32x4 __attribute__((ext_vector_type(4)));
#define LAS __attribute__((address_space(3)))
#define GAS __attribute__((address_space(1)))

constexpr int L = 2048, D = 512, NS = 128, F = 1792, FH = 896;

__device__ __forceinline__ int xt_off(int d, int t) { return (((t >> 6) * D + d) << 6) + (t & 63); }

__device__ __forceinline__ float bf_lo(u32 w) { return __uint_as_float(w << 16); }
__device__ __forceinline__ float bf_hi(u32 w) { return __uint_as_float(w & 0xffff0000u); }
__device__ __forceinline__ float bf1(u16 h) { return __uint_as_float(((u32)h) << 16); }
__device__ __forceinline__ u32 pk_bf16(float lo, float hi) {
  f32x2 a = {lo, hi};
  bf16x2 p = __builtin_convertvector(a, bf16x2);
  return __builtin_bit_cast(u32, p);
}
__device__ __forceinline__ float rbf(float x) { return bf_lo(pk_bf16(x, x)); }

template <int CTRL, int ROWMASK = 0xf>
__device__ __forceinline__ float dpp_add(float x) {
  int y = __builtin_amdgcn_update_dpp(0, __float_as_int(x), CTRL, ROWMASK, 0xf, true);
  return x + __int_as_float(y);
}
__device__ __forceinline__ float row16_sum(float x) {
  x = dpp_add<0xB1>(x);
  x = dpp_add<0x4E>(x);
  x = dpp_add<0x141>(x);
  x = dpp_add<0x140>(x);
  return x;
}
__device__ __forceinline__ float wave_sum(float x) {
  x = row16_sum(x);
  float a = __int_as_float(__builtin_amdgcn_readlane(__float_as_int(x), 0));
  float b = __int_as_float(__builtin_amdgcn_readlane(__float_as_int(x), 16));
  float c = __int_as_float(__builtin_amdgcn_readlane(__float_as_int(x), 32));
  float d = __int_as_float(__builtin_amdgcn_readlane(__float_as_int(x), 48));
  return (a + b) + (c + d);
}
__device__ __forceinline__ s16x4 tr_read(const char LAS* p) {
  return __builtin_amdgcn_ds_read_tr16_b64_v4i16((s16x4 LAS*)p);
}

#define STAMP(kern, i)
__device__ __forceinline__ void glds16(const void* gsrc, u32 lds_dst) {
  u32 keep;
  asm volatile("s_mov_b32 %0, m0\n\ts_mov_b32 m0, %2\n\ts_nop 0\n\tglobal_load_lds_dwordx4 %1, off\n\ts_mov_b32 m0, %0"
               : "=&s"(keep) : "v"(gsrc), "s"(lds_dst) : "memory");
}
#define SCHED_FENCE() __builtin_amdgcn_sched_barrier(0)
#define VM_WAIT(n) asm volatile("s_waitcnt vmcnt(" #n ")" ::: "memory")
__device__ __forceinline__ float xor16_sum(float x) {
  const u32 v = __float_as_uint(x);
  auto r = __builtin_amdgcn_permlane16_swap(v, v, false, false);
  return __uint_as_float(r[0]) + __uint_as_float(r[1]);
}
__device__ __forceinline__ float xor32_sum(float x) {
  const u32 v = __float_as_uint(x);
  auto r = __builtin_amdgcn_permlane32_swap(v, v, false, false);
  return __uint_as_float(r[0]) + __uint_as_float(r[1]);
}
__device__ __forceinline__ float xor4_8_sum(float x) {
  x = x + __int_as_float(__builtin_amdgcn_update_dpp(0, __float_as_int(x), 0x124, 0xf, 0xf, true));
  x = x + __int_as_float(__builtin_amdgcn_update_dpp(0, __float_as_int(x), 0x128, 0xf, 0xf, true));
  return x;
}
__device__ __forceinline__ void lds_rendezvous(u32 LAS* cnt, u32 target) {
  asm volatile("s_waitcnt lgkmcnt(0)" ::: "memory");
  if ((threadIdx.x & 63) == 0) __hip_atomic_fetch_add(cnt, 1u, __ATOMIC_RELAXED, __HIP_MEMORY_SCOPE_WORKGROUP);
  while (__hip_atomic_load(cnt, __ATOMIC_RELAXED, __HIP_MEMORY_SCOPE_WORKGROUP) < target) __builtin_amdgcn_s_sleep(1);
  asm volatile("" ::: "memory");
}

typedef __amdgpu_buffer_rsrc_t rsrc_t;
__device__ __forceinline__ rsrc_t make_rsrc(const void* base, u32 bytes) {
  return __builtin_amdgcn_make_buffer_rsrc(const_cast<void*>(base), 0, bytes, 0x00020000);
}
__device__ __forceinline__ void wt_store16(rsrc_t r, u32 byte_off, u32x4 v) { __builtin_amdgcn_raw_buffer_store_b128(v, r, byte_off, 0, 16); }
__device__ __forceinline__ u32x4 pair16(u32x2 a, u32x2 b) {
  const auto r0 = __builtin_amdgcn_permlane16_swap(a[0], b[0], false, false);
  const auto r1 = __builtin_amdgcn_permlane16_swap(a[1], b[1], false, false);
  return u32x4{r0[0], r1[0], r0[1], r1[1]};
}

__device__ __forceinline__ float mod_get(const float* mp, int i) { return rbf((mp[i] + mp[3072 + i]) + (mp[6144 + i] + mp[9216 + i])); }
__device__ __forceinline__ f32x4 mod_get4(const float* mp, int i) {
  const f32x4 a = *(const f32x4*)(mp + i), b = *(const f32x4*)(mp + 3072 + i), c = *(const f32x4*)(mp + 6144 + i), d = *(const f32x4*)(mp + 9216 + i);
  f32x4 r;
#pragma unroll
  for (int e = 0; e < 4; ++e) r[e] = rbf((a[e] + b[e]) + (c[e] + d[e]));
  return r;
}
__device__ __forceinline__ f32x4 uv_get4(const float* uv, int which, int f) {
  const float* p = uv + which * F + f;
  const f32x4 a = *(const f32x4*)p, b = *(const f32x4*)(p + 2 * F), c = *(const f32x4*)(p + 4 * F), d = *(const f32x4*)(p + 6 * F);
  return (a + b) + (c + d);
}
struct UV4 { f32x4 p[4]; };
__device__ __forceinline__ UV4 uv_ld(const float* uv, int which, int f) {
  const float* p = uv + which * F + f;
  UV4 r;
#pragma unroll
  for (int q = 0; q < 4; ++q) r.p[q] = *(const f32x4*)(p + 2 * q * F);
  return r;
}
__device__ __forceinline__ f32x4 uv_sum(const UV4& r) { return (r.p[0] + r.p[1]) + (r.p[2] + r.p[3]); }
constexpr int CODE_LANES = 84;
struct AArgs {
  const u16 *x, *Are, *Aim, *Bre, *Bim, *Cre, *Cim, *logdt, *c, *wmod, *win, *wout;
  float* mod; u32* stats1; u16* xT; u16* Krev; u32x4* gran;
};
constexpr int A_RS = 528;
constexpr int A_TAB = 0;
constexpr int A_KC = A_TAB + 2 * 64 * A_RS;
constexpr int A_TILE = A_KC + 2 * 768 * 4;
constexpr int A_RED = A_TILE + 8 * 1088;
constexpr int A_CNT = A_RED + 4 * 64 * 4;
constexpr int A_LDS = A_CNT + 16;

struct KIdx { u16 v[2048]; };
constexpr int bf16_round_int(int l) {
  int sh = 0;
  while ((l >> sh) >= 256) ++sh;
  if (sh == 0) return l;
  const int q = l >> sh, rem = l & ((1 << sh) - 1), half = 1 << (sh - 1);
  const int up = (rem > half || (rem == half && (q & 1))) ? 1 : 0;
  return (q + up) << sh;
}
constexpr KIdx make_kidx() {
  KIdx t{};
  for (int x = 0; x < 2048; ++x) {
    const int lr = bf16_round_int(2047 - x);
    const int aa = lr >> 7, b = (lr >> 3) & 15, r = lr & 7;
    const int row = lr < 256 ? 8 * aa + r : lr < 512 ? 16 + 4 * (aa - 2) + (r >> 1) : lr < 1024 ? 24 + 2 * (aa - 4) + (r >> 2) : 32 + (aa - 8);
    t.v[x] = (u16)(row * 16 + b);
  }
  return t;
}
__device__ const KIdx g_kidx = make_kidx();

struct cpx { float r, i; };
__device__ __forceinline__ cpx cmul(cpx a, cpx b) { return cpx{a.r * b.r - a.i * b.i, a.r * b.i + a.i * b.r}; }

__global__ __launch_bounds__(768) void kA(AArgs a) {
  extern __shared__ __attribute__((aligned(16))) char smem[];
  const int wg = blockIdx.x, tid = threadIdx.x, lane = tid & 63;
  const int w = __builtin_amdgcn_readfirstlane(tid >> 6);
  STAMP(0, 0);
  const int tw = w - 8, st = tid - 512;
  const int t0 = 8 * ((wg & 7) * 32 + (wg >> 3));
  const bool doM = wg < 192;
  const int mrb = wg / 48, mcb = wg - 48 * mrb;
  const int rr = lane >> 3, q = lane & 7;
  u32x4 ld[14];
  u16 sv[8];
  if (w < 8) {
    const int d = 2 * wg + (w >> 2), o = d * NS + 64 * ((w >> 1) & 1) + lane;
    sv[0] = a.Are[o]; sv[1] = a.Aim[o]; sv[2] = a.Bre[o]; sv[3] = a.Bim[o]; sv[4] = a.Cre[o]; sv[5] = a.Cim[o]; sv[6] = a.logdt[d]; sv[7] = 0;
    ld[0] = *(const u32x4*)(g_kidx.v + 8 * (tid & 255));
  } else {
#pragma unroll
    for (int j = 0; j < 2; ++j) ld[j] = *(const u32x4*)(a.x + (size_t)(t0 + 2 * tw + j) * D + 8 * lane);
    if (tw == 0 && lane < 4) *(u32*)(smem + A_CNT + 4 * lane) = 0;
    asm volatile("s_waitcnt lgkmcnt(0)" ::: "memory");
  }
  SCHED_FENCE();
  STAMP(0, 9);
  __builtin_amdgcn_s_barrier();
  STAMP(0, 10);
  SCHED_FENCE();
  if (w < 8) {
    const int ch = w >> 2, nh = (w >> 1) & 1, part = (w ^ ch) & 1, d = 2 * wg + ch;
    const int n = 64 * nh + lane;
    const u16 hare = sv[0], haim = sv[1], hbre = sv[2], hbim = sv[3], hcre = sv[4], hcim = sv[5], hdt = sv[6];
    const u32x4 kix = ld[0];
    asm volatile("" ::"v"((u32)hare), "v"((u32)hcim), "v"((u32)hdt));
    STAMP(0, 11);
    const float dt = rbf(__expf(bf1(hdt)));
    const float lr = (dt * bf1(hare)) * 1.4426950408889634f, lp = (dt * bf1(haim)) * 0.15915494309189535f;
    auto cexp = [&](float l) -> cpx {
      const float e = __builtin_amdgcn_exp2f(lr * l), ph = __builtin_amdgcn_fractf(lp * l);
      return cpx{e * __builtin_amdgcn_cosf(ph), e * __builtin_amdgcn_sinf(ph)};
    };
    char* tab = smem + A_TAB + ch * (64 * A_RS) + n * 4;
    auto put = [&](int row, cpx v) { *(u32*)(tab + row * A_RS) = pk_bf16(v.r, v.i); };
    if (part == 0) {
      const cpx E1 = cexp(1.f), E128 = cexp(128.f), E512 = cexp(512.f);
      const cpx E2 = cmul(E1, E1), E3 = cmul(E2, E1), E4 = cmul(E2, E2), E5 = cmul(E4, E1), E6 = cmul(E4, E2), E7 = cmul(E4, E3);
      put(0, cpx{1.f, 0.f}); put(1, E1); put(2, E2); put(3, E3); put(4, E4); put(5, E5); put(6, E6); put(7, E7);
      put(8, E128); put(9, cmul(E128, E1)); put(10, cmul(E128, E2)); put(11, cmul(E128, E3));
      put(12, cmul(E128, E4)); put(13, cmul(E128, E5)); put(14, cmul(E128, E6)); put(15, cmul(E128, E7));
      const cpx E256 = cmul(E128, E128), E384 = cmul(E256, E128);
      put(16, E256); put(17, cmul(E256, E2)); put(18, cmul(E256, E4)); put(19, cmul(E256, E6));
      put(20, E384); put(21, cmul(E384, E2)); put(22, cmul(E384, E4)); put(23, cmul(E384, E6));
      const cpx E640 = cmul(E512, E128), E768 = cmul(E512, E256), E896 = cmul(E512, E384);
      put(24, E512); put(25, cmul(E512, E4)); put(26, E640); put(27, cmul(E640, E4));
      put(28, E768); put(29, cmul(E768, E4)); put(30, E896); put(31, cmul(E896, E4));
    } else {
      const cpx E8 = cexp(8.f), E128 = cexp(128.f), E1024 = cexp(1024.f);
      const float br = dt * bf1(hbre), bi = dt * bf1(hbim), cr = bf1(hcre), ci = bf1(hcim);
      const cpx W = cmul(cpx{cr, ci}, cpx{br, bi});
      const cpx E16 = cmul(E8, E8), E32 = cmul(E16, E16), E64 = cmul(E32, E32);
      cpx Y[8];
      Y[0] = W; Y[1] = cmul(W, E8); Y[2] = cmul(W, E16); Y[3] = cmul(Y[1], E16);
#pragma unroll
      for (int b = 0; b < 4; ++b) Y[4 + b] = cmul(Y[b], E32);
#pragma unroll
      for (int b = 0; b < 8; ++b) {
        put(48 + b, cpx{Y[b].r, -Y[b].i});
        const cpx t = cmul(Y[b], E64);
        put(56 + b, cpx{t.r, -t.i});
      }
      const cpx E256 = cmul(E128, E128), E512 = cmul(E256, E256);
      cpx P[4];
      P[0] = E1024; P[1] = cmul(E1024, E128); P[2] = cmul(E1024, E256); P[3] = cmul(P[1], E256);
#pragma unroll
      for (int b = 0; b < 4; ++b) { put(32 + b, P[b]); put(36 + b, cmul(P[b], E512)); }
      put(40, cmul(E1024, E1024));
    }
    STAMP(0, 1);
    lds_rendezvous((u32 LAS*)(smem + A_CNT), 8);
    STAMP(0, 2);
    if (w < 6) {
      const int c2 = w >= 3 ? 1 : 0, t = w - 3 * c2, i = lane & 15, g = lane >> 4;
      const char* tb = smem + A_TAB + c2 * (64 * A_RS) + 16 * g;
      const char* pa = tb + (16 * t + i) * A_RS, *pb = tb + (48 + i) * A_RS;
      f32x4 acc = {0, 0, 0, 0};
      bf16x8 Af[8], Bf[8];
#pragma unroll
      for (int s = 0; s < 8; ++s) {
        Af[s] = *(const bf16x8*)(pa + s * 64); Bf[s] = *(const bf16x8*)(pb + s * 64);
      }
#pragma unroll
      for (int s = 0; s < 8; ++s) acc = __builtin_amdgcn_mfma_f32_16x16x32_bf16(Af[s], Bf[s], acc, 0, 0, 0);
      float* Kc = (float*)(smem + A_KC) + c2 * 768 + t * 256;
#pragma unroll
      for (int r = 0; r < 4; ++r) Kc[(4 * g + r) * 16 + i] = acc[r];
    }
    STAMP(0, 3);
    lds_rendezvous((u32 LAS*)(smem + A_CNT + 4), 8);
    STAMP(0, 4);
    const float* Kc = (const float*)(smem + A_KC) + ch * 768;
    float kv[8];
#pragma unroll
    for (int j = 0; j < 4; ++j) { kv[2 * j] = Kc[kix[j] & 0xffffu]; kv[2 * j + 1] = Kc[kix[j] >> 16]; }
    wt_store16(make_rsrc(a.Krev, D * L * 2), (u32)(d * L + 8 * (tid & 255)) * 2,
               u32x4{pk_bf16(kv[0], kv[1]), pk_bf16(kv[2], kv[3]), pk_bf16(kv[4], kv[5]), pk_bf16(kv[6], kv[7])});
    STAMP(0, 5);
  } else {
    __builtin_amdgcn_s_setprio(3);
    const u32x4 xr[2] = {ld[0], ld[1]};
    asm volatile("" ::"v"(xr[1]));
    STAMP(0, 11);
    char* tile = smem + A_TILE;
#pragma unroll
    for (int j = 0; j < 2; ++j) *(u32x4*)(tile + (2 * tw + j) * 1088 + 16 * lane) = xr[j];
    SCHED_FENCE();
    if (doM) {
#pragma unroll
      for (int p = 0; p < 4; ++p) ld[2 + p] = *(const u32x4*)(a.wmod + (size_t)(128 * mrb + p * 32 + tw * 8 + rr) * 3072 + 64 * mcb + 8 * q);
#pragma unroll
      for (int p = 0; p < 4; ++p) sv[p] = a.c[128 * mrb + p * 32 + tw * 8 + rr];
    }
    ld[10] = *(const u32x4*)(a.win + (size_t)wg * 3584 + st * 8);
    ld[11] = *(const u32x4*)(a.win + (size_t)wg * 3584 + (st < 192 ? 2048 + st * 8 : 0));
    ld[12] = *(const u32x4*)(a.wout + (size_t)wg * 1792 + (st < 224 ? st * 8 : 0));
    u64 base;
    asm volatile("s_getpc_b64 s[40:41]\n\ts_add_u32 s40, s40, _Z2kB5BArgs@rel32@lo+4\n\ts_addc_u32 s41, s41, _Z2kB5BArgs@rel32@hi+12\n\ts_mov_b64 %0, s[40:41]"
                 : "=s"(base) : : "s40", "s41", "scc");
    ld[13] = *(const u32x4*)((const char*)base + (size_t)(st < CODE_LANES ? (wg >> 3) * CODE_LANES + st : 0) * 16);
    SCHED_FENCE();
    STAMP(0, 1);
    lds_rendezvous((u32 LAS*)(smem + A_CNT + 8), 4);
    STAMP(0, 2);
#pragma unroll
    for (int ps = 0; ps < 2; ++ps) {
      const int d0 = 128 * tw + 64 * ps + 16 * (lane >> 4);
      const char LAS* src = (const char LAS*)tile + ((lane & 15) >> 2) * 1088 + 2 * (d0 + 4 * (lane & 3));
      const s16x4 lo = tr_read(src), hi = tr_read(src + 4 * 1088);
      u32x2 a0 = __builtin_bit_cast(u32x2, lo), a1 = __builtin_bit_cast(u32x2, hi);
      *(u32x4*)(a.xT + xt_off(d0 + (lane & 15), t0)) = u32x4{a0[0], a0[1], a1[0], a1[1]};
    }
    STAMP(0, 6);
    if (tw == 0) a.gran[wg * 64 + lane] = u32x4{0, 0, 0, 0};
    if (tw == 1 && wg < 8) a.gran[(256 + wg) * 64 + lane] = u32x4{0, 0, 0, 0};
    {
      float v[2][8], s[2];
#pragma unroll
      for (int j = 0; j < 2; ++j) {
#pragma unroll
        for (int k = 0; k < 4; ++k) { v[j][2 * k] = bf_lo(xr[j][k]); v[j][2 * k + 1] = bf_hi(xr[j][k]); }
        s[j] = ((v[j][0] + v[j][1]) + (v[j][2] + v[j][3])) + ((v[j][4] + v[j][5]) + (v[j][6] + v[j][7]));
      }
      float mean[2], q2[2];
#pragma unroll
      for (int j = 0; j < 2; ++j) mean[j] = wave_sum(s[j]) * (1.f / D);
#pragma unroll
      for (int j = 0; j < 2; ++j) {
        q2[j] = 0;
#pragma unroll
        for (int k = 0; k < 8; ++k) { const float dv = v[j][k] - mean[j]; q2[j] += dv * dv; }
      }
#pragma unroll
      for (int j = 0; j < 2; ++j) {
        const float var = wave_sum(q2[j]) * (1.f / D);
        const float sd = rbf(__builtin_amdgcn_sqrtf(rbf(rbf(var) + 1e-5f)));
        if (lane == 0) *(f32x2*)(a.stats1 + 2 * (t0 + 2 * tw + j)) = f32x2{rbf(mean[j]), __builtin_amdgcn_rcpf(sd)};
      }
    }
    STAMP(0, 7);
    float* red = (float*)(smem + A_RED);
    if (doM) {
      asm volatile("" ::"v"(ld[5]));
      STAMP(0, 8);
      float acc[8] = {0, 0, 0, 0, 0, 0, 0, 0};
#pragma unroll
      for (int p = 0; p < 4; ++p) {
        const float cx = bf1(sv[p]);
        const float s = rbf(cx * __builtin_amdgcn_rcpf(1.f + __expf(-cx)));
#pragma unroll
        for (int k = 0; k < 4; ++k) { acc[2 * k] += s * bf_lo(ld[2 + p][k]); acc[2 * k + 1] += s * bf_hi(ld[2 + p][k]); }
      }
#pragma unroll
      for (int k = 0; k < 8; ++k) {
        float v = acc[k];
        v += __int_as_float(__builtin_amdgcn_update_dpp(0, __float_as_int(v), 0x128, 0xf, 0xf, true));
        v = xor32_sum(xor16_sum(v));
        if (rr == 0) red[tw * 64 + 8 * q + k] = v;
      }
      STAMP(0, 3);
      lds_rendezvous((u32 LAS*)(smem + A_CNT + 12), 4);
      STAMP(0, 4);
      if (tw == 0) a.mod[3072 * mrb + 64 * mcb + lane] = (red[lane] + red[64 + lane]) + (red[128 + lane] + red[192 + lane]);
    }
#pragma unroll
    for (int k = 10; k < 14; ++k) asm volatile("" ::"v"(ld[k]));
    STAMP(0, 5);
  }
}
constexpr int F_CODE_LANES = 72;
struct BArgs {
  const u16 *xT, *Krev, *Dskip, *win, *wout, *wsp;
  const u32* stats1; const float* mod;
  u16 *x1T, *Wt, *Wot; float* uv;
};
constexpr int B_H = 0;
constexpr int B_HS = (256 + 2048) * 2;
constexpr int B_KC = B_H + 2 * B_HS;
constexpr int B_KS = 4160;
constexpr int B_PART = B_KC + 2 * 4 * B_KS;
constexpr int B_RED = B_PART + 2 * 4 * 8 * 1280;
constexpr int B_STG = B_RED + 4 * 64 * 4;
constexpr int B_CNT = B_STG + 4 * 2048;
constexpr int B_LDS = B_CNT + 32;

__global__ __launch_bounds__(768) void kB(BArgs a) {
  extern __shared__ __attribute__((aligned(16))) char smem[];
  const int wg = blockIdx.x, tid = threadIdx.x, lane = tid & 63;
  const int w = __builtin_amdgcn_readfirstlane(tid >> 6);
  STAMP(1, 0);
  if (w < 8) {
    __builtin_amdgcn_s_setprio(2);
    const int ch = w >> 2, q = w & 3, d = 2 * wg + ch, tj = tid & 255;
    const u32x4 xv = *(const u32x4*)(a.xT + xt_off(d, 8 * tj));
    f32x4 st[4];
#pragma unroll
    for (int k = 0; k < 4; ++k) st[k] = *(const f32x4*)(a.stats1 + 16 * tj + 4 * k);
    const u32x4 k0 = *(const u32x4*)(a.Krev + (size_t)d * L + 8 * tj);
    const u32x4 k1r = *(const u32x4*)(a.Krev + (size_t)d * L + 8 * (tj < 255 ? tj + 1 : tj));
    const float sa = mod_get(a.mod, d), Ac = rbf(1.f + mod_get(a.mod, 512 + d)), ga = mod_get(a.mod, 1024 + d), dsk = bf1(a.Dskip[d]);
    SCHED_FENCE();
    __builtin_amdgcn_s_barrier();
    SCHED_FENCE();
    char* hb = smem + B_H + ch * B_HS;
    char* kc = smem + B_KC + ch * 4 * B_KS;
    if (tj < 32) *(u32x4*)(hb + 16 * tj) = u32x4{0, 0, 0, 0};
    if (tj < 16) *(u32x4*)(kc + (tj >> 2) * B_KS + 16 * (256 + (tj & 3))) = u32x4{0, 0, 0, 0};
    {
      const u32 z = tj < 255 ? 0xffffffffu : 0u;
      const u32 wd[8] = {k0[0], k0[1], k0[2], k0[3], k1r[0] & z, k1r[1] & z, 0, 0};
      u32 od[5];
#pragma unroll
      for (int k = 0; k < 5; ++k) od[k] = __builtin_amdgcn_alignbit(wd[k + 1], wd[k], 16);
      *(u32x4*)(kc + 0 * B_KS + 16 * tj) = u32x4{wd[0], wd[1], wd[2], wd[3]};
      *(u32x4*)(kc + 1 * B_KS + 16 * tj) = u32x4{od[0], od[1], od[2], od[3]};
      *(u32x4*)(kc + 2 * B_KS + 16 * tj) = u32x4{wd[1], wd[2], wd[3], wd[4]};
      *(u32x4*)(kc + 3 * B_KS + 16 * tj) = u32x4{od[1], od[2], od[3], od[4]};
    }
    u32x4 hv;
    {
      float hh[8];
#pragma unroll
      for (int k = 0; k < 8; ++k) {
        const float m = st[k >> 1][2 * (k & 1)], ri = st[k >> 1][2 * (k & 1) + 1];
        const float xx = (k & 1) ? bf_hi(xv[k >> 1]) : bf_lo(xv[k >> 1]);
        hh[k] = (xx - m) * (ri * Ac) + sa;
      }
      hv = u32x4{pk_bf16(hh[0], hh[1]), pk_bf16(hh[2], hh[3]), pk_bf16(hh[4], hh[5]), pk_bf16(hh[6], hh[7])};
    }
    *(u32x4*)(hb + 512 + 16 * tj) = hv;
    STAMP(1, 1);
    lds_rendezvous((u32 LAS*)(smem + B_CNT + 12 + 4 * 0) + ch * 2, 4);
    STAMP(1, 2);
    const int c = lane & 15, g = lane >> 4;
    const char* bbase = hb + 2 * (256 + 16 * c - 16 * (g >> 1) + 8 * (g & 1) - 32 * q) - 256;
    const int X = 2047 - 32 * q - 16 * (g >> 1) + 8 * (g & 1) - c;
    const char* abase = kc + (X & 3) * B_KS + 2 * (X & ~3) - 256 * 15;
    const char LAS* ab0 = (const char LAS*)abase;
    bf16x8 Bf[16], Af[16];
    auto lda = [&](int u) {
      const u32x2 lo = *(const volatile u32x2 LAS*)(ab0 + 256 * (15 - u)), hi = *(const volatile u32x2 LAS*)(ab0 + 256 * (15 - u) + 8);
      Af[u] = __builtin_bit_cast(bf16x8, u32x4{lo[0], lo[1], hi[0], hi[1]});
    };
    lda(0);
#pragma unroll
    for (int v = 1; v < 16; v += 2) Bf[v] = *(const bf16x8*)(bbase + 256 * v);
    lda(1);
#pragma unroll
    for (int v = 0; v < 16; v += 2) Bf[v] = *(const bf16x8*)(bbase + 256 * v);
    lda(2); lda(3);
    SCHED_FENCE();
    f32x4 acc[8];
#pragma unroll
    for (int i = 0; i < 8; ++i) acc[i] = f32x4{0, 0, 0, 0};
#pragma unroll
    for (int u = 0; u < 16; ++u) {
      if (u + 4 < 16) lda(u + 4);
      SCHED_FENCE();
#pragma unroll
      for (int i = u >> 1; i < 8; ++i) acc[i] = __builtin_amdgcn_mfma_f32_16x16x32_bf16(Af[u], Bf[2 * i - u + 1], acc[i], 0, 0, 0);
      SCHED_FENCE();
    }
    STAMP(1, 3);
    char* part = smem + B_PART + ch * (4 * 8 * 1280);
#pragma unroll
    for (int i = 0; i < 8; ++i) *(f32x4*)(part + (q * 8 + i) * 1280 + 16 * (20 * g + c)) = acc[i];
    lds_rendezvous((u32 LAS*)(smem + B_CNT + 4 * ch), 4);
    STAMP(1, 4);
    {
      const int i = tj >> 5, j5 = tj & 31, cc = j5 >> 1;
      float y[8];
#pragma unroll
      for (int hh = 0; hh < 2; ++hh) {
        const char* p = part + i * 1280 + 16 * (20 * (2 * (j5 & 1) + hh) + cc);
        const f32x4 p0 = *(const f32x4*)(p), p1 = *(const f32x4*)(p + 8 * 1280), p2 = *(const f32x4*)(p + 16 * 1280), p3 = *(const f32x4*)(p + 24 * 1280);
#pragma unroll
        for (int r = 0; r < 4; ++r) y[4 * hh + r] = (p0[r] + p1[r]) + (p2[r] + p3[r]);
      }
      float o[8];
#pragma unroll
      for (int k = 0; k < 8; ++k) {
        const float hx = (k & 1) ? bf_hi(hv[k >> 1]) : bf_lo(hv[k >> 1]);
        const float xx = (k & 1) ? bf_hi(xv[k >> 1]) : bf_lo(xv[k >> 1]);
        o[k] = xx + ga * (y[k] + hx * dsk);
      }
      wt_store16(make_rsrc(a.x1T, D * L * 2), (u32)xt_off(d, 8 * tj) * 2, u32x4{pk_bf16(o[0], o[1]), pk_bf16(o[2], o[3]), pk_bf16(o[4], o[5]), pk_bf16(o[6], o[7])});
    }
    STAMP(1, 5);
  } else {
#define B_SIDE_PRIO 3
    __builtin_amdgcn_s_setprio(B_SIDE_PRIO);
    const int st = tid - 512, sw = w - 8;
    if (st < 8) *(u32*)(smem + B_CNT + 4 * st) = 0;
    asm volatile("s_waitcnt lgkmcnt(0)" ::: "memory");
    __builtin_amdgcn_s_barrier();
    SCHED_FENCE();
    float* red = (float*)(smem + B_RED);
    const bool act = wg < 224;
    const int dq = wg / 56, f0 = 32 * (wg - 56 * dq);
    const int d8 = 16 * dq + (st >> 4), fp = st & 15;
    const int f8 = wg >> 1, dp = (wg & 1) * 128 + (st & 127);
    u32 wv[8], ov[8];
    f32x4 cb0, cb1, sb0, sb1;
    if (act) {
#pragma unroll
      for (int j = 0; j < 8; ++j) wv[j] = *(const u32*)(a.win + (size_t)(8 * d8 + j) * F + f0 + 2 * fp);
      cb0 = mod_get4(a.mod, 2048 + 8 * d8); cb1 = mod_get4(a.mod, 2048 + 8 * d8 + 4);
      sb0 = mod_get4(a.mod, 1536 + 8 * d8); sb1 = mod_get4(a.mod, 1536 + 8 * d8 + 4);
      if (st < 128) {
#pragma unroll
        for (int j = 0; j < 8; ++j) ov[j] = *(const u32*)(a.wout + (size_t)(8 * f8 + j) * D + 2 * dp);
      }
    }
    u32x4 pf[4];
    {
#pragma unroll
      for (int j = 0; j < 4; ++j) {
        const int idx = st + 256 * j;
        pf[j] = *(const u32x4*)(a.wsp + (size_t)(8 * wg + (idx >> 7)) * L + ((idx >> 5) & 3) * 512 + 256 + (idx & 31) * 8);
      }
    }
    SCHED_FENCE();
    if (act) {
      float lo[8], hi[8], u0 = 0, u1 = 0, v0 = 0, v1 = 0;
#pragma unroll
      for (int j = 0; j < 8; ++j) {
        const float Ac = rbf(1.f + (j < 4 ? cb0[j] : cb1[j - 4])), sb = j < 4 ? sb0[j] : sb1[j - 4];
        const float wl = bf_lo(wv[j]), wh = bf_hi(wv[j]);
        lo[j] = rbf(Ac * wl); hi[j] = rbf(Ac * wh);
        u0 += lo[j]; u1 += hi[j]; v0 += sb * wl; v1 += sb * wh;
      }
      u32x4* stg = (u32x4*)(smem + B_STG + sw * 2048);
      stg[2 * lane] = u32x4{pk_bf16(lo[0], lo[1]), pk_bf16(lo[2], lo[3]), pk_bf16(lo[4], lo[5]), pk_bf16(lo[6], lo[7])};
      stg[2 * lane + 1] = u32x4{pk_bf16(hi[0], hi[1]), pk_bf16(hi[2], hi[3]), pk_bf16(hi[4], hi[5]), pk_bf16(hi[6], hi[7])};
      const rsrc_t rw = make_rsrc(a.Wt, D * F * 2);
#pragma unroll
      for (int k = 0; k < 2; ++k) {
        const int pc = 64 * k + lane, dd8 = 16 * dq + 4 * sw + (pc >> 5), col = f0 + (pc & 31);
        wt_store16(rw, (u32)((((col >> 4) * 64 + dd8) * 16 + (col & 15)) * 16), stg[pc]);
      }
      float rv[4] = {u0, u1, v0, v1};
#pragma unroll
      for (int k = 0; k < 4; ++k) {
        const float v = xor32_sum(xor16_sum(rv[k]));
        if (lane < 16) red[sw * 64 + lane * 4 + k] = v;
      }
      if (st < 128) {
        u32x4 e, o;
#pragma unroll
        for (int k = 0; k < 4; ++k) {
          e[k] = (ov[2 * k] & 0xffffu) | (ov[2 * k + 1] << 16);
          o[k] = (ov[2 * k] >> 16) | (ov[2 * k + 1] & 0xffff0000u);
        }
        stg[2 * lane] = e; stg[2 * lane + 1] = o;
        const rsrc_t ro = make_rsrc(a.Wot, FH * D * 2);
#pragma unroll
        for (int k = 0; k < 2; ++k) {
          const int col = (wg & 1) * 256 + 128 * sw + 64 * k + lane;
          wt_store16(ro, (u32)((((col >> 4) * 112 + f8) * 16 + (col & 15)) * 16), stg[64 * k + lane]);
        }
      }
      lds_rendezvous((u32 LAS*)(smem + B_CNT + 8), 4);
      if (st < 64) {
        const float s = (red[st] + red[64 + st]) + (red[128 + st] + red[192 + st]);
        const int fq = st >> 2, k = st & 3;
        a.uv[(dq * 2 + (k >> 1)) * F + f0 + 2 * fq + (k & 1)] = s;
      }
    }
    u32x4 cf;
    {
      u64 base;
      asm volatile("s_getpc_b64 s[40:41]\n\ts_add_u32 s40, s40, _Z2kF5FArgs@rel32@lo+4\n\ts_addc_u32 s41, s41, _Z2kF5FArgs@rel32@hi+12\n\ts_mov_b64 %0, s[40:41]"
                   : "=s"(base) : : "s40", "s41", "scc");
      cf = *(const u32x4*)((const char*)base + (size_t)(st < F_CODE_LANES ? (wg >> 3) * F_CODE_LANES + st : 0) * 16);
    }
    STAMP(1, 1);
    asm volatile("" ::"v"(cf));
#pragma unroll
    for (int j = 0; j < (int)(sizeof(pf) / sizeof(pf[0])); ++j) asm volatile("" ::"v"(pf[j]));
    STAMP(1, 5);
  }
}
struct FArgs {
  const u16 *x1T, *Wt, *wsp, *bsp, *Wot; const float *uv, *mod;
  u16 *z2t, *Pt, *out; u64* gran; u32* flags;
};
constexpr int F_X = 0;
constexpr int F_ST = 65536;
constexpr int F_MR = F_ST + 512;
constexpr int F_PS = F_MR + 8 * 512;
constexpr int F_FS = F_PS + 7 * 64 * 8;
constexpr int F_ZS = F_FS + 512;
constexpr int F_RS = 232;
constexpr int F_A0 = 90112;
constexpr int F_PF = F_A0 + 8 * 8192;
constexpr int F_LDS = F_PF + 1024;
constexpr int F_EX = 112 * 1024;
#define F_SLEEP0 16
#define F_SLEEP1 8
#define F_BDEPTH 3
constexpr int F_BD = F_BDEPTH;
constexpr int F_BR = F_BD;
static_assert(F_ZS + 64 * F_RS <= F_A0 && F_EX + 64 * 132 <= F_LDS, "LDS map");

__device__ __forceinline__ float gelu_tanh(float z) {
  const float k0 = -2.f * 0.7978845608028654f * 1.4426950408889634f, k1 = k0 * 0.044715f;
  const float e = __builtin_amdgcn_exp2f(z * (k0 + k1 * (z * z)));
  return z * __builtin_amdgcn_rcpf(1.f + e);
}
__device__ __forceinline__ float pick4(f32x4 v, int r) { return r == 0 ? v[0] : r == 1 ? v[1] : r == 2 ? v[2] : v[3]; }
template <int N> __device__ __forceinline__ void vm_wait_c() { asm volatile("s_waitcnt vmcnt(%0)" ::"i"(N > 63 ? 63 : N) : "memory"); }
__device__ __forceinline__ u32 f_slot(int kq, int sg) { return (u32)(((sg & 2) ? 0 : F_A0) + (kq * 2 + (sg & 1)) * 8192); }
__device__ __forceinline__ bf16x8 ld_dev16(rsrc_t r, u32 off) { return __builtin_bit_cast(bf16x8, __builtin_amdgcn_raw_buffer_load_b128(r, off, 0, 16)); }
__device__ __forceinline__ void f_lda(const u16* const (&pa)[2], u32 lds0, int kq, int nh, int sg) {
#pragma unroll
  for (int i = 0; i < 4; ++i) glds16(pa[i & 1] + (size_t)(i >> 1) * 16 * L + 64 * sg, lds0 + f_slot(kq, sg) + (4 * nh + i) * 1024);
}
__device__ __forceinline__ void f_lda_full(const u16* wsp, u32 lds0, int pi, int kq, int sg) {
  const int lane = threadIdx.x & 63;
#pragma unroll
  for (int i = 0; i < 8; ++i) {
    const int row = 8 * i + (lane >> 3);
    glds16(wsp + (size_t)(64 * pi + row) * L + 512 * kq + 64 * sg + 8 * ((lane & 7) ^ ((row >> 1) & 7)), lds0 + f_slot(kq, sg) + i * 1024);
  }
}
__device__ __forceinline__ void f_ldx(const u16* x1T, u32 lds0, int w, int pi, int sg) {
  const int lane = threadIdx.x & 63;
#pragma unroll
  for (int i = 0; i < 2; ++i) {
    const int d = 128 * sg + 16 * w + 8 * i + (lane >> 3);
    const int fz = (((d >> 1) & 1) | ((d >> 2) & 2)) << 1;
    glds16(x1T + xt_off(d, 64 * pi) + 8 * ((lane & 7) ^ fz), lds0 + F_X + (128 * sg + 16 * w + 8 * i) * 128);
  }
}
__device__ __forceinline__ void wait_flags(const u32* p, int n) {
  const int lane = threadIdx.x & 63;
  for (u32 spin = 0; spin < (1u << 22); ++spin) {
    const u32 v = __hip_atomic_load((GAS u32*)p + (lane < n ? lane : 0), __ATOMIC_RELAXED, __HIP_MEMORY_SCOPE_AGENT);
    if (__all(v == 1u)) break;
    __builtin_amdgcn_s_sleep(1);
  }
}

__device__ __forceinline__ u32 poll8(const u32* p, int n) {
  const int lane = threadIdx.x & 63;
  const u32 v = __hip_atomic_load((GAS u32*)p + (lane < n ? lane : 0), __ATOMIC_RELAXED, __HIP_MEMORY_SCOPE_AGENT);
  return (u32)__ballot(v == 1u) & 0xffu;
}
__device__ __forceinline__ void need_flag(const u32* p, int n, u32& mask, int k) {
  if (!((mask >> k) & 1u)) {
    for (u32 spin = 0; spin < (1u << 22); ++spin) {
      __builtin_amdgcn_s_sleep(F_SLEEP1);
      mask = poll8(p, n);
      if ((mask >> k) & 1u) break;
    }
  }
  asm volatile("" ::: "memory");
}

struct Z1 { f32x4 y[4]; };

template <int ROLE>
__device__ __forceinline__ void f_phase_c(const FArgs& a, char* smem, const int w, const int pi, const int j, const u16* const (&pa)[2], Z1& z1) {
  const int tid = threadIdx.x, lane = tid & 63;
  const int c = lane & 15, g = lane >> 4;
  const u32 lds0 = (u32)(size_t)(const char LAS*)smem;
  const int nt2 = 56 + 7 * j + (w < 7 ? w : 0), nt1 = 7 * j + (w > 0 ? w - 1 : 0);
  const u16* pw2 = a.Wt + (size_t)nt2 * 64 * 128 + lane * 8;
  const u16* pw1 = a.Wt + (size_t)nt1 * 64 * 128 + lane * 8;
  const int q4 = (lane & 15) >> 2, p4 = lane & 3;
  const int drow = 8 * g + q4;
  const int fz0 = (((drow >> 1) & 1) | ((drow >> 2) & 2)) << 1;
  const char LAS* xb = (const char LAS*)smem + F_X + drow * 128 + 8 * (p4 & 1);
  const char LAS* xt[4];
  int tb[4];
#pragma unroll
  for (int s = 0; s < 4; ++s) { tb[s] = (s + w) & 3; xt[s] = xb + 16 * ((2 * tb[s] + (p4 >> 1)) ^ fz0); }
  auto xfrag = [&](int s, int ks) -> bf16x8 {
    const char LAS* p = xt[s] + ks * 4096;
    const s16x4 lo = tr_read(p), hi = tr_read(p + 512);
    const u32x2 a0 = __builtin_bit_cast(u32x2, lo), a1 = __builtin_bit_cast(u32x2, hi);
    return __builtin_bit_cast(bf16x8, u32x4{a0[0], a0[1], a1[0], a1[1]});
  };
  f32x4 acc[4];
#pragma unroll
  for (int s = 0; s < 4; ++s) acc[s] = f32x4{0, 0, 0, 0};
  bf16x8 W1[16];
  f32x4 u2, v2;
  UV4 ru1, rv1;
  float* xs2 = (float*)(smem + F_ST);
  if constexpr (ROLE < 2) {
    bf16x8 W2[16];
#pragma unroll
    for (int ks = 0; ks < 4; ++ks) W2[ks] = *(const bf16x8*)(pw2 + 4 * ks * 128);
    SCHED_FENCE();
    f_ldx(a.x1T, lds0, w, pi, 1);
#pragma unroll
    for (int ks = 4; ks < 8; ++ks) W2[ks] = *(const bf16x8*)(pw2 + 4 * ks * 128);
    SCHED_FENCE();
    VM_WAIT(10);
    __builtin_amdgcn_s_barrier();
    SCHED_FENCE();
    STAMP(2, 1);
    f32x4 sq = {0, 0, 0, 0};
    UV4 ru2, rv2;
    bf16x8 X[2][4];
#pragma unroll
    for (int s = 0; s < 4; ++s) X[0][s] = xfrag(s, 0);
#pragma unroll
    for (int ks = 0; ks < 16; ++ks) {
      if (ks == 3) { VM_WAIT(9); __builtin_amdgcn_s_barrier(); }
      if (ks == 7) { VM_WAIT(9); __builtin_amdgcn_s_barrier(); }
      if (ks == 11) { VM_WAIT(4); __builtin_amdgcn_s_barrier(); }
      if (ks == 0) f_ldx(a.x1T, lds0, w, pi, 2);
      if (ks == 4) f_ldx(a.x1T, lds0, w, pi, 3);
      if (ks < 8) W2[8 + ks] = *(const bf16x8*)(pw2 + 4 * (8 + ks) * 128);
      if (ks == 13) { ru2 = uv_ld(a.uv, 0, 16 * nt2 + 4 * g); rv2 = uv_ld(a.uv, 1, 16 * nt2 + 4 * g); }
      SCHED_FENCE();
#pragma unroll
      for (int s = 0; s < 4; ++s) {
        if (ks + 1 < 16) X[(ks + 1) & 1][s] = xfrag(s, ks + 1);
        acc[s] = __builtin_amdgcn_mfma_f32_16x16x32_bf16(W2[ks], X[ks & 1][s], acc[s], 0, 0, 0);
        if (s == 3) sq = __builtin_amdgcn_mfma_f32_16x16x32_bf16(X[ks & 1][0], X[ks & 1][0], sq, 0, 0, 0);
        SCHED_FENCE();
      }
    }
    u2 = uv_sum(ru2); v2 = uv_sum(rv2);
    if (w < 4 && (c >> 2) == g) xs2[64 + 16 * w + c] = pick4(sq, c & 3);
  } else {
    f_ldx(a.x1T, lds0, w, pi, 1);
    VM_WAIT(2);
    __builtin_amdgcn_s_barrier();
    SCHED_FENCE();
    STAMP(2, 1);
    const u32x4 ones = {0x3f803f80u, 0x3f803f80u, 0x3f803f80u, 0x3f803f80u};
    bf16x8 X[2][4];
#pragma unroll
    for (int s = 0; s < 4; ++s) X[0][s] = xfrag(s, 0);
#pragma unroll
    for (int ks = 0; ks < 16; ++ks) {
      if (ks == 3) { VM_WAIT(2); __builtin_amdgcn_s_barrier(); }
      if (ks == 7) { VM_WAIT(2); __builtin_amdgcn_s_barrier(); }
      if (ks == 11) { VM_WAIT(6); __builtin_amdgcn_s_barrier(); }
      if (ks == 0) f_ldx(a.x1T, lds0, w, pi, 2);
      if (ks == 4) f_ldx(a.x1T, lds0, w, pi, 3);
      if (ks >= 8) { W1[2 * ks - 16] = *(const bf16x8*)(pw1 + 4 * (2 * ks - 16) * 128); W1[2 * ks - 15] = *(const bf16x8*)(pw1 + 4 * (2 * ks - 15) * 128); }
      SCHED_FENCE();
#pragma unroll
      for (int s = 0; s < 4; ++s) {
        if (ks + 1 < 16) X[(ks + 1) & 1][s] = xfrag(s, ks + 1);
        acc[s] = __builtin_amdgcn_mfma_f32_16x16x32_bf16(__builtin_bit_cast(bf16x8, ones), X[ks & 1][s], acc[s], 0, 0, 0);
        SCHED_FENCE();
      }
    }
    if (g == 0) {
#pragma unroll
      for (int s = 0; s < 4; ++s) xs2[16 * tb[s] + c] = acc[s][0];
    }
  }
  STAMP(2, 2);
  __syncthreads();
  STAMP(2, 3);
  float mu[4], ri[4];
  {
    const float mean = xs2[lane] * (1.f / D);
    const float var = fmaxf(xs2[64 + lane] * (1.f / D) - mean * mean, 0.f);
    f32x2* mr = (f32x2*)(smem + F_MR + w * 512);
    mr[lane] = f32x2{rbf(mean), __builtin_amdgcn_rcpf(rbf(__builtin_amdgcn_sqrtf(rbf(rbf(var) + 1e-5f))))};
#pragma unroll
    for (int s = 0; s < 4; ++s) { const f32x2 v = mr[16 * tb[s] + c]; mu[s] = v[0]; ri[s] = v[1]; }
  }
  u32x2 z2p[4];
  if constexpr (ROLE < 2) {
    float* ps = (float*)(smem + F_PS) + w * 128;
    const s16x4 ones4 = {0x3f80, 0x3f80, 0x3f80, 0x3f80};
#pragma unroll
    for (int s = 0; s < 4; ++s) {
      if constexpr (ROLE == 1) {
#pragma unroll
        for (int ks = 4 * s; ks < 4 * s + 4; ++ks) W1[ks] = *(const bf16x8*)(pw1 + 4 * ks * 128);
        SCHED_FENCE();
      }
      float z[4];
#pragma unroll
      for (int r = 0; r < 4; ++r) z[r] = gelu_tanh(ri[s] * (acc[s][r] - mu[s] * u2[r]) + v2[r]);
      z2p[s] = u32x2{pk_bf16(z[0], z[1]), pk_bf16(z[2], z[3])};
      const s16x4 zz = __builtin_bit_cast(s16x4, z2p[s]);
      const f32x4 zero = {0, 0, 0, 0};
      const f32x4 s1 = __builtin_amdgcn_mfma_f32_16x16x16bf16_1k(ones4, zz, zero, 0, 0, 0);
      const f32x4 s2 = __builtin_amdgcn_mfma_f32_16x16x16bf16_1k(zz, zz, zero, 0, 0, 0);
      if ((c >> 2) == g) *(f32x2*)(ps + (16 * tb[s] + c) * 2) = f32x2{s1[0], pick4(s2, c & 3)};
      SCHED_FENCE();
    }
  }
  if constexpr (ROLE > 0) { f_lda(pa, lds0, w & 3, w >> 2, 0); f_lda(pa, lds0, w & 3, w >> 2, 1); }
  STAMP(2, 4);
  __syncthreads();
  STAMP(2, 5);
  if constexpr (ROLE == 0) {
    const float* ps = (const float*)(smem + F_PS);
    float s1 = 0, s2 = 0;
#pragma unroll
    for (int k = 0; k < 7; ++k) { const f32x2 v = *(const f32x2*)(ps + k * 128 + lane * 2); s1 += v[0]; s2 += v[1]; }
    const rsrc_t rg = make_rsrc(a.gran, 256 * 1024);
    const u32 goff = (u32)(pi * 8 * 64 + lane) * 16;
    wt_store16(rg, goff + (u32)j * 1024, u32x4{__float_as_uint(s1), 1u, __float_as_uint(s2), 1u});
    float t1 = 0, t2 = 0;
    for (u32 spin = 0; spin < (1u << 22); ++spin) {
      u32x4 x[8];
#pragma unroll
      for (int k = 0; k < 8; ++k) x[k] = __builtin_amdgcn_raw_buffer_load_b128(rg, goff + k * 1024, 0, 16);
      bool ok = true; t1 = 0; t2 = 0;
#pragma unroll
      for (int k = 0; k < 8; ++k) {
        ok = ok && x[k][1] == 1u && x[k][3] == 1u;
        t1 += __uint_as_float(x[k][0]); t2 += __uint_as_float(x[k][2]);
      }
      if (__all(ok)) break;
      __builtin_amdgcn_s_sleep(1);
    }
    const float mean = t1 * (1.f / FH);
    const float var = fmaxf(t2 * (1.f / FH) - mean * mean, 0.f);
    *(f32x2*)(smem + F_FS + lane * 8) = f32x2{rbf(mean), __builtin_amdgcn_rcpf(rbf(__builtin_amdgcn_sqrtf(rbf(rbf(var) + 1e-5f))))};
    f_lda(pa, lds0, w & 3, w >> 2, 0);
    f_lda(pa, lds0, w & 3, w >> 2, 1);
  } else {
#pragma unroll
    for (int s = 0; s < 4; ++s) acc[s] = f32x4{0, 0, 0, 0};
    bf16x8 X[2][4];
#pragma unroll
    for (int s = 0; s < 4; ++s) X[0][s] = xfrag(s, 0);
#pragma unroll
    for (int ks = 0; ks < 16; ++ks) {
      if (ks == 13) { ru1 = uv_ld(a.uv, 0, 16 * nt1 + 4 * g); rv1 = uv_ld(a.uv, 1, 16 * nt1 + 4 * g); SCHED_FENCE(); }
#pragma unroll
      for (int s = 0; s < 4; ++s) {
        if (ks + 1 < 16) X[(ks + 1) & 1][s] = xfrag(s, ks + 1);
        acc[s] = __builtin_amdgcn_mfma_f32_16x16x32_bf16(W1[ks], X[ks & 1][s], acc[s], 0, 0, 0);
        SCHED_FENCE();
      }
    }
  }
  STAMP(2, 6);
  __syncthreads();
  STAMP(2, 7);
  if constexpr (ROLE < 2) {
#pragma unroll
    for (int s = 0; s < 4; ++s) {
      const f32x2 fs = *(const f32x2*)(smem + F_FS + (16 * tb[s] + c) * 8);
      const float a0 = (bf_lo(z2p[s][0]) - fs[0]) * fs[1], a1 = (bf_hi(z2p[s][0]) - fs[0]) * fs[1];
      const float a2 = (bf_lo(z2p[s][1]) - fs[0]) * fs[1], a3 = (bf_hi(z2p[s][1]) - fs[0]) * fs[1];
      *(u32x2*)(smem + F_ZS + (16 * tb[s] + c) * F_RS + (16 * w + 4 * g) * 2) = u32x2{pk_bf16(a0, a1), pk_bf16(a2, a3)};
    }
#pragma unroll
    for (int kk = 0; kk < 2; ++kk) {
      const char LAS* p = (const char LAS*)smem + F_ZS + (32 * kk + 8 * g + q4) * F_RS + (16 * w + 4 * p4) * 2;
      const s16x4 lo = tr_read(p), hi = tr_read(p + 4 * F_RS);
      const u32x2 a0 = __builtin_bit_cast(u32x2, lo), a1 = __builtin_bit_cast(u32x2, hi);
      wt_store16(make_rsrc(a.z2t, L * FH * 2), (u32)((((7 * j + w) * 256 + 8 * pi + 4 * kk) * 64 + lane * 4) * 4), u32x4{a0[0], a0[1], a1[0], a1[1]});
    }
  }
  VM_WAIT(0);
  __syncthreads();
  if (tid == 0) __hip_atomic_store((GAS u32*)a.flags + j * 32 + pi, 1u, __ATOMIC_RELAXED, __HIP_MEMORY_SCOPE_AGENT);
  STAMP(2, 8);
  if constexpr (ROLE > 0) {
    const f32x4 u1 = uv_sum(ru1), v1 = uv_sum(rv1);
#pragma unroll
    for (int s = 0; s < 4; ++s)
#pragma unroll
      for (int r = 0; r < 4; ++r) z1.y[s][r] = ri[s] * (acc[s][r] - mu[s] * u1[r]) + v1[r];
  } else {
#pragma unroll
    for (int s = 0; s < 4; ++s) z1.y[s] = f32x4{0, 0, 0, 0};
  }
  STAMP(2, 9);
}

template <int NT> constexpr int fd_glds(int sg) { return (sg == 2 || sg == 4) ? 8 : 0; }
template <int NT> constexpr int fd_bld(int sg) { return sg < 0 ? 0 : (2 * sg + F_BD < 16 ? NT : 0) + (2 * sg + 1 + F_BD < 16 ? NT : 0); }
template <int NT> constexpr int fd_ops(int sg) { return fd_glds<NT>(sg) + fd_bld<NT>(sg); }

template <int NT, int SG>
__device__ __forceinline__ void fd_stage(f32x4 (&acc)[4][NT], bf16x8 (&Bf)[F_BR][NT], bf16x8 (&Af)[2][4], const rsrc_t rb, u32 boff,
                                         const u16* const (&pa)[2], u32 lds0, const char* const (&ap)[2][2][4], int kq, int nh,
                                         const u32* fl, u32& mask, const Z1& z1, char* zdst, int w) {
  if constexpr ((SG & 1) == 0 && SG > 0) {
    if constexpr (SG == 2) vm_wait_c<F_BD * NT + fd_ops<NT>(0) + fd_ops<NT>(1)>();
    else vm_wait_c<fd_bld<NT>(SG - 2) + fd_ops<NT>(SG - 1)>();
    __builtin_amdgcn_s_barrier();
    if constexpr (fd_glds<NT>(SG) > 0) { f_lda(pa, lds0, kq, nh, SG + 2); f_lda(pa, lds0, kq, nh, SG + 3); }
#pragma unroll
    for (int m = 0; m < 4; ++m) Af[0][m] = *(const bf16x8*)(ap[(SG >> 1) & 1][0][m]);
  }
#pragma unroll
  for (int kk = 0; kk < 2; ++kk) {
    constexpr int ks0 = 2 * SG;
    if (kk == 0 || (SG & 1) == 0) {
#pragma unroll
      for (int m = 0; m < 4; ++m) Af[(kk + 1) & 1][m] = *(const bf16x8*)(ap[(SG >> 1) & 1][(kk + 1) & 1][m] + (kk == 0 ? (SG & 1) : 1) * 8192);
    }
    SCHED_FENCE();
#pragma unroll
    for (int n = 0; n < NT; ++n) {
#pragma unroll
      for (int m = 0; m < 4; ++m) acc[m][n] = __builtin_amdgcn_mfma_f32_16x16x32_bf16(Bf[(ks0 + kk) % F_BR][n], Af[kk][m], acc[m][n], 0, 0, 0);
      SCHED_FENCE();
      if (ks0 + kk + F_BD < 16) {
        if (n == 0 && ((ks0 + kk + F_BD) & 1) == 0) need_flag(fl, 8, mask, (ks0 + kk + F_BD) >> 1);
        Bf[(ks0 + kk + F_BD) % F_BR][n] = ld_dev16(rb, boff + (u32)((n * 256 + 4 * (ks0 + kk + F_BD)) * 256));
      }
      if constexpr (SG < 4) {
        if (kk == 0 && n == 1 && w > 0) {
          float z[4];
#pragma unroll
          for (int r = 0; r < 4; ++r) z[r] = gelu_tanh(z1.y[SG][r]);
          *(u32x2*)(zdst + ((SG + w) & 3) * 16 * F_RS) = u32x2{pk_bf16(z[0], z[1]), pk_bf16(z[2], z[3])};
        }
      }
      SCHED_FENCE();
    }
  }
}

template <int NT>
__device__ __forceinline__ void f_phase_d(const FArgs& a, char* smem, const int w, const int rt, const int ct, const u16* const (&pa)[2], const Z1& z1) {
  const int tid = threadIdx.x, lane = tid & 63;
  const int kq = w & 3, nh = w >> 2, n0 = nh * 4;
  const int c = lane & 15, g = lane >> 4;
  const u32 lds0 = (u32)(size_t)(const char LAS*)smem;
  const rsrc_t rb = make_rsrc(a.z2t, L * FH * 2);
  const u32 boff = (u32)((((7 * ct + n0) * 256 + 64 * kq) * 64 + lane * 4) * 4);
  f_lda(pa, lds0, kq, nh, 2);
  f_lda(pa, lds0, kq, nh, 3);
  const u32* fl = a.flags + ct * 32 + 8 * kq;
  __builtin_amdgcn_s_sleep(F_SLEEP0);
  u32 mask = poll8(fl, 8);
  bf16x8 Bf[F_BR][NT], Af[2][4];
#pragma unroll
  for (int ks = 0; ks < F_BD; ++ks) {
    if ((ks & 1) == 0) need_flag(fl, 8, mask, ks >> 1);
#pragma unroll
    for (int n = 0; n < NT; ++n) Bf[ks][n] = ld_dev16(rb, boff + (u32)((n * 256 + 4 * ks) * 256));
  }
  SCHED_FENCE();
  f32x4 acc[4][NT];
#pragma unroll
  for (int m = 0; m < 4; ++m)
#pragma unroll
    for (int n = 0; n < NT; ++n) acc[m][n] = f32x4{0, 0, 0, 0};
  const int asw = (c >> 1) & 7;
  const char* ap[2][2][4];
#pragma unroll
  for (int hi = 0; hi < 2; ++hi)
#pragma unroll
    for (int m = 0; m < 4; ++m) {
      const char* base = smem + f_slot(kq, 2 * hi) + ((m + kq) & 3) * 2048 + c * 128;
      ap[hi][0][m] = base + 16 * (g ^ asw); ap[hi][1][m] = base + 16 * ((4 + g) ^ asw);
    }
#pragma unroll
  for (int m = 0; m < 4; ++m) Af[0][m] = *(const bf16x8*)(ap[0][0][m]);
  char* zdst = smem + F_ZS + c * F_RS + (16 * (w - 1) + 4 * g) * 2;
  STAMP(3, 4);
  fd_stage<NT, 0>(acc, Bf, Af, rb, boff, pa, lds0, ap, kq, nh, fl, mask, z1, zdst, w);
  fd_stage<NT, 1>(acc, Bf, Af, rb, boff, pa, lds0, ap, kq, nh, fl, mask, z1, zdst, w);
  STAMP(3, 5);
  fd_stage<NT, 2>(acc, Bf, Af, rb, boff, pa, lds0, ap, kq, nh, fl, mask, z1, zdst, w);
  fd_stage<NT, 3>(acc, Bf, Af, rb, boff, pa, lds0, ap, kq, nh, fl, mask, z1, zdst, w);
  STAMP(3, 6);
  fd_stage<NT, 4>(acc, Bf, Af, rb, boff, pa, lds0, ap, kq, nh, fl, mask, z1, zdst, w);
  fd_stage<NT, 5>(acc, Bf, Af, rb, boff, pa, lds0, ap, kq, nh, fl, mask, z1, zdst, w);
  STAMP(3, 7);
  fd_stage<NT, 6>(acc, Bf, Af, rb, boff, pa, lds0, ap, kq, nh, fl, mask, z1, zdst, w);
  if (w < 7) {
    const int wg = blockIdx.x;
    const u16* p = a.Wot + (size_t)(wg & 1) * 16 * 112 * 128 + (size_t)(wg >> 3) * 7168 + w * 1024 + lane * 8;
    glds16(p, lds0 + F_PF); glds16(p + 512, lds0 + F_PF);
  }
  const float bs = bf1(a.bsp[64 * rt + 16 * kq + c]);
  fd_stage<NT, 7>(acc, Bf, Af, rb, boff, pa, lds0, ap, kq, nh, fl, mask, z1, zdst, w);
  STAMP(3, 1);
  __syncthreads();
#pragma unroll
  for (int m = 1; m < 4; ++m) {
    const int o = (m + kq) & 3;
    char* dst = smem + o * 10752 + ((m - 1) * 7 * 64 + n0 * 64 + lane) * 8;
#pragma unroll
    for (int n = 0; n < NT; ++n) *(u32x2*)(dst + n * 512) = u32x2{pk_bf16(acc[m][n][0], acc[m][n][1]), pk_bf16(acc[m][n][2], acc[m][n][3])};
  }
  __syncthreads();
  STAMP(3, 2);
  const char* src = smem + kq * 10752 + (n0 * 64 + lane) * 8;
  const char* zs = smem + F_ZS + (16 * kq + c) * F_RS + (16 * n0 + 4 * g) * 2;
  const rsrc_t rp = make_rsrc(a.Pt, L * FH * 2);
  u32x2 pv[NT];
#pragma unroll
  for (int n = 0; n < NT; ++n) {
    const u32x2 p0 = *(const u32x2*)(src + n * 512), p1 = *(const u32x2*)(src + (7 + n) * 512), p2 = *(const u32x2*)(src + (14 + n) * 512);
    const u32x2 zz = *(const u32x2*)(zs + n * 32);
    float gg[4];
#pragma unroll
    for (int e = 0; e < 2; ++e) {
      gg[2 * e] = ((acc[0][n][2 * e] + bf_lo(p0[e])) + (bf_lo(p1[e]) + bf_lo(p2[e]))) + bs;
      gg[2 * e + 1] = ((acc[0][n][2 * e + 1] + bf_hi(p0[e])) + (bf_hi(p1[e]) + bf_hi(p2[e]))) + bs;
    }
    pv[n] = u32x2{pk_bf16(bf_lo(zz[0]) * gg[0], bf_hi(zz[0]) * gg[1]), pk_bf16(bf_lo(zz[1]) * gg[2], bf_hi(zz[1]) * gg[3])};
    if (n & 1) wt_store16(rp, (u32)((((4 * rt + kq) * 112 + 14 * ct + 2 * (n0 + n - 1 + (g & 1)) + (g >> 1)) * 16 + c) * 16), pair16(pv[n - 1], pv[n]));
  }
  if constexpr (NT & 1) {
    const u32x4 v = pair16(pv[NT - 1], pv[NT - 1]);
    if ((g & 1) == 0) wt_store16(rp, (u32)((((4 * rt + kq) * 112 + 14 * ct + 2 * (n0 + NT - 1) + (g >> 1)) * 16 + c) * 16), v);
  }
  STAMP(3, 3);
}

__device__ __forceinline__ void f_phase_e(const FArgs& a, char* smem, const int w, const int rt, const int dt) {
  const int tid = threadIdx.x, lane = tid & 63;
  const int kq = w & 3, mh = w >> 2;
  const int c = lane & 15, g = lane >> 4;
  const u32 lds0 = (u32)(size_t)(const char LAS*)smem;
  VM_WAIT(0);
  __syncthreads();
  if (tid == 0) __hip_atomic_store((GAS u32*)a.flags + 256 + rt * 32 + dt, 1u, __ATOMIC_RELAXED, __HIP_MEMORY_SCOPE_AGENT);
  STAMP(4, 5);
#pragma unroll
  for (int k = 0; k < 7; ++k)
#pragma unroll
    for (int nn = 0; nn < 2; ++nn) {
      const int n = 2 * mh + nn;
      glds16(a.Wot + ((size_t)(4 * dt + n) * 112 + 4 * (7 * kq + k)) * 128 + lane * 8, lds0 + (n * 28 + 7 * kq + k) * 1024);
    }
  const u32x4 xr = *(const u32x4*)(a.x1T + xt_off(64 * dt + (tid >> 3), 64 * rt) + 8 * (tid & 7));
  const f32x4 gbv = mod_get4(a.mod, 2560 + 64 * dt + 16 * kq + 4 * g);
  SCHED_FENCE();
  {
    u32* p = (u32*)(smem + F_EX + (tid >> 3) * 132 + (tid & 7) * 16);
    p[0] = xr[0]; p[1] = xr[1]; p[2] = xr[2]; p[3] = xr[3];
  }
  VM_WAIT(0);
  __builtin_amdgcn_s_barrier();
  SCHED_FENCE();
  bf16x8 Wf[7][4];
  const char* wp = smem + (7 * kq) * 1024 + lane * 16;
#pragma unroll
  for (int k = 0; k < 7; ++k)
#pragma unroll
    for (int n = 0; n < 4; ++n) Wf[k][n] = *(const bf16x8*)(wp + (((n + kq) & 3) * 28 + k) * 1024);
  SCHED_FENCE();
  {
    const u32* fl = a.flags + 256 + rt * 32 + 2 * kq;
    u32 mask = poll8(fl, 2);
    need_flag(fl, 2, mask, 0); need_flag(fl, 2, mask, 1);
  }
  STAMP(4, 4);
  const rsrc_t rp = make_rsrc(a.Pt, L * FH * 2);
  bf16x8 Pf[7][2];
#pragma unroll
  for (int k = 0; k < 7; ++k)
#pragma unroll
    for (int m = 0; m < 2; ++m) Pf[k][m] = ld_dev16(rp, (u32)((((4 * rt + 2 * mh + m) * 112 + 4 * (7 * kq + k)) * 64 + lane * 4) * 4));
  SCHED_FENCE();
  f32x4 acc[2][4];
#pragma unroll
  for (int m = 0; m < 2; ++m)
#pragma unroll
    for (int n = 0; n < 4; ++n) acc[m][n] = f32x4{0, 0, 0, 0};
#pragma unroll
  for (int k = 0; k < 7; ++k)
#pragma unroll
    for (int n = 0; n < 4; ++n)
#pragma unroll
      for (int m = 0; m < 2; ++m) acc[m][n] = __builtin_amdgcn_mfma_f32_16x16x32_bf16(Wf[k][n], Pf[k][m], acc[m][n], 0, 0, 0);
  STAMP(4, 1);
  __syncthreads();
#pragma unroll
  for (int n = 1; n < 4; ++n)
#pragma unroll
    for (int m = 0; m < 2; ++m)
      *(f32x4*)(smem + (((((n + kq) & 3) * 2 + mh) * 3 + (n - 1)) * 2 + m) * 1024 + lane * 16) = acc[m][n];
  __syncthreads();
  STAMP(4, 2);
  {
    u32x2 ov[2];
#pragma unroll
    for (int m = 0; m < 2; ++m) {
      const char* p = smem + (((kq * 2 + mh) * 3) * 2 + m) * 1024 + lane * 16;
      const f32x4 p0 = *(const f32x4*)p, p1 = *(const f32x4*)(p + 2048), p2 = *(const f32x4*)(p + 4096);
      const char* xp = smem + F_EX + (16 * kq + 4 * g) * 132 + (16 * (2 * mh + m) + c) * 2;
      float o[4];
#pragma unroll
      for (int e = 0; e < 4; ++e) o[e] = bf1(*(const u16*)(xp + e * 132)) + gbv[e] * ((acc[m][0][e] + p0[e]) + (p1[e] + p2[e]));
      ov[m] = u32x2{pk_bf16(o[0], o[1]), pk_bf16(o[2], o[3])};
    }
    *(u32x4*)(a.out + (size_t)(64 * rt + 16 * (2 * mh + (g & 1)) + c) * D + 64 * dt + 16 * kq + 8 * (g >> 1)) = pair16(ov[0], ov[1]);
  }
  STAMP(4, 3);
}

__global__ __launch_bounds__(512, 2) void kF(FArgs a) {
  extern __shared__ __attribute__((aligned(16))) char smem[];
  const int wg = blockIdx.x, lane = threadIdx.x & 63;
  const int w = __builtin_amdgcn_readfirstlane(threadIdx.x >> 6);
  const int xc = wg & 7, ii = wg >> 3;
  const int pi = (xc >> 1) * 8 + (ii & 7), j = (xc & 1) * 4 + (ii >> 3);
  STAMP(2, 0);
  f_ldx(a.x1T, (u32)(size_t)(const char LAS*)smem, w, pi, 0);
  const u16* pa[2];
#pragma unroll
  for (int par = 0; par < 2; ++par) {
    const int row = 32 * (w >> 2) + 8 * par + (lane >> 3);
    pa[par] = a.wsp + (size_t)(64 * pi + row) * L + 512 * (w & 3) + 8 * ((lane & 7) ^ ((row >> 1) & 7));
  }
  Z1 z1;
  if (w == 0) f_phase_c<0>(a, smem, w, pi, j, pa, z1);
  else if (w < 7) f_phase_c<1>(a, smem, w, pi, j, pa, z1);
  else f_phase_c<2>(a, smem, w, pi, j, pa, z1);
  STAMP(3, 0);
  if (w < 4) f_phase_d<4>(a, smem, w, pi, j, pa, z1);
  else f_phase_d<3>(a, smem, w, pi, j, pa, z1);
  STAMP(4, 0);
  f_phase_e(a, smem, w, pi, j);
}
#define KMASK(b)
extern "C" void kernel_launch(void* const* d_in, const int* in_sizes, int n_in, void* d_out, int out_size,
                              void* d_ws, size_t ws_size, hipStream_t stream) {
  const u16* x = (const u16*)d_in[0];
  char* ws = (char*)d_ws;
  float* mod = (float*)(ws + 524288);
  u32* stats1 = (u32*)(ws + 16384);
  float* uv = (float*)(ws + 589824);
  u64* gran = (u64*)(ws + 65536);
  u32* flags = (u32*)(ws + 65536 + 262144);
  u16* xT = (u16*)(ws + (1 << 20));
  u16* Krev = (u16*)(ws + (3 << 20));
  u16* x1T = xT;
  u16* Wt = (u16*)(ws + (7 << 20));
  u16* Wot = (u16*)(ws + (9 << 20));
  u16* Pt = (u16*)(ws + (10 << 20));
  u16* z2t = Krev;
  AArgs aa;
  aa.x = x; aa.Are = (const u16*)d_in[1]; aa.Aim = (const u16*)d_in[2]; aa.Bre = (const u16*)d_in[3]; aa.Bim = (const u16*)d_in[4];
  aa.Cre = (const u16*)d_in[5]; aa.Cim = (const u16*)d_in[6]; aa.logdt = (const u16*)d_in[7]; aa.c = (const u16*)d_in[14];
  aa.wmod = (const u16*)d_in[15]; aa.win = (const u16*)d_in[10]; aa.wout = (const u16*)d_in[13];
  aa.mod = mod; aa.stats1 = stats1; aa.xT = xT; aa.Krev = Krev; aa.gran = (u32x4*)gran;
  (void)hipFuncSetAttribute((const void*)kA, hipFuncAttributeMaxDynamicSharedMemorySize, A_LDS);
  KMASK(1) kA<<<256, 768, A_LDS, stream>>>(aa);
  BArgs ba;
  ba.xT = xT; ba.Krev = Krev; ba.Dskip = (const u16*)d_in[8]; ba.win = (const u16*)d_in[10]; ba.wout = (const u16*)d_in[13];
  ba.wsp = (const u16*)d_in[11]; ba.stats1 = stats1; ba.mod = mod; ba.x1T = x1T; ba.Wt = Wt; ba.Wot = Wot; ba.uv = uv;
  (void)hipFuncSetAttribute((const void*)kB, hipFuncAttributeMaxDynamicSharedMemorySize, B_LDS);
  KMASK(2) kB<<<256, 768, B_LDS, stream>>>(ba);
  FArgs fa;
  fa.x1T = x1T; fa.Wt = Wt; fa.wsp = (const u16*)d_in[11]; fa.bsp = (const u16*)d_in[12]; fa.Wot = Wot; fa.uv = uv; fa.mod = mod;
  fa.z2t = z2t; fa.Pt = Pt; fa.out = (u16*)d_out; fa.gran = gran; fa.flags = flags;
  (void)hipFuncSetAttribute((const void*)kF, hipFuncAttributeMaxDynamicSharedMemorySize, F_LDS);
  KMASK(4) kF<<<256, 512, F_LDS, stream>>>(fa);
}
```

```cpp
#include <hip/hip_runtime.h>
#include <hip/hip_bf16.h>
#include <stdint.h>

typedef unsigned short u16;
typedef unsigned int u32;
typedef unsigned long long u64;
typedef __bf16 bf16x8 __attribute__((ext_vector_type(8)));
typedef __bf16 bf16x2 __attribute__((ext_vector_type(2)));
typedef short s16x4 __attribute__((ext_vector_type(4)));
typedef float f32x2 __attribute__((ext_vector_type(2)));
typedef float f32x4 __attribute__((ext_vector_type(4)));
typedef u32 u32x2 __attribute__((ext_vector_type(2)));
typedef u32 u32x4 __attribute__((ext_vector_type(4)));
#define LAS __attribute__((address_space(3)))
#define GAS __attribute__((address_space(1)))

constexpr int L = 2048, D = 512, NS = 128, F = 1792, FH = 896;

__device__ __forceinline__ float bf_lo(u32 w) { return __uint_as_float(w << 16); }
__device__ __forceinline__ float bf_hi(u32 w) { return __uint_as_float(w & 0xffff0000u); }
__device__ __forceinline__ float bf1(u16 h) { return __uint_as_float(((u32)h) << 16); }
__device__ __forceinline__ u32 pk_bf16(float lo, float hi) {
  f32x2 a = {lo, hi};
  bf16x2 p = __builtin_convertvector(a, bf16x2);
  return __builtin_bit_cast(u32, p);
}
__device__ __forceinline__ float rbf(float x) { return bf_lo(pk_bf16(x, x)); }

template <int CTRL, int ROWMASK = 0xf>
__device__ __forceinline__ float dpp_add(float x) {
  int y = __builtin_amdgcn_update_dpp(0, __float_as_int(x), CTRL, ROWMASK, 0xf, true);
  return x + __int_as_float(y);
}
__device__ __forceinline__ float row16_sum(float x) {
  x = dpp_add<0xB1>(x);
  x = dpp_add<0x4E>(x);
  x = dpp_add<0x141>(x);
  x = dpp_add<0x140>(x);
  return x;
}
__device__ __forceinline__ float wave_sum(float x) {
  x = row16_sum(x);
  float a = __int_as_float(__builtin_amdgcn_readlane(__float_as_int(x), 0));
  float b = __int_as_float(__builtin_amdgcn_readlane(__float_as_int(x), 16));
  float c = __int_as_float(__builtin_amdgcn_readlane(__float_as_int(x), 32));
  float d = __int_as_float(__builtin_amdgcn_readlane(__float_as_int(x), 48));
  return (a + b) + (c + d);
}
__device__ __forceinline__ s16x4 tr_read(const char LAS* p) {
  return __builtin_amdgcn_ds_read_tr16_b64_v4i16((s16x4 LAS*)p);
}

#define STAMP(kern, i)
__device__ __forceinline__ void glds16(const void* gsrc, u32 lds_dst) {
  u32 keep;
  asm volatile("s_mov_b32 %0, m0\n\ts_mov_b32 m0, %2\n\ts_nop 0\n\tglobal_load_lds_dwordx4 %1, off\n\ts_mov_b32 m0, %0"
               : "=&s"(keep) : "v"(gsrc), "s"(lds_dst) : "memory");
}
#define SCHED_FENCE() __builtin_amdgcn_sched_barrier(0)
#define VM_WAIT(n) asm volatile("s_waitcnt vmcnt(" #n ")" ::: "memory")
__device__ __forceinline__ float xor16_sum(float x) {
  const u32 v = __float_as_uint(x);
  auto r = __builtin_amdgcn_permlane16_swap(v, v, false, false);
  return __uint_as_float(r[0]) + __uint_as_float(r[1]);
}
__device__ __forceinline__ float xor32_sum(float x) {
  const u32 v = __float_as_uint(x);
  auto r = __builtin_amdgcn_permlane32_swap(v, v, false, false);
  return __uint_as_float(r[0]) + __uint_as_float(r[1]);
}
__device__ __forceinline__ float xor4_8_sum(float x) {
  x = x + __int_as_float(__builtin_amdgcn_update_dpp(0, __float_as_int(x), 0x124, 0xf, 0xf, true));
  x = x + __int_as_float(__builtin_amdgcn_update_dpp(0, __float_as_int(x), 0x128, 0xf, 0xf, true));
  return x;
}
__device__ __forceinline__ void lds_rendezvous(u32 LAS* cnt, u32 target) {
  asm volatile("s_waitcnt lgkmcnt(0)" ::: "memory");
  if ((threadIdx.x & 63) == 0) __hip_atomic_fetch_add(cnt, 1u, __ATOMIC_RELAXED, __HIP_MEMORY_SCOPE_WORKGROUP);
  while (__hip_atomic_load(cnt, __ATOMIC_RELAXED, __HIP_MEMORY_SCOPE_WORKGROUP) < target) __builtin_amdgcn_s_sleep(1);
  asm volatile("" ::: "memory");
}

typedef __amdgpu_buffer_rsrc_t rsrc_t;
__device__ __forceinline__ rsrc_t make_rsrc(const void* base, u32 bytes) {
  return __builtin_amdgcn_make_buffer_rsrc(const_cast<void*>(base), 0, bytes, 0x00020000);
}
__device__ __forceinline__ void wt_store16(rsrc_t r, u32 byte_off, u32x4 v) { __builtin_amdgcn_raw_buffer_store_b128(v, r, byte_off, 0, 16); }
__device__ __forceinline__ u32x4 pair16(u32x2 a, u32x2 b) {
  const auto r0 = __builtin_amdgcn_permlane16_swap(a[0], b[0], false, false);
  const auto r1 = __builtin_amdgcn_permlane16_swap(a[1], b[1], false, false);
  return u32x4{r0[0], r1[0], r0[1], r1[1]};
}

__device__ __forceinline__ float mod_get(const float* mp, int i) { return rbf((mp[i] + mp[3072 + i]) + (mp[6144 + i] + mp[9216 + i])); }
__device__ __forceinline__ f32x4 mod_get4(const float* mp, int i) {
  const f32x4 a = *(const f32x4*)(mp + i), b = *(const f32x4*)(mp + 3072 + i), c = *(const f32x4*)(mp + 6144 + i), d = *(const f32x4*)(mp + 9216 + i);
  f32x4 r;
#pragma unroll
  for (int e = 0; e < 4; ++e) r[e] = rbf((a[e] + b[e]) + (c[e] + d[e]));
  return r;
}
__device__ __forceinline__ f32x4 uv_get4(const float* uv, int which, int f) {
  const float* p = uv + which * F + f;
  const f32x4 a = *(const f32x4*)p, b = *(const f32x4*)(p + 2 * F), c = *(const f32x4*)(p + 4 * F), d = *(const f32x4*)(p + 6 * F);
  return (a + b) + (c + d);
}
struct UV4 { f32x4 p[4]; };
__device__ __forceinline__ UV4 uv_ld(const float* uv, int which, int f) {
  const float* p = uv + which * F + f;
  UV4 r;
#pragma unroll
  for (int q = 0; q < 4; ++q) r.p[q] = *(const f32x4*)(p + 2 * q * F);
  return r;
}
__device__ __forceinline__ f32x4 uv_sum(const UV4& r) { return (r.p[0] + r.p[1]) + (r.p[2] + r.p[3]); }
constexpr int CODE_LANES = 84;
struct AArgs {
  const u16 *x, *Are, *Aim, *Bre, *Bim, *Cre, *Cim, *logdt, *c, *wmod, *win, *wout;
  float* mod; u32* stats1; u16* xT; u16* Krev; u32x4* gran;
};
constexpr int A_RS = 528;
constexpr int A_TAB = 0;
constexpr int A_KC = A_TAB + 2 * 64 * A_RS;
constexpr int A_TILE = A_KC + 2 * 768 * 4;
constexpr int A_RED = A_TILE + 8 * 1088;
constexpr int A_CNT = A_RED + 4 * 64 * 4;
constexpr int A_LDS = A_CNT + 16;

struct KIdx { u16 v[2048]; };
constexpr int bf16_round_int(int l) {
  int sh = 0;
  while ((l >> sh) >= 256) ++sh;
  if (sh == 0) return l;
  const int q = l >> sh, rem = l & ((1 << sh) - 1), half = 1 << (sh - 1);
  const int up = (rem > half || (rem == half && (q & 1))) ? 1 : 0;
  return (q + up) << sh;
}
constexpr KIdx make_kidx() {
  KIdx t{};
  for (int x = 0; x < 2048; ++x) {
    const int lr = bf16_round_int(2047 - x);
    const int aa = lr >> 7, b = (lr >> 3) & 15, r = lr & 7;
    const int row = lr < 256 ? 8 * aa + r : lr < 512 ? 16 + 4 * (aa - 2) + (r >> 1) : lr < 1024 ? 24 + 2 * (aa - 4) + (r >> 2) : 32 + (aa - 8);
    t.v[x] = (u16)(row * 16 + b);
  }
  return t;
}
__device__ const KIdx g_kidx = make_kidx();

struct cpx { float r, i; };
__device__ __forceinline__ cpx cmul(cpx a, cpx b) { return cpx{a.r * b.r - a.i * b.i, a.r * b.i + a.i * b.r}; }

__global__ __launch_bounds__(768) void kA(AArgs a) {
  extern __shared__ __attribute__((aligned(16))) char smem[];
  const int wg = blockIdx.x, tid = threadIdx.x, lane = tid & 63;
  const int w = __builtin_amdgcn_readfirstlane(tid >> 6);
  STAMP(0, 0);
  const int tw = w - 8, st = tid - 512;
  const int t0 = 8 * ((wg & 7) * 32 + (wg >> 3));
  const bool doM = wg < 192;
  const int mrb = wg / 48, mcb = wg - 48 * mrb;
  const int rr = lane >> 3, q = lane & 7;
  u32x4 ld[14];
  u16 sv[8];
  if (w < 8) {
    const int d = 2 * wg + (w >> 2), o = d * NS + 64 * ((w >> 1) & 1) + lane;
    sv[0] = a.Are[o]; sv[1] = a.Aim[o]; sv[2] = a.Bre[o]; sv[3] = a.Bim[o]; sv[4] = a.Cre[o]; sv[5] = a.Cim[o]; sv[6] = a.logdt[d]; sv[7] = 0;
    ld[0] = *(const u32x4*)(g_kidx.v + 8 * (tid & 255));
  } else {
#pragma unroll
    for (int j = 0; j < 2; ++j) ld[j] = *(const u32x4*)(a.x + (size_t)(t0 + 2 * tw + j) * D + 8 * lane);
    if (tw == 0 && lane < 4) *(u32*)(smem + A_CNT + 4 * lane) = 0;
    asm volatile("s_waitcnt lgkmcnt(0)" ::: "memory");
  }
  SCHED_FENCE();
  STAMP(0, 9);
  __builtin_amdgcn_s_barrier();
  STAMP(0, 10);
  SCHED_FENCE();
  if (w < 8) {
    const int ch = w >> 2, nh = (w >> 1) & 1, part = (w ^ ch) & 1, d = 2 * wg + ch;
    const int n = 64 * nh + lane;
    const u16 hare = sv[0], haim = sv[1], hbre = sv[2], hbim = sv[3], hcre = sv[4], hcim = sv[5], hdt = sv[6];
    const u32x4 kix = ld[0];
    asm volatile("" ::"v"((u32)hare), "v"((u32)hcim), "v"((u32)hdt));
    STAMP(0, 11);
    const float dt = rbf(__expf(bf1(hdt)));
    const float lr = (dt * bf1(hare)) * 1.4426950408889634f, lp = (dt * bf1(haim)) * 0.15915494309189535f;
    auto cexp = [&](float l) -> cpx {
      const float e = __builtin_amdgcn_exp2f(lr * l), ph = __builtin_amdgcn_fractf(lp * l);
      return cpx{e * __builtin_amdgcn_cosf(ph), e * __builtin_amdgcn_sinf(ph)};
    };
    char* tab = smem + A_TAB + ch * (64 * A_RS) + n * 4;
    auto put = [&](int row, cpx v) { *(u32*)(tab + row * A_RS) = pk_bf16(v.r, v.i); };
    if (part == 0) {
      const cpx E1 = cexp(1.f), E128 = cexp(128.f), E512 = cexp(512.f);
      const cpx E2 = cmul(E1, E1), E3 = cmul(E2, E1), E4 = cmul(E2, E2), E5 = cmul(E4, E1), E6 = cmul(E4, E2), E7 = cmul(E4, E3);
      put(0, cpx{1.f, 0.f}); put(1, E1); put(2, E2); put(3, E3); put(4, E4); put(5, E5); put(6, E6); put(7, E7);
      put(8, E128); put(9, cmul(E128, E1)); put(10, cmul(E128, E2)); put(11, cmul(E128, E3));
      put(12, cmul(E128, E4)); put(13, cmul(E128, E5)); put(14, cmul(E128, E6)); put(15, cmul(E128, E7));
      const cpx E256 = cmul(E128, E128), E384 = cmul(E256, E128);
      put(16, E256); put(17, cmul(E256, E2)); put(18, cmul(E256, E4)); put(19, cmul(E256, E6));
      put(20, E384); put(21, cmul(E384, E2)); put(22, cmul(E384, E4)); put(23, cmul(E384, E6));
      const cpx E640 = cmul(E512, E128), E768 = cmul(E512, E256), E896 = cmul(E512, E384);
      put(24, E512); put(25, cmul(E512, E4)); put(26, E640); put(27, cmul(E640, E4));
      put(28, E768); put(29, cmul(E768, E4)); put(30, E896); put(31, cmul(E896, E4));
    } else {
      const cpx E8 = cexp(8.f), E128 = cexp(128.f), E1024 = cexp(1024.f);
      const float br = dt * bf1(hbre), bi = dt * bf1(hbim), cr = bf1(hcre), ci = bf1(hcim);
      const cpx W = cmul(cpx{cr, ci}, cpx{br, bi});
      const cpx E16 = cmul(E8, E8), E32 = cmul(E16, E16), E64 = cmul(E32, E32);
      cpx Y[8];
      Y[0] = W; Y[1] = cmul(W, E8); Y[2] = cmul(W, E16); Y[3] = cmul(Y[1], E16);
#pragma unroll
      for (int b = 0; b < 4; ++b) Y[4 + b] = cmul(Y[b], E32);
#pragma unroll
      for (int b = 0; b < 8; ++b) {
        put(48 + b, cpx{Y[b].r, -Y[b].i});
        const cpx t = cmul(Y[b], E64);
        put(56 + b, cpx{t.r, -t.i});
      }
      const cpx E256 = cmul(E128, E128), E512 = cmul(E256, E256);
      cpx P[4];
      P[0] = E1024; P[1] = cmul(E1024, E128); P[2] = cmul(E1024, E256); P[3] = cmul(P[1], E256);
#pragma unroll
      for (int b = 0; b < 4; ++b) { put(32 + b, P[b]); put(36 + b, cmul(P[b], E512)); }
      put(40, cmul(E1024, E1024));
    }
    STAMP(0, 1);
    lds_rendezvous((u32 LAS*)(smem + A_CNT), 8);
    STAMP(0, 2);
    if (w < 6) {
      const int c2 = w >= 3 ? 1 : 0, t = w - 3 * c2, i = lane & 15, g = lane >> 4;
      const char* tb = smem + A_TAB + c2 * (64 * A_RS) + 16 * g;
      const char* pa = tb + (16 * t + i) * A_RS, *pb = tb + (48 + i) * A_RS;
      f32x4 acc = {0, 0, 0, 0};
      bf16x8 Af[8], Bf[8];
#pragma unroll
      for (int s = 0; s < 8; ++s) {
        Af[s] = *(const bf16x8*)(pa + s * 64); Bf[s] = *(const bf16x8*)(pb + s * 64);
      }
#pragma unroll
      for (int s = 0; s < 8; ++s) acc = __builtin_amdgcn_mfma_f32_16x16x32_bf16(Af[s], Bf[s], acc, 0, 0, 0);
      float* Kc = (float*)(smem + A_KC) + c2 * 768 + t * 256;
#pragma unroll
      for (int r = 0; r < 4; ++r) Kc[(4 * g + r) * 16 + i] = acc[r];
    }
    STAMP(0, 3);
    lds_rendezvous((u32 LAS*)(smem + A_CNT + 4), 8);
    STAMP(0, 4);
    const float* Kc = (const float*)(smem + A_KC) + ch * 768;
    float kv[8];
#pragma unroll
    for (int j = 0; j < 4; ++j) { kv[2 * j] = Kc[kix[j] & 0xffffu]; kv[2 * j + 1] = Kc[kix[j] >> 16]; }
    wt_store16(make_rsrc(a.Krev, D * L * 2), (u32)(d * L + 8 * (tid & 255)) * 2,
               u32x4{pk_bf16(kv[0], kv[1]), pk_bf16(kv[2], kv[3]), pk_bf16(kv[4], kv[5]), pk_bf16(kv[6], kv[7])});
    STAMP(0, 5);
  } else {
    __builtin_amdgcn_s_setprio(3);
    const u32x4 xr[2] = {ld[0], ld[1]};
    asm volatile("" ::"v"(xr[1]));
    STAMP(0, 11);
    char* tile = smem + A_TILE;
#pragma unroll
    for (int j = 0; j < 2; ++j) *(u32x4*)(tile + (2 * tw + j) * 1088 + 16 * lane) = xr[j];
    SCHED_FENCE();
    if (doM) {
#pragma unroll
      for (int p = 0; p < 4; ++p) ld[2 + p] = *(const u32x4*)(a.wmod + (size_t)(128 * mrb + p * 32 + tw * 8 + rr) * 3072 + 64 * mcb + 8 * q);
#pragma unroll
      for (int p = 0; p < 4; ++p) sv[p] = a.c[128 * mrb + p * 32 + tw * 8 + rr];
    }
    ld[10] = *(const u32x4*)(a.win + (size_t)wg * 3584 + st * 8);
    ld[11] = *(const u32x4*)(a.win + (size_t)wg * 3584 + (st < 192 ? 2048 + st * 8 : 0));
    ld[12] = *(const u32x4*)(a.wout + (size_t)wg * 1792 + (st < 224 ? st * 8 : 0));
    u64 base;
    asm volatile("s_getpc_b64 s[40:41]\n\ts_add_u32 s40, s40, _Z2kB5BArgs@rel32@lo+4\n\ts_addc_u32 s41, s41, _Z2kB5BArgs@rel32@hi+12\n\ts_mov_b64 %0, s[40:41]"
                 : "=s"(base) : : "s40", "s41", "scc");
    ld[13] = *(const u32x4*)((const char*)base + (size_t)(st < CODE_LANES ? (wg >> 3) * CODE_LANES + st : 0) * 16);
    SCHED_FENCE();
    STAMP(0, 1);
    lds_rendezvous((u32 LAS*)(smem + A_CNT + 8), 4);
    STAMP(0, 2);
#pragma unroll
    for (int ps = 0; ps < 2; ++ps) {
      const int d0 = 128 * tw + 64 * ps + 16 * (lane >> 4);
      const char LAS* src = (const char LAS*)tile + ((lane & 15) >> 2) * 1088 + 2 * (d0 + 4 * (lane & 3));
      const s16x4 lo = tr_read(src), hi = tr_read(src + 4 * 1088);
      u32x2 a0 = __builtin_bit_cast(u32x2, lo), a1 = __builtin_bit_cast(u32x2, hi);
      *(u32x4*)(a.xT + (size_t)(d0 + (lane & 15)) * L + t0) = u32x4{a0[0], a0[1], a1[0], a1[1]};
    }
    STAMP(0, 6);
    if (tw == 0) a.gran[wg * 64 + lane] = u32x4{0, 0, 0, 0};
    if (tw == 1 && wg < 8) a.gran[(256 + wg) * 64 + lane] = u32x4{0, 0, 0, 0};
    {
      float v[2][8], s[2];
#pragma unroll
      for (int j = 0; j < 2; ++j) {
#pragma unroll
        for (int k = 0; k < 4; ++k) { v[j][2 * k] = bf_lo(xr[j][k]); v[j][2 * k + 1] = bf_hi(xr[j][k]); }
        s[j] = ((v[j][0] + v[j][1]) + (v[j][2] + v[j][3])) + ((v[j][4] + v[j][5]) + (v[j][6] + v[j][7]));
      }
      float mean[2], q2[2];
#pragma unroll
      for (int j = 0; j < 2; ++j) mean[j] = wave_sum(s[j]) * (1.f / D);
#pragma unroll
      for (int j = 0; j < 2; ++j) {
        q2[j] = 0;
#pragma unroll
        for (int k = 0; k < 8; ++k) { const float dv = v[j][k] - mean[j]; q2[j] += dv * dv; }
      }
#pragma unroll
      for (int j = 0; j < 2; ++j) {
        const float var = wave_sum(q2[j]) * (1.f / D);
        const float sd = rbf(__builtin_amdgcn_sqrtf(rbf(rbf(var) + 1e-5f)));
        if (lane == 0) *(f32x2*)(a.stats1 + 2 * (t0 + 2 * tw + j)) = f32x2{rbf(mean[j]), __builtin_amdgcn_rcpf(sd)};
      }
    }
    STAMP(0, 7);
    float* red = (float*)(smem + A_RED);
    if (doM) {
      asm volatile("" ::"v"(ld[5]));
      STAMP(0, 8);
      float acc[8] = {0, 0, 0, 0, 0, 0, 0, 0};
#pragma unroll
      for (int p = 0; p < 4; ++p) {
        const float cx = bf1(sv[p]);
        const float s = rbf(cx * __builtin_amdgcn_rcpf(1.f + __expf(-cx)));
#pragma unroll
        for (int k = 0; k < 4; ++k) { acc[2 * k] += s * bf_lo(ld[2 + p][k]); acc[2 * k + 1] += s * bf_hi(ld[2 + p][k]); }
      }
#pragma unroll
      for (int k = 0; k < 8; ++k) {
        float v = acc[k];
        v += __int_as_float(__builtin_amdgcn_update_dpp(0, __float_as_int(v), 0x128, 0xf, 0xf, true));
        v = xor32_sum(xor16_sum(v));
        if (rr == 0) red[tw * 64 + 8 * q + k] = v;
      }
      STAMP(0, 3);
      lds_rendezvous((u32 LAS*)(smem + A_CNT + 12), 4);
      STAMP(0, 4);
      if (tw == 0) a.mod[3072 * mrb + 64 * mcb + lane] = (red[lane] + red[64 + lane]) + (red[128 + lane] + red[192 + lane]);
    }
#pragma unroll
    for (int k = 10; k < 14; ++k) asm volatile("" ::"v"(ld[k]));
    STAMP(0, 5);
  }
}
constexpr int F_CODE_LANES = 72;
struct BArgs {
  const u16 *xT, *Krev, *Dskip, *win, *wout, *wsp;
  const u32* stats1; const float* mod;
  u16 *x1T, *Wt, *Wot; float* uv;
};
constexpr int B_H = 0;
constexpr int B_HS = (256 + 2048) * 2;
constexpr int B_KC = B_H + 2 * B_HS;
constexpr int B_KS = 4160;
constexpr int B_PART = B_KC + 2 * 4 * B_KS;
constexpr int B_RED = B_PART + 2 * 4 * 8 * 1280;
constexpr int B_STG = B_RED + 4 * 64 * 4;
constexpr int B_CNT = B_STG + 4 * 2048;
constexpr int B_LDS = B_CNT + 32;

__global__ __launch_bounds__(768) void kB(BArgs a) {
  extern __shared__ __attribute__((aligned(16))) char smem[];
  const int wg = blockIdx.x, tid = threadIdx.x, lane = tid & 63;
  const int w = __builtin_amdgcn_readfirstlane(tid >> 6);
  STAMP(1, 0);
  if (w < 8) {
    __builtin_amdgcn_s_setprio(2);
    const int ch = w >> 2, q = w & 3, d = 2 * wg + ch, tj = tid & 255;
    const u32x4 xv = *(const u32x4*)(a.xT + (size_t)d * L + 8 * tj);
    f32x4 st[4];
#pragma unroll
    for (int k = 0; k < 4; ++k) st[k] = *(const f32x4*)(a.stats1 + 16 * tj + 4 * k);
    const u32x4 k0 = *(const u32x4*)(a.Krev + (size_t)d * L + 8 * tj);
    const u32x4 k1r = *(const u32x4*)(a.Krev + (size_t)d * L + 8 * (tj < 255 ? tj + 1 : tj));
    const float sa = mod_get(a.mod, d), Ac = rbf(1.f + mod_get(a.mod, 512 + d)), ga = mod_get(a.mod, 1024 + d), dsk = bf1(a.Dskip[d]);
    SCHED_FENCE();
    __builtin_amdgcn_s_barrier();
    SCHED_FENCE();
    char* hb = smem + B_H + ch * B_HS;
    char* kc = smem + B_KC + ch * 4 * B_KS;
    if (tj < 32) *(u32x4*)(hb + 16 * tj) = u32x4{0, 0, 0, 0};
    if (tj < 16) *(u32x4*)(kc + (tj >> 2) * B_KS + 16 * (256 + (tj & 3))) = u32x4{0, 0, 0, 0};
    {
      const u32 z = tj < 255 ? 0xffffffffu : 0u;
      const u32 wd[8] = {k0[0], k0[1], k0[2], k0[3], k1r[0] & z, k1r[1] & z, 0, 0};
      u32 od[5];
#pragma unroll
      for (int k = 0; k < 5; ++k) od[k] = __builtin_amdgcn_alignbit(wd[k + 1], wd[k], 16);
      *(u32x4*)(kc + 0 * B_KS + 16 * tj) = u32x4{wd[0], wd[1], wd[2], wd[3]};
      *(u32x4*)(kc + 1 * B_KS + 16 * tj) = u32x4{od[0], od[1], od[2], od[3]};
      *(u32x4*)(kc + 2 * B_KS + 16 * tj) = u32x4{wd[1], wd[2], wd[3], wd[4]};
      *(u32x4*)(kc + 3 * B_KS + 16 * tj) = u32x4{od[1], od[2], od[3], od[4]};
    }
    u32x4 hv;
    {
      float hh[8];
#pragma unroll
      for (int k = 0; k < 8; ++k) {
        const float m = st[k >> 1][2 * (k & 1)], ri = st[k >> 1][2 * (k & 1) + 1];
        const float xx = (k & 1) ? bf_hi(xv[k >> 1]) : bf_lo(xv[k >> 1]);
        hh[k] = (xx - m) * (ri * Ac) + sa;
      }
      hv = u32x4{pk_bf16(hh[0], hh[1]), pk_bf16(hh[2], hh[3]), pk_bf16(hh[4], hh[5]), pk_bf16(hh[6], hh[7])};
    }
    *(u32x4*)(hb + 512 + 16 * tj) = hv;
    STAMP(1, 1);
    lds_rendezvous((u32 LAS*)(smem + B_CNT + 12 + 4 * 0) + ch * 2, 4);
    STAMP(1, 2);
    const int c = lane & 15, g = lane >> 4;
    const char* bbase = hb + 2 * (256 + 16 * c - 16 * (g >> 1) + 8 * (g & 1) - 32 * q) - 256;
    const int X = 2047 - 32 * q - 16 * (g >> 1) + 8 * (g & 1) - c;
    const char* abase = kc + (X & 3) * B_KS + 2 * (X & ~3) - 256 * 15;
    const char LAS* ab0 = (const char LAS*)abase;
    bf16x8 Bf[16], Af[16];
    auto lda = [&](int u) {
      const u32x2 lo = *(const volatile u32x2 LAS*)(ab0 + 256 * (15 - u)), hi = *(const volatile u32x2 LAS*)(ab0 + 256 * (15 - u) + 8);
      Af[u] = __builtin_bit_cast(bf16x8, u32x4{lo[0], lo[1], hi[0], hi[1]});
    };
    lda(0);
#pragma unroll
    for (int v = 1; v < 16; v += 2) Bf[v] = *(const bf16x8*)(bbase + 256 * v);
    lda(1);
#pragma unroll
    for (int v = 0; v < 16; v += 2) Bf[v] = *(const bf16x8*)(bbase + 256 * v);
    lda(2); lda(3);
    SCHED_FENCE();
    f32x4 acc[8];
#pragma unroll
    for (int i = 0; i < 8; ++i) acc[i] = f32x4{0, 0, 0, 0};
#pragma unroll
    for (int u = 0; u < 16; ++u) {
      if (u + 4 < 16) lda(u + 4);
      SCHED_FENCE();
#pragma unroll
      for (int i = u >> 1; i < 8; ++i) acc[i] = __builtin_amdgcn_mfma_f32_16x16x32_bf16(Af[u], Bf[2 * i - u + 1], acc[i], 0, 0, 0);
      SCHED_FENCE();
    }
    STAMP(1, 3);
    char* part = smem + B_PART + ch * (4 * 8 * 1280);
#pragma unroll
    for (int i = 0; i < 8; ++i) *(f32x4*)(part + (q * 8 + i) * 1280 + 16 * (20 * g + c)) = acc[i];
    lds_rendezvous((u32 LAS*)(smem + B_CNT + 4 * ch), 4);
    STAMP(1, 4);
    {
      const int i = tj >> 5, j5 = tj & 31, cc = j5 >> 1;
      float y[8];
#pragma unroll
      for (int hh = 0; hh < 2; ++hh) {
        const char* p = part + i * 1280 + 16 * (20 * (2 * (j5 & 1) + hh) + cc);
        const f32x4 p0 = *(const f32x4*)(p), p1 = *(const f32x4*)(p + 8 * 1280), p2 = *(const f32x4*)(p + 16 * 1280), p3 = *(const f32x4*)(p + 24 * 1280);
#pragma unroll
        for (int r = 0; r < 4; ++r) y[4 * hh + r] = (p0[r] + p1[r]) + (p2[r] + p3[r]);
      }
      float o[8];
#pragma unroll
      for (int k = 0; k < 8; ++k) {
        const float hx = (k & 1) ? bf_hi(hv[k >> 1]) : bf_lo(hv[k >> 1]);
        const float xx = (k & 1) ? bf_hi(xv[k >> 1]) : bf_lo(xv[k >> 1]);
        o[k] = xx + ga * (y[k] + hx * dsk);
      }
      wt_store16(make_rsrc(a.x1T, D * L * 2), (u32)(d * L + 8 * tj) * 2, u32x4{pk_bf16(o[0], o[1]), pk_bf16(o[2], o[3]), pk_bf16(o[4], o[5]), pk_bf16(o[6], o[7])});
    }
    STAMP(1, 5);
  } else {
#define B_SIDE_PRIO 3
    __builtin_amdgcn_s_setprio(B_SIDE_PRIO);
    const int st = tid - 512, sw = w - 8;
    if (st < 8) *(u32*)(smem + B_CNT + 4 * st) = 0;
    asm volatile("s_waitcnt lgkmcnt(0)" ::: "memory");
    __builtin_amdgcn_s_barrier();
    SCHED_FENCE();
    float* red = (float*)(smem + B_RED);
    const bool act = wg < 224;
    const int dq = wg / 56, f0 = 32 * (wg - 56 * dq);
    const int d8 = 16 * dq + (st >> 4), fp = st & 15;
    const int f8 = wg >> 1, dp = (wg & 1) * 128 + (st & 127);
    u32 wv[8], ov[8];
    f32x4 cb0, cb1, sb0, sb1;
    if (act) {
#pragma unroll
      for (int j = 0; j < 8; ++j) wv[j] = *(const u32*)(a.win + (size_t)(8 * d8 + j) * F + f0 + 2 * fp);
      cb0 = mod_get4(a.mod, 2048 + 8 * d8); cb1 = mod_get4(a.mod, 2048 + 8 * d8 + 4);
      sb0 = mod_get4(a.mod, 1536 + 8 * d8); sb1 = mod_get4(a.mod, 1536 + 8 * d8 + 4);
      if (st < 128) {
#pragma unroll
        for (int j = 0; j < 8; ++j) ov[j] = *(const u32*)(a.wout + (size_t)(8 * f8 + j) * D + 2 * dp);
      }
    }
    u32x4 pf[4];
    {
#pragma unroll
      for (int j = 0; j < 4; ++j) {
        const int idx = st + 256 * j;
        pf[j] = *(const u32x4*)(a.wsp + (size_t)(8 * wg + (idx >> 7)) * L + ((idx >> 5) & 3) * 512 + 256 + (idx & 31) * 8);
      }
    }
    SCHED_FENCE();
    if (act) {
      float lo[8], hi[8], u0 = 0, u1 = 0, v0 = 0, v1 = 0;
#pragma unroll
      for (int j = 0; j < 8; ++j) {
        const float Ac = rbf(1.f + (j < 4 ? cb0[j] : cb1[j - 4])), sb = j < 4 ? sb0[j] : sb1[j - 4];
        const float wl = bf_lo(wv[j]), wh = bf_hi(wv[j]);
        lo[j] = rbf(Ac * wl); hi[j] = rbf(Ac * wh);
        u0 += lo[j]; u1 += hi[j]; v0 += sb * wl; v1 += sb * wh;
      }
      u32x4* stg = (u32x4*)(smem + B_STG + sw * 2048);
      stg[2 * lane] = u32x4{pk_bf16(lo[0], lo[1]), pk_bf16(lo[2], lo[3]), pk_bf16(lo[4], lo[5]), pk_bf16(lo[6], lo[7])};
      stg[2 * lane + 1] = u32x4{pk_bf16(hi[0], hi[1]), pk_bf16(hi[2], hi[3]), pk_bf16(hi[4], hi[5]), pk_bf16(hi[6], hi[7])};
      const rsrc_t rw = make_rsrc(a.Wt, D * F * 2);
#pragma unroll
      for (int k = 0; k < 2; ++k) {
        const int pc = 64 * k + lane, dd8 = 16 * dq + 4 * sw + (pc >> 5), col = f0 + (pc & 31);
        wt_store16(rw, (u32)((((col >> 4) * 64 + dd8) * 16 + (col & 15)) * 16), stg[pc]);
      }
      float rv[4] = {u0, u1, v0, v1};
#pragma unroll
      for (int k = 0; k < 4; ++k) {
        const float v = xor32_sum(xor16_sum(rv[k]));
        if (lane < 16) red[sw * 64 + lane * 4 + k] = v;
      }
      if (st < 128) {
        u32x4 e, o;
#pragma unroll
        for (int k = 0; k < 4; ++k) {
          e[k] = (ov[2 * k] & 0xffffu) | (ov[2 * k + 1] << 16);
          o[k] = (ov[2 * k] >> 16) | (ov[2 * k + 1] & 0xffff0000u);
        }
        stg[2 * lane] = e; stg[2 * lane + 1] = o;
        const rsrc_t ro = make_rsrc(a.Wot, FH * D * 2);
#pragma unroll
        for (int k = 0; k < 2; ++k) {
          const int col = (wg & 1) * 256 + 128 * sw + 64 * k + lane;
          wt_store16(ro, (u32)((((col >> 4) * 112 + f8) * 16 + (col & 15)) * 16), stg[64 * k + lane]);
        }
      }
      lds_rendezvous((u32 LAS*)(smem + B_CNT + 8), 4);
      if (st < 64) {
        const float s = (red[st] + red[64 + st]) + (red[128 + st] + red[192 + st]);
        const int fq = st >> 2, k = st & 3;
        a.uv[(dq * 2 + (k >> 1)) * F + f0 + 2 * fq + (k & 1)] = s;
      }
    }
    u32x4 cf;
    {
      u64 base;
      asm volatile("s_getpc_b64 s[40:41]\n\ts_add_u32 s40, s40, _Z2kF5FArgs@rel32@lo+4\n\ts_addc_u32 s41, s41, _Z2kF5FArgs@rel32@hi+12\n\ts_mov_b64 %0, s[40:41]"
                   : "=s"(base) : : "s40", "s41", "scc");
      cf = *(const u32x4*)((const char*)base + (size_t)(st < F_CODE_LANES ? (wg >> 3) * F_CODE_LANES + st : 0) * 16);
    }
    STAMP(1, 1);
    asm volatile("" ::"v"(cf));
#pragma unroll
    for (int j = 0; j < (int)(sizeof(pf) / sizeof(pf[0])); ++j) asm volatile("" ::"v"(pf[j]));
    STAMP(1, 5);
  }
}
struct FArgs {
  const u16 *x1T, *Wt, *wsp, *bsp, *Wot; const float *uv, *mod;
  u16 *z2t, *Pt, *out; u64* gran; u32* flags;
};
constexpr int F_X = 0;
constexpr int F_ST = 65536;
constexpr int F_MR = F_ST + 512;
constexpr int F_PS = F_MR + 8 * 512;
constexpr int F_FS = F_PS + 7 * 64 * 8;
constexpr int F_ZS = F_FS + 512;
constexpr int F_RS = 232;
constexpr int F_A0 = 90112;
constexpr int F_PF = F_A0 + 8 * 8192;
constexpr int F_LDS = F_PF + 1024;
constexpr int F_EX = 112 * 1024;
#define F_SLEEP0 16
#define F_SLEEP1 8
#define F_BDEPTH 3
constexpr int F_BD = F_BDEPTH;
constexpr int F_BR = F_BD;
static_assert(F_ZS + 64 * F_RS <= F_A0 && F_EX + 64 * 132 <= F_LDS, "LDS map");

__device__ __forceinline__ float gelu_tanh(float z) {
  const float k0 = -2.f * 0.7978845608028654f * 1.4426950408889634f, k1 = k0 * 0.044715f;
  const float e = __builtin_amdgcn_exp2f(z * (k0 + k1 * (z * z)));
  return z * __builtin_amdgcn_rcpf(1.f + e);
}
__device__ __forceinline__ float pick4(f32x4 v, int r) { return r == 0 ? v[0] : r == 1 ? v[1] : r == 2 ? v[2] : v[3]; }
template <int N> __device__ __forceinline__ void vm_wait_c() { asm volatile("s_waitcnt vmcnt(%0)" ::"i"(N > 63 ? 63 : N) : "memory"); }
__device__ __forceinline__ u32 f_slot(int kq, int sg) { return (u32)(((sg & 2) ? 0 : F_A0) + (kq * 2 + (sg & 1)) * 8192); }
__device__ __forceinline__ bf16x8 ld_dev16(rsrc_t r, u32 off) { return __builtin_bit_cast(bf16x8, __builtin_amdgcn_raw_buffer_load_b128(r, off, 0, 16)); }
__device__ __forceinline__ void f_lda(const u16* const (&pa)[2], u32 lds0, int kq, int nh, int sg) {
#pragma unroll
  for (int i = 0; i < 4; ++i) glds16(pa[i & 1] + (size_t)(i >> 1) * 16 * L + 64 * sg, lds0 + f_slot(kq, sg) + (4 * nh + i) * 1024);
}
__device__ __forceinline__ void f_lda_full(const u16* wsp, u32 lds0, int pi, int kq, int sg) {
  const int lane = threadIdx.x & 63;
#pragma unroll
  for (int i = 0; i < 8; ++i) {
    const int row = 8 * i + (lane >> 3);
    glds16(wsp + (size_t)(64 * pi + row) * L + 512 * kq + 64 * sg + 8 * ((lane & 7) ^ ((row >> 1) & 7)), lds0 + f_slot(kq, sg) + i * 1024);
  }
}
__device__ __forceinline__ void f_ldx(const u16* x1T, u32 lds0, int w, int pi, int sg) {
  const int lane = threadIdx.x & 63;
#pragma unroll
  for (int i = 0; i < 2; ++i) {
    const int d = 128 * sg + 16 * w + 8 * i + (lane >> 3);
    const int fz = (((d >> 1) & 1) | ((d >> 2) & 2)) << 1;
    glds16(x1T + (size_t)d * L + 64 * pi + 8 * ((lane & 7) ^ fz), lds0 + F_X + (128 * sg + 16 * w + 8 * i) * 128);
  }
}
__device__ __forceinline__ void wait_flags(const u32* p, int n) {
  const int lane = threadIdx.x & 63;
  for (u32 spin = 0; spin < (1u << 22); ++spin) {
    const u32 v = __hip_atomic_load((GAS u32*)p + (lane < n ? lane : 0), __ATOMIC_RELAXED, __HIP_MEMORY_SCOPE_AGENT);
    if (__all(v == 1u)) break;
    __builtin_amdgcn_s_sleep(1);
  }
}

__device__ __forceinline__ u32 poll8(const u32* p, int n) {
  const int lane = threadIdx.x & 63;
  const u32 v = __hip_atomic_load((GAS u32*)p + (lane < n ? lane : 0), __ATOMIC_RELAXED, __HIP_MEMORY_SCOPE_AGENT);
  return (u32)__ballot(v == 1u) & 0xffu;
}
__device__ __forceinline__ void need_flag(const u32* p, int n, u32& mask, int k) {
  if (!((mask >> k) & 1u)) {
    for (u32 spin = 0; spin < (1u << 22); ++spin) {
      __builtin_amdgcn_s_sleep(F_SLEEP1);
      mask = poll8(p, n);
      if ((mask >> k) & 1u) break;
    }
  }
  asm volatile("" ::: "memory");
}

struct Z1 { f32x4 y[4]; };

template <int ROLE>
__device__ __forceinline__ void f_phase_c(const FArgs& a, char* smem, const int w, const int pi, const int j, const u16* const (&pa)[2], Z1& z1) {
  const int tid = threadIdx.x, lane = tid & 63;
  const int c = lane & 15, g = lane >> 4;
  const u32 lds0 = (u32)(size_t)(const char LAS*)smem;
  const int nt2 = 56 + 7 * j + (w < 7 ? w : 0), nt1 = 7 * j + (w > 0 ? w - 1 : 0);
  const u16* pw2 = a.Wt + (size_t)nt2 * 64 * 128 + lane * 8;
  const u16* pw1 = a.Wt + (size_t)nt1 * 64 * 128 + lane * 8;
  const int q4 = (lane & 15) >> 2, p4 = lane & 3;
  const int drow = 8 * g + q4;
  const int fz0 = (((drow >> 1) & 1) | ((drow >> 2) & 2)) << 1;
  const char LAS* xb = (const char LAS*)smem + F_X + drow * 128 + 8 * (p4 & 1);
  const char LAS* xt[4];
  int tb[4];
#pragma unroll
  for (int s = 0; s < 4; ++s) { tb[s] = (s + w) & 3; xt[s] = xb + 16 * ((2 * tb[s] + (p4 >> 1)) ^ fz0); }
  auto xfrag = [&](int s, int ks) -> bf16x8 {
    const char LAS* p = xt[s] + ks * 4096;
    const s16x4 lo = tr_read(p), hi = tr_read(p + 512);
    const u32x2 a0 = __builtin_bit_cast(u32x2, lo), a1 = __builtin_bit_cast(u32x2, hi);
    return __builtin_bit_cast(bf16x8, u32x4{a0[0], a0[1], a1[0], a1[1]});
  };
  f32x4 acc[4];
#pragma unroll
  for (int s = 0; s < 4; ++s) acc[s] = f32x4{0, 0, 0, 0};
  bf16x8 W1[16];
  f32x4 u2, v2;
  UV4 ru1, rv1;
  float* xs2 = (float*)(smem + F_ST);
  if constexpr (ROLE < 2) {
    bf16x8 W2[16];
#pragma unroll
    for (int ks = 0; ks < 4; ++ks) W2[ks] = *(const bf16x8*)(pw2 + 4 * ks * 128);
    SCHED_FENCE();
    f_ldx(a.x1T, lds0, w, pi, 1);
#pragma unroll
    for (int ks = 4; ks < 8; ++ks) W2[ks] = *(const bf16x8*)(pw2 + 4 * ks * 128);
    SCHED_FENCE();
    VM_WAIT(10);
    __builtin_amdgcn_s_barrier();
    SCHED_FENCE();
    STAMP(2, 1);
    f32x4 sq = {0, 0, 0, 0};
    UV4 ru2, rv2;
    bf16x8 X[2][4];
#pragma unroll
    for (int s = 0; s < 4; ++s) X[0][s] = xfrag(s, 0);
#pragma unroll
    for (int ks = 0; ks < 16; ++ks) {
      if (ks == 3) { VM_WAIT(9); __builtin_amdgcn_s_barrier(); }
      if (ks == 7) { VM_WAIT(9); __builtin_amdgcn_s_barrier(); }
      if (ks == 11) { VM_WAIT(4); __builtin_amdgcn_s_barrier(); }
      if (ks == 0) f_ldx(a.x1T, lds0, w, pi, 2);
      if (ks == 4) f_ldx(a.x1T, lds0, w, pi, 3);
      if (ks < 8) W2[8 + ks] = *(const bf16x8*)(pw2 + 4 * (8 + ks) * 128);
      if (ks == 13) { ru2 = uv_ld(a.uv, 0, 16 * nt2 + 4 * g); rv2 = uv_ld(a.uv, 1, 16 * nt2 + 4 * g); }
      SCHED_FENCE();
#pragma unroll
      for (int s = 0; s < 4; ++s) {
        if (ks + 1 < 16) X[(ks + 1) & 1][s] = xfrag(s, ks + 1);
        acc[s] = __builtin_amdgcn_mfma_f32_16x16x32_bf16(W2[ks], X[ks & 1][s], acc[s], 0, 0, 0);
        if (s == 3) sq = __builtin_amdgcn_mfma_f32_16x16x32_bf16(X[ks & 1][0], X[ks & 1][0], sq, 0, 0, 0);
        SCHED_FENCE();
      }
    }
    u2 = uv_sum(ru2); v2 = uv_sum(rv2);
    if (w < 4 && (c >> 2) == g) xs2[64 + 16 * w + c] = pick4(sq, c & 3);
  } else {
    f_ldx(a.x1T, lds0, w, pi, 1);
    VM_WAIT(2);
    __builtin_amdgcn_s_barrier();
    SCHED_FENCE();
    STAMP(2, 1);
    const u32x4 ones = {0x3f803f80u, 0x3f803f80u, 0x3f803f80u, 0x3f803f80u};
    bf16x8 X[2][4];
#pragma unroll
    for (int s = 0; s < 4; ++s) X[0][s] = xfrag(s, 0);
#pragma unroll
    for (int ks = 0; ks < 16; ++ks) {
      if (ks == 3) { VM_WAIT(2); __builtin_amdgcn_s_barrier(); }
      if (ks == 7) { VM_WAIT(2); __builtin_amdgcn_s_barrier(); }
      if (ks == 11) { VM_WAIT(6); __builtin_amdgcn_s_barrier(); }
      if (ks == 0) f_ldx(a.x1T, lds0, w, pi, 2);
      if (ks == 4) f_ldx(a.x1T, lds0, w, pi, 3);
      if (ks >= 8) { W1[2 * ks - 16] = *(const bf16x8*)(pw1 + 4 * (2 * ks - 16) * 128); W1[2 * ks - 15] = *(const bf16x8*)(pw1 + 4 * (2 * ks - 15) * 128); }
      SCHED_FENCE();
#pragma unroll
      for (int s = 0; s < 4; ++s) {
        if (ks + 1 < 16) X[(ks + 1) & 1][s] = xfrag(s, ks + 1);
        acc[s] = __builtin_amdgcn_mfma_f32_16x16x32_bf16(__builtin_bit_cast(bf16x8, ones), X[ks & 1][s], acc[s], 0, 0, 0);
        SCHED_FENCE();
      }
    }
    if (g == 0) {
#pragma unroll
      for (int s = 0; s < 4; ++s) xs2[16 * tb[s] + c] = acc[s][0];
    }
  }
  STAMP(2, 2);
  __syncthreads();
  STAMP(2, 3);
  float mu[4], ri[4];
  {
    const float mean = xs2[lane] * (1.f / D);
    const float var = fmaxf(xs2[64 + lane] * (1.f / D) - mean * mean, 0.f);
    f32x2* mr = (f32x2*)(smem + F_MR + w * 512);
    mr[lane] = f32x2{rbf(mean), __builtin_amdgcn_rcpf(rbf(__builtin_amdgcn_sqrtf(rbf(rbf(var) + 1e-5f))))};
#pragma unroll
    for (int s = 0; s < 4; ++s) { const f32x2 v = mr[16 * tb[s] + c]; mu[s] = v[0]; ri[s] = v[1]; }
  }
  u32x2 z2p[4];
  if constexpr (ROLE < 2) {
    float* ps = (float*)(smem + F_PS) + w * 128;
    const s16x4 ones4 = {0x3f80, 0x3f80, 0x3f80, 0x3f80};
#pragma unroll
    for (int s = 0; s < 4; ++s) {
      if constexpr (ROLE == 1) {
#pragma unroll
        for (int ks = 4 * s; ks < 4 * s + 4; ++ks) W1[ks] = *(const bf16x8*)(pw1 + 4 * ks * 128);
        SCHED_FENCE();
      }
      float z[4];
#pragma unroll
      for (int r = 0; r < 4; ++r) z[r] = gelu_tanh(ri[s] * (acc[s][r] - mu[s] * u2[r]) + v2[r]);
      z2p[s] = u32x2{pk_bf16(z[0], z[1]), pk_bf16(z[2], z[3])};
      const s16x4 zz = __builtin_bit_cast(s16x4, z2p[s]);
      const f32x4 zero = {0, 0, 0, 0};
      const f32x4 s1 = __builtin_amdgcn_mfma_f32_16x16x16bf16_1k(ones4, zz, zero, 0, 0, 0);
      const f32x4 s2 = __builtin_amdgcn_mfma_f32_16x16x16bf16_1k(zz, zz, zero, 0, 0, 0);
      if ((c >> 2) == g) *(f32x2*)(ps + (16 * tb[s] + c) * 2) = f32x2{s1[0], pick4(s2, c & 3)};
      SCHED_FENCE();
    }
  }
  if constexpr (ROLE > 0) { f_lda(pa, lds0, w & 3, w >> 2, 0); f_lda(pa, lds0, w & 3, w >> 2, 1); }
  STAMP(2, 4);
  __syncthreads();
  STAMP(2, 5);
  if constexpr (ROLE == 0) {
    const float* ps = (const float*)(smem + F_PS);
    float s1 = 0, s2 = 0;
#pragma unroll
    for (int k = 0; k < 7; ++k) { const f32x2 v = *(const f32x2*)(ps + k * 128 + lane * 2); s1 += v[0]; s2 += v[1]; }
    const rsrc_t rg = make_rsrc(a.gran, 256 * 1024);
    const u32 goff = (u32)(pi * 8 * 64 + lane) * 16;
    wt_store16(rg, goff + (u32)j * 1024, u32x4{__float_as_uint(s1), 1u, __float_as_uint(s2), 1u});
    float t1 = 0, t2 = 0;
    for (u32 spin = 0; spin < (1u << 22); ++spin) {
      u32x4 x[8];
#pragma unroll
      for (int k = 0; k < 8; ++k) x[k] = __builtin_amdgcn_raw_buffer_load_b128(rg, goff + k * 1024, 0, 16);
      bool ok = true; t1 = 0; t2 = 0;
#pragma unroll
      for (int k = 0; k < 8; ++k) {
        ok = ok && x[k][1] == 1u && x[k][3] == 1u;
        t1 += __uint_as_float(x[k][0]); t2 += __uint_as_float(x[k][2]);
      }
      if (__all(ok)) break;
      __builtin_amdgcn_s_sleep(1);
    }
    const float mean = t1 * (1.f / FH);
    const float var = fmaxf(t2 * (1.f / FH) - mean * mean, 0.f);
    *(f32x2*)(smem + F_FS + lane * 8) = f32x2{rbf(mean), __builtin_amdgcn_rcpf(rbf(__builtin_amdgcn_sqrtf(rbf(rbf(var) + 1e-5f))))};
    f_lda(pa, lds0, w & 3, w >> 2, 0);
    f_lda(pa, lds0, w & 3, w >> 2, 1);
  } else {
#pragma unroll
    for (int s = 0; s < 4; ++s) acc[s] = f32x4{0, 0, 0, 0};
    bf16x8 X[2][4];
#pragma unroll
    for (int s = 0; s < 4; ++s) X[0][s] = xfrag(s, 0);
#pragma unroll
    for (int ks = 0; ks < 16; ++ks) {
      if (ks == 13) { ru1 = uv_ld(a.uv, 0, 16 * nt1 + 4 * g); rv1 = uv_ld(a.uv, 1, 16 * nt1 + 4 * g); SCHED_FENCE(); }
#pragma unroll
      for (int s = 0; s < 4; ++s) {
        if (ks + 1 < 16) X[(ks + 1) & 1][s] = xfrag(s, ks + 1);
        acc[s] = __builtin_amdgcn_mfma_f32_16x16x32_bf16(W1[ks], X[ks & 1][s], acc[s], 0, 0, 0);
        SCHED_FENCE();
      }
    }
  }
  STAMP(2, 6);
  __syncthreads();
  STAMP(2, 7);
  if constexpr (ROLE < 2) {
#pragma unroll
    for (int s = 0; s < 4; ++s) {
      const f32x2 fs = *(const f32x2*)(smem + F_FS + (16 * tb[s] + c) * 8);
      const float a0 = (bf_lo(z2p[s][0]) - fs[0]) * fs[1], a1 = (bf_hi(z2p[s][0]) - fs[0]) * fs[1];
      const float a2 = (bf_lo(z2p[s][1]) - fs[0]) * fs[1], a3 = (bf_hi(z2p[s][1]) - fs[0]) * fs[1];
      *(u32x2*)(smem + F_ZS + (16 * tb[s] + c) * F_RS + (16 * w + 4 * g) * 2) = u32x2{pk_bf16(a0, a1), pk_bf16(a2, a3)};
    }
#pragma unroll
    for (int kk = 0; kk < 2; ++kk) {
      const char LAS* p = (const char LAS*)smem + F_ZS + (32 * kk + 8 * g + q4) * F_RS + (16 * w + 4 * p4) * 2;
      const s16x4 lo = tr_read(p), hi = tr_read(p + 4 * F_RS);
      const u32x2 a0 = __builtin_bit_cast(u32x2, lo), a1 = __builtin_bit_cast(u32x2, hi);
      wt_store16(make_rsrc(a.z2t, L * FH * 2), (u32)((((7 * j + w) * 256 + 8 * pi + 4 * kk) * 64 + lane * 4) * 4), u32x4{a0[0], a0[1], a1[0], a1[1]});
    }
  }
  VM_WAIT(0);
  __syncthreads();
  if (tid == 0) __hip_atomic_store((GAS u32*)a.flags + j * 32 + pi, 1u, __ATOMIC_RELAXED, __HIP_MEMORY_SCOPE_AGENT);
  STAMP(2, 8);
  if constexpr (ROLE > 0) {
    const f32x4 u1 = uv_sum(ru1), v1 = uv_sum(rv1);
#pragma unroll
    for (int s = 0; s < 4; ++s)
#pragma unroll
      for (int r = 0; r < 4; ++r) z1.y[s][r] = ri[s] * (acc[s][r] - mu[s] * u1[r]) + v1[r];
  } else {
#pragma unroll
    for (int s = 0; s < 4; ++s) z1.y[s] = f32x4{0, 0, 0, 0};
  }
  STAMP(2, 9);
}

template <int NT> constexpr int fd_glds(int sg) { return (sg == 2 || sg == 4) ? 8 : 0; }
template <int NT> constexpr int fd_bld(int sg) { return sg < 0 ? 0 : (2 * sg + F_BD < 16 ? NT : 0) + (2 * sg + 1 + F_BD < 16 ? NT : 0); }
template <int NT> constexpr int fd_ops(int sg) { return fd_glds<NT>(sg) + fd_bld<NT>(sg); }

template <int NT, int SG>
__device__ __forceinline__ void fd_stage(f32x4 (&acc)[4][NT], bf16x8 (&Bf)[F_BR][NT], bf16x8 (&Af)[2][4], const rsrc_t rb, u32 boff,
                                         const u16* const (&pa)[2], u32 lds0, const char* const (&ap)[2][2][4], int kq, int nh,
                                         const u32* fl, u32& mask, const Z1& z1, char* zdst, int w) {
  if constexpr ((SG & 1) == 0 && SG > 0) {
    if constexpr (SG == 2) vm_wait_c<F_BD * NT + fd_ops<NT>(0) + fd_ops<NT>(1)>();
    else vm_wait_c<fd_bld<NT>(SG - 2) + fd_ops<NT>(SG - 1)>();
    __builtin_amdgcn_s_barrier();
    if constexpr (fd_glds<NT>(SG) > 0) { f_lda(pa, lds0, kq, nh, SG + 2); f_lda(pa, lds0, kq, nh, SG + 3); }
#pragma unroll
    for (int m = 0; m < 4; ++m) Af[0][m] = *(const bf16x8*)(ap[(SG >> 1) & 1][0][m]);
  }
#pragma unroll
  for (int kk = 0; kk < 2; ++kk) {
    constexpr int ks0 = 2 * SG;
    if (kk == 0 || (SG & 1) == 0) {
#pragma unroll
      for (int m = 0; m < 4; ++m) Af[(kk + 1) & 1][m] = *(const bf16x8*)(ap[(SG >> 1) & 1][(kk + 1) & 1][m] + (kk == 0 ? (SG & 1) : 1) * 8192);
    }
    SCHED_FENCE();
#pragma unroll
    for (int n = 0; n < NT; ++n) {
#pragma unroll
      for (int m = 0; m < 4; ++m) acc[m][n] = __builtin_amdgcn_mfma_f32_16x16x32_bf16(Bf[(ks0 + kk) % F_BR][n], Af[kk][m], acc[m][n], 0, 0, 0);
      SCHED_FENCE();
      if (ks0 + kk + F_BD < 16) {
        if (n == 0 && ((ks0 + kk + F_BD) & 1) == 0) need_flag(fl, 8, mask, (ks0 + kk + F_BD) >> 1);
        Bf[(ks0 + kk + F_BD) % F_BR][n] = ld_dev16(rb, boff + (u32)((n * 256 + 4 * (ks0 + kk + F_BD)) * 256));
      }
      if constexpr (SG < 4) {
        if (kk == 0 && n == 1 && w > 0) {
          float z[4];
#pragma unroll
          for (int r = 0; r < 4; ++r) z[r] = gelu_tanh(z1.y[SG][r]);
          *(u32x2*)(zdst + ((SG + w) & 3) * 16 * F_RS) = u32x2{pk_bf16(z[0], z[1]), pk_bf16(z[2], z[3])};
        }
      }
      SCHED_FENCE();
    }
  }
}

template <int NT>
__device__ __forceinline__ void f_phase_d(const FArgs& a, char* smem, const int w, const int rt, const int ct, const u16* const (&pa)[2], const Z1& z1) {
  const int tid = threadIdx.x, lane = tid & 63;
  const int kq = w & 3, nh = w >> 2, n0 = nh * 4;
  const int c = lane & 15, g = lane >> 4;
  const u32 lds0 = (u32)(size_t)(const char LAS*)smem;
  const rsrc_t rb = make_rsrc(a.z2t, L * FH * 2);
  const u32 boff = (u32)((((7 * ct + n0) * 256 + 64 * kq) * 64 + lane * 4) * 4);
  f_lda(pa, lds0, kq, nh, 2);
  f_lda(pa, lds0, kq, nh, 3);
  const u32* fl = a.flags + ct * 32 + 8 * kq;
  __builtin_amdgcn_s_sleep(F_SLEEP0);
  u32 mask = poll8(fl, 8);
  bf16x8 Bf[F_BR][NT], Af[2][4];
#pragma unroll
  for (int ks = 0; ks < F_BD; ++ks) {
    if ((ks & 1) == 0) need_flag(fl, 8, mask, ks >> 1);
#pragma unroll
    for (int n = 0; n < NT; ++n) Bf[ks][n] = ld_dev16(rb, boff + (u32)((n * 256 + 4 * ks) * 256));
  }
  SCHED_FENCE();
  f32x4 acc[4][NT];
#pragma unroll
  for (int m = 0; m < 4; ++m)
#pragma unroll
    for (int n = 0; n < NT; ++n) acc[m][n] = f32x4{0, 0, 0, 0};
  const int asw = (c >> 1) & 7;
  const char* ap[2][2][4];
#pragma unroll
  for (int hi = 0; hi < 2; ++hi)
#pragma unroll
    for (int m = 0; m < 4; ++m) {
      const char* base = smem + f_slot(kq, 2 * hi) + ((m + kq) & 3) * 2048 + c * 128;
      ap[hi][0][m] = base + 16 * (g ^ asw); ap[hi][1][m] = base + 16 * ((4 + g) ^ asw);
    }
#pragma unroll
  for (int m = 0; m < 4; ++m) Af[0][m] = *(const bf16x8*)(ap[0][0][m]);
  char* zdst = smem + F_ZS + c * F_RS + (16 * (w - 1) + 4 * g) * 2;
  STAMP(3, 4);
  fd_stage<NT, 0>(acc, Bf, Af, rb, boff, pa, lds0, ap, kq, nh, fl, mask, z1, zdst, w);
  fd_stage<NT, 1>(acc, Bf, Af, rb, boff, pa, lds0, ap, kq, nh, fl, mask, z1, zdst, w);
  STAMP(3, 5);
  fd_stage<NT, 2>(acc, Bf, Af, rb, boff, pa, lds0, ap, kq, nh, fl, mask, z1, zdst, w);
  fd_stage<NT, 3>(acc, Bf, Af, rb, boff, pa, lds0, ap, kq, nh, fl, mask, z1, zdst, w);
  STAMP(3, 6);
  fd_stage<NT, 4>(acc, Bf, Af, rb, boff, pa, lds0, ap, kq, nh, fl, mask, z1, zdst, w);
  fd_stage<NT, 5>(acc, Bf, Af, rb, boff, pa, lds0, ap, kq, nh, fl, mask, z1, zdst, w);
  STAMP(3, 7);
  fd_stage<NT, 6>(acc, Bf, Af, rb, boff, pa, lds0, ap, kq, nh, fl, mask, z1, zdst, w);
  if (w < 7) {
    const int wg = blockIdx.x;
    const u16* p = a.Wot + (size_t)(wg & 1) * 16 * 112 * 128 + (size_t)(wg >> 3) * 7168 + w * 1024 + lane * 8;
    glds16(p, lds0 + F_PF); glds16(p + 512, lds0 + F_PF);
  }
  const float bs = bf1(a.bsp[64 * rt + 16 * kq + c]);
  fd_stage<NT, 7>(acc, Bf, Af, rb, boff, pa, lds0, ap, kq, nh, fl, mask, z1, zdst, w);
  STAMP(3, 1);
  __syncthreads();
#pragma unroll
  for (int m = 1; m < 4; ++m) {
    const int o = (m + kq) & 3;
    char* dst = smem + o * 10752 + ((m - 1) * 7 * 64 + n0 * 64 + lane) * 8;
#pragma unroll
    for (int n = 0; n < NT; ++n) *(u32x2*)(dst + n * 512) = u32x2{pk_bf16(acc[m][n][0], acc[m][n][1]), pk_bf16(acc[m][n][2], acc[m][n][3])};
  }
  __syncthreads();
  STAMP(3, 2);
  const char* src = smem + kq * 10752 + (n0 * 64 + lane) * 8;
  const char* zs = smem + F_ZS + (16 * kq + c) * F_RS + (16 * n0 + 4 * g) * 2;
  const rsrc_t rp = make_rsrc(a.Pt, L * FH * 2);
  u32x2 pv[NT];
#pragma unroll
  for (int n = 0; n < NT; ++n) {
    const u32x2 p0 = *(const u32x2*)(src + n * 512), p1 = *(const u32x2*)(src + (7 + n) * 512), p2 = *(const u32x2*)(src + (14 + n) * 512);
    const u32x2 zz = *(const u32x2*)(zs + n * 32);
    float gg[4];
#pragma unroll
    for (int e = 0; e < 2; ++e) {
      gg[2 * e] = ((acc[0][n][2 * e] + bf_lo(p0[e])) + (bf_lo(p1[e]) + bf_lo(p2[e]))) + bs;
      gg[2 * e + 1] = ((acc[0][n][2 * e + 1] + bf_hi(p0[e])) + (bf_hi(p1[e]) + bf_hi(p2[e]))) + bs;
    }
    pv[n] = u32x2{pk_bf16(bf_lo(zz[0]) * gg[0], bf_hi(zz[0]) * gg[1]), pk_bf16(bf_lo(zz[1]) * gg[2], bf_hi(zz[1]) * gg[3])};
    if (n & 1) wt_store16(rp, (u32)((((4 * rt + kq) * 112 + 14 * ct + 2 * (n0 + n - 1 + (g & 1)) + (g >> 1)) * 16 + c) * 16), pair16(pv[n - 1], pv[n]));
  }
  if constexpr (NT & 1) {
    const u32x4 v = pair16(pv[NT - 1], pv[NT - 1]);
    if ((g & 1) == 0) wt_store16(rp, (u32)((((4 * rt + kq) * 112 + 14 * ct + 2 * (n0 + NT - 1) + (g >> 1)) * 16 + c) * 16), v);
  }
  STAMP(3, 3);
}

__device__ __forceinline__ void f_phase_e(const FArgs& a, char* smem, const int w, const int rt, const int dt) {
  const int tid = threadIdx.x, lane = tid & 63;
  const int kq = w & 3, mh = w >> 2;
  const int c = lane & 15, g = lane >> 4;
  const u32 lds0 = (u32)(size_t)(const char LAS*)smem;
  VM_WAIT(0);
  __syncthreads();
  if (tid == 0) __hip_atomic_store((GAS u32*)a.flags + 256 + rt * 32 + dt, 1u, __ATOMIC_RELAXED, __HIP_MEMORY_SCOPE_AGENT);
  STAMP(4, 5);
#pragma unroll
  for (int k = 0; k < 7; ++k)
#pragma unroll
    for (int nn = 0; nn < 2; ++nn) {
      const int n = 2 * mh + nn;
      glds16(a.Wot + ((size_t)(4 * dt + n) * 112 + 4 * (7 * kq + k)) * 128 + lane * 8, lds0 + (n * 28 + 7 * kq + k) * 1024);
    }
  const u32x4 xr = *(const u32x4*)(a.x1T + (size_t)(64 * dt + (tid >> 3)) * L + 64 * rt + 8 * (tid & 7));
  const f32x4 gbv = mod_get4(a.mod, 2560 + 64 * dt + 16 * kq + 4 * g);
  SCHED_FENCE();
  {
    u32* p = (u32*)(smem + F_EX + (tid >> 3) * 132 + (tid & 7) * 16);
    p[0] = xr[0]; p[1] = xr[1]; p[2] = xr[2]; p[3] = xr[3];
  }
  VM_WAIT(0);
  __builtin_amdgcn_s_barrier();
  SCHED_FENCE();
  bf16x8 Wf[7][4];
  const char* wp = smem + (7 * kq) * 1024 + lane * 16;
#pragma unroll
  for (int k = 0; k < 7; ++k)
#pragma unroll
    for (int n = 0; n < 4; ++n) Wf[k][n] = *(const bf16x8*)(wp + (((n + kq) & 3) * 28 + k) * 1024);
  SCHED_FENCE();
  {
    const u32* fl = a.flags + 256 + rt * 32 + 2 * kq;
    u32 mask = poll8(fl, 2);
    need_flag(fl, 2, mask, 0); need_flag(fl, 2, mask, 1);
  }
  STAMP(4, 4);
  const rsrc_t rp = make_rsrc(a.Pt, L * FH * 2);
  bf16x8 Pf[7][2];
#pragma unroll
  for (int k = 0; k < 7; ++k)
#pragma unroll
    for (int m = 0; m < 2; ++m) Pf[k][m] = ld_dev16(rp, (u32)((((4 * rt + 2 * mh + m) * 112 + 4 * (7 * kq + k)) * 64 + lane * 4) * 4));
  SCHED_FENCE();
  f32x4 acc[2][4];
#pragma unroll
  for (int m = 0; m < 2; ++m)
#pragma unroll
    for (int n = 0; n < 4; ++n) acc[m][n] = f32x4{0, 0, 0, 0};
#pragma unroll
  for (int k = 0; k < 7; ++k)
#pragma unroll
    for (int n = 0; n < 4; ++n)
#pragma unroll
      for (int m = 0; m < 2; ++m) acc[m][n] = __builtin_amdgcn_mfma_f32_16x16x32_bf16(Wf[k][n], Pf[k][m], acc[m][n], 0, 0, 0);
  STAMP(4, 1);
  __syncthreads();
#pragma unroll
  for (int n = 1; n < 4; ++n)
#pragma unroll
    for (int m = 0; m < 2; ++m)
      *(f32x4*)(smem + (((((n + kq) & 3) * 2 + mh) * 3 + (n - 1)) * 2 + m) * 1024 + lane * 16) = acc[m][n];
  __syncthreads();
  STAMP(4, 2);
  {
    u32x2 ov[2];
#pragma unroll
    for (int m = 0; m < 2; ++m) {
      const char* p = smem + (((kq * 2 + mh) * 3) * 2 + m) * 1024 + lane * 16;
      const f32x4 p0 = *(const f32x4*)p, p1 = *(const f32x4*)(p + 2048), p2 = *(const f32x4*)(p + 4096);
      const char* xp = smem + F_EX + (16 * kq + 4 * g) * 132 + (16 * (2 * mh + m) + c) * 2;
      float o[4];
#pragma unroll
      for (int e = 0; e < 4; ++e) o[e] = bf1(*(const u16*)(xp + e * 132)) + gbv[e] * ((acc[m][0][e] + p0[e]) + (p1[e] + p2[e]));
      ov[m] = u32x2{pk_bf16(o[0], o[1]), pk_bf16(o[2], o[3])};
    }
    *(u32x4*)(a.out + (size_t)(64 * rt + 16 * (2 * mh + (g & 1)) + c) * D + 64 * dt + 16 * kq + 8 * (g >> 1)) = pair16(ov[0], ov[1]);
  }
  STAMP(4, 3);
}

__global__ __launch_bounds__(512, 2) void kF(FArgs a) {
  extern __shared__ __attribute__((aligned(16))) char smem[];
  const int wg = blockIdx.x, lane = threadIdx.x & 63;
  const int w = __builtin_amdgcn_readfirstlane(threadIdx.x >> 6);
  const int xc = wg & 7, ii = wg >> 3;
  const int pi = (xc >> 1) * 8 + (ii & 7), j = (xc & 1) * 4 + (ii >> 3);
  STAMP(2, 0);
  f_ldx(a.x1T, (u32)(size_t)(const char LAS*)smem, w, pi, 0);
  const u16* pa[2];
#pragma unroll
  for (int par = 0; par < 2; ++par) {
    const int row = 32 * (w >> 2) + 8 * par + (lane >> 3);
    pa[par] = a.wsp + (size_t)(64 * pi + row) * L + 512 * (w & 3) + 8 * ((lane & 7) ^ ((row >> 1) & 7));
  }
  Z1 z1;
  if (w == 0) f_phase_c<0>(a, smem, w, pi, j, pa, z1);
  else if (w < 7) f_phase_c<1>(a, smem, w, pi, j, pa, z1);
  else f_phase_c<2>(a, smem, w, pi, j, pa, z1);
  STAMP(3, 0);
  if (w < 4) f_phase_d<4>(a, smem, w, pi, j, pa, z1);
  else f_phase_d<3>(a, smem, w, pi, j, pa, z1);
  STAMP(4, 0);
  f_phase_e(a, smem, w, pi, j);
}
#define KMASK(b)
extern "C" void kernel_launch(void* const* d_in, const int* in_sizes, int n_in, void* d_out, int out_size,
                              void* d_ws, size_t ws_size, hipStream_t stream) {
  const u16* x = (const u16*)d_in[0];
  char* ws = (char*)d_ws;
  float* mod = (float*)(ws + 524288);
  u32* stats1 = (u32*)(ws + 16384);
  float* uv = (float*)(ws + 589824);
  u64* gran = (u64*)(ws + 65536);
  u32* flags = (u32*)(ws + 65536 + 262144);
  u16* xT = (u16*)(ws + (1 << 20));
  u16* Krev = (u16*)(ws + (3 << 20));
  u16* x1T = xT;
  u16* Wt = (u16*)(ws + (7 << 20));
  u16* Wot = (u16*)(ws + (9 << 20));
  u16* Pt = (u16*)(ws + (10 << 20));
  u16* z2t = Krev;
  AArgs aa;
  aa.x = x; aa.Are = (const u16*)d_in[1]; aa.Aim = (const u16*)d_in[2]; aa.Bre = (const u16*)d_in[3]; aa.Bim = (const u16*)d_in[4];
  aa.Cre = (const u16*)d_in[5]; aa.Cim = (const u16*)d_in[6]; aa.logdt = (const u16*)d_in[7]; aa.c = (const u16*)d_in[14];
  aa.wmod = (const u16*)d_in[15]; aa.win = (const u16*)d_in[10]; aa.wout = (const u16*)d_in[13];
  aa.mod = mod; aa.stats1 = stats1; aa.xT = xT; aa.Krev = Krev; aa.gran = (u32x4*)gran;
  (void)hipFuncSetAttribute((const void*)kA, hipFuncAttributeMaxDynamicSharedMemorySize, A_LDS);
  KMASK(1) kA<<<256, 768, A_LDS, stream>>>(aa);
  BArgs ba;
  ba.xT = xT; ba.Krev = Krev; ba.Dskip = (const u16*)d_in[8]; ba.win = (const u16*)d_in[10]; ba.wout = (const u16*)d_in[13];
  ba.wsp = (const u16*)d_in[11]; ba.stats1 = stats1; ba.mod = mod; ba.x1T = x1T; ba.Wt = Wt; ba.Wot = Wot; ba.uv = uv;
  (void)hipFuncSetAttribute((const void*)kB, hipFuncAttributeMaxDynamicSharedMemorySize, B_LDS);
  KMASK(2) kB<<<256, 768, B_LDS, stream>>>(ba);
  FArgs fa;
  fa.x1T = x1T; fa.Wt = Wt; fa.wsp = (const u16*)d_in[11]; fa.bsp = (const u16*)d_in[12]; fa.Wot = Wot; fa.uv = uv; fa.mod = mod;
  fa.z2t = z2t; fa.Pt = Pt; fa.out = (u16*)d_out; fa.gran = gran; fa.flags = flags;
  (void)hipFuncSetAttribute((const void*)kF, hipFuncAttributeMaxDynamicSharedMemorySize, F_LDS);
  KMASK(4) kF<<<256, 512, F_LDS, stream>>>(fa);
}
```
